```python
import torch

def _softmax(x, axis=-1):
    return torch.softmax(x, dim=axis)
def _gelu(x):
    return torch.nn.functional.gelu(x, approximate="tanh")
def _silu(x):
    return torch.nn.functional.silu(x)
def _sigmoid(x):
    return torch.sigmoid(x)
def _layernorm(x, axis=-1, eps=1e-5):
    m = x.mean(dim=axis, keepdim=True)
    v = x.var(dim=axis, keepdim=True, unbiased=False)
    return (x - m) / torch.sqrt(v + eps)
def _scatter_add(tgt, idx, src, axis=0):
    return tgt.clone().index_add_(axis, idx, src)
def _segment_sum(src, seg, n):
    out = torch.zeros((n,) + src.shape[1:], dtype=src.dtype)
    return out.index_add_(0, seg, src)
def _pad(x, pw):
    flat = tuple(v for pair in reversed(pw) for v in pair)
    return torch.nn.functional.pad(x, flat)

kernel_name = 'parallel_sum__linear_kernel_hedgehog__mixer_token_mlp__prompt'

def setup_inputs():

    _w = (lambda a: (lambda t: t.to(torch.bfloat16) if t.is_floating_point() else t)(torch.as_tensor(a)))
    def _A_setup():
        B, S, D, H = 1, 2048, 448, 16
        Dh = D // H
        return {
            "x": _w((lambda *s: torch.randn(*s, dtype=torch.bfloat16) * 0.05)(B, S, D)),
            "wqkv": _w((lambda *s: torch.randn(*s, dtype=torch.bfloat16) * 0.2)(D, 3 * D)),
            "w_phi": _w((lambda *s: torch.randn(*s, dtype=torch.bfloat16) * 0.2)(H, Dh, Dh) * (Dh ** -0.5)),
            "wo": _w((lambda *s: torch.randn(*s, dtype=torch.bfloat16) * 0.05)(D, D)),
        }
    _A_in = _A_setup()

    def _B_setup():
        B, S, D, F = 1, 2048, 448, 1792
        return {
            "x": _w((lambda *s: torch.randn(*s, dtype=torch.bfloat16) * 0.05)(B, S, D)),
            "w1": _w((lambda *s: torch.randn(*s, dtype=torch.bfloat16) * 0.8)(S, F) * (S ** -0.5)),
            "b1": _w((lambda *s: torch.randn(*s, dtype=torch.bfloat16) * 3.2)(F) * 0.02),
            "w2": _w((lambda *s: torch.randn(*s, dtype=torch.bfloat16) * 0.4)(F, S) * (F ** -0.5)),
            "b2": _w((lambda *s: torch.randn(*s, dtype=torch.bfloat16) * 1.6)(S) * 0.02),
        }
    _B_in = _B_setup()

    return {
        "A_x": _A_in["x"],
        "A_wqkv": _A_in["wqkv"],
        "A_w_phi": _A_in["w_phi"],
        "A_wo": _A_in["wo"],
        "B_x": _B_in["x"],
        "B_w1": _B_in["w1"],
        "B_b1": _B_in["b1"],
        "B_w2": _B_in["w2"],
        "B_b2": _B_in["b2"],
    }

def reference(A_x, A_wqkv, A_w_phi, A_wo, B_x, B_w1, B_b1, B_w2, B_b2):
    def _A_ref(x, wqkv, w_phi, wo):
        B, S, D = x.shape
        H = 16; Dh = D // H
        qkv = (lambda x, *d: x.permute(*d))((x @ wqkv).reshape(B, S, 3, H, Dh), 2, 0, 3, 1, 4)
        q, k, v = qkv[0], qkv[1], qkv[2]
        pq = torch.einsum("bhsd,hdf->bhsf", q, w_phi)
        pk = torch.einsum("bhsd,hdf->bhsf", k, w_phi)
        phi_q = torch.concatenate([_softmax(pq, axis=-1), _softmax(-pq, axis=-1)], axis=-1)
        phi_k = torch.concatenate([_softmax(pk, axis=-1), _softmax(-pk, axis=-1)], axis=-1)
        kv = torch.einsum("bhsf,bhsd->bhfd", phi_k, v)
        z  = torch.einsum("bhsf->bhf", phi_k)
        num = torch.einsum("bhqf,bhfd->bhqd", phi_q, kv)
        den = torch.einsum("bhqf,bhf->bhq", phi_q, z)[..., None] + 1e-6
        o = num / den
        return (lambda x, *d: x.permute(*d))(o, 0, 2, 1, 3).reshape(B, S, D) @ wo

    def _B_ref(x, w1, b1, w2, b2):
        B, S, D = x.shape
        h = _layernorm(x, axis=-1)
        u = torch.einsum("bsd,sf->bdf", h, w1) + b1
        u = _gelu(u)
        y = torch.einsum("bdf,fs->bds", u, w2) + b2
        return (lambda x, *d: x.permute(*d))(y, 0, 2, 1)

    x = A_x
    xn = _layernorm(x, axis=-1)
    return x + _A_ref(xn, A_wqkv, A_w_phi, A_wo) + _B_ref(xn, B_w1, B_b1, B_w2, B_b2)
```

```cpp
#include <hip/hip_runtime.h>
#include <hip/hip_bf16.h>
typedef __hip_bfloat16 bf16;
constexpr int S=2048, D=448, H=16, DH=28, F=1792;
__device__ inline float b2f(bf16 v){ return __bfloat162float(v); }
__device__ inline float rb(float v){ return __bfloat162float(__float2bfloat16(v)); }

__global__ void k_ln(const bf16* x, float* xn, float* h){
  int s=blockIdx.x, t=threadIdx.x;
  float v[7]; float sum=0;
  for(int i=0;i<7;i++){ v[i]=b2f(x[s*D+t+64*i]); sum+=v[i]; }
  for(int o=32;o;o>>=1) sum+=__shfl_xor(sum,o);
  float m=sum/D, q=0;
  for(int i=0;i<7;i++){ float d=v[i]-m; q+=d*d; }
  for(int o=32;o;o>>=1) q+=__shfl_xor(q,o);
  float r=1.f/sqrtf(q/D+1e-5f); sum=0;
  for(int i=0;i<7;i++){ v[i]=rb((v[i]-m)*r); xn[s*D+t+64*i]=v[i]; sum+=v[i]; }
  for(int o=32;o;o>>=1) sum+=__shfl_xor(sum,o);
  m=sum/D; q=0;
  for(int i=0;i<7;i++){ float d=v[i]-m; q+=d*d; }
  for(int o=32;o;o>>=1) q+=__shfl_xor(q,o);
  r=1.f/sqrtf(q/D+1e-5f);
  for(int i=0;i<7;i++) h[s*D+t+64*i]=rb((v[i]-m)*r);
}
__global__ void k_qkv(const float* xn,const bf16* w,float* qkv){
  int s=blockIdx.y, n=blockIdx.x*64+threadIdx.x; float a=0;
  for(int k=0;k<D;k++) a+=xn[s*D+k]*b2f(w[k*3*D+n]);
  qkv[s*3*D+n]=rb(a);
}
__global__ __launch_bounds__(64) void k_phi(const float* qkv,const bf16* wphi,float* phi){
  int s=blockIdx.x, t=threadIdx.x; if(t>=32) return; int which=t/16,hh=t%16;
  const float* q=qkv+s*3*D+which*D+hh*DH; float p[DH]; float mx=-1e30f,mn=1e30f;
  #pragma unroll
  for(int f=0;f<DH;f++){ float a=0;
 _Pragma("unroll 1") for(int d=0;d<DH;d++) a+=q[d]*b2f(wphi[(hh*DH+d)*DH+f]); p[f]=rb(a); mx=fmaxf(mx,p[f]); mn=fminf(mn,p[f]); }
  float s1=0,s2=0;
  #pragma unroll
  for(int f=0;f<DH;f++){ s1+=__expf(p[f]-mx); s2+=__expf(mn-p[f]); }
  float* o=phi+((size_t)(which*S+s)*H+hh)*56;
  #pragma unroll
  for(int f=0;f<DH;f++){ o[f]=rb(__expf(p[f]-mx)/s1); o[28+f]=rb(__expf(mn-p[f])/s2); }
}
__global__ void k_kv(const float* qkv,const float* phi,float* kv){
  int hh=blockIdx.x,f=blockIdx.y,e=threadIdx.x; if(e>=29) return; float a=0;
  for(int s=0;s<S;s++){ float pk=phi[((size_t)(S+s)*H+hh)*56+f]; a+=pk*(e<28?qkv[s*3*D+2*D+hh*DH+e]:1.f); }
  kv[(hh*56+f)*29+e]=rb(a);
}
__global__ void k_o(const float* phi,const float* kv,float* o){
  int s=blockIdx.x,j=threadIdx.x; int hh=j/DH,e=j%DH; float n=0,dn=0;
  for(int f=0;f<56;f++){ float p=phi[((size_t)s*H+hh)*56+f]; n+=p*kv[(hh*56+f)*29+e]; dn+=p*kv[(hh*56+f)*29+28]; }
  o[s*D+j]=rb(rb(n)/(rb(dn)+1e-6f));
}
__global__ void k_u(const float* h,const bf16* w1,const bf16* b1,float* u){
  int d=blockIdx.y,f=blockIdx.x*64+threadIdx.x; float a=0;
  for(int s=0;s<S;s++) a+=h[s*D+d]*b2f(w1[s*F+f]);
  a+=b2f(b1[f]);
  float t=tanhf(0.7978845608f*(a+0.044715f*a*a*a));
  u[d*F+f]=rb(0.5f*a*(1.f+t));
}
__global__ void k_out(const bf16* x,const float* o,const bf16* wo,const float* u,const bf16* w2,const bf16* b2,bf16* out){
  int s=blockIdx.x,d=threadIdx.x; float a=0,y=0;
  for(int j=0;j<D;j++) a+=o[s*D+j]*b2f(wo[j*D+d]);
  for(int f=0;f<F;f++) y+=u[d*F+f]*b2f(w2[f*S+s]);
  out[s*D+d]=__float2bfloat16(b2f(x[s*D+d])+a+y+b2f(b2[s]));
}
extern "C" void kernel_launch(void* const* d_in,const int* in_sizes,int n_in,void* d_out,int out_size,void* d_ws,size_t ws_size,hipStream_t stream){
  const bf16* x=(const bf16*)d_in[0]; const bf16* wqkv=(const bf16*)d_in[1]; const bf16* wphi=(const bf16*)d_in[2]; const bf16* wo=(const bf16*)d_in[3];
  const bf16* w1=(const bf16*)d_in[5]; const bf16* b1=(const bf16*)d_in[6]; const bf16* w2=(const bf16*)d_in[7]; const bf16* b2=(const bf16*)d_in[8];
  float* ws=(float*)d_ws; float* xn=ws; float* h=xn+S*D; float* qkv=h+S*D; float* phi=qkv+S*3*D; float* kv=phi+2*S*H*56; float* o=kv+H*56*29; float* u=o+S*D;
  k_ln<<<S,64,0,stream>>>(x,xn,h);
  k_qkv<<<dim3(3*D/64,S),64,0,stream>>>(xn,wqkv,qkv);
  k_phi<<<S,64,0,stream>>>(qkv,wphi,phi);
  k_kv<<<dim3(H,56),64,0,stream>>>(qkv,phi,kv);
  k_o<<<S,D,0,stream>>>(phi,kv,o);
  k_u<<<dim3(F/64,D),64,0,stream>>>(h,w1,b1,u);
  k_out<<<S,D,0,stream>>>(x,o,wo,u,w2,b2,(bf16*)d_out);
}
```

```cpp
#include <hip/hip_runtime.h>
#include <hip/hip_bf16.h>
typedef __hip_bfloat16 bf16;
constexpr int S=2048, D=448, H=16, DH=28, F=1792;
__device__ inline float b2f(bf16 v){ return __bfloat162float(v); }
__device__ inline float rb(float v){ return __bfloat162float(__float2bfloat16(v)); }

__global__ void k_ln(const bf16* x, float* xn, float* h){
  int s=blockIdx.x, t=threadIdx.x;
  float v[7]; float sum=0;
  for(int i=0;i<7;i++){ v[i]=b2f(x[s*D+t+64*i]); sum+=v[i]; }
  for(int o=32;o;o>>=1) sum+=__shfl_xor(sum,o);
  float m=sum/D, q=0;
  for(int i=0;i<7;i++){ float d=v[i]-m; q+=d*d; }
  for(int o=32;o;o>>=1) q+=__shfl_xor(q,o);
  float r=1.f/sqrtf(q/D+1e-5f); sum=0;
  for(int i=0;i<7;i++){ v[i]=rb((v[i]-m)*r); xn[s*D+t+64*i]=v[i]; sum+=v[i]; }
  for(int o=32;o;o>>=1) sum+=__shfl_xor(sum,o);
  m=sum/D; q=0;
  for(int i=0;i<7;i++){ float d=v[i]-m; q+=d*d; }
  for(int o=32;o;o>>=1) q+=__shfl_xor(q,o);
  r=1.f/sqrtf(q/D+1e-5f);
  for(int i=0;i<7;i++) h[s*D+t+64*i]=rb((v[i]-m)*r);
}
__global__ void k_qkv(const float* xn,const bf16* w,float* qkv){
  int s=blockIdx.y, n=blockIdx.x*64+threadIdx.x; float a=0;
  for(int k=0;k<D;k++) a+=xn[s*D+k]*b2f(w[k*3*D+n]);
  qkv[s*3*D+n]=rb(a);
}
__global__ __launch_bounds__(64) void k_phi(const float* qkv,const bf16* wphi,float* phi){
  int s=blockIdx.x, t=threadIdx.x; if(t>=32) return; int which=t/16,hh=t%16;
  const float* q=qkv+s*3*D+which*D+hh*DH; float p[DH]; float mx=-1e30f,mn=1e30f;
  #pragma unroll
  for(int f=0;f<DH;f++){ float a=0;
 _Pragma("unroll 1") for(int d=0;d<DH;d++) a+=q[d]*b2f(wphi[(hh*DH+d)*DH+f]); p[f]=rb(a); mx=fmaxf(mx,p[f]); mn=fminf(mn,p[f]); }
  float s1=0,s2=0;
  #pragma unroll
  for(int f=0;f<DH;f++){ s1+=__expf(p[f]-mx); s2+=__expf(mn-p[f]); }
  float* o=phi+((size_t)(which*S+s)*H+hh)*56;
  #pragma unroll
  for(int f=0;f<DH;f++){ o[f]=rb(__expf(p[f]-mx)/s1); o[28+f]=rb(__expf(mn-p[f])/s2); }
}
__global__ void k_kv(const float* qkv,const float* phi,float* kv){
  int hh=blockIdx.x,f=blockIdx.y,e=threadIdx.x; if(e>=29) return; float a=0;
  for(int s=0;s<S;s++){ float pk=phi[((size_t)(S+s)*H+hh)*56+f]; a+=pk*(e<28?qkv[s*3*D+2*D+hh*DH+e]:1.f); }
  kv[(hh*56+f)*29+e]=rb(a);
}
__global__ void k_o(const float* phi,const float* kv,float* o){
  int s=blockIdx.x,j=threadIdx.x; int hh=j/DH,e=j%DH; float n=0,dn=0;
  for(int f=0;f<56;f++){ float p=phi[((size_t)s*H+hh)*56+f]; n+=p*kv[(hh*56+f)*29+e]; dn+=p*kv[(hh*56+f)*29+28]; }
  o[s*D+j]=rb(rb(n)/(rb(dn)+1e-6f));
}
__global__ void k_u(const float* h,const bf16* w1,const bf16* b1,float* u){
  int d=blockIdx.y,f=blockIdx.x*64+threadIdx.x; float a=0;
  for(int s=0;s<S;s++) a+=h[s*D+d]*b2f(w1[s*F+f]);
  a+=b2f(b1[f]);
  float t=tanhf(0.7978845608f*(a+0.044715f*a*a*a));
  u[d*F+f]=rb(0.5f*a*(1.f+t));
}
__global__ void k_out(const bf16* x,const float* o,const bf16* wo,const float* u,const bf16* w2,const bf16* b2,bf16* out){
  int s=blockIdx.x,d=threadIdx.x; float a=0,y=0;
  for(int j=0;j<D;j++) a+=o[s*D+j]*b2f(wo[j*D+d]);
  for(int f=0;f<F;f++) y+=u[d*F+f]*b2f(w2[f*S+s]);
  out[s*D+d]=__float2bfloat16(b2f(x[s*D+d])+a+y+b2f(b2[s]));
}

typedef unsigned u32x4 __attribute__((ext_vector_type(4)));
typedef unsigned long long u64;
template<int U> __global__ __launch_bounds__(512) void streamk(const u32x4* a,int na,const u32x4* b,int nb,const u32x4* c,int nc,const u32x4* d,int nd,u64* ts,unsigned* sink){
  u64 r0=__builtin_amdgcn_s_memrealtime();
  u32x4 acc={0,0,0,0};
  const int t=blockIdx.x*512+threadIdx.x, T=gridDim.x*512;
  u32x4 v[12];
  #pragma unroll
  for(int j=0;j<4;j++){ int i=t+j*T; v[j]=(i<na)?a[i]:acc; }
  #pragma unroll
  for(int j=0;j<4;j++){ int i=t+j*T; v[4+j]=(i<nb)?b[i]:acc; }
  v[8]=(t<nc)?c[t]:acc; v[9]=(t<nd)?d[t]:acc;
  #pragma unroll
  for(int j=0;j<10;j++) acc^=v[j];
  if((acc.x^acc.y^acc.z^acc.w)==0x12345678) sink[0]=1;
  __syncthreads();
  if(threadIdx.x==0){ ts[blockIdx.x*2]=r0; ts[blockIdx.x*2+1]=__builtin_amdgcn_s_memrealtime(); }
}
__global__ void latk(const unsigned* a,int n,int stride,u64* ts,unsigned* sink){
  u64 r0=__builtin_amdgcn_s_memrealtime();
  unsigned off=0,acc=0;
  for(int i=0;i<n;i++){ unsigned v=__builtin_nontemporal_load(a+off); acc^=v; off+=stride+(v==0x12345678u); }
  if(acc==0x9abcdef1u) sink[0]=1;
  u64 r1=__builtin_amdgcn_s_memrealtime();
  for(int i=threadIdx.x;i<256;i+=64){ ts[i*2]=r0; ts[i*2+1]=r1; }
}
__global__ void spink(const u64* ts,int mult){
  u64 mn=~0ull,mx=0; for(int i=0;i<256;i++){ mn=min(mn,ts[2*i]); mx=max(mx,ts[2*i+1]); }
  u64 d=(mx-mn); if(d>5000) d=5000; d*=mult;
  u64 r0=__builtin_amdgcn_s_memrealtime();
  while(__builtin_amdgcn_s_memrealtime()-r0<d) __builtin_amdgcn_s_sleep(8);
}
extern "C" void kernel_launch(void* const* d_in,const int* in_sizes,int n_in,void* d_out,int out_size,void* d_ws,size_t ws_size,hipStream_t stream){
  const bf16* x=(const bf16*)d_in[0]; const bf16* wqkv=(const bf16*)d_in[1]; const bf16* wphi=(const bf16*)d_in[2]; const bf16* wo=(const bf16*)d_in[3];
  const bf16* w1=(const bf16*)d_in[5]; const bf16* b1=(const bf16*)d_in[6]; const bf16* w2=(const bf16*)d_in[7]; const bf16* b2=(const bf16*)d_in[8];
  float* ws=(float*)d_ws; float* xn=ws; float* h=xn+S*D; float* qkv=h+S*D; float* phi=qkv+S*3*D; float* kv=phi+2*S*H*56; float* o=kv+H*56*29; float* u=o+S*D;
  u64* ts=(u64*)((char*)d_ws+(200<<20)); unsigned* sink=(unsigned*)((char*)d_ws+(201<<20));
  streamk<0><<<256,512,0,stream>>>((const u32x4*)w1,F*S/8,(const u32x4*)w2,F*S/8,(const u32x4*)x,S*D/8,(const u32x4*)wqkv,D*3*D/8,ts,sink);
  k_ln<<<S,64,0,stream>>>(x,xn,h);
  k_qkv<<<dim3(3*D/64,S),64,0,stream>>>(xn,wqkv,qkv);
  k_phi<<<S,64,0,stream>>>(qkv,wphi,phi);
  k_kv<<<dim3(H,56),64,0,stream>>>(qkv,phi,kv);
  k_o<<<S,D,0,stream>>>(phi,kv,o);
  k_u<<<dim3(F/64,D),64,0,stream>>>(h,w1,b1,u);
  k_out<<<S,D,0,stream>>>(x,o,wo,u,w2,b2,(bf16*)d_out);
  spink<<<1,1,0,stream>>>(ts,0);
}
```

```cpp
#include <hip/hip_runtime.h>
#include <hip/hip_bf16.h>
typedef __hip_bfloat16 bf16;
constexpr int S=2048, D=448, H=16, DH=28, F=1792;
__device__ inline float b2f(bf16 v){ return __bfloat162float(v); }
__device__ inline float rb(float v){ return __bfloat162float(__float2bfloat16(v)); }

__global__ void k_ln(const bf16* x, float* xn, float* h){
  int s=blockIdx.x, t=threadIdx.x;
  float v[7]; float sum=0;
  for(int i=0;i<7;i++){ v[i]=b2f(x[s*D+t+64*i]); sum+=v[i]; }
  for(int o=32;o;o>>=1) sum+=__shfl_xor(sum,o);
  float m=sum/D, q=0;
  for(int i=0;i<7;i++){ float d=v[i]-m; q+=d*d; }
  for(int o=32;o;o>>=1) q+=__shfl_xor(q,o);
  float r=1.f/sqrtf(q/D+1e-5f); sum=0;
  for(int i=0;i<7;i++){ v[i]=rb((v[i]-m)*r); xn[s*D+t+64*i]=v[i]; sum+=v[i]; }
  for(int o=32;o;o>>=1) sum+=__shfl_xor(sum,o);
  m=sum/D; q=0;
  for(int i=0;i<7;i++){ float d=v[i]-m; q+=d*d; }
  for(int o=32;o;o>>=1) q+=__shfl_xor(q,o);
  r=1.f/sqrtf(q/D+1e-5f);
  for(int i=0;i<7;i++) h[s*D+t+64*i]=rb((v[i]-m)*r);
}
__global__ void k_qkv(const float* xn,const bf16* w,float* qkv){
  int s=blockIdx.y, n=blockIdx.x*64+threadIdx.x; float a=0;
  for(int k=0;k<D;k++) a+=xn[s*D+k]*b2f(w[k*3*D+n]);
  qkv[s*3*D+n]=rb(a);
}
__global__ __launch_bounds__(64) void k_phi(const float* qkv,const bf16* wphi,float* phi){
  int s=blockIdx.x, t=threadIdx.x; if(t>=32) return; int which=t/16,hh=t%16;
  const float* q=qkv+s*3*D+which*D+hh*DH; float p[DH]; float mx=-1e30f,mn=1e30f;
  #pragma unroll
  for(int f=0;f<DH;f++){ float a=0;
 _Pragma("unroll 1") for(int d=0;d<DH;d++) a+=q[d]*b2f(wphi[(hh*DH+d)*DH+f]); p[f]=rb(a); mx=fmaxf(mx,p[f]); mn=fminf(mn,p[f]); }
  float s1=0,s2=0;
  #pragma unroll
  for(int f=0;f<DH;f++){ s1+=__expf(p[f]-mx); s2+=__expf(mn-p[f]); }
  float* o=phi+((size_t)(which*S+s)*H+hh)*56;
  #pragma unroll
  for(int f=0;f<DH;f++){ o[f]=rb(__expf(p[f]-mx)/s1); o[28+f]=rb(__expf(mn-p[f])/s2); }
}
__global__ void k_kv(const float* qkv,const float* phi,float* kv){
  int hh=blockIdx.x,f=blockIdx.y,e=threadIdx.x; if(e>=29) return; float a=0;
  for(int s=0;s<S;s++){ float pk=phi[((size_t)(S+s)*H+hh)*56+f]; a+=pk*(e<28?qkv[s*3*D+2*D+hh*DH+e]:1.f); }
  kv[(hh*56+f)*29+e]=rb(a);
}
__global__ void k_o(const float* phi,const float* kv,float* o){
  int s=blockIdx.x,j=threadIdx.x; int hh=j/DH,e=j%DH; float n=0,dn=0;
  for(int f=0;f<56;f++){ float p=phi[((size_t)s*H+hh)*56+f]; n+=p*kv[(hh*56+f)*29+e]; dn+=p*kv[(hh*56+f)*29+28]; }
  o[s*D+j]=rb(rb(n)/(rb(dn)+1e-6f));
}
__global__ void k_u(const float* h,const bf16* w1,const bf16* b1,float* u){
  int d=blockIdx.y,f=blockIdx.x*64+threadIdx.x; float a=0;
  for(int s=0;s<S;s++) a+=h[s*D+d]*b2f(w1[s*F+f]);
  a+=b2f(b1[f]);
  float t=tanhf(0.7978845608f*(a+0.044715f*a*a*a));
  u[d*F+f]=rb(0.5f*a*(1.f+t));
}
__global__ void k_out(const bf16* x,const float* o,const bf16* wo,const float* u,const bf16* w2,const bf16* b2,bf16* out){
  int s=blockIdx.x,d=threadIdx.x; float a=0,y=0;
  for(int j=0;j<D;j++) a+=o[s*D+j]*b2f(wo[j*D+d]);
  for(int f=0;f<F;f++) y+=u[d*F+f]*b2f(w2[f*S+s]);
  out[s*D+d]=__float2bfloat16(b2f(x[s*D+d])+a+y+b2f(b2[s]));
}

typedef unsigned long long u64;
#define R4(x) x x x x
#define R16(x) R4(R4(x))
#define R256(x) R16(R16(x))
template<int N256,int NT> __global__ __launch_bounds__(NT) void codek(u64* ts,unsigned* sink){
  u64 r0=__builtin_amdgcn_s_memrealtime();
  unsigned a=threadIdx.x,b=threadIdx.x*3;
  if(N256>=1) asm volatile(R256("v_add3_u32 %0, %0, %1, %1\n\t"):"+v"(a):"v"(b));
  if(N256>=2) asm volatile(R256("v_add3_u32 %0, %0, %1, %1\n\t"):"+v"(a):"v"(b));
  if(N256>=4) asm volatile(R256("v_add3_u32 %0, %0, %1, %1\n\t" "v_add3_u32 %0, %0, %1, %1\n\t"):"+v"(a):"v"(b));
  if(N256>=8) asm volatile(R256(R4("v_add3_u32 %0, %0, %1, %1\n\t")):"+v"(a):"v"(b));
  if(N256>=16) asm volatile(R256(R4("v_add3_u32 %0, %0, %1, %1\n\t" "v_add3_u32 %0, %0, %1, %1\n\t")):"+v"(a):"v"(b));
  if(a==0x12345678) sink[0]=1;
  __syncthreads();
  if(threadIdx.x==0){ ts[blockIdx.x*2]=r0; ts[blockIdx.x*2+1]=__builtin_amdgcn_s_memrealtime(); }
}
__global__ void spink(const u64* ts,int mult){
  u64 mn=~0ull,mx=0; for(int i=0;i<256;i++){ mn=min(mn,ts[2*i]); mx=max(mx,ts[2*i+1]); }
  u64 d=(mx-mn); if(d>5000) d=5000; d*=mult;
  u64 r0=__builtin_amdgcn_s_memrealtime();
  while(__builtin_amdgcn_s_memrealtime()-r0<d) __builtin_amdgcn_s_sleep(8);
}
extern "C" void kernel_launch(void* const* d_in,const int* in_sizes,int n_in,void* d_out,int out_size,void* d_ws,size_t ws_size,hipStream_t stream){
  const bf16* x=(const bf16*)d_in[0]; const bf16* wqkv=(const bf16*)d_in[1]; const bf16* wphi=(const bf16*)d_in[2]; const bf16* wo=(const bf16*)d_in[3];
  const bf16* w1=(const bf16*)d_in[5]; const bf16* b1=(const bf16*)d_in[6]; const bf16* w2=(const bf16*)d_in[7]; const bf16* b2=(const bf16*)d_in[8];
  float* ws=(float*)d_ws; float* xn=ws; float* h=xn+S*D; float* qkv=h+S*D; float* phi=qkv+S*3*D; float* kv=phi+2*S*H*56; float* o=kv+H*56*29; float* u=o+S*D;
  u64* ts=(u64*)((char*)d_ws+(200<<20)); unsigned* sink=(unsigned*)((char*)d_ws+(201<<20));
  codek<0,256><<<256,256,0,stream>>>(ts,sink);
  k_ln<<<S,64,0,stream>>>(x,xn,h);
  k_qkv<<<dim3(3*D/64,S),64,0,stream>>>(xn,wqkv,qkv);
  k_phi<<<S,64,0,stream>>>(qkv,wphi,phi);
  k_kv<<<dim3(H,56),64,0,stream>>>(qkv,phi,kv);
  k_o<<<S,D,0,stream>>>(phi,kv,o);
  k_u<<<dim3(F/64,D),64,0,stream>>>(h,w1,b1,u);
  k_out<<<S,D,0,stream>>>(x,o,wo,u,w2,b2,(bf16*)d_out);
  spink<<<1,1,0,stream>>>(ts,0);
}
```

```cpp
#include <hip/hip_runtime.h>
#include <hip/hip_bf16.h>
typedef __hip_bfloat16 bf16;
constexpr int S=2048, D=448, H=16, DH=28, F=1792;
__device__ inline float b2f(bf16 v){ return __bfloat162float(v); }
__device__ inline float rb(float v){ return __bfloat162float(__float2bfloat16(v)); }

__global__ void k_ln(const bf16* x, float* xn, float* h){
  int s=blockIdx.x, t=threadIdx.x;
  float v[7]; float sum=0;
  for(int i=0;i<7;i++){ v[i]=b2f(x[s*D+t+64*i]); sum+=v[i]; }
  for(int o=32;o;o>>=1) sum+=__shfl_xor(sum,o);
  float m=sum/D, q=0;
  for(int i=0;i<7;i++){ float d=v[i]-m; q+=d*d; }
  for(int o=32;o;o>>=1) q+=__shfl_xor(q,o);
  float r=1.f/sqrtf(q/D+1e-5f); sum=0;
  for(int i=0;i<7;i++){ v[i]=rb((v[i]-m)*r); xn[s*D+t+64*i]=v[i]; sum+=v[i]; }
  for(int o=32;o;o>>=1) sum+=__shfl_xor(sum,o);
  m=sum/D; q=0;
  for(int i=0;i<7;i++){ float d=v[i]-m; q+=d*d; }
  for(int o=32;o;o>>=1) q+=__shfl_xor(q,o);
  r=1.f/sqrtf(q/D+1e-5f);
  for(int i=0;i<7;i++) h[s*D+t+64*i]=rb((v[i]-m)*r);
}
__global__ void k_qkv(const float* xn,const bf16* w,float* qkv){
  int s=blockIdx.y, n=blockIdx.x*64+threadIdx.x; float a=0;
  for(int k=0;k<D;k++) a+=xn[s*D+k]*b2f(w[k*3*D+n]);
  qkv[s*3*D+n]=rb(a);
}
__global__ __launch_bounds__(64) void k_phi(const float* qkv,const bf16* wphi,float* phi){
  int s=blockIdx.x, t=threadIdx.x; if(t>=32) return; int which=t/16,hh=t%16;
  const float* q=qkv+s*3*D+which*D+hh*DH; float p[DH]; float mx=-1e30f,mn=1e30f;
  #pragma unroll
  for(int f=0;f<DH;f++){ float a=0;
 _Pragma("unroll 1") for(int d=0;d<DH;d++) a+=q[d]*b2f(wphi[(hh*DH+d)*DH+f]); p[f]=rb(a); mx=fmaxf(mx,p[f]); mn=fminf(mn,p[f]); }
  float s1=0,s2=0;
  #pragma unroll
  for(int f=0;f<DH;f++){ s1+=__expf(p[f]-mx); s2+=__expf(mn-p[f]); }
  float* o=phi+((size_t)(which*S+s)*H+hh)*56;
  #pragma unroll
  for(int f=0;f<DH;f++){ o[f]=rb(__expf(p[f]-mx)/s1); o[28+f]=rb(__expf(mn-p[f])/s2); }
}
__global__ void k_kv(const float* qkv,const float* phi,float* kv){
  int hh=blockIdx.x,f=blockIdx.y,e=threadIdx.x; if(e>=29) return; float a=0;
  for(int s=0;s<S;s++){ float pk=phi[((size_t)(S+s)*H+hh)*56+f]; a+=pk*(e<28?qkv[s*3*D+2*D+hh*DH+e]:1.f); }
  kv[(hh*56+f)*29+e]=rb(a);
}
__global__ void k_o(const float* phi,const float* kv,float* o){
  int s=blockIdx.x,j=threadIdx.x; int hh=j/DH,e=j%DH; float n=0,dn=0;
  for(int f=0;f<56;f++){ float p=phi[((size_t)s*H+hh)*56+f]; n+=p*kv[(hh*56+f)*29+e]; dn+=p*kv[(hh*56+f)*29+28]; }
  o[s*D+j]=rb(rb(n)/(rb(dn)+1e-6f));
}
__global__ void k_u(const float* h,const bf16* w1,const bf16* b1,float* u){
  int d=blockIdx.y,f=blockIdx.x*64+threadIdx.x; float a=0;
  for(int s=0;s<S;s++) a+=h[s*D+d]*b2f(w1[s*F+f]);
  a+=b2f(b1[f]);
  float t=tanhf(0.7978845608f*(a+0.044715f*a*a*a));
  u[d*F+f]=rb(0.5f*a*(1.f+t));
}
__global__ void k_out(const bf16* x,const float* o,const bf16* wo,const float* u,const bf16* w2,const bf16* b2,bf16* out){
  int s=blockIdx.x,d=threadIdx.x; float a=0,y=0;
  for(int j=0;j<D;j++) a+=o[s*D+j]*b2f(wo[j*D+d]);
  for(int f=0;f<F;f++) y+=u[d*F+f]*b2f(w2[f*S+s]);
  out[s*D+d]=__float2bfloat16(b2f(x[s*D+d])+a+y+b2f(b2[s]));
}

typedef unsigned long long u64;
#define R4(x) x x x x
#define R16(x) R4(R4(x))
#define R256(x) R16(R16(x))
template<int N256,int NT,int PF,int SPLIT> __global__ __launch_bounds__(NT) void codek(u64* ts,unsigned* sink){
  u64 r0=__builtin_amdgcn_s_memrealtime();
  unsigned a=threadIdx.x,b=threadIdx.x*3;
  unsigned d0=0;
  if(PF){
    u64 pc; asm volatile("s_getpc_b64 %0":"=s"(pc));
    const char* p=(const char*)(pc&~127ull)+threadIdx.x*128;
    if(threadIdx.x*128<N256*2048) asm volatile("global_load_dword %0, %1, off":"=v"(d0):"v"(p):"memory");
    if(PF==2) asm volatile("s_waitcnt vmcnt(0)\n\ts_barrier":::"memory");
  }
  if(SPLIT){
    const int w=__builtin_amdgcn_readfirstlane(threadIdx.x>>6);
    if(w==0) asm volatile(R256("v_add3_u32 %0, %0, %1, %1\n\t" "v_add3_u32 %0, %0, %1, %1\n\t"):"+v"(a):"v"(b));
    else if(w==1) asm volatile(R256("v_add3_u32 %0, %1, %0, %1\n\t" "v_add3_u32 %0, %0, %1, %1\n\t"):"+v"(a):"v"(b));
    else if(w==2) asm volatile(R256("v_add3_u32 %0, %1, %1, %0\n\t" "v_add3_u32 %0, %0, %1, %1\n\t"):"+v"(a):"v"(b));
    else asm volatile(R256("v_add3_u32 %0, %0, %0, %1\n\t" "v_add3_u32 %0, %0, %1, %1\n\t"):"+v"(a):"v"(b));
  } else {
  if(N256>=1) asm volatile(R256("v_add3_u32 %0, %0, %1, %1\n\t"):"+v"(a):"v"(b));
  if(N256>=2) asm volatile(R256("v_add3_u32 %0, %0, %1, %1\n\t"):"+v"(a):"v"(b));
  if(N256>=4) asm volatile(R256("v_add3_u32 %0, %0, %1, %1\n\t" "v_add3_u32 %0, %0, %1, %1\n\t"):"+v"(a):"v"(b));
  if(N256>=8) asm volatile(R256(R4("v_add3_u32 %0, %0, %1, %1\n\t")):"+v"(a):"v"(b));
  if(N256>=16) asm volatile(R256(R4("v_add3_u32 %0, %0, %1, %1\n\t" "v_add3_u32 %0, %0, %1, %1\n\t")):"+v"(a):"v"(b));
  }
  asm volatile("s_waitcnt vmcnt(0)":"+v"(d0)::"memory");
  if(a==0x12345678) sink[0]=d0;
  __syncthreads();
  if(threadIdx.x==0){ ts[blockIdx.x*2]=r0; ts[blockIdx.x*2+1]=__builtin_amdgcn_s_memrealtime(); }
}
__global__ void spink(const u64* ts,int mult){
  u64 mn=~0ull,mx=0; for(int i=0;i<256;i++){ mn=min(mn,ts[2*i]); mx=max(mx,ts[2*i+1]); }
  u64 d=(mx-mn); if(d>5000) d=5000; d*=mult;
  u64 r0=__builtin_amdgcn_s_memrealtime();
  while(__builtin_amdgcn_s_memrealtime()-r0<d) __builtin_amdgcn_s_sleep(8);
}
extern "C" void kernel_launch(void* const* d_in,const int* in_sizes,int n_in,void* d_out,int out_size,void* d_ws,size_t ws_size,hipStream_t stream){
  const bf16* x=(const bf16*)d_in[0]; const bf16* wqkv=(const bf16*)d_in[1]; const bf16* wphi=(const bf16*)d_in[2]; const bf16* wo=(const bf16*)d_in[3];
  const bf16* w1=(const bf16*)d_in[5]; const bf16* b1=(const bf16*)d_in[6]; const bf16* w2=(const bf16*)d_in[7]; const bf16* b2=(const bf16*)d_in[8];
  float* ws=(float*)d_ws; float* xn=ws; float* h=xn+S*D; float* qkv=h+S*D; float* phi=qkv+S*3*D; float* kv=phi+2*S*H*56; float* o=kv+H*56*29; float* u=o+S*D;
  u64* ts=(u64*)((char*)d_ws+(200<<20)); unsigned* sink=(unsigned*)((char*)d_ws+(201<<20));
  codek<0,256,0,0><<<256,256,0,stream>>>(ts,sink);
  k_ln<<<S,64,0,stream>>>(x,xn,h);
  k_qkv<<<dim3(3*D/64,S),64,0,stream>>>(xn,wqkv,qkv);
  k_phi<<<S,64,0,stream>>>(qkv,wphi,phi);
  k_kv<<<dim3(H,56),64,0,stream>>>(qkv,phi,kv);
  k_o<<<S,D,0,stream>>>(phi,kv,o);
  k_u<<<dim3(F/64,D),64,0,stream>>>(h,w1,b1,u);
  k_out<<<S,D,0,stream>>>(x,o,wo,u,w2,b2,(bf16*)d_out);
  spink<<<1,1,0,stream>>>(ts,0);
}
```

```cpp
#include <hip/hip_runtime.h>
#include <stdint.h>

typedef short bf16x8 __attribute__((ext_vector_type(8)));
typedef short s16x4 __attribute__((ext_vector_type(4)));
typedef float f32x2 __attribute__((ext_vector_type(2)));
typedef float f32x4 __attribute__((ext_vector_type(4)));
typedef float f32x16 __attribute__((ext_vector_type(16)));
typedef unsigned u32x2 __attribute__((ext_vector_type(2)));
typedef unsigned u32x4 __attribute__((ext_vector_type(4)));
typedef __bf16 b16x2 __attribute__((ext_vector_type(2)));
typedef __attribute__((address_space(3))) char* ldsp;
typedef __amdgpu_buffer_rsrc_t rsrc_t;

#define DEV __device__ __forceinline__
#define SB() __builtin_amdgcn_sched_barrier(0)
#define MFMA(a, b, c) __builtin_amdgcn_mfma_f32_32x32x16_bf16(a, b, c, 0, 0, 0)

constexpr int S = 2048, D = 448, F = 1792;
constexpr unsigned FL_H = 8, FL_X = FL_H + 128, FL_P = FL_X + 128, FL_R = FL_P + 128, FL_O = FL_R + 16, FL_U = FL_O + 128;
constexpr unsigned WS_FLAG_BYTES = 131072;
constexpr unsigned WS_HF = WS_FLAG_BYTES, SZ_HF = 256 * 448 * 16;
constexpr unsigned WS_XN = WS_HF + SZ_HF, SZ_XN = 56 * 2048 * 16;
constexpr unsigned WS_UF = WS_XN + SZ_XN, SZ_UF = 224 * 448 * 16;
constexpr unsigned WS_OF = WS_UF + SZ_UF, SZ_OF = 56 * 2048 * 16;
constexpr unsigned WS_KVP = WS_OF + SZ_OF, SZ_KVP = 16 * 16 * 2 * 4096;
constexpr unsigned WS_KVF = WS_KVP + SZ_KVP, SZ_KVF = 16 * 4 * 1024;
constexpr unsigned WS_END = WS_KVF + SZ_KVF;
constexpr unsigned AREA = 40960;
constexpr unsigned L_WK = 0, L_WV = 50816, L_WQ = 101632, L_WPHI = 152448, L_PHI = 0, L_VX = 32768;
constexpr unsigned OOB = 0x40000000u;
constexpr int SPIN = 1 << 18;

DEV u32x4 mkrs(const void* p, unsigned bytes) {
  unsigned long long a = (unsigned long long)p;
  u32x4 r = {(unsigned)a, (unsigned)(a >> 32), bytes, 0x00020000u};
  return r;
}
DEV rsrc_t mkst(void* p, unsigned bytes) { return __builtin_amdgcn_make_buffer_rsrc(p, 0, bytes, 0x00020000); }
DEV void stc(rsrc_t rs, unsigned voff, u32x4 v) { __builtin_amdgcn_raw_buffer_store_b128(v, rs, voff, 0, 16); }
template <int OFF> DEV bf16x8 ldp(u32x4 rs, unsigned voff, unsigned soff) {
  bf16x8 r;
  asm volatile("buffer_load_dwordx4 %0, %2, %1, %3 offen offset:%4" : "=v"(r) : "s"(rs), "v"(voff), "s"(soff), "n"(OFF) : "memory");
  return r;
}
template <int OFF> DEV bf16x8 ldc(u32x4 rs, unsigned voff, unsigned soff) {
  bf16x8 r;
  asm volatile("buffer_load_dwordx4 %0, %2, %1, %3 offen offset:%4 sc1" : "=v"(r) : "s"(rs), "v"(voff), "s"(soff), "n"(OFF) : "memory");
  return r;
}
template <int OFF> DEV u32x2 ldp2(u32x4 rs, unsigned voff, unsigned soff) {
  u32x2 r;
  asm volatile("buffer_load_dwordx2 %0, %2, %1, %3 offen offset:%4" : "=v"(r) : "s"(rs), "v"(voff), "s"(soff), "n"(OFF) : "memory");
  return r;
}
DEV u32x2 ldc2(u32x4 rs, unsigned voff) {
  u32x2 r;
  asm volatile("buffer_load_dwordx2 %0, %2, %1, 0 offen sc1" : "=v"(r) : "s"(rs), "v"(voff) : "memory");
  return r;
}
template <int OFF> DEV unsigned ldu16(u32x4 rs, unsigned voff, unsigned soff) {
  unsigned r;
  asm volatile("buffer_load_ushort %0, %2, %1, %3 offen offset:%4" : "=v"(r) : "s"(rs), "v"(voff), "s"(soff), "n"(OFF) : "memory");
  return r;
}
DEV void setm0(unsigned a) { asm volatile("s_mov_b32 m0, %0\n\ts_nop 0" ::"s"(a) : "memory"); }
template <int OFF> DEV void dma(u32x4 rs, unsigned voff, unsigned soff) {
  asm volatile("buffer_load_dwordx4 %1, %0, %2 offen offset:%3 lds" ::"s"(rs), "v"(voff), "s"(soff), "n"(OFF) : "memory");
}
DEV void dma4(u32x4 rs, const unsigned (&v)[4], unsigned soff, unsigned m0) {
  setm0(m0); dma<0>(rs, v[0], soff); dma<1024>(rs, v[1], soff); dma<2048>(rs, v[2], soff); dma<3072>(rs, v[3], soff);
}
template <int J> DEV void dma1(u32x4 rs, const unsigned (&v)[4], unsigned soff, unsigned m0) {
  if (J == 0) setm0(m0);
  dma<J * 1024>(rs, v[J], soff);
}
template <int N> DEV void waitv() { asm volatile("s_waitcnt vmcnt(%0)" ::"n"(N) : "memory"); }
template <int N, class T> DEV void waitv(T& a) { asm volatile("s_waitcnt vmcnt(%1)" : "+v"(a) : "n"(N) : "memory"); }
template <int N, class T> DEV void waitv(T& a, T& b) { asm volatile("s_waitcnt vmcnt(%2)" : "+v"(a), "+v"(b) : "n"(N) : "memory"); }
template <int N, class T> DEV void waitv(T& a, T& b, T& c, T& d) {
  asm volatile("s_waitcnt vmcnt(%4)" : "+v"(a), "+v"(b), "+v"(c), "+v"(d) : "n"(N) : "memory");
}
template <class T> DEV void tie(T& a) { asm volatile("" : "+v"(a)::"memory"); }
DEV void bar() { asm volatile("s_waitcnt lgkmcnt(0)\n\ts_barrier" ::: "memory"); }
DEV void barv() { asm volatile("s_waitcnt vmcnt(0) lgkmcnt(0)\n\ts_barrier" ::: "memory"); }
DEV s16x4 vtr(ldsp p) { return __builtin_amdgcn_ds_read_tr16_b64_v4i16((__attribute__((address_space(3))) s16x4*)p); }
DEV bf16x8 cat(s16x4 a, s16x4 b) { return __builtin_shufflevector(a, b, 0, 1, 2, 3, 4, 5, 6, 7); }
DEV unsigned pk(float a, float b) {
  f32x2 v = {a, b};
  return __builtin_bit_cast(unsigned, __builtin_convertvector(v, b16x2));
}
DEV float blo(unsigned u) { return __builtin_bit_cast(float, u << 16); }
DEV float bhi(unsigned u) { return __builtin_bit_cast(float, u & 0xffff0000u); }
DEV void swap32(float& a, float& b) {
  auto r = __builtin_amdgcn_permlane32_swap(__builtin_bit_cast(unsigned, a), __builtin_bit_cast(unsigned, b), false, false);
  const unsigned r0 = r[0], r1 = r[1];
  a = __builtin_bit_cast(float, r0); b = __builtin_bit_cast(float, r1);
}
DEV void swap16(float& a, float& b) {
  auto r = __builtin_amdgcn_permlane16_swap(__builtin_bit_cast(unsigned, a), __builtin_bit_cast(unsigned, b), false, false);
  const unsigned r0 = r[0], r1 = r[1];
  a = __builtin_bit_cast(float, r0); b = __builtin_bit_cast(float, r1);
}
template <int C> DEV float ror(float z) {
  return __builtin_bit_cast(float, __builtin_amdgcn_update_dpp(0, __builtin_bit_cast(int, z), C, 0xf, 0xf, true));
}
DEV float rowsum(float z) { z += ror<0x128>(z); z += ror<0x124>(z); z += ror<0x122>(z); z += ror<0x121>(z); return z; }
DEV float rdl(float z, int l) { return __builtin_bit_cast(float, __builtin_amdgcn_readlane(__builtin_bit_cast(int, z), l)); }
template <class T> DEV void lst(ldsp p, T v) { *(__attribute__((address_space(3))) T*)p = v; }
template <class T> DEV T lld(ldsp p) { return *(__attribute__((address_space(3))) T*)p; }

struct Epoch { unsigned lo, hi; };
DEV void publish(char* ws, unsigned flag, Epoch e) {
  barv();
  if (threadIdx.x == 0)
    __hip_atomic_store((unsigned long long*)(ws + flag * 128), ((unsigned long long)e.hi << 32) | e.lo, __ATOMIC_RELAXED, __HIP_MEMORY_SCOPE_AGENT);
}
DEV bool match(u32x2 f, Epoch e) { return __all(f.x == e.lo && f.y == e.hi); }
DEV void poll(u32x4 rsF, unsigned voff, Epoch e) {
#pragma unroll 1
  for (int it = 0; it < SPIN; ++it) {
    u32x2 f = ldc2(rsF, voff);
    waitv<0>(f);
    if (match(f, e)) break;
    __builtin_amdgcn_s_sleep(1);
  }
}

template <bool TWICE> DEV void ln_rows(const bf16x8 (&v)[4], float (&xf)[4][8]) {
  float s[4], q[4];
#pragma unroll
  for (int r = 0; r < 4; r++) {
    u32x4 u = __builtin_bit_cast(u32x4, v[r]);
#pragma unroll
    for (int e = 0; e < 4; e++) { xf[r][2 * e] = blo(u[e]); xf[r][2 * e + 1] = bhi(u[e]); }
    s[r] = 0.f; q[r] = 0.f;
#pragma unroll
    for (int e = 0; e < 8; e++) { s[r] += xf[r][e]; q[r] = fmaf(xf[r][e], xf[r][e], q[r]); }
  }
  swap32(s[0], s[1]); swap32(s[2], s[3]); swap32(q[0], q[1]); swap32(q[2], q[3]);
  float sa = s[0] + s[1], sb = s[2] + s[3], qa = q[0] + q[1], qb = q[2] + q[3];
  swap16(sa, sb); swap16(qa, qb);
  float mu = rowsum(sa + sb) * (1.f / D), ms = rowsum(qa + qb) * (1.f / D);
  float var = fmaxf(ms - mu * mu, 0.f);
  float a = __builtin_amdgcn_rsqf(var + 1e-5f);
  if (TWICE) a *= __builtin_amdgcn_rsqf(var * a * a + 1e-5f);
  float c = -mu * a;
#pragma unroll
  for (int r = 0; r < 4; r++) {
    const int l = (r & 1) * 32 + (r >> 1) * 16;
    float ar = rdl(a, l), cr = rdl(c, l);
#pragma unroll
    for (int e = 0; e < 8; e++) xf[r][e] = fmaf(xf[r][e], ar, cr);
  }
}

DEV void features(const f32x16& p, bool h, unsigned (&ph)[2][8]) {
  constexpr float L = 1.4426950408889634f;
  float mx = fmaxf(p[0], p[1]), mn = fminf(p[0], p[1]);
#pragma unroll
  for (int r = 2; r < 12; r += 2) { mx = fmaxf(fmaxf(mx, p[r]), p[r + 1]); mn = fminf(fminf(mn, p[r]), p[r + 1]); }
  float mx2 = fmaxf(fmaxf(fmaxf(fmaxf(mx, p[12]), p[13]), p[14]), p[15]);
  float mn2 = fminf(fminf(fminf(fminf(mn, p[12]), p[13]), p[14]), p[15]);
  mx = h ? mx : mx2; mn = h ? -mn : -mn2;
  swap32(mx, mn);
  float c0 = -L * fmaxf(mx, mn), c1 = c0;
  swap32(c0, c1);
  const float ninf = -__builtin_inff();
  const float d0 = h ? ninf : c0, d1 = h ? ninf : c1;
  float e[2][16], s0 = 0.f, s1 = 0.f;
#pragma unroll
  for (int r = 0; r < 16; r++) {
    e[0][r] = __builtin_amdgcn_exp2f(fmaf(p[r], L, r < 12 ? c0 : d0));
    e[1][r] = __builtin_amdgcn_exp2f(fmaf(p[r], -L, r < 12 ? c1 : d1));
    s0 += e[0][r]; s1 += e[1][r];
  }
  swap32(s0, s1);
  float i0 = __builtin_amdgcn_rcpf(s0 + s1), i1 = i0;
  swap32(i0, i1);
#pragma unroll
  for (int r = 0; r < 8; r++) { ph[0][r] = pk(e[0][2 * r] * i0, e[0][2 * r + 1] * i0); ph[1][r] = pk(e[1][2 * r] * i1, e[1][2 * r + 1] * i1); }
}
DEV bf16x8 pack8(const f32x16& a, int o) {
  u32x4 u = {pk(a[o], a[o + 1]), pk(a[o + 2], a[o + 3]), pk(a[o + 4], a[o + 5]), pk(a[o + 6], a[o + 7])};
  return __builtin_bit_cast(bf16x8, u);
}
DEV float gelu(float a) {
  constexpr float c1 = -2.302208198f, c2 = c1 * 0.044715f;
  return a * __builtin_amdgcn_rcpf(1.f + __builtin_amdgcn_exp2f(a * fmaf(a * a, c2, c1)));
}

#define STAMP(n)

template <int T, int N, class Fn> DEV void unroll(Fn&& f) {
  if constexpr (T < N) { f(std::integral_constant<int, T>()); unroll<T + 1, N>(f); }
}
constexpr int g1cnt(int s) { return s < 0 ? 4 : (s + 3 < 16 ? (s >= 1 ? 12 : 4) : 0); }
constexpr int G2R = 4, G2POLL = 5;
constexpr int g2ld(int t) { return t < 17 ? 4 : (t == 17 ? 2 : 0); }
constexpr int g2dm(int t) { return t < 17 ? 4 : (t == 17 ? 2 : 0); }
constexpr int g2cnt(int s) { return s < 0 ? 4 : g2ld(s + G2R) + (s >= 1 ? g2dm(s + 9) : 0) + (s == G2POLL ? 1 : 0); }
constexpr int kvn(int ks) { return 8 + 2 * ks < 41 ? 8 + 2 * ks : 41; }

__global__ __launch_bounds__(256, 1) void fused_kernel(const short* __restrict__ x, const short* __restrict__ wqkv, const short* __restrict__ wphi,
                                                       const short* __restrict__ wo, const short* __restrict__ w1, const short* __restrict__ b1,
                                                       const short* __restrict__ w2, const short* __restrict__ b2, short* __restrict__ out,
                                                       char* __restrict__ ws) {
  __shared__ __attribute__((aligned(16))) char lds_[163840];
  const ldsp lds = (ldsp)lds_;
  const unsigned lds0 = (unsigned)(uintptr_t)lds;
  const int tid = threadIdx.x, lane = tid & 63, w = __builtin_amdgcn_readfirstlane(tid >> 6);
  asm volatile("s_nop 4" ::"s"(w), "s"(x), "s"(wqkv), "s"(wphi), "s"(wo), "s"(w1), "s"(b1), "s"(w2), "s"(b2), "s"(out), "s"(ws) : "memory");
  const int xc = blockIdx.x & 7, i = blockIdx.x >> 3;
  const int h = lane >> 5, n = lane & 31, q = (lane & 15) >> 2, pp = lane & 3, blk = (lane >> 4) & 1;
  const u32x4 rsF = mkrs(ws, WS_FLAG_BYTES), rsX = mkrs(x, S * D * 2), rsW2 = mkrs(w2, F * S * 2);
  const unsigned area = lds0 + w * AREA;
  const int tile2 = 28 * (xc & 1) + i - 4, dt2 = tile2 >> 3, st2 = 8 * (xc >> 1) + (tile2 & 7);
  const unsigned swz = (lane >> 3), swc = ((lane & 7) ^ (((lane >> 4) & 1) << 2)) * 16;
  unsigned v2[4];
#pragma unroll
  for (int j = 0; j < 4; j++) v2[j] = swz * (S * 2) + swc + j * (8 * S * 2 - 1024);
  const unsigned so2 = (w * 448) * (S * 2) + st2 * 128;
  auto pre2 = [&](int t0, int t1, int slot0) {
#pragma unroll 1
    for (int t = t0; t < t1; t++) dma4(rsW2, v2, so2 + t * (32 * S * 2), area + (slot0 + t - t0) * 4096);
  };

  const bool isA = i >= 16;
  const int T = 4 * (xc >> 1) + (i & 3), pr = 4 * (xc & 1) + ((i - 16) >> 2);
  const int row0 = isA ? 128 * T + 16 * pr : 256 * xc + 16 * i;
  auto ldrows = [&](bf16x8 (&xv)[4], u32x2& gen) {
    const unsigned vo = lane < 56 ? lane * 16 : OOB, so = (row0 + 4 * w) * (D * 2);
    xv[0] = ldp<0>(rsX, vo, so); xv[1] = ldp<896>(rsX, vo, so); xv[2] = ldp<1792>(rsX, vo, so); xv[3] = ldp<2688>(rsX, vo, so);
    gen = ldc2(rsF, 0);
  };
  Epoch ep;

  if (!isA) {
    bf16x8 xv[4];
    u32x2 gen;
    ldrows(xv, gen);
    const int g0 = (49 * xc + 3) >> 2, g1 = (49 * xc + 52) >> 2;
    const bool isG = g0 + i < g1;
    const int ft = (g0 + i) / 7, dt = (g0 + i) % 7;
    const u32x4 rsW1 = mkrs(w1, S * F * 2);
    unsigned v1[4];
#pragma unroll
    for (int j = 0; j < 4; j++) v1[j] = swz * (F * 2) + swc + j * (8 * F * 2 - 1024);
    const unsigned so1 = (w * 512) * (F * 2) + ft * 256;
    if (isG) {
#pragma unroll
      for (int t = 0; t < 2; t++) { dma4(rsW1, v1, so1 + t * (32 * F * 2), area + t * 8192); dma4(rsW1, v1, so1 + t * (32 * F * 2) + 128, area + t * 8192 + 4096); }
    } else {
      pre2(4, 8, 0);
    }
    waitv<16>(xv[0], xv[1], xv[2], xv[3]);
    tie(gen);
    STAMP(0);
    {
      unsigned long long g = (((unsigned long long)__builtin_amdgcn_readfirstlane(gen.y) << 32) | (unsigned)__builtin_amdgcn_readfirstlane(gen.x)) + 1;
      ep.lo = (unsigned)g; ep.hi = (unsigned)(g >> 32);
    }
    {
      float xf[4][8];
      ln_rows<true>(xv, xf);
      if (lane < 56) {
        const ldsp p = lds + (w >> 1) * AREA + 32768 + (w & 1) * 8 + lane * 16;
#pragma unroll
        for (int e = 0; e < 8; e++) lst(p + e * (57 * 16), (u32x2){pk(xf[0][e], xf[1][e]), pk(xf[2][e], xf[3][e])});
      }
      bar();
      const ldsp p = lds + 32768 + ((tid & 7) * 57 + (tid >> 3)) * 16;
      const rsrc_t st = mkst(ws + WS_HF, SZ_HF);
      const unsigned vo = (row0 >> 3) * (D * 16) + tid * 16;
      u32x4 e0 = lld<u32x4>(p), e1 = lld<u32x4>(p + (w < 3 ? 512 : AREA - 384)), e2 = lld<u32x4>(p + AREA + 128);
      stc(st, vo, e0); stc(st, vo + 4096, e1); stc(st, vo + 8192, e2);
      if (w < 2) stc(st, vo + 12288, lld<u32x4>(p + AREA + 640));
    }
    STAMP(1);
    publish(ws, FL_H + (row0 >> 4), ep);
    STAMP(2);

    if (isG) {
#pragma unroll
      for (int t = 2; t < 4; t++) { dma4(rsW1, v1, so1 + t * (32 * F * 2), area + t * 8192); dma4(rsW1, v1, so1 + t * (32 * F * 2) + 128, area + t * 8192 + 4096); }
      u32x2 bb[4];
      {
        const u32x4 rsB = mkrs(b1, F * 2);
        const unsigned so = (ft * 128 + w * 32) * 2;
        bb[0] = ldp2<0>(rsB, h * 8, so); bb[1] = ldp2<16>(rsB, h * 8, so); bb[2] = ldp2<32>(rsB, h * 8, so); bb[3] = ldp2<48>(rsB, h * 8, so);
      }
      poll(rsF, (FL_H + w * 32 + n) * 128, ep);
      STAMP(3);
      const u32x4 rsH = mkrs(ws + WS_HF, SZ_HF);
      const unsigned vh0 = h * (D * 16) + (dt * 64 + n) * 16, vh1 = vh0 + 2 * D * 16, soh = w * 64 * (D * 16);
      bf16x8 fb[16][4];
      auto ldH = [&](auto tt, auto jj) {
        constexpr int t = tt.value, j = jj.value;
        fb[t][j] = ldc<(j & 1) * 512>(rsH, (j >> 1) ? vh1 : vh0, soh + t * (4 * D * 16));
      };
      unroll<0, 3>([&](auto tt) { unroll<0, 4>([&](auto jj) { ldH(tt, jj); }); });
      waitv<12>(bb[0], bb[1], bb[2], bb[3]);
      f32x16 acc[4][2];
#pragma unroll
      for (int g = 0; g < 4; g++) {
        acc[0][0][4 * g] = blo(bb[g].x); acc[0][0][4 * g + 1] = bhi(bb[g].x); acc[0][0][4 * g + 2] = blo(bb[g].y); acc[0][0][4 * g + 3] = bhi(bb[g].y);
      }
      acc[0][1] = acc[0][0];
#pragma unroll
      for (int a = 1; a < 4; a++) { acc[a][0] = (f32x16)(0.f); acc[a][1] = (f32x16)(0.f); }
      ldsp pa[4];
#pragma unroll
      for (int a = 0; a < 4; a++) {
        const int s = a ^ w;
        pa[a] = lds + w * AREA + (s >> 1) * 4096 + (8 * h + q) * 128 + (((4 * (s & 1) + 2 * blk + (pp >> 1)) ^ ((q >> 1) << 2)) * 16) + (pp & 1) * 8;
      }
      unroll<0, 16>([&](auto tt) {
        constexpr int t = tt.value, sl = t & 3;
        waitv<g1cnt(t - 2) + g1cnt(t - 1)>(fb[t][0], fb[t][1], fb[t][2], fb[t][3]);
        s16x4 lo[2][4], hi[2][4];
#pragma unroll
        for (int ks = 0; ks < 2; ks++)
#pragma unroll
          for (int a = 0; a < 4; a++) { lo[ks][a] = vtr(pa[a] + sl * 8192 + ks * 2048); hi[ks][a] = vtr(pa[a] + sl * 8192 + ks * 2048 + 512); }
        SB();
        unroll<0, 16>([&](auto mm) {
          constexpr int m = mm.value, ks = m >> 3, a = (m >> 1) & 3, dh = m & 1;
          if constexpr (t + 3 < 16) {
            if constexpr (m < 4) ldH(std::integral_constant<int, t + 3>(), mm);
            else if constexpr (m < 12 && t >= 1)
              dma1<(m & 3)>(rsW1, v1, so1 + (t + 3) * (32 * F * 2) + (m >= 8 ? 128 : 0), area + ((t + 3) & 3) * 8192 + (m >= 8 ? 4096 : 0));
          }
          acc[a][dh] = MFMA(cat(lo[ks][a], hi[ks][a]), fb[t][ks * 2 + dh], acc[a][dh]);
          SB();
        });
      });
      STAMP(4);
      if (i >= 4) pre2(0, 4, 6);
      {
        const ldsp p = lds + w * AREA + lane * 16;
#pragma unroll
        for (int a = 1; a < 4; a++)
#pragma unroll
          for (int dh = 0; dh < 2; dh++)
#pragma unroll
            for (int g = 0; g < 4; g++)
              lst(p + (a - 1) * 8192 + dh * 4096 + g * 1024, (f32x4){acc[a][dh][4 * g], acc[a][dh][4 * g + 1], acc[a][dh][4 * g + 2], acc[a][dh][4 * g + 3]});
      }
      bar();
      STAMP(5);
      {
        const rsrc_t st = mkst(ws + WS_UF, SZ_UF);
        const unsigned vo = (ft * 16 + w * 4 + h) * (D * 16) + (dt * 64 + n) * 16;
        f32x4 v[2][3][4];
#pragma unroll
        for (int dh = 0; dh < 2; dh++)
#pragma unroll
          for (int a = 1; a < 4; a++)
#pragma unroll
            for (int g = 0; g < 4; g++) v[dh][a - 1][g] = lld<f32x4>(lds + (w ^ a) * AREA + lane * 16 + (a - 1) * 8192 + dh * 4096 + g * 1024);
        SB();
#pragma unroll
        for (int dh = 0; dh < 2; dh++) {
#pragma unroll
          for (int g = 0; g < 4; g += 2) {
            float r[8];
#pragma unroll
            for (int e = 0; e < 8; e++) {
              const int gg = g + (e >> 2), c = e & 3;
              r[e] = gelu((acc[0][dh][4 * gg + c] + v[dh][0][gg][c]) + (v[dh][1][gg][c] + v[dh][2][gg][c]));
            }
            auto r0 = __builtin_amdgcn_permlane32_swap(pk(r[0], r[1]), pk(r[4], r[5]), false, false);
            auto r1 = __builtin_amdgcn_permlane32_swap(pk(r[2], r[3]), pk(r[6], r[7]), false, false);
            stc(st, vo + g * (D * 16) + dh * 512, (u32x4){r0[0], r1[0], r0[1], r1[1]});
          }
        }
      }
      STAMP(6);
      publish(ws, FL_U + dt * 14 + ft, ep);
      STAMP(7);
      if (i >= 4) pre2(4, 10, 0);
    } else {
      pre2(0, 4, 6);
      pre2(4, 10, 0);
      if (i >= 14) {
        const int head = 2 * xc + i - 14;
        poll(rsF, (FL_P + xc * 16 + (lane & 15)) * 128, ep);
        STAMP(3);
        const u32x4 rsP = mkrs(ws + WS_KVP, SZ_KVP);
        const unsigned so = head * 131072 + (w >> 1) * 4096 + (w & 1) * 2048;
        bf16x8 pv[16][2];
        unroll<0, 16>([&](auto tt) { constexpr int t = tt.value; pv[t][0] = ldc<0>(rsP, lane * 16, so + t * 8192); pv[t][1] = ldc<1024>(rsP, lane * 16, so + t * 8192); });
        f32x4 a0 = {0.f, 0.f, 0.f, 0.f}, a1 = a0;
        unroll<0, 16>([&](auto tt) {
          constexpr int t = tt.value;
          waitv<30 - 2 * t>(pv[t][0], pv[t][1]);
          a0 += __builtin_bit_cast(f32x4, pv[t][0]); a1 += __builtin_bit_cast(f32x4, pv[t][1]);
        });
        stc(mkst(ws + WS_KVF, SZ_KVF), head * 4096 + w * 1024 + lane * 16, (u32x4){pk(a0[0], a0[1]), pk(a0[2], a0[3]), pk(a1[0], a1[1]), pk(a1[2], a1[3])});
        STAMP(4);
        publish(ws, FL_R + head, ep);
        STAMP(5);
      }
    }
  } else {
    bf16x8 xv[4];
    u32x2 gen;
    ldrows(xv, gen);
    const u32x4 rsW = mkrs(wqkv, D * 3 * D * 2);
    unsigned vw[4];
    {
      const unsigned r7 = (lane * 37) >> 8;
#pragma unroll
      for (int j = 0; j < 4; j++) vw[j] = r7 * 2688 + (lane - r7 * 7) * 16 + j * (9 * 2688 - 1008);
    }
    const unsigned sow = w * 112 * 2688 + pr * 112, mw = lds0 + w * 112 * 112;
    auto dmaW = [&](auto kk, unsigned col, unsigned base) {
      constexpr int k = kk.value;
      if constexpr ((k & 3) == 0) setm0(mw + base + (k >> 2) * 4032);
      dma<(k & 3) * 1008>(rsW, vw[k & 3], sow + col + (k >> 2) * (36 * 2688));
    };
    setm0(lds0 + L_WPHI + w * 1024);
    dma<0>(mkrs(wphi + pr * 1568, 3136), tid * 16, 0);
    unroll<0, 13>([&](auto kk) { dmaW(kk, 896, L_WK); });
    waitv<14>(xv[0], xv[1], xv[2], xv[3]);
    tie(gen);
    STAMP(0);
    {
      unsigned long long g = (((unsigned long long)__builtin_amdgcn_readfirstlane(gen.y) << 32) | (unsigned)__builtin_amdgcn_readfirstlane(gen.x)) + 1;
      ep.lo = (unsigned)g; ep.hi = (unsigned)(g >> 32);
    }
    {
      float xf[4][8];
      ln_rows<false>(xv, xf);
      if (lane < 56) {
        const ldsp p = lds + L_WQ + (lane >> 1) * 544 + (lane & 1) * 4 + w * 64;
#pragma unroll
        for (int r = 0; r < 4; r++)
#pragma unroll
          for (int e = 0; e < 4; e++) lst(p + r * 16 + (e >> 1) * 272 + (e & 1) * 8, pk(xf[r][e], xf[r][e + 4]));
      }
      bar();
      const ldsp p = lds + L_WQ + (tid >> 4) * 272 + (tid & 15) * 16;
      const rsrc_t st = mkst(ws + WS_XN, SZ_XN);
      const unsigned vo = (tid >> 4) * (S * 16) + (row0 + (tid & 15)) * 16;
      u32x4 e0 = lld<u32x4>(p), e1 = lld<u32x4>(p + 16 * 272), e2 = lld<u32x4>(p + 32 * 272);
      stc(st, vo, e0); stc(st, vo + 16 * S * 16, e1); stc(st, vo + 32 * S * 16, e2);
      if (w < 2) stc(st, vo + 48 * S * 16, lld<u32x4>(p + 48 * 272));
    }
    STAMP(1);
    publish(ws, FL_X + T * 8 + pr, ep);
    STAMP(2);
    unroll<0, 13>([&](auto kk) { dmaW(kk, 1792, L_WV); });
    poll(rsF, (FL_X + T * 8 + 2 * w + (lane & 1)) * 128, ep);
    STAMP(3);
    const u32x4 rsN = mkrs(ws + WS_XN, SZ_XN);
    const unsigned vx = h * (S * 16) + (128 * T + 32 * w + n) * 16;
    bf16x8 xa[28];
    auto item = [&](auto kk) {
      constexpr int k = kk.value;
      if constexpr (k < 28) xa[k] = ldc<0>(rsN, vx, k * (2 * S * 16));
      else if constexpr (k < 41) dmaW(std::integral_constant<int, k - 28>(), 0, L_WQ);
    };
    unroll<0, 8>(item);
    bar();
    bf16x8 wf[2][2];
    {
      const ldsp p = lds + L_WPHI + (4 * h + q) * 56 + 32 * blk + 8 * pp;
#pragma unroll
      for (int hd = 0; hd < 2; hd++) {
        wf[hd][0] = cat(vtr(p + hd * 1568), vtr(p + hd * 1568 + 8 * 56));
        s16x4 z = vtr(p + hd * 1568 + 24 * 56);
        wf[hd][1] = cat(vtr(p + hd * 1568 + 16 * 56), h ? (s16x4){0, 0, 0, 0} : z);
      }
    }
    const unsigned wlane = (2 * h + 4 * q) * 112 + 32 * blk + 8 * pp;
    f32x16 kT[2] = {}, vv[2] = {};
    {
      const ldsp pk_ = lds + L_WK + wlane, pv_ = lds + L_WV + wlane;
      s16x4 fr[29][8];
      auto rd = [&](auto kk, auto cc) {
        constexpr int ks = kk.value, c = cc.value;
        if constexpr (ks < 28) fr[ks][c] = vtr(((c & 4) ? pv_ : pk_) + ks * 1792 + ((c >> 1) & 1) * 56 + (c & 1) * 112);
      };
      unroll<0, 8>([&](auto cc) { rd(std::integral_constant<int, 0>(), cc); });
      unroll<0, 28>([&](auto kk) {
        constexpr int ks = kk.value;
        waitv<kvn(ks) - ks - 1>(xa[ks]);
        if (ks == 0) STAMP(4);
        unroll<0, 4>([&](auto mm) {
          constexpr int m = mm.value;
          rd(std::integral_constant<int, ks + 1>(), std::integral_constant<int, 2 * m>());
          rd(std::integral_constant<int, ks + 1>(), std::integral_constant<int, 2 * m + 1>());
          if constexpr (m & 1) item(std::integral_constant<int, 8 + 2 * ks + (m >> 1)>());
          const bf16x8 wv = cat(fr[ks][2 * m], fr[ks][2 * m + 1]);
          if constexpr (m < 2) kT[m] = MFMA(wv, xa[ks], kT[m]);
          else vv[m - 2] = MFMA(xa[ks], wv, vv[m - 2]);
          SB();
        });
      });
    }
    STAMP(5);
    {
      f32x16 p[2];
#pragma unroll
      for (int hd = 0; hd < 2; hd++) { p[hd] = MFMA(wf[hd][0], pack8(kT[hd], 0), (f32x16)(0.f)); p[hd] = MFMA(wf[hd][1], pack8(kT[hd], 8), p[hd]); }
      bar();
      {
        const bf16x8 ones = {0x3f80, 0x3f80, 0x3f80, 0x3f80, 0x3f80, 0x3f80, 0x3f80, 0x3f80};
        const ldsp pvx = lds + L_VX + w * 2048 + lane * 16;
#pragma unroll
        for (int hd = 0; hd < 2; hd++)
#pragma unroll
          for (int j = 0; j < 2; j++) { bf16x8 f = pack8(vv[hd], 8 * j); lst(pvx + hd * 8192 + j * 1024, n == 28 ? ones : f); }
      }
      const unsigned pb = (32 * w + n) * 64 + ((h ^ ((n >> 1) & 7)) * 8);
#pragma unroll
      for (int hd = 0; hd < 2; hd++) {
        unsigned ph[2][8];
        features(p[hd], h, ph);
#pragma unroll
        for (int sg = 0; sg < 2; sg++)
#pragma unroll
          for (int g = 0; g < 4; g++) lst(lds + L_PHI + (hd * 2 + sg) * 8192 + (pb ^ (g << 4)), (u32x2){ph[sg][2 * g], ph[sg][2 * g + 1]});
      }
    }
    STAMP(6);
    barv();
    {
      const unsigned sw = 2 * h + (q >> 1);
      const ldsp pl = lds + L_PHI + w * 8192 + (4 * h + q) * 64 + (((4 * blk + pp) ^ sw) * 8);
      const ldsp ph_ = lds + L_PHI + w * 8192 + (8 + 4 * h + q) * 64 + (((4 * blk + pp) ^ sw ^ 4) * 8);
      const ldsp pvx = lds + L_VX + (w >> 1) * 8192 + lane * 16;
      f32x16 kv = {};
#pragma unroll
      for (int jj = 0; jj < 8; jj++) kv = MFMA(cat(vtr(pl + jj * 1024), vtr(ph_ + jj * 1024)), lld<bf16x8>(pvx + jj * 1024), kv);
      const rsrc_t st = mkst(ws + WS_KVP, SZ_KVP);
      const unsigned vo = (2 * pr + (w >> 1)) * 131072 + T * 8192 + (w & 1) * 4096 + lane * 16;
#pragma unroll
      for (int g = 0; g < 4; g++) stc(st, vo + g * 1024, __builtin_bit_cast(u32x4, (f32x4){kv[4 * g], kv[4 * g + 1], kv[4 * g + 2], kv[4 * g + 3]}));
    }
    STAMP(7);
    f32x16 qT[2] = {};
    {
      const ldsp pq_ = lds + L_WQ + wlane;
      s16x4 fr[29][4];
      auto rd = [&](auto kk, auto cc) {
        constexpr int ks = kk.value, c = cc.value;
        if constexpr (ks < 28) fr[ks][c] = vtr(pq_ + ks * 1792 + (c >> 1) * 56 + (c & 1) * 112);
      };
      unroll<0, 4>([&](auto cc) { rd(std::integral_constant<int, 0>(), cc); });
      unroll<0, 28>([&](auto kk) {
        constexpr int ks = kk.value;
        if constexpr (ks == 14) { publish(ws, FL_P + pr * 16 + T, ep); STAMP(8); SB(); }
        unroll<0, 2>([&](auto mm) {
          constexpr int m = mm.value;
          rd(std::integral_constant<int, ks + 1>(), std::integral_constant<int, 2 * m>());
          rd(std::integral_constant<int, ks + 1>(), std::integral_constant<int, 2 * m + 1>());
          qT[m] = MFMA(cat(fr[ks][2 * m], fr[ks][2 * m + 1]), xa[ks], qT[m]);
          SB();
        });
      });
    }
    unsigned phq[2][2][8];
    {
      f32x16 p[2];
#pragma unroll
      for (int hd = 0; hd < 2; hd++) { p[hd] = MFMA(wf[hd][0], pack8(qT[hd], 0), (f32x16)(0.f)); p[hd] = MFMA(wf[hd][1], pack8(qT[hd], 8), p[hd]); }
      bar();
      STAMP(9);
      pre2(0, 4, 6);
      pre2(4, 10, 0);
      features(p[0], h, phq[0]);
      features(p[1], h, phq[1]);
    }
    STAMP(10);
    poll(rsF, (FL_R + 2 * pr + (lane & 1)) * 128, ep);
    STAMP(11);
    {
      const u32x4 rsK = mkrs(ws + WS_KVF, SZ_KVF);
      bf16x8 kf[2][4];
#pragma unroll
      for (int hd = 0; hd < 2; hd++) {
        const unsigned so = (2 * pr + hd) * 4096;
        kf[hd][0] = ldc<0>(rsK, lane * 16, so); kf[hd][1] = ldc<1024>(rsK, lane * 16, so);
        kf[hd][2] = ldc<2048>(rsK, lane * 16, so); kf[hd][3] = ldc<3072>(rsK, lane * 16, so);
      }
      f32x16 o[2] = {};
      waitv<4>(kf[0][0], kf[0][1], kf[0][2], kf[0][3]);
#pragma unroll
      for (int hd = 0; hd < 2; hd++) {
        if (hd) waitv<0>(kf[1][0], kf[1][1], kf[1][2], kf[1][3]);
#pragma unroll
        for (int c = 0; c < 4; c++) {
          const unsigned* s = &phq[hd][c >> 1][4 * (c & 1)];
          o[hd] = MFMA(kf[hd][c], __builtin_bit_cast(bf16x8, (u32x4){s[0], s[1], s[2], s[3]}), o[hd]);
        }
      }
      float da = o[0][12], db = o[1][12];
      swap32(da, db);
      float ia = __builtin_amdgcn_rcpf(db + 1e-6f), ib = ia;
      swap32(ia, ib);
      unsigned a[8], bq[8];
#pragma unroll
      for (int r = 0; r < 8; r++) { a[r] = pk(o[0][2 * r] * ia, o[0][2 * r + 1] * ia); bq[r] = pk(o[1][2 * r] * ib, o[1][2 * r + 1] * ib); }
      const rsrc_t st = mkst(ws + WS_OF, SZ_OF);
      const unsigned tk = (128 * T + 32 * w + n) * 16, jb = 14 * (pr >> 1);
      const unsigned vo = (jb + 6 * (pr & 1) + h) * (S * 16) + tk;
      stc(st, vo, (u32x4){a[0], a[1], a[2], a[3]});
      stc(st, vo + 2 * S * 16, (u32x4){a[4], a[5], bq[0], bq[1]});
      stc(st, vo + 4 * S * 16, (u32x4){bq[2], bq[3], bq[4], bq[5]});
      stc(st, h ? OOB : (jb + 12 + (pr & 1)) * (S * 16) + tk, (u32x4){a[6], a[7], bq[6], bq[7]});
    }
    publish(ws, FL_O + T * 8 + pr, ep);
    STAMP(12);
  }

  if (i >= 4) {
    const unsigned orow = st2 * 64 + 16 * w + (lane >> 3);
    const unsigned vo = orow * (D * 2) + dt2 * 128 + (lane & 7) * 16;
    bf16x8 xr0 = ldp<0>(rsX, vo, 0), xr1 = ldp<0>(rsX, vo, 8 * D * 2);
    const u32x4 rsB = mkrs(b2, S * 2);
    unsigned bs0 = ldu16<0>(rsB, orow * 2, 0), bs1 = ldu16<16>(rsB, orow * 2, 0);
    poll(rsF, (FL_U + dt2 * 14 + ((7 * w) >> 1) + (lane & 3)) * 128, ep);
    tie(xr0); tie(xr1); tie(bs0); tie(bs1);
    STAMP(13);
    const u32x4 rsU = mkrs(ws + WS_UF, SZ_UF), rsO = mkrs(ws + WS_OF, SZ_OF), rsWo = mkrs(wo, D * D * 2);
    const unsigned vu0 = h * (D * 16) + (dt2 * 64 + n) * 16, vu1 = vu0 + 2 * D * 16, sou = w * 56 * (D * 16);
    const unsigned vof = h * (S * 16) + (st2 * 64 + n) * 16, soo = w * 14 * (S * 16);
    const unsigned vfl = (FL_O + (st2 >> 1) * 8 + 2 * w + (lane & 1)) * 128;
    unsigned vwo[4];
#pragma unroll
    for (int j = 0; j < 4; j++) vwo[j] = swz * (D * 2) + swc + j * (8 * D * 2 - 1024);
    const unsigned sowo = w * 112 * (D * 2) + dt2 * 128;
    bf16x8 fa[18][4];
    u32x2 fl;
    auto ldD = [&](auto tt, auto jj) {
      constexpr int t = tt.value, j = jj.value;
      if constexpr (t < 14) fa[t][j] = ldc<(j & 1) * 512>(rsU, (j >> 1) ? vu1 : vu0, sou + t * (4 * D * 16));
      else if constexpr (j < g2ld(t)) fa[t][j] = ldc<(j & 1) * 512>(rsO, vof, soo + (2 * (t - 14) + (j >> 1)) * (2 * S * 16));
    };
    auto dmaS = [&](auto tt, auto jj) {
      constexpr int t = tt.value, j = jj.value;
      if constexpr (t < 14) dma1<j>(rsW2, v2, so2 + t * (32 * S * 2), area + ((t + 6) % 10) * 4096);
      else if constexpr (j < g2dm(t)) dma1<j>(rsWo, vwo, sowo + (t - 14) * (32 * D * 2), area + (t - 14) * 4096);
    };
    unroll<0, G2R>([&](auto tt) { unroll<0, 4>([&](auto jj) { ldD(tt, jj); }); });
    ldsp pb[2], pw[2];
#pragma unroll
    for (int a = 0; a < 2; a++) {
      const unsigned c = q * 128 + (((4 * a + 2 * blk + (pp >> 1)) ^ ((q >> 1) << 2)) * 16) + (pp & 1) * 8;
      pb[a] = lds + w * AREA + h * 1024 + c;
      pw[a] = lds + w * AREA + h * 512 + c;
    }
    f32x16 acc[2][2] = {};
    unroll<0, 18>([&](auto tt) {
      constexpr int t = tt.value, nk = t < 17 ? 2 : 1;
      constexpr int left = g2cnt(t - 3) + g2cnt(t - 2) + g2cnt(t - 1);
      if constexpr (nk == 2) waitv<left>(fa[t][0], fa[t][1], fa[t][2], fa[t][3]); else waitv<left>(fa[t][0], fa[t][1]);
      if constexpr (t == G2POLL + G2R) {
        tie(fl);
        if (!match(fl, ep)) poll(rsF, vfl, ep);
      }
      s16x4 lo[2][2], hi[2][2];
#pragma unroll
      for (int ks = 0; ks < nk; ks++)
#pragma unroll
        for (int a = 0; a < 2; a++) {
          if (t < 14) {
            lo[ks][a] = vtr(pb[a] + ((t + 6) % 10) * 4096 + ks * 2048); hi[ks][a] = vtr(pb[a] + ((t + 6) % 10) * 4096 + ks * 2048 + 512);
          } else {
            const int j = 2 * (t - 14) + ks;
            if (j < 6) {
              const int u = j % 3, rl = 56 * (j / 3) + (u == 0 ? 0 : u == 1 ? 16 : 36), rh = 56 * (j / 3) + (u == 0 ? 8 : u == 1 ? 28 : 44);
              lo[ks][a] = vtr(pw[a] + rl * 128); hi[ks][a] = vtr(pw[a] + rh * 128);
            } else {
              lo[ks][a] = vtr(pw[a] + h * (52 * 128) + 24 * 128); hi[ks][a] = vtr(pw[a] + h * (52 * 128) + 52 * 128);
            }
          }
        }
      SB();
      unroll<0, 4 * nk>([&](auto mm) {
        constexpr int m = mm.value, ks = m >> 2, nh = (m >> 1) & 1, mh = m & 1;
        if constexpr (m < 4) ldD(std::integral_constant<int, t + G2R>(), mm);
        else if constexpr (t >= 1) dmaS(std::integral_constant<int, t + 9>(), std::integral_constant<int, m - 4>());
        if constexpr (t == G2POLL && m == 7) fl = ldc2(rsF, vfl);
        if constexpr (t < 14) acc[nh][mh] = MFMA(fa[t][ks * 2 + mh], cat(lo[ks][nh], hi[ks][nh]), acc[nh][mh]);
        else acc[nh][mh] = MFMA(cat(lo[ks][mh], hi[ks][mh]), fa[t][ks * 2 + nh], acc[nh][mh]);
        SB();
      });
    });
    STAMP(14);
#pragma unroll
    for (int nh = 0; nh < 2; nh++)
#pragma unroll
      for (int mh = 0; mh < 2; mh++)
#pragma unroll
        for (int g = 0; g < 4; g++)
          lst(lds + w * AREA + (32 * nh + n) * 256 + (((8 * mh + 2 * g + h) ^ (n & 15)) * 16),
              (f32x4){acc[nh][mh][4 * g], acc[nh][mh][4 * g + 1], acc[nh][mh][4 * g + 2], acc[nh][mh][4 * g + 3]});
    bar();
    const rsrc_t sto = mkst(out, S * D * 2);
#pragma unroll
    for (int u = 0; u < 2; u++) {
      const int r = 16 * w + 8 * u + (lane >> 3);
      const unsigned c0 = r * 256 + (((2 * (lane & 7)) ^ (r & 15)) * 16), c1 = c0 ^ 16;
      f32x4 a = lld<f32x4>(lds + c0), bq = lld<f32x4>(lds + c1);
#pragma unroll
      for (int s = 1; s < 4; s++) { a += lld<f32x4>(lds + s * AREA + c0); bq += lld<f32x4>(lds + s * AREA + c1); }
      const u32x4 xu = __builtin_bit_cast(u32x4, u ? xr1 : xr0);
      const float bv = blo(u ? bs1 : bs0);
      u32x4 res = {pk(a[0] + bv + blo(xu[0]), a[1] + bv + bhi(xu[0])), pk(a[2] + bv + blo(xu[1]), a[3] + bv + bhi(xu[1])),
                   pk(bq[0] + bv + blo(xu[2]), bq[1] + bv + bhi(xu[2])), pk(bq[2] + bv + blo(xu[3]), bq[3] + bv + bhi(xu[3]))};
      __builtin_amdgcn_raw_buffer_store_b128(res, sto, vo + u * (8 * D * 2), 0, 0);
    }
    if (blockIdx.x == 255 && tid == 0)
      __hip_atomic_store((unsigned long long*)ws, ((unsigned long long)ep.hi << 32) | ep.lo, __ATOMIC_RELAXED, __HIP_MEMORY_SCOPE_AGENT);
  }
}

extern "C" void kernel_launch(void* const* d_in, const int* in_sizes, int n_in, void* d_out, int out_size, void* d_ws, size_t ws_size,
                              hipStream_t stream) {
  fused_kernel<<<256, 256, 0, stream>>>((const short*)d_in[0], (const short*)d_in[1], (const short*)d_in[2], (const short*)d_in[3],
                                        (const short*)d_in[5], (const short*)d_in[6], (const short*)d_in[7], (const short*)d_in[8],
                                        (short*)d_out, (char*)d_ws);
}
```

```cpp
#include <hip/hip_runtime.h>
#include <stdint.h>

typedef short bf16x8 __attribute__((ext_vector_type(8)));
typedef short s16x4 __attribute__((ext_vector_type(4)));
typedef float f32x2 __attribute__((ext_vector_type(2)));
typedef float f32x4 __attribute__((ext_vector_type(4)));
typedef float f32x16 __attribute__((ext_vector_type(16)));
typedef unsigned u32x2 __attribute__((ext_vector_type(2)));
typedef unsigned u32x4 __attribute__((ext_vector_type(4)));
typedef __bf16 b16x2 __attribute__((ext_vector_type(2)));
typedef __attribute__((address_space(3))) char* ldsp;
typedef __amdgpu_buffer_rsrc_t rsrc_t;

#define DEV __device__ __forceinline__
#define SB() __builtin_amdgcn_sched_barrier(0)
#define MFMA(a, b, c) __builtin_amdgcn_mfma_f32_32x32x16_bf16(a, b, c, 0, 0, 0)

constexpr int S = 2048, D = 448, F = 1792;
constexpr unsigned FL_H = 8, FL_X = FL_H + 128, FL_P = FL_X + 128, FL_R = FL_P + 128, FL_O = FL_R + 16, FL_U = FL_O + 128;
constexpr unsigned WS_FLAG_BYTES = 131072;
constexpr unsigned WS_HF = WS_FLAG_BYTES, SZ_HF = 256 * 448 * 16;
constexpr unsigned WS_XN = WS_HF + SZ_HF, SZ_XN = 56 * 2048 * 16;
constexpr unsigned WS_UF = WS_XN + SZ_XN, SZ_UF = 224 * 448 * 16;
constexpr unsigned WS_OF = WS_UF + SZ_UF, SZ_OF = 56 * 2048 * 16;
constexpr unsigned WS_KVP = WS_OF + SZ_OF, SZ_KVP = 16 * 16 * 2 * 4096;
constexpr unsigned WS_KVF = WS_KVP + SZ_KVP, SZ_KVF = 16 * 4 * 1024;
constexpr unsigned WS_END = WS_KVF + SZ_KVF;
constexpr unsigned AREA = 40960;
constexpr unsigned L_WK = 0, L_WV = 50816, L_WQ = 101632, L_WPHI = 152448, L_PHI = 0, L_VX = 32768;
constexpr unsigned OOB = 0x40000000u;
constexpr int SPIN = 1 << 18;

DEV u32x4 mkrs(const void* p, unsigned bytes) {
  unsigned long long a = (unsigned long long)p;
  u32x4 r = {(unsigned)a, (unsigned)(a >> 32), bytes, 0x00020000u};
  return r;
}
DEV rsrc_t mkst(void* p, unsigned bytes) { return __builtin_amdgcn_make_buffer_rsrc(p, 0, bytes, 0x00020000); }
DEV void stc(rsrc_t rs, unsigned voff, u32x4 v) { __builtin_amdgcn_raw_buffer_store_b128(v, rs, voff, 0, 16); }
template <int OFF> DEV bf16x8 ldp(u32x4 rs, unsigned voff, unsigned soff) {
  bf16x8 r;
  asm volatile("buffer_load_dwordx4 %0, %2, %1, %3 offen offset:%4" : "=v"(r) : "s"(rs), "v"(voff), "s"(soff), "n"(OFF) : "memory");
  return r;
}
template <int OFF> DEV bf16x8 ldc(u32x4 rs, unsigned voff, unsigned soff) {
  bf16x8 r;
  asm volatile("buffer_load_dwordx4 %0, %2, %1, %3 offen offset:%4 sc1" : "=v"(r) : "s"(rs), "v"(voff), "s"(soff), "n"(OFF) : "memory");
  return r;
}
template <int OFF> DEV u32x2 ldp2(u32x4 rs, unsigned voff, unsigned soff) {
  u32x2 r;
  asm volatile("buffer_load_dwordx2 %0, %2, %1, %3 offen offset:%4" : "=v"(r) : "s"(rs), "v"(voff), "s"(soff), "n"(OFF) : "memory");
  return r;
}
DEV u32x2 ldc2(u32x4 rs, unsigned voff) {
  u32x2 r;
  asm volatile("buffer_load_dwordx2 %0, %2, %1, 0 offen sc1" : "=v"(r) : "s"(rs), "v"(voff) : "memory");
  return r;
}
template <int OFF> DEV unsigned ldu16(u32x4 rs, unsigned voff, unsigned soff) {
  unsigned r;
  asm volatile("buffer_load_ushort %0, %2, %1, %3 offen offset:%4" : "=v"(r) : "s"(rs), "v"(voff), "s"(soff), "n"(OFF) : "memory");
  return r;
}
DEV void setm0(unsigned a) { asm volatile("s_mov_b32 m0, %0\n\ts_nop 0" ::"s"(a) : "memory"); }
template <int OFF> DEV void dma(u32x4 rs, unsigned voff, unsigned soff) {
  asm volatile("buffer_load_dwordx4 %1, %0, %2 offen offset:%3 lds" ::"s"(rs), "v"(voff), "s"(soff), "n"(OFF) : "memory");
}
DEV void dma4(u32x4 rs, const unsigned (&v)[4], unsigned soff, unsigned m0) {
  setm0(m0); dma<0>(rs, v[0], soff); dma<1024>(rs, v[1], soff); dma<2048>(rs, v[2], soff); dma<3072>(rs, v[3], soff);
}
template <int J> DEV void dma1(u32x4 rs, const unsigned (&v)[4], unsigned soff, unsigned m0) {
  if (J == 0) setm0(m0);
  dma<J * 1024>(rs, v[J], soff);
}
template <int N> DEV void waitv() { asm volatile("s_waitcnt vmcnt(%0)" ::"n"(N) : "memory"); }
template <int N, class T> DEV void waitv(T& a) { asm volatile("s_waitcnt vmcnt(%1)" : "+v"(a) : "n"(N) : "memory"); }
template <int N, class T> DEV void waitv(T& a, T& b) { asm volatile("s_waitcnt vmcnt(%2)" : "+v"(a), "+v"(b) : "n"(N) : "memory"); }
template <int N, class T> DEV void waitv(T& a, T& b, T& c, T& d) {
  asm volatile("s_waitcnt vmcnt(%4)" : "+v"(a), "+v"(b), "+v"(c), "+v"(d) : "n"(N) : "memory");
}
template <class T> DEV void tie(T& a) { asm volatile("" : "+v"(a)::"memory"); }
DEV void bar() { asm volatile("s_waitcnt lgkmcnt(0)\n\ts_barrier" ::: "memory"); }
DEV void barv() { asm volatile("s_waitcnt vmcnt(0) lgkmcnt(0)\n\ts_barrier" ::: "memory"); }
DEV s16x4 vtr(ldsp p) { return __builtin_amdgcn_ds_read_tr16_b64_v4i16((__attribute__((address_space(3))) s16x4*)p); }
DEV bf16x8 cat(s16x4 a, s16x4 b) { return __builtin_shufflevector(a, b, 0, 1, 2, 3, 4, 5, 6, 7); }
DEV unsigned pk(float a, float b) {
  f32x2 v = {a, b};
  return __builtin_bit_cast(unsigned, __builtin_convertvector(v, b16x2));
}
DEV float blo(unsigned u) { return __builtin_bit_cast(float, u << 16); }
DEV float bhi(unsigned u) { return __builtin_bit_cast(float, u & 0xffff0000u); }
DEV void swap32(float& a, float& b) {
  auto r = __builtin_amdgcn_permlane32_swap(__builtin_bit_cast(unsigned, a), __builtin_bit_cast(unsigned, b), false, false);
  const unsigned r0 = r[0], r1 = r[1];
  a = __builtin_bit_cast(float, r0); b = __builtin_bit_cast(float, r1);
}
DEV void swap16(float& a, float& b) {
  auto r = __builtin_amdgcn_permlane16_swap(__builtin_bit_cast(unsigned, a), __builtin_bit_cast(unsigned, b), false, false);
  const unsigned r0 = r[0], r1 = r[1];
  a = __builtin_bit_cast(float, r0); b = __builtin_bit_cast(float, r1);
}
template <int C> DEV float ror(float z) {
  return __builtin_bit_cast(float, __builtin_amdgcn_update_dpp(0, __builtin_bit_cast(int, z), C, 0xf, 0xf, true));
}
DEV float rowsum(float z) { z += ror<0x128>(z); z += ror<0x124>(z); z += ror<0x122>(z); z += ror<0x121>(z); return z; }
DEV float rdl(float z, int l) { return __builtin_bit_cast(float, __builtin_amdgcn_readlane(__builtin_bit_cast(int, z), l)); }
template <class T> DEV void lst(ldsp p, T v) { *(__attribute__((address_space(3))) T*)p = v; }
template <class T> DEV T lld(ldsp p) { return *(__attribute__((address_space(3))) T*)p; }

struct Epoch { unsigned lo, hi; };
DEV void publish(char* ws, unsigned flag, Epoch e) {
  barv();
  if (threadIdx.x == 0) __builtin_amdgcn_raw_buffer_store_b64((u32x2){e.lo, e.hi}, mkst(ws, WS_FLAG_BYTES), flag * 128, 0, 16);
}
DEV bool match(u32x2 f, Epoch e) { return __all(f.x == e.lo && f.y == e.hi); }
DEV void poll(u32x4 rsF, unsigned voff, Epoch e) {
#pragma unroll 1
  for (int it = 0; it < SPIN; ++it) {
    u32x2 f = ldc2(rsF, voff);
    waitv<0>(f);
    if (match(f, e)) break;
    __builtin_amdgcn_s_sleep(1);
  }
}

template <bool TWICE> DEV void ln_rows(const bf16x8 (&v)[4], float (&xf)[4][8]) {
  float s[4], q[4];
#pragma unroll
  for (int r = 0; r < 4; r++) {
    u32x4 u = __builtin_bit_cast(u32x4, v[r]);
#pragma unroll
    for (int e = 0; e < 4; e++) { xf[r][2 * e] = blo(u[e]); xf[r][2 * e + 1] = bhi(u[e]); }
    s[r] = 0.f; q[r] = 0.f;
#pragma unroll
    for (int e = 0; e < 8; e++) { s[r] += xf[r][e]; q[r] = fmaf(xf[r][e], xf[r][e], q[r]); }
  }
  swap32(s[0], s[1]); swap32(s[2], s[3]); swap32(q[0], q[1]); swap32(q[2], q[3]);
  float sa = s[0] + s[1], sb = s[2] + s[3], qa = q[0] + q[1], qb = q[2] + q[3];
  swap16(sa, sb); swap16(qa, qb);
  float mu = rowsum(sa + sb) * (1.f / D), ms = rowsum(qa + qb) * (1.f / D);
  float var = fmaxf(ms - mu * mu, 0.f);
  float a = __builtin_amdgcn_rsqf(var + 1e-5f);
  if (TWICE) a *= __builtin_amdgcn_rsqf(var * a * a + 1e-5f);
  float c = -mu * a;
#pragma unroll
  for (int r = 0; r < 4; r++) {
    const int l = (r & 1) * 32 + (r >> 1) * 16;
    float ar = rdl(a, l), cr = rdl(c, l);
#pragma unroll
    for (int e = 0; e < 8; e++) xf[r][e] = fmaf(xf[r][e], ar, cr);
  }
}

DEV void features(const f32x16& p, bool h, unsigned (&ph)[2][8]) {
  constexpr float L = 1.4426950408889634f;
  float mx = fmaxf(p[0], p[1]), mn = fminf(p[0], p[1]);
#pragma unroll
  for (int r = 2; r < 12; r += 2) { mx = fmaxf(fmaxf(mx, p[r]), p[r + 1]); mn = fminf(fminf(mn, p[r]), p[r + 1]); }
  float mx2 = fmaxf(fmaxf(fmaxf(fmaxf(mx, p[12]), p[13]), p[14]), p[15]);
  float mn2 = fminf(fminf(fminf(fminf(mn, p[12]), p[13]), p[14]), p[15]);
  mx = h ? mx : mx2; mn = h ? -mn : -mn2;
  swap32(mx, mn);
  float c0 = -L * fmaxf(mx, mn), c1 = c0;
  swap32(c0, c1);
  const float ninf = -__builtin_inff();
  const float d0 = h ? ninf : c0, d1 = h ? ninf : c1;
  float e[2][16], s0 = 0.f, s1 = 0.f;
#pragma unroll
  for (int r = 0; r < 16; r++) {
    e[0][r] = __builtin_amdgcn_exp2f(fmaf(p[r], L, r < 12 ? c0 : d0));
    e[1][r] = __builtin_amdgcn_exp2f(fmaf(p[r], -L, r < 12 ? c1 : d1));
    s0 += e[0][r]; s1 += e[1][r];
  }
  swap32(s0, s1);
  float i0 = __builtin_amdgcn_rcpf(s0 + s1), i1 = i0;
  swap32(i0, i1);
#pragma unroll
  for (int r = 0; r < 8; r++) { ph[0][r] = pk(e[0][2 * r] * i0, e[0][2 * r + 1] * i0); ph[1][r] = pk(e[1][2 * r] * i1, e[1][2 * r + 1] * i1); }
}
DEV bf16x8 pack8(const f32x16& a, int o) {
  u32x4 u = {pk(a[o], a[o + 1]), pk(a[o + 2], a[o + 3]), pk(a[o + 4], a[o + 5]), pk(a[o + 6], a[o + 7])};
  return __builtin_bit_cast(bf16x8, u);
}
DEV float gelu(float a) {
  constexpr float c1 = -2.302208198f, c2 = c1 * 0.044715f;
  return a * __builtin_amdgcn_rcpf(1.f + __builtin_amdgcn_exp2f(a * fmaf(a * a, c2, c1)));
}

#define STAMP(n)

template <int T, int N, class Fn> DEV void unroll(Fn&& f) {
  if constexpr (T < N) { f(std::integral_constant<int, T>()); unroll<T + 1, N>(f); }
}
constexpr int g1cnt(int s) { return s < 0 ? 4 : (s + 3 < 16 ? (s >= 1 ? 12 : 4) : 0); }
constexpr int G2R = 4, G2POLL = 5;
constexpr int g2ld(int t) { return t < 17 ? 4 : (t == 17 ? 2 : 0); }
constexpr int g2dm(int t) { return t < 17 ? 4 : (t == 17 ? 2 : 0); }
constexpr int g2cnt(int s) { return s < 0 ? 4 : g2ld(s + G2R) + (s >= 1 ? g2dm(s + 9) : 0) + (s == G2POLL ? 1 : 0); }
constexpr int kvn(int ks) { return 8 + 2 * ks < 41 ? 8 + 2 * ks : 41; }

__global__ __launch_bounds__(256, 1) void fused_kernel(const short* __restrict__ x, const short* __restrict__ wqkv, const short* __restrict__ wphi,
                                                       const short* __restrict__ wo, const short* __restrict__ w1, const short* __restrict__ b1,
                                                       const short* __restrict__ w2, const short* __restrict__ b2, short* __restrict__ out,
                                                       char* __restrict__ ws) {
  __shared__ __attribute__((aligned(16))) char lds_[163840];
  const ldsp lds = (ldsp)lds_;
  const unsigned lds0 = (unsigned)(uintptr_t)lds;
  const int tid = threadIdx.x, lane = tid & 63, w = __builtin_amdgcn_readfirstlane(tid >> 6);
  asm volatile("s_nop 4" ::"s"(w), "s"(x), "s"(wqkv), "s"(wphi), "s"(wo), "s"(w1), "s"(b1), "s"(w2), "s"(b2), "s"(out), "s"(ws) : "memory");
  const int xc = blockIdx.x & 7, i = blockIdx.x >> 3;
  const int h = lane >> 5, n = lane & 31, q = (lane & 15) >> 2, pp = lane & 3, blk = (lane >> 4) & 1;
  const u32x4 rsF = mkrs(ws, WS_FLAG_BYTES), rsX = mkrs(x, S * D * 2), rsW2 = mkrs(w2, F * S * 2);
  const unsigned area = lds0 + w * AREA;
  const int tile2 = 28 * (xc & 1) + i - 4, dt2 = tile2 >> 3, st2 = 8 * (xc >> 1) + (tile2 & 7);
  const unsigned swz = (lane >> 3), swc = ((lane & 7) ^ (((lane >> 4) & 1) << 2)) * 16;
  unsigned v2[4];
#pragma unroll
  for (int j = 0; j < 4; j++) v2[j] = swz * (S * 2) + swc + j * (8 * S * 2 - 1024);
  const unsigned so2 = (w * 448) * (S * 2) + st2 * 128;
  auto pre2 = [&](int t0, int t1, int slot0) {
#pragma unroll 1
    for (int t = t0; t < t1; t++) dma4(rsW2, v2, so2 + t * (32 * S * 2), area + (slot0 + t - t0) * 4096);
  };

  const bool isA = i >= 16;
  const int T = 4 * (xc >> 1) + (i & 3), pr = 4 * (xc & 1) + ((i - 16) >> 2);
  const int row0 = isA ? 128 * T + 16 * pr : 256 * xc + 16 * i;
  auto ldrows = [&](bf16x8 (&xv)[4], u32x2& gen) {
    const unsigned vo = lane < 56 ? lane * 16 : OOB, so = (row0 + 4 * w) * (D * 2);
    xv[0] = ldp<0>(rsX, vo, so); xv[1] = ldp<896>(rsX, vo, so); xv[2] = ldp<1792>(rsX, vo, so); xv[3] = ldp<2688>(rsX, vo, so);
    gen = ldc2(rsF, 0);
  };
  Epoch ep;

  if (!isA) {
    bf16x8 xv[4];
    u32x2 gen;
    ldrows(xv, gen);
    const int g0 = (49 * xc + 3) >> 2, g1 = (49 * xc + 52) >> 2;
    const bool isG = g0 + i < g1;
    const int ft = (g0 + i) / 7, dt = (g0 + i) % 7;
    const u32x4 rsW1 = mkrs(w1, S * F * 2);
    unsigned v1[4];
#pragma unroll
    for (int j = 0; j < 4; j++) v1[j] = swz * (F * 2) + swc + j * (8 * F * 2 - 1024);
    const unsigned so1 = (w * 512) * (F * 2) + ft * 256;
    if (isG) {
#pragma unroll
      for (int t = 0; t < 2; t++) { dma4(rsW1, v1, so1 + t * (32 * F * 2), area + t * 8192); dma4(rsW1, v1, so1 + t * (32 * F * 2) + 128, area + t * 8192 + 4096); }
    } else {
      pre2(4, 8, 0);
    }
    waitv<16>(xv[0], xv[1], xv[2], xv[3]);
    tie(gen);
    STAMP(0);
    {
      unsigned long long g = (((unsigned long long)__builtin_amdgcn_readfirstlane(gen.y) << 32) | (unsigned)__builtin_amdgcn_readfirstlane(gen.x)) + 1;
      ep.lo = (unsigned)g; ep.hi = (unsigned)(g >> 32);
    }
    {
      float xf[4][8];
      ln_rows<true>(xv, xf);
      if (lane < 56) {
        const ldsp p = lds + (w >> 1) * AREA + 32768 + (w & 1) * 8 + lane * 16;
#pragma unroll
        for (int e = 0; e < 8; e++) lst(p + e * (57 * 16), (u32x2){pk(xf[0][e], xf[1][e]), pk(xf[2][e], xf[3][e])});
      }
      bar();
      const ldsp p = lds + 32768 + ((tid & 7) * 57 + (tid >> 3)) * 16;
      const rsrc_t st = mkst(ws + WS_HF, SZ_HF);
      const unsigned vo = (row0 >> 3) * (D * 16) + tid * 16;
      u32x4 e0 = lld<u32x4>(p), e1 = lld<u32x4>(p + (w < 3 ? 512 : AREA - 384)), e2 = lld<u32x4>(p + AREA + 128);
      stc(st, vo, e0); stc(st, vo + 4096, e1); stc(st, vo + 8192, e2);
      if (w < 2) stc(st, vo + 12288, lld<u32x4>(p + AREA + 640));
    }
    STAMP(1);
    publish(ws, FL_H + (row0 >> 4), ep);
    STAMP(2);

    if (isG) {
#pragma unroll
      for (int t = 2; t < 4; t++) { dma4(rsW1, v1, so1 + t * (32 * F * 2), area + t * 8192); dma4(rsW1, v1, so1 + t * (32 * F * 2) + 128, area + t * 8192 + 4096); }
      u32x2 bb[4];
      {
        const u32x4 rsB = mkrs(b1, F * 2);
        const unsigned so = (ft * 128 + w * 32) * 2;
        bb[0] = ldp2<0>(rsB, h * 8, so); bb[1] = ldp2<16>(rsB, h * 8, so); bb[2] = ldp2<32>(rsB, h * 8, so); bb[3] = ldp2<48>(rsB, h * 8, so);
      }
      poll(rsF, (FL_H + w * 32 + n) * 128, ep);
      STAMP(3);
      const u32x4 rsH = mkrs(ws + WS_HF, SZ_HF);
      const unsigned vh0 = h * (D * 16) + (dt * 64 + n) * 16, vh1 = vh0 + 2 * D * 16, soh = w * 64 * (D * 16);
      bf16x8 fb[16][4];
      auto ldH = [&](auto tt, auto jj) {
        constexpr int t = tt.value, j = jj.value;
        fb[t][j] = ldc<(j & 1) * 512>(rsH, (j >> 1) ? vh1 : vh0, soh + t * (4 * D * 16));
      };
      unroll<0, 3>([&](auto tt) { unroll<0, 4>([&](auto jj) { ldH(tt, jj); }); });
      waitv<12>(bb[0], bb[1], bb[2], bb[3]);
      f32x16 acc[4][2];
#pragma unroll
      for (int g = 0; g < 4; g++) {
        acc[0][0][4 * g] = blo(bb[g].x); acc[0][0][4 * g + 1] = bhi(bb[g].x); acc[0][0][4 * g + 2] = blo(bb[g].y); acc[0][0][4 * g + 3] = bhi(bb[g].y);
      }
      acc[0][1] = acc[0][0];
#pragma unroll
      for (int a = 1; a < 4; a++) { acc[a][0] = (f32x16)(0.f); acc[a][1] = (f32x16)(0.f); }
      ldsp pa[4];
#pragma unroll
      for (int a = 0; a < 4; a++) {
        const int s = a ^ w;
        pa[a] = lds + w * AREA + (s >> 1) * 4096 + (8 * h + q) * 128 + (((4 * (s & 1) + 2 * blk + (pp >> 1)) ^ ((q >> 1) << 2)) * 16) + (pp & 1) * 8;
      }
      unroll<0, 16>([&](auto tt) {
        constexpr int t = tt.value, sl = t & 3;
        waitv<g1cnt(t - 2) + g1cnt(t - 1)>(fb[t][0], fb[t][1], fb[t][2], fb[t][3]);
        s16x4 lo[2][4], hi[2][4];
#pragma unroll
        for (int ks = 0; ks < 2; ks++)
#pragma unroll
          for (int a = 0; a < 4; a++) { lo[ks][a] = vtr(pa[a] + sl * 8192 + ks * 2048); hi[ks][a] = vtr(pa[a] + sl * 8192 + ks * 2048 + 512); }
        SB();
        unroll<0, 16>([&](auto mm) {
          constexpr int m = mm.value, ks = m >> 3, a = (m >> 1) & 3, dh = m & 1;
          if constexpr (t + 3 < 16) {
            if constexpr (m < 4) ldH(std::integral_constant<int, t + 3>(), mm);
            else if constexpr (m < 12 && t >= 1)
              dma1<(m & 3)>(rsW1, v1, so1 + (t + 3) * (32 * F * 2) + (m >= 8 ? 128 : 0), area + ((t + 3) & 3) * 8192 + (m >= 8 ? 4096 : 0));
          }
          acc[a][dh] = MFMA(cat(lo[ks][a], hi[ks][a]), fb[t][ks * 2 + dh], acc[a][dh]);
          SB();
        });
      });
      STAMP(4);
      if (i >= 4) pre2(0, 4, 6);
      {
        const ldsp p = lds + w * AREA + lane * 16;
#pragma unroll
        for (int a = 1; a < 4; a++)
#pragma unroll
          for (int dh = 0; dh < 2; dh++)
#pragma unroll
            for (int g = 0; g < 4; g++)
              lst(p + (a - 1) * 8192 + dh * 4096 + g * 1024, (f32x4){acc[a][dh][4 * g], acc[a][dh][4 * g + 1], acc[a][dh][4 * g + 2], acc[a][dh][4 * g + 3]});
      }
      bar();
      STAMP(5);
      {
        const rsrc_t st = mkst(ws + WS_UF, SZ_UF);
        const unsigned vo = (ft * 16 + w * 4 + h) * (D * 16) + (dt * 64 + n) * 16;
        f32x4 v[2][3][4];
#pragma unroll
        for (int dh = 0; dh < 2; dh++)
#pragma unroll
          for (int a = 1; a < 4; a++)
#pragma unroll
            for (int g = 0; g < 4; g++) v[dh][a - 1][g] = lld<f32x4>(lds + (w ^ a) * AREA + lane * 16 + (a - 1) * 8192 + dh * 4096 + g * 1024);
        SB();
#pragma unroll
        for (int dh = 0; dh < 2; dh++) {
#pragma unroll
          for (int g = 0; g < 4; g += 2) {
            float r[8];
#pragma unroll
            for (int e = 0; e < 8; e++) {
              const int gg = g + (e >> 2), c = e & 3;
              r[e] = gelu((acc[0][dh][4 * gg + c] + v[dh][0][gg][c]) + (v[dh][1][gg][c] + v[dh][2][gg][c]));
            }
            auto r0 = __builtin_amdgcn_permlane32_swap(pk(r[0], r[1]), pk(r[4], r[5]), false, false);
            auto r1 = __builtin_amdgcn_permlane32_swap(pk(r[2], r[3]), pk(r[6], r[7]), false, false);
            stc(st, vo + g * (D * 16) + dh * 512, (u32x4){r0[0], r1[0], r0[1], r1[1]});
          }
        }
      }
      STAMP(6);
      publish(ws, FL_U + dt * 14 + ft, ep);
      STAMP(7);
      if (i >= 4) pre2(4, 10, 0);
    } else {
      pre2(0, 4, 6);
      pre2(4, 10, 0);
      if (i >= 14) {
        const int head = 2 * xc + i - 14;
        poll(rsF, (FL_P + xc * 16 + (lane & 15)) * 128, ep);
        STAMP(3);
        const u32x4 rsP = mkrs(ws + WS_KVP, SZ_KVP);
        const unsigned so = head * 131072 + (w >> 1) * 4096 + (w & 1) * 2048;
        bf16x8 pv[16][2];
        unroll<0, 16>([&](auto tt) { constexpr int t = tt.value; pv[t][0] = ldc<0>(rsP, lane * 16, so + t * 8192); pv[t][1] = ldc<1024>(rsP, lane * 16, so + t * 8192); });
        f32x4 a0 = {0.f, 0.f, 0.f, 0.f}, a1 = a0;
        unroll<0, 16>([&](auto tt) {
          constexpr int t = tt.value;
          waitv<30 - 2 * t>(pv[t][0], pv[t][1]);
          a0 += __builtin_bit_cast(f32x4, pv[t][0]); a1 += __builtin_bit_cast(f32x4, pv[t][1]);
        });
        stc(mkst(ws + WS_KVF, SZ_KVF), head * 4096 + w * 1024 + lane * 16, (u32x4){pk(a0[0], a0[1]), pk(a0[2], a0[3]), pk(a1[0], a1[1]), pk(a1[2], a1[3])});
        STAMP(4);
        publish(ws, FL_R + head, ep);
        STAMP(5);
      }
    }
  } else {
    bf16x8 xv[4];
    u32x2 gen;
    ldrows(xv, gen);
    const u32x4 rsW = mkrs(wqkv, D * 3 * D * 2);
    unsigned vw[4];
    {
      const unsigned r7 = (lane * 37) >> 8;
#pragma unroll
      for (int j = 0; j < 4; j++) vw[j] = r7 * 2688 + (lane - r7 * 7) * 16 + j * (9 * 2688 - 1008);
    }
    const unsigned sow = w * 112 * 2688 + pr * 112, mw = lds0 + w * 112 * 112;
    auto dmaW = [&](auto kk, unsigned col, unsigned base) {
      constexpr int k = kk.value;
      if constexpr ((k & 3) == 0) setm0(mw + base + (k >> 2) * 4032);
      dma<(k & 3) * 1008>(rsW, vw[k & 3], sow + col + (k >> 2) * (36 * 2688));
    };
    setm0(lds0 + L_WPHI + w * 1024);
    dma<0>(mkrs(wphi + pr * 1568, 3136), tid * 16, 0);
    unroll<0, 13>([&](auto kk) { dmaW(kk, 896, L_WK); });
    waitv<14>(xv[0], xv[1], xv[2], xv[3]);
    tie(gen);
    STAMP(0);
    {
      unsigned long long g = (((unsigned long long)__builtin_amdgcn_readfirstlane(gen.y) << 32) | (unsigned)__builtin_amdgcn_readfirstlane(gen.x)) + 1;
      ep.lo = (unsigned)g; ep.hi = (unsigned)(g >> 32);
    }
    {
      float xf[4][8];
      ln_rows<false>(xv, xf);
      if (lane < 56) {
        const ldsp p = lds + L_WQ + (lane >> 1) * 544 + (lane & 1) * 4 + w * 64;
#pragma unroll
        for (int r = 0; r < 4; r++)
#pragma unroll
          for (int e = 0; e < 4; e++) lst(p + r * 16 + (e >> 1) * 272 + (e & 1) * 8, pk(xf[r][e], xf[r][e + 4]));
      }
      bar();
      const ldsp p = lds + L_WQ + (tid >> 4) * 272 + (tid & 15) * 16;
      const rsrc_t st = mkst(ws + WS_XN, SZ_XN);
      const unsigned vo = (tid >> 4) * (S * 16) + (row0 + (tid & 15)) * 16;
      u32x4 e0 = lld<u32x4>(p), e1 = lld<u32x4>(p + 16 * 272), e2 = lld<u32x4>(p + 32 * 272);
      stc(st, vo, e0); stc(st, vo + 16 * S * 16, e1); stc(st, vo + 32 * S * 16, e2);
      if (w < 2) stc(st, vo + 48 * S * 16, lld<u32x4>(p + 48 * 272));
    }
    STAMP(1);
    publish(ws, FL_X + T * 8 + pr, ep);
    STAMP(2);
    unroll<0, 13>([&](auto kk) { dmaW(kk, 1792, L_WV); });
    poll(rsF, (FL_X + T * 8 + 2 * w + (lane & 1)) * 128, ep);
    STAMP(3);
    const u32x4 rsN = mkrs(ws + WS_XN, SZ_XN);
    const unsigned vx = h * (S * 16) + (128 * T + 32 * w + n) * 16;
    bf16x8 xa[28];
    auto item = [&](auto kk) {
      constexpr int k = kk.value;
      if constexpr (k < 28) xa[k] = ldc<0>(rsN, vx, k * (2 * S * 16));
      else if constexpr (k < 41) dmaW(std::integral_constant<int, k - 28>(), 0, L_WQ);
    };
    unroll<0, 8>(item);
    bar();
    bf16x8 wf[2][2];
    {
      const ldsp p = lds + L_WPHI + (4 * h + q) * 56 + 32 * blk + 8 * pp;
#pragma unroll
      for (int hd = 0; hd < 2; hd++) {
        wf[hd][0] = cat(vtr(p + hd * 1568), vtr(p + hd * 1568 + 8 * 56));
        s16x4 z = vtr(p + hd * 1568 + 24 * 56);
        wf[hd][1] = cat(vtr(p + hd * 1568 + 16 * 56), h ? (s16x4){0, 0, 0, 0} : z);
      }
    }
    const unsigned wlane = (2 * h + 4 * q) * 112 + 32 * blk + 8 * pp;
    f32x16 kT[2] = {}, vv[2] = {};
    {
      const ldsp pk_ = lds + L_WK + wlane, pv_ = lds + L_WV + wlane;
      s16x4 fr[29][8];
      auto rd = [&](auto kk, auto cc) {
        constexpr int ks = kk.value, c = cc.value;
        if constexpr (ks < 28) fr[ks][c] = vtr(((c & 4) ? pv_ : pk_) + ks * 1792 + ((c >> 1) & 1) * 56 + (c & 1) * 112);
      };
      unroll<0, 8>([&](auto cc) { rd(std::integral_constant<int, 0>(), cc); });
      unroll<0, 28>([&](auto kk) {
        constexpr int ks = kk.value;
        waitv<kvn(ks) - ks - 1>(xa[ks]);
        if (ks == 0) STAMP(4);
        unroll<0, 4>([&](auto mm) {
          constexpr int m = mm.value;
          rd(std::integral_constant<int, ks + 1>(), std::integral_constant<int, 2 * m>());
          rd(std::integral_constant<int, ks + 1>(), std::integral_constant<int, 2 * m + 1>());
          if constexpr (m & 1) item(std::integral_constant<int, 8 + 2 * ks + (m >> 1)>());
          const bf16x8 wv = cat(fr[ks][2 * m], fr[ks][2 * m + 1]);
          if constexpr (m < 2) kT[m] = MFMA(wv, xa[ks], kT[m]);
          else vv[m - 2] = MFMA(xa[ks], wv, vv[m - 2]);
          SB();
        });
      });
    }
    STAMP(5);
    {
      f32x16 p[2];
#pragma unroll
      for (int hd = 0; hd < 2; hd++) { p[hd] = MFMA(wf[hd][0], pack8(kT[hd], 0), (f32x16)(0.f)); p[hd] = MFMA(wf[hd][1], pack8(kT[hd], 8), p[hd]); }
      bar();
      {
        const bf16x8 ones = {0x3f80, 0x3f80, 0x3f80, 0x3f80, 0x3f80, 0x3f80, 0x3f80, 0x3f80};
        const ldsp pvx = lds + L_VX + w * 2048 + lane * 16;
#pragma unroll
        for (int hd = 0; hd < 2; hd++)
#pragma unroll
          for (int j = 0; j < 2; j++) { bf16x8 f = pack8(vv[hd], 8 * j); lst(pvx + hd * 8192 + j * 1024, n == 28 ? ones : f); }
      }
      const unsigned pb = (32 * w + n) * 64 + ((h ^ ((n >> 1) & 7)) * 8);
#pragma unroll
      for (int hd = 0; hd < 2; hd++) {
        unsigned ph[2][8];
        features(p[hd], h, ph);
#pragma unroll
        for (int sg = 0; sg < 2; sg++)
#pragma unroll
          for (int g = 0; g < 4; g++) lst(lds + L_PHI + (hd * 2 + sg) * 8192 + (pb ^ (g << 4)), (u32x2){ph[sg][2 * g], ph[sg][2 * g + 1]});
      }
    }
    STAMP(6);
    barv();
    {
      const unsigned sw = 2 * h + (q >> 1);
      const ldsp pl = lds + L_PHI + w * 8192 + (4 * h + q) * 64 + (((4 * blk + pp) ^ sw) * 8);
      const ldsp ph_ = lds + L_PHI + w * 8192 + (8 + 4 * h + q) * 64 + (((4 * blk + pp) ^ sw ^ 4) * 8);
      const ldsp pvx = lds + L_VX + (w >> 1) * 8192 + lane * 16;
      f32x16 kv = {};
#pragma unroll
      for (int jj = 0; jj < 8; jj++) kv = MFMA(cat(vtr(pl + jj * 1024), vtr(ph_ + jj * 1024)), lld<bf16x8>(pvx + jj * 1024), kv);
      const rsrc_t st = mkst(ws + WS_KVP, SZ_KVP);
      const unsigned vo = (2 * pr + (w >> 1)) * 131072 + T * 8192 + (w & 1) * 4096 + lane * 16;
#pragma unroll
      for (int g = 0; g < 4; g++) stc(st, vo + g * 1024, __builtin_bit_cast(u32x4, (f32x4){kv[4 * g], kv[4 * g + 1], kv[4 * g + 2], kv[4 * g + 3]}));
    }
    STAMP(7);
    f32x16 qT[2] = {};
    {
      const ldsp pq_ = lds + L_WQ + wlane;
      s16x4 fr[29][4];
      auto rd = [&](auto kk, auto cc) {
        constexpr int ks = kk.value, c = cc.value;
        if constexpr (ks < 28) fr[ks][c] = vtr(pq_ + ks * 1792 + (c >> 1) * 56 + (c & 1) * 112);
      };
      unroll<0, 4>([&](auto cc) { rd(std::integral_constant<int, 0>(), cc); });
      unroll<0, 28>([&](auto kk) {
        constexpr int ks = kk.value;
        if constexpr (ks == 14) { publish(ws, FL_P + pr * 16 + T, ep); STAMP(8); SB(); }
        unroll<0, 2>([&](auto mm) {
          constexpr int m = mm.value;
          rd(std::integral_constant<int, ks + 1>(), std::integral_constant<int, 2 * m>());
          rd(std::integral_constant<int, ks + 1>(), std::integral_constant<int, 2 * m + 1>());
          qT[m] = MFMA(cat(fr[ks][2 * m], fr[ks][2 * m + 1]), xa[ks], qT[m]);
          SB();
        });
      });
    }
    unsigned phq[2][2][8];
    {
      f32x16 p[2];
#pragma unroll
      for (int hd = 0; hd < 2; hd++) { p[hd] = MFMA(wf[hd][0], pack8(qT[hd], 0), (f32x16)(0.f)); p[hd] = MFMA(wf[hd][1], pack8(qT[hd], 8), p[hd]); }
      bar();
      STAMP(9);
      pre2(0, 4, 6);
      pre2(4, 10, 0);
      features(p[0], h, phq[0]);
      features(p[1], h, phq[1]);
    }
    STAMP(10);
    poll(rsF, (FL_R + 2 * pr + (lane & 1)) * 128, ep);
    STAMP(11);
    {
      const u32x4 rsK = mkrs(ws + WS_KVF, SZ_KVF);
      bf16x8 kf[2][4];
#pragma unroll
      for (int hd = 0; hd < 2; hd++) {
        const unsigned so = (2 * pr + hd) * 4096;
        kf[hd][0] = ldc<0>(rsK, lane * 16, so); kf[hd][1] = ldc<1024>(rsK, lane * 16, so);
        kf[hd][2] = ldc<2048>(rsK, lane * 16, so); kf[hd][3] = ldc<3072>(rsK, lane * 16, so);
      }
      f32x16 o[2] = {};
      waitv<4>(kf[0][0], kf[0][1], kf[0][2], kf[0][3]);
#pragma unroll
      for (int hd = 0; hd < 2; hd++) {
        if (hd) waitv<0>(kf[1][0], kf[1][1], kf[1][2], kf[1][3]);
#pragma unroll
        for (int c = 0; c < 4; c++) {
          const unsigned* s = &phq[hd][c >> 1][4 * (c & 1)];
          o[hd] = MFMA(kf[hd][c], __builtin_bit_cast(bf16x8, (u32x4){s[0], s[1], s[2], s[3]}), o[hd]);
        }
      }
      float da = o[0][12], db = o[1][12];
      swap32(da, db);
      float ia = __builtin_amdgcn_rcpf(db + 1e-6f), ib = ia;
      swap32(ia, ib);
      unsigned a[8], bq[8];
#pragma unroll
      for (int r = 0; r < 8; r++) { a[r] = pk(o[0][2 * r] * ia, o[0][2 * r + 1] * ia); bq[r] = pk(o[1][2 * r] * ib, o[1][2 * r + 1] * ib); }
      const rsrc_t st = mkst(ws + WS_OF, SZ_OF);
      const unsigned tk = (128 * T + 32 * w + n) * 16, jb = 14 * (pr >> 1);
      const unsigned vo = (jb + 6 * (pr & 1) + h) * (S * 16) + tk;
      stc(st, vo, (u32x4){a[0], a[1], a[2], a[3]});
      stc(st, vo + 2 * S * 16, (u32x4){a[4], a[5], bq[0], bq[1]});
      stc(st, vo + 4 * S * 16, (u32x4){bq[2], bq[3], bq[4], bq[5]});
      stc(st, h ? OOB : (jb + 12 + (pr & 1)) * (S * 16) + tk, (u32x4){a[6], a[7], bq[6], bq[7]});
    }
    publish(ws, FL_O + T * 8 + pr, ep);
    STAMP(12);
  }

  if (i >= 4) {
    const unsigned orow = st2 * 64 + 16 * w + (lane >> 3);
    const unsigned vo = orow * (D * 2) + dt2 * 128 + (lane & 7) * 16;
    bf16x8 xr0 = ldp<0>(rsX, vo, 0), xr1 = ldp<0>(rsX, vo, 8 * D * 2);
    const u32x4 rsB = mkrs(b2, S * 2);
    unsigned bs0 = ldu16<0>(rsB, orow * 2, 0), bs1 = ldu16<16>(rsB, orow * 2, 0);
    poll(rsF, (FL_U + dt2 * 14 + ((7 * w) >> 1) + (lane & 3)) * 128, ep);
    tie(xr0); tie(xr1); tie(bs0); tie(bs1);
    STAMP(13);
    const u32x4 rsU = mkrs(ws + WS_UF, SZ_UF), rsO = mkrs(ws + WS_OF, SZ_OF), rsWo = mkrs(wo, D * D * 2);
    const unsigned vu0 = h * (D * 16) + (dt2 * 64 + n) * 16, vu1 = vu0 + 2 * D * 16, sou = w * 56 * (D * 16);
    const unsigned vof = h * (S * 16) + (st2 * 64 + n) * 16, soo = w * 14 * (S * 16);
    const unsigned vfl = (FL_O + (st2 >> 1) * 8 + 2 * w + (lane & 1)) * 128;
    unsigned vwo[4];
#pragma unroll
    for (int j = 0; j < 4; j++) vwo[j] = swz * (D * 2) + swc + j * (8 * D * 2 - 1024);
    const unsigned sowo = w * 112 * (D * 2) + dt2 * 128;
    bf16x8 fa[18][4];
    u32x2 fl;
    auto ldD = [&](auto tt, auto jj) {
      constexpr int t = tt.value, j = jj.value;
      if constexpr (t < 14) fa[t][j] = ldc<(j & 1) * 512>(rsU, (j >> 1) ? vu1 : vu0, sou + t * (4 * D * 16));
      else if constexpr (j < g2ld(t)) fa[t][j] = ldc<(j & 1) * 512>(rsO, vof, soo + (2 * (t - 14) + (j >> 1)) * (2 * S * 16));
    };
    auto dmaS = [&](auto tt, auto jj) {
      constexpr int t = tt.value, j = jj.value;
      if constexpr (t < 14) dma1<j>(rsW2, v2, so2 + t * (32 * S * 2), area + ((t + 6) % 10) * 4096);
      else if constexpr (j < g2dm(t)) dma1<j>(rsWo, vwo, sowo + (t - 14) * (32 * D * 2), area + (t - 14) * 4096);
    };
    unroll<0, G2R>([&](auto tt) { unroll<0, 4>([&](auto jj) { ldD(tt, jj); }); });
    ldsp pb[2], pw[2];
#pragma unroll
    for (int a = 0; a < 2; a++) {
      const unsigned c = q * 128 + (((4 * a + 2 * blk + (pp >> 1)) ^ ((q >> 1) << 2)) * 16) + (pp & 1) * 8;
      pb[a] = lds + w * AREA + h * 1024 + c;
      pw[a] = lds + w * AREA + h * 512 + c;
    }
    f32x16 acc[2][2] = {};
    unroll<0, 18>([&](auto tt) {
      constexpr int t = tt.value, nk = t < 17 ? 2 : 1;
      constexpr int left = g2cnt(t - 3) + g2cnt(t - 2) + g2cnt(t - 1);
      if constexpr (nk == 2) waitv<left>(fa[t][0], fa[t][1], fa[t][2], fa[t][3]); else waitv<left>(fa[t][0], fa[t][1]);
      if constexpr (t == G2POLL + G2R) {
        tie(fl);
        if (!match(fl, ep)) poll(rsF, vfl, ep);
      }
      s16x4 lo[2][2], hi[2][2];
#pragma unroll
      for (int ks = 0; ks < nk; ks++)
#pragma unroll
        for (int a = 0; a < 2; a++) {
          if (t < 14) {
            lo[ks][a] = vtr(pb[a] + ((t + 6) % 10) * 4096 + ks * 2048); hi[ks][a] = vtr(pb[a] + ((t + 6) % 10) * 4096 + ks * 2048 + 512);
          } else {
            const int j = 2 * (t - 14) + ks;
            if (j < 6) {
              const int u = j % 3, rl = 56 * (j / 3) + (u == 0 ? 0 : u == 1 ? 16 : 36), rh = 56 * (j / 3) + (u == 0 ? 8 : u == 1 ? 28 : 44);
              lo[ks][a] = vtr(pw[a] + rl * 128); hi[ks][a] = vtr(pw[a] + rh * 128);
            } else {
              lo[ks][a] = vtr(pw[a] + h * (52 * 128) + 24 * 128); hi[ks][a] = vtr(pw[a] + h * (52 * 128) + 52 * 128);
            }
          }
        }
      SB();
      unroll<0, 4 * nk>([&](auto mm) {
        constexpr int m = mm.value, ks = m >> 2, nh = (m >> 1) & 1, mh = m & 1;
        if constexpr (m < 4) ldD(std::integral_constant<int, t + G2R>(), mm);
        else if constexpr (t >= 1) dmaS(std::integral_constant<int, t + 9>(), std::integral_constant<int, m - 4>());
        if constexpr (t == G2POLL && m == 7) fl = ldc2(rsF, vfl);
        if constexpr (t < 14) acc[nh][mh] = MFMA(fa[t][ks * 2 + mh], cat(lo[ks][nh], hi[ks][nh]), acc[nh][mh]);
        else acc[nh][mh] = MFMA(cat(lo[ks][mh], hi[ks][mh]), fa[t][ks * 2 + nh], acc[nh][mh]);
        SB();
      });
    });
    STAMP(14);
#pragma unroll
    for (int nh = 0; nh < 2; nh++)
#pragma unroll
      for (int mh = 0; mh < 2; mh++)
#pragma unroll
        for (int g = 0; g < 4; g++)
          lst(lds + w * AREA + (32 * nh + n) * 256 + (((8 * mh + 2 * g + h) ^ (n & 15)) * 16),
              (f32x4){acc[nh][mh][4 * g], acc[nh][mh][4 * g + 1], acc[nh][mh][4 * g + 2], acc[nh][mh][4 * g + 3]});
    bar();
    const rsrc_t sto = mkst(out, S * D * 2);
#pragma unroll
    for (int u = 0; u < 2; u++) {
      const int r = 16 * w + 8 * u + (lane >> 3);
      const unsigned c0 = r * 256 + (((2 * (lane & 7)) ^ (r & 15)) * 16), c1 = c0 ^ 16;
      f32x4 a = lld<f32x4>(lds + c0), bq = lld<f32x4>(lds + c1);
#pragma unroll
      for (int s = 1; s < 4; s++) { a += lld<f32x4>(lds + s * AREA + c0); bq += lld<f32x4>(lds + s * AREA + c1); }
      const u32x4 xu = __builtin_bit_cast(u32x4, u ? xr1 : xr0);
      const float bv = blo(u ? bs1 : bs0);
      u32x4 res = {pk(a[0] + bv + blo(xu[0]), a[1] + bv + bhi(xu[0])), pk(a[2] + bv + blo(xu[1]), a[3] + bv + bhi(xu[1])),
                   pk(bq[0] + bv + blo(xu[2]), bq[1] + bv + bhi(xu[2])), pk(bq[2] + bv + blo(xu[3]), bq[3] + bv + bhi(xu[3]))};
      __builtin_amdgcn_raw_buffer_store_b128(res, sto, vo + u * (8 * D * 2), 0, 0);
    }
    if (blockIdx.x == 255 && tid == 0) __builtin_amdgcn_raw_buffer_store_b64((u32x2){ep.lo, ep.hi}, mkst(ws, WS_FLAG_BYTES), 0, 0, 16);
  }
}

extern "C" void kernel_launch(void* const* d_in, const int* in_sizes, int n_in, void* d_out, int out_size, void* d_ws, size_t ws_size,
                              hipStream_t stream) {
  fused_kernel<<<256, 256, 0, stream>>>((const short*)d_in[0], (const short*)d_in[1], (const short*)d_in[2], (const short*)d_in[3],
                                        (const short*)d_in[5], (const short*)d_in[6], (const short*)d_in[7], (const short*)d_in[8],
                                        (short*)d_out, (char*)d_ws);
}
```

```cpp
#include <hip/hip_runtime.h>
#include <stdint.h>

typedef short bf16x8 __attribute__((ext_vector_type(8)));
typedef short s16x4 __attribute__((ext_vector_type(4)));
typedef float f32x2 __attribute__((ext_vector_type(2)));
typedef float f32x4 __attribute__((ext_vector_type(4)));
typedef float f32x16 __attribute__((ext_vector_type(16)));
typedef unsigned u32x2 __attribute__((ext_vector_type(2)));
typedef unsigned u32x4 __attribute__((ext_vector_type(4)));
typedef __bf16 b16x2 __attribute__((ext_vector_type(2)));
typedef __attribute__((address_space(3))) char* ldsp;
typedef __amdgpu_buffer_rsrc_t rsrc_t;

#define DEV __device__ __forceinline__
#define SB() __builtin_amdgcn_sched_barrier(0)
#define MFMA(a, b, c) __builtin_amdgcn_mfma_f32_32x32x16_bf16(a, b, c, 0, 0, 0)

constexpr int S = 2048, D = 448, F = 1792;
constexpr unsigned FL_H = 8, FL_X = FL_H + 128, FL_P = FL_X + 128, FL_R = FL_P + 128, FL_O = FL_R + 16, FL_U = FL_O + 128;
constexpr unsigned WS_FLAG_BYTES = 131072;
constexpr unsigned WS_HF = WS_FLAG_BYTES, SZ_HF = 256 * 448 * 16;
constexpr unsigned WS_XN = WS_HF + SZ_HF, SZ_XN = 56 * 2048 * 16;
constexpr unsigned WS_UF = WS_XN + SZ_XN, SZ_UF = 224 * 448 * 16;
constexpr unsigned WS_OF = WS_UF + SZ_UF, SZ_OF = 56 * 2048 * 16;
constexpr unsigned WS_KVP = WS_OF + SZ_OF, SZ_KVP = 16 * 16 * 2 * 4096;
constexpr unsigned WS_KVF = WS_KVP + SZ_KVP, SZ_KVF = 16 * 4 * 1024;
constexpr unsigned WS_END = WS_KVF + SZ_KVF;
constexpr unsigned AREA = 40960;
constexpr unsigned L_WK = 0, L_WV = 50816, L_WQ = 101632, L_WPHI = 152448, L_PHI = 0, L_VX = 32768;
constexpr unsigned OOB = 0x40000000u;
constexpr int SPIN = 1 << 18;

DEV u32x4 mkrs(const void* p, unsigned bytes) {
  unsigned long long a = (unsigned long long)p;
  u32x4 r = {(unsigned)a, (unsigned)(a >> 32), bytes, 0x00020000u};
  return r;
}
DEV rsrc_t mkst(void* p, unsigned bytes) { return __builtin_amdgcn_make_buffer_rsrc(p, 0, bytes, 0x00020000); }
DEV void stc(rsrc_t rs, unsigned voff, u32x4 v) { __builtin_amdgcn_raw_buffer_store_b128(v, rs, voff, 0, 16); }
template <int OFF> DEV bf16x8 ldp(u32x4 rs, unsigned voff, unsigned soff) {
  bf16x8 r;
  asm volatile("buffer_load_dwordx4 %0, %2, %1, %3 offen offset:%4" : "=v"(r) : "s"(rs), "v"(voff), "s"(soff), "n"(OFF) : "memory");
  return r;
}
template <int OFF> DEV bf16x8 ldc(u32x4 rs, unsigned voff, unsigned soff) {
  bf16x8 r;
  asm volatile("buffer_load_dwordx4 %0, %2, %1, %3 offen offset:%4 sc1" : "=v"(r) : "s"(rs), "v"(voff), "s"(soff), "n"(OFF) : "memory");
  return r;
}
template <int OFF> DEV u32x2 ldp2(u32x4 rs, unsigned voff, unsigned soff) {
  u32x2 r;
  asm volatile("buffer_load_dwordx2 %0, %2, %1, %3 offen offset:%4" : "=v"(r) : "s"(rs), "v"(voff), "s"(soff), "n"(OFF) : "memory");
  return r;
}
DEV u32x2 ldc2(u32x4 rs, unsigned voff) {
  u32x2 r;
  asm volatile("buffer_load_dwordx2 %0, %2, %1, 0 offen sc1" : "=v"(r) : "s"(rs), "v"(voff) : "memory");
  return r;
}
template <int OFF> DEV unsigned ldu16(u32x4 rs, unsigned voff, unsigned soff) {
  unsigned r;
  asm volatile("buffer_load_ushort %0, %2, %1, %3 offen offset:%4" : "=v"(r) : "s"(rs), "v"(voff), "s"(soff), "n"(OFF) : "memory");
  return r;
}
DEV void setm0(unsigned a) { asm volatile("s_mov_b32 m0, %0\n\ts_nop 0" ::"s"(a) : "memory"); }
template <int OFF> DEV void dma(u32x4 rs, unsigned voff, unsigned soff) {
  asm volatile("buffer_load_dwordx4 %1, %0, %2 offen offset:%3 lds" ::"s"(rs), "v"(voff), "s"(soff), "n"(OFF) : "memory");
}
DEV void dma4(u32x4 rs, const unsigned (&v)[4], unsigned soff, unsigned m0) {
  setm0(m0); dma<0>(rs, v[0], soff); dma<1024>(rs, v[1], soff); dma<2048>(rs, v[2], soff); dma<3072>(rs, v[3], soff);
}
template <int J> DEV void dma1(u32x4 rs, const unsigned (&v)[4], unsigned soff, unsigned m0) {
  if (J == 0) setm0(m0);
  dma<J * 1024>(rs, v[J], soff);
}
template <int N> DEV void waitv() { asm volatile("s_waitcnt vmcnt(%0)" ::"n"(N) : "memory"); }
template <int N, class T> DEV void waitv(T& a) { asm volatile("s_waitcnt vmcnt(%1)" : "+v"(a) : "n"(N) : "memory"); }
template <int N, class T> DEV void waitv(T& a, T& b) { asm volatile("s_waitcnt vmcnt(%2)" : "+v"(a), "+v"(b) : "n"(N) : "memory"); }
template <int N, class T> DEV void waitv(T& a, T& b, T& c, T& d) {
  asm volatile("s_waitcnt vmcnt(%4)" : "+v"(a), "+v"(b), "+v"(c), "+v"(d) : "n"(N) : "memory");
}
template <class T> DEV void tie(T& a) { asm volatile("" : "+v"(a)::"memory"); }
DEV void bar() { asm volatile("s_waitcnt lgkmcnt(0)\n\ts_barrier" ::: "memory"); }
DEV void barv() { asm volatile("s_waitcnt vmcnt(0) lgkmcnt(0)\n\ts_barrier" ::: "memory"); }
DEV s16x4 vtr(ldsp p) { return __builtin_amdgcn_ds_read_tr16_b64_v4i16((__attribute__((address_space(3))) s16x4*)p); }
DEV bf16x8 cat(s16x4 a, s16x4 b) { return __builtin_shufflevector(a, b, 0, 1, 2, 3, 4, 5, 6, 7); }
DEV unsigned pk(float a, float b) {
  f32x2 v = {a, b};
  return __builtin_bit_cast(unsigned, __builtin_convertvector(v, b16x2));
}
DEV float blo(unsigned u) { return __builtin_bit_cast(float, u << 16); }
DEV float bhi(unsigned u) { return __builtin_bit_cast(float, u & 0xffff0000u); }
DEV void swap32(float& a, float& b) {
  auto r = __builtin_amdgcn_permlane32_swap(__builtin_bit_cast(unsigned, a), __builtin_bit_cast(unsigned, b), false, false);
  const unsigned r0 = r[0], r1 = r[1];
  a = __builtin_bit_cast(float, r0); b = __builtin_bit_cast(float, r1);
}
DEV void swap16(float& a, float& b) {
  auto r = __builtin_amdgcn_permlane16_swap(__builtin_bit_cast(unsigned, a), __builtin_bit_cast(unsigned, b), false, false);
  const unsigned r0 = r[0], r1 = r[1];
  a = __builtin_bit_cast(float, r0); b = __builtin_bit_cast(float, r1);
}
template <int C> DEV float ror(float z) {
  return __builtin_bit_cast(float, __builtin_amdgcn_update_dpp(0, __builtin_bit_cast(int, z), C, 0xf, 0xf, true));
}
DEV float rowsum(float z) { z += ror<0x128>(z); z += ror<0x124>(z); z += ror<0x122>(z); z += ror<0x121>(z); return z; }
DEV float rdl(float z, int l) { return __builtin_bit_cast(float, __builtin_amdgcn_readlane(__builtin_bit_cast(int, z), l)); }
template <class T> DEV void lst(ldsp p, T v) { *(__attribute__((address_space(3))) T*)p = v; }
template <class T> DEV T lld(ldsp p) { return *(__attribute__((address_space(3))) T*)p; }

struct Epoch { unsigned lo, hi; };
DEV void publish(char* ws, unsigned flag, Epoch e) {
  barv();
  if (threadIdx.x == 0) __builtin_amdgcn_raw_buffer_store_b64((u32x2){e.lo, e.hi}, mkst(ws, WS_FLAG_BYTES), flag * 128, 0, 16);
}
DEV bool match(u32x2 f, Epoch e) { return __all(f.x == e.lo && f.y == e.hi); }
DEV void poll(u32x4 rsF, unsigned voff, Epoch e) {
#pragma unroll 1
  for (int it = 0; it < SPIN; ++it) {
    u32x2 f = ldc2(rsF, voff);
    waitv<0>(f);
    if (match(f, e)) break;
    __builtin_amdgcn_s_sleep(1);
  }
}

template <bool TWICE> DEV void ln_rows(const bf16x8 (&v)[4], float (&xf)[4][8]) {
  float s[4], q[4];
#pragma unroll
  for (int r = 0; r < 4; r++) {
    u32x4 u = __builtin_bit_cast(u32x4, v[r]);
#pragma unroll
    for (int e = 0; e < 4; e++) { xf[r][2 * e] = blo(u[e]); xf[r][2 * e + 1] = bhi(u[e]); }
    s[r] = 0.f; q[r] = 0.f;
#pragma unroll
    for (int e = 0; e < 8; e++) { s[r] += xf[r][e]; q[r] = fmaf(xf[r][e], xf[r][e], q[r]); }
  }
  swap32(s[0], s[1]); swap32(s[2], s[3]); swap32(q[0], q[1]); swap32(q[2], q[3]);
  float sa = s[0] + s[1], sb = s[2] + s[3], qa = q[0] + q[1], qb = q[2] + q[3];
  swap16(sa, sb); swap16(qa, qb);
  float mu = rowsum(sa + sb) * (1.f / D), ms = rowsum(qa + qb) * (1.f / D);
  float var = fmaxf(ms - mu * mu, 0.f);
  float a = __builtin_amdgcn_rsqf(var + 1e-5f);
  if (TWICE) a *= __builtin_amdgcn_rsqf(var * a * a + 1e-5f);
  float c = -mu * a;
#pragma unroll
  for (int r = 0; r < 4; r++) {
    const int l = (r & 1) * 32 + (r >> 1) * 16;
    float ar = rdl(a, l), cr = rdl(c, l);
#pragma unroll
    for (int e = 0; e < 8; e++) xf[r][e] = fmaf(xf[r][e], ar, cr);
  }
}

DEV void features(const f32x16& p, bool h, unsigned (&ph)[2][8]) {
  constexpr float L = 1.4426950408889634f;
  float mx = fmaxf(p[0], p[1]), mn = fminf(p[0], p[1]);
#pragma unroll
  for (int r = 2; r < 12; r += 2) { mx = fmaxf(fmaxf(mx, p[r]), p[r + 1]); mn = fminf(fminf(mn, p[r]), p[r + 1]); }
  float mx2 = fmaxf(fmaxf(fmaxf(fmaxf(mx, p[12]), p[13]), p[14]), p[15]);
  float mn2 = fminf(fminf(fminf(fminf(mn, p[12]), p[13]), p[14]), p[15]);
  mx = h ? mx : mx2; mn = h ? -mn : -mn2;
  swap32(mx, mn);
  float c0 = -L * fmaxf(mx, mn), c1 = c0;
  swap32(c0, c1);
  const float ninf = -__builtin_inff();
  const float d0 = h ? ninf : c0, d1 = h ? ninf : c1;
  float e[2][16], s0 = 0.f, s1 = 0.f;
#pragma unroll
  for (int r = 0; r < 16; r++) {
    e[0][r] = __builtin_amdgcn_exp2f(fmaf(p[r], L, r < 12 ? c0 : d0));
    e[1][r] = __builtin_amdgcn_exp2f(fmaf(p[r], -L, r < 12 ? c1 : d1));
    s0 += e[0][r]; s1 += e[1][r];
  }
  swap32(s0, s1);
  float i0 = __builtin_amdgcn_rcpf(s0 + s1), i1 = i0;
  swap32(i0, i1);
#pragma unroll
  for (int r = 0; r < 8; r++) { ph[0][r] = pk(e[0][2 * r] * i0, e[0][2 * r + 1] * i0); ph[1][r] = pk(e[1][2 * r] * i1, e[1][2 * r + 1] * i1); }
}
DEV bf16x8 pack8(const f32x16& a, int o) {
  u32x4 u = {pk(a[o], a[o + 1]), pk(a[o + 2], a[o + 3]), pk(a[o + 4], a[o + 5]), pk(a[o + 6], a[o + 7])};
  return __builtin_bit_cast(bf16x8, u);
}
DEV float gelu(float a) {
  constexpr float c1 = -2.302208198f, c2 = c1 * 0.044715f;
  return a * __builtin_amdgcn_rcpf(1.f + __builtin_amdgcn_exp2f(a * fmaf(a * a, c2, c1)));
}

#define STAMP(n)

template <int T, int N, class Fn> DEV void unroll(Fn&& f) {
  if constexpr (T < N) { f(std::integral_constant<int, T>()); unroll<T + 1, N>(f); }
}
constexpr int g1cnt(int s) { return s < 0 ? 4 : (s + 3 < 16 ? (s >= 1 ? 12 : 4) : 0); }
constexpr int G2R = 4, G2POLL = 5;
constexpr int g2ld(int t) { return t < 17 ? 4 : (t == 17 ? 2 : 0); }
constexpr int g2dm(int t) { return t < 17 ? 4 : (t == 17 ? 2 : 0); }
constexpr int g2cnt(int s) { return s < 0 ? 4 : g2ld(s + G2R) + (s >= 1 ? g2dm(s + 9) : 0) + (s == G2POLL ? 1 : 0); }
constexpr int kvn(int ks) { return 8 + 2 * ks < 41 ? 8 + 2 * ks : 41; }

__global__ __launch_bounds__(256, 1) void fused_kernel(const short* __restrict__ x, const short* __restrict__ wqkv, const short* __restrict__ wphi,
                                                       const short* __restrict__ wo, const short* __restrict__ w1, const short* __restrict__ b1,
                                                       const short* __restrict__ w2, const short* __restrict__ b2, short* __restrict__ out,
                                                       char* __restrict__ ws) {
  __shared__ __attribute__((aligned(16))) char lds_[163840];
  const ldsp lds = (ldsp)lds_;
  const unsigned lds0 = (unsigned)(uintptr_t)lds;
  const int tid = threadIdx.x, lane = tid & 63, w = __builtin_amdgcn_readfirstlane(tid >> 6);
  asm volatile("s_nop 4" ::"s"(w), "s"(x), "s"(wqkv), "s"(wphi), "s"(wo), "s"(w1), "s"(b1), "s"(w2), "s"(b2), "s"(out), "s"(ws) : "memory");
  const int xc = blockIdx.x & 7, i = blockIdx.x >> 3;
  const int h = lane >> 5, n = lane & 31, q = (lane & 15) >> 2, pp = lane & 3, blk = (lane >> 4) & 1;
  const u32x4 rsF = mkrs(ws, WS_FLAG_BYTES), rsX = mkrs(x, S * D * 2), rsW2 = mkrs(w2, F * S * 2);
  const unsigned area = lds0 + w * AREA;
  const int tile2 = 28 * (xc & 1) + i - 4, dt2 = tile2 >> 3, st2 = 8 * (xc >> 1) + (tile2 & 7);
  const unsigned swz = (lane >> 3), swc = ((lane & 7) ^ (((lane >> 4) & 1) << 2)) * 16;
  unsigned v2[4];
#pragma unroll
  for (int j = 0; j < 4; j++) v2[j] = swz * (S * 2) + swc + j * (8 * S * 2 - 1024);
  const unsigned so2 = (w * 448) * (S * 2) + st2 * 128;
  auto pre2 = [&](int t0, int t1, int slot0) {
#pragma unroll 1
    for (int t = t0; t < t1; t++) dma4(rsW2, v2, so2 + t * (32 * S * 2), area + (slot0 + t - t0) * 4096);
  };

  const bool isA = i >= 16;
  const int T = 4 * (xc >> 1) + (i & 3), pr = 4 * (xc & 1) + ((i - 16) >> 2);
  const int row0 = isA ? 128 * T + 16 * pr : 256 * xc + 16 * i;
  auto ldrows = [&](bf16x8 (&xv)[4], u32x2& gen) {
    const unsigned vo = lane < 56 ? lane * 16 : OOB, so = (row0 + 4 * w) * (D * 2);
    xv[0] = ldp<0>(rsX, vo, so); xv[1] = ldp<896>(rsX, vo, so); xv[2] = ldp<1792>(rsX, vo, so); xv[3] = ldp<2688>(rsX, vo, so);
    gen = ldc2(rsF, 0);
  };
  Epoch ep;

  if (!isA) {
    bf16x8 xv[4];
    u32x2 gen;
    ldrows(xv, gen);
    const int g0 = (49 * xc + 3) >> 2, g1 = (49 * xc + 52) >> 2;
    const bool isG = g0 + i < g1;
    const int ft = (g0 + i) / 7, dt = (g0 + i) % 7;
    const u32x4 rsW1 = mkrs(w1, S * F * 2);
    unsigned v1[4];
#pragma unroll
    for (int j = 0; j < 4; j++) v1[j] = swz * (F * 2) + swc + j * (8 * F * 2 - 1024);
    const unsigned so1 = (w * 512) * (F * 2) + ft * 256;
    if (isG) {
#pragma unroll
      for (int t = 0; t < 2; t++) { dma4(rsW1, v1, so1 + t * (32 * F * 2), area + t * 8192); dma4(rsW1, v1, so1 + t * (32 * F * 2) + 128, area + t * 8192 + 4096); }
    } else {
      pre2(4, 8, 0);
    }
    waitv<16>(xv[0], xv[1], xv[2], xv[3]);
    tie(gen);
    STAMP(0);
    {
      unsigned long long g = (((unsigned long long)__builtin_amdgcn_readfirstlane(gen.y) << 32) | (unsigned)__builtin_amdgcn_readfirstlane(gen.x)) + 1;
      ep.lo = (unsigned)g; ep.hi = (unsigned)(g >> 32);
    }
    {
      float xf[4][8];
      ln_rows<true>(xv, xf);
      if (lane < 56) {
        const ldsp p = lds + (w >> 1) * AREA + 32768 + (w & 1) * 8 + lane * 16;
#pragma unroll
        for (int e = 0; e < 8; e++) lst(p + e * (57 * 16), (u32x2){pk(xf[0][e], xf[1][e]), pk(xf[2][e], xf[3][e])});
      }
      bar();
      const ldsp p = lds + 32768 + ((tid & 7) * 57 + (tid >> 3)) * 16;
      const rsrc_t st = mkst(ws + WS_HF, SZ_HF);
      const unsigned vo = (row0 >> 3) * (D * 16) + tid * 16;
      u32x4 e0 = lld<u32x4>(p), e1 = lld<u32x4>(p + (w < 3 ? 512 : AREA - 384)), e2 = lld<u32x4>(p + AREA + 128);
      stc(st, vo, e0); stc(st, vo + 4096, e1); stc(st, vo + 8192, e2);
      if (w < 2) stc(st, vo + 12288, lld<u32x4>(p + AREA + 640));
    }
    STAMP(1);
    publish(ws, FL_H + (row0 >> 4), ep);
    STAMP(2);

    if (isG) {
#pragma unroll
      for (int t = 2; t < 4; t++) { dma4(rsW1, v1, so1 + t * (32 * F * 2), area + t * 8192); dma4(rsW1, v1, so1 + t * (32 * F * 2) + 128, area + t * 8192 + 4096); }
      u32x2 bb[4];
      {
        const u32x4 rsB = mkrs(b1, F * 2);
        const unsigned so = (ft * 128 + w * 32) * 2;
        bb[0] = ldp2<0>(rsB, h * 8, so); bb[1] = ldp2<16>(rsB, h * 8, so); bb[2] = ldp2<32>(rsB, h * 8, so); bb[3] = ldp2<48>(rsB, h * 8, so);
      }
      poll(rsF, (FL_H + w * 32 + n) * 128, ep);
      STAMP(3);
      const u32x4 rsH = mkrs(ws + WS_HF, SZ_HF);
      const unsigned vh0 = h * (D * 16) + (dt * 64 + n) * 16, vh1 = vh0 + 2 * D * 16, soh = w * 64 * (D * 16);
      bf16x8 fb[16][4];
      auto ldH = [&](auto tt, auto jj) {
        constexpr int t = tt.value, j = jj.value;
        fb[t][j] = ldc<(j & 1) * 512>(rsH, (j >> 1) ? vh1 : vh0, soh + t * (4 * D * 16));
      };
      unroll<0, 3>([&](auto tt) { unroll<0, 4>([&](auto jj) { ldH(tt, jj); }); });
      waitv<12>(bb[0], bb[1], bb[2], bb[3]);
      f32x16 acc[4][2];
#pragma unroll
      for (int g = 0; g < 4; g++) {
        acc[0][0][4 * g] = blo(bb[g].x); acc[0][0][4 * g + 1] = bhi(bb[g].x); acc[0][0][4 * g + 2] = blo(bb[g].y); acc[0][0][4 * g + 3] = bhi(bb[g].y);
      }
      acc[0][1] = acc[0][0];
#pragma unroll
      for (int a = 1; a < 4; a++) { acc[a][0] = (f32x16)(0.f); acc[a][1] = (f32x16)(0.f); }
      ldsp pa[4];
#pragma unroll
      for (int a = 0; a < 4; a++) {
        const int s = a ^ w;
        pa[a] = lds + w * AREA + (s >> 1) * 4096 + (8 * h + q) * 128 + (((4 * (s & 1) + 2 * blk + (pp >> 1)) ^ ((q >> 1) << 2)) * 16) + (pp & 1) * 8;
      }
      unroll<0, 16>([&](auto tt) {
        constexpr int t = tt.value, sl = t & 3;
        waitv<g1cnt(t - 2) + g1cnt(t - 1)>(fb[t][0], fb[t][1], fb[t][2], fb[t][3]);
        s16x4 lo[2][4], hi[2][4];
#pragma unroll
        for (int ks = 0; ks < 2; ks++)
#pragma unroll
          for (int a = 0; a < 4; a++) { lo[ks][a] = vtr(pa[a] + sl * 8192 + ks * 2048); hi[ks][a] = vtr(pa[a] + sl * 8192 + ks * 2048 + 512); }
        SB();
        unroll<0, 16>([&](auto mm) {
          constexpr int m = mm.value, ks = m >> 3, a = (m >> 1) & 3, dh = m & 1;
          if constexpr (t + 3 < 16) {
            if constexpr (m < 4) ldH(std::integral_constant<int, t + 3>(), mm);
            else if constexpr (m < 12 && t >= 1)
              dma1<(m & 3)>(rsW1, v1, so1 + (t + 3) * (32 * F * 2) + (m >= 8 ? 128 : 0), area + ((t + 3) & 3) * 8192 + (m >= 8 ? 4096 : 0));
          }
          acc[a][dh] = MFMA(cat(lo[ks][a], hi[ks][a]), fb[t][ks * 2 + dh], acc[a][dh]);
          SB();
        });
      });
      STAMP(4);
      if (i >= 4) pre2(0, 4, 6);
      {
        const ldsp p = lds + w * AREA + lane * 16;
#pragma unroll
        for (int a = 1; a < 4; a++)
#pragma unroll
          for (int dh = 0; dh < 2; dh++)
#pragma unroll
            for (int g = 0; g < 4; g++)
              lst(p + (a - 1) * 8192 + dh * 4096 + g * 1024, (f32x4){acc[a][dh][4 * g], acc[a][dh][4 * g + 1], acc[a][dh][4 * g + 2], acc[a][dh][4 * g + 3]});
      }
      bar();
      STAMP(5);
      {
        const rsrc_t st = mkst(ws + WS_UF, SZ_UF);
        const unsigned vo = (ft * 16 + w * 4 + h) * (D * 16) + (dt * 64 + n) * 16;
        f32x4 v[2][3][4];
#pragma unroll
        for (int dh = 0; dh < 2; dh++)
#pragma unroll
          for (int a = 1; a < 4; a++)
#pragma unroll
            for (int g = 0; g < 4; g++) v[dh][a - 1][g] = lld<f32x4>(lds + (w ^ a) * AREA + lane * 16 + (a - 1) * 8192 + dh * 4096 + g * 1024);
        SB();
#pragma unroll
        for (int dh = 0; dh < 2; dh++) {
#pragma unroll
          for (int g = 0; g < 4; g += 2) {
            float r[8];
#pragma unroll
            for (int e = 0; e < 8; e++) {
              const int gg = g + (e >> 2), c = e & 3;
              r[e] = gelu((acc[0][dh][4 * gg + c] + v[dh][0][gg][c]) + (v[dh][1][gg][c] + v[dh][2][gg][c]));
            }
            auto r0 = __builtin_amdgcn_permlane32_swap(pk(r[0], r[1]), pk(r[4], r[5]), false, false);
            auto r1 = __builtin_amdgcn_permlane32_swap(pk(r[2], r[3]), pk(r[6], r[7]), false, false);
            stc(st, vo + g * (D * 16) + dh * 512, (u32x4){r0[0], r1[0], r0[1], r1[1]});
          }
        }
      }
      STAMP(6);
      publish(ws, FL_U + dt * 14 + ft, ep);
      STAMP(7);
      if (i >= 4) pre2(4, 10, 0);
    } else {
      pre2(0, 4, 6);
      pre2(4, 10, 0);
      if (i >= 14) {
        const int head = 2 * xc + i - 14;
        poll(rsF, (FL_P + xc * 16 + (lane & 15)) * 128, ep);
        STAMP(3);
        const u32x4 rsP = mkrs(ws + WS_KVP, SZ_KVP);
        const unsigned so = head * 131072 + (w >> 1) * 4096 + (w & 1) * 2048;
        bf16x8 pv[16][2];
        unroll<0, 16>([&](auto tt) { constexpr int t = tt.value; pv[t][0] = ldc<0>(rsP, lane * 16, so + t * 8192); pv[t][1] = ldc<1024>(rsP, lane * 16, so + t * 8192); });
        f32x4 a0 = {0.f, 0.f, 0.f, 0.f}, a1 = a0;
        unroll<0, 16>([&](auto tt) {
          constexpr int t = tt.value;
          waitv<30 - 2 * t>(pv[t][0], pv[t][1]);
          a0 += __builtin_bit_cast(f32x4, pv[t][0]); a1 += __builtin_bit_cast(f32x4, pv[t][1]);
        });
        stc(mkst(ws + WS_KVF, SZ_KVF), head * 4096 + w * 1024 + lane * 16, (u32x4){pk(a0[0], a0[1]), pk(a0[2], a0[3]), pk(a1[0], a1[1]), pk(a1[2], a1[3])});
        STAMP(4);
        publish(ws, FL_R + head, ep);
        STAMP(5);
      }
    }
  } else {
    bf16x8 xv[4];
    u32x2 gen;
    ldrows(xv, gen);
    const u32x4 rsW = mkrs(wqkv, D * 3 * D * 2);
    unsigned vw[4];
    {
      const unsigned r7 = (lane * 37) >> 8;
#pragma unroll
      for (int j = 0; j < 4; j++) vw[j] = r7 * 2688 + (lane - r7 * 7) * 16 + j * (9 * 2688 - 1008);
    }
    const unsigned sow = w * 112 * 2688 + pr * 112, mw = lds0 + w * 112 * 112;
    auto dmaW = [&](auto kk, unsigned col, unsigned base) {
      constexpr int k = kk.value;
      if constexpr ((k & 3) == 0) setm0(mw + base + (k >> 2) * 4032);
      dma<(k & 3) * 1008>(rsW, vw[k & 3], sow + col + (k >> 2) * (36 * 2688));
    };
    setm0(lds0 + L_WPHI + w * 1024);
    dma<0>(mkrs(wphi + pr * 1568, 3136), tid * 16, 0);
    unroll<0, 13>([&](auto kk) { dmaW(kk, 896, L_WK); });
    waitv<14>(xv[0], xv[1], xv[2], xv[3]);
    tie(gen);
    STAMP(0);
    {
      unsigned long long g = (((unsigned long long)__builtin_amdgcn_readfirstlane(gen.y) << 32) | (unsigned)__builtin_amdgcn_readfirstlane(gen.x)) + 1;
      ep.lo = (unsigned)g; ep.hi = (unsigned)(g >> 32);
    }
    {
      float xf[4][8];
      ln_rows<false>(xv, xf);
      if (lane < 56) {
        const ldsp p = lds + L_WQ + (lane >> 1) * 544 + (lane & 1) * 4 + w * 64;
#pragma unroll
        for (int r = 0; r < 4; r++)
#pragma unroll
          for (int e = 0; e < 4; e++) lst(p + r * 16 + (e >> 1) * 272 + (e & 1) * 8, pk(xf[r][e], xf[r][e + 4]));
      }
      bar();
      const ldsp p = lds + L_WQ + (tid >> 4) * 272 + (tid & 15) * 16;
      const rsrc_t st = mkst(ws + WS_XN, SZ_XN);
      const unsigned vo = (tid >> 4) * (S * 16) + (row0 + (tid & 15)) * 16;
      u32x4 e0 = lld<u32x4>(p), e1 = lld<u32x4>(p + 16 * 272), e2 = lld<u32x4>(p + 32 * 272);
      stc(st, vo, e0); stc(st, vo + 16 * S * 16, e1); stc(st, vo + 32 * S * 16, e2);
      if (w < 2) stc(st, vo + 48 * S * 16, lld<u32x4>(p + 48 * 272));
    }
    STAMP(1);
    publish(ws, FL_X + T * 8 + pr, ep);
    STAMP(2);
    unroll<0, 13>([&](auto kk) { dmaW(kk, 1792, L_WV); });
    poll(rsF, (FL_X + T * 8 + 2 * w + (lane & 1)) * 128, ep);
    STAMP(3);
    const u32x4 rsN = mkrs(ws + WS_XN, SZ_XN);
    const unsigned vx = h * (S * 16) + (128 * T + 32 * w + n) * 16;
    bf16x8 xa[28];
    auto item = [&](auto kk) {
      constexpr int k = kk.value;
      if constexpr (k < 28) xa[k] = ldc<0>(rsN, vx, k * (2 * S * 16));
      else if constexpr (k < 41) dmaW(std::integral_constant<int, k - 28>(), 0, L_WQ);
    };
    unroll<0, 8>(item);
    bar();
    bf16x8 wf[2][2];
    {
      const ldsp p = lds + L_WPHI + (4 * h + q) * 56 + 32 * blk + 8 * pp;
#pragma unroll
      for (int hd = 0; hd < 2; hd++) {
        wf[hd][0] = cat(vtr(p + hd * 1568), vtr(p + hd * 1568 + 8 * 56));
        s16x4 z = vtr(p + hd * 1568 + 24 * 56);
        wf[hd][1] = cat(vtr(p + hd * 1568 + 16 * 56), h ? (s16x4){0, 0, 0, 0} : z);
      }
    }
    const unsigned wlane = (2 * h + 4 * q) * 112 + 32 * blk + 8 * pp;
    f32x16 kT[2] = {}, vv[2] = {};
    {
      const ldsp pk_ = lds + L_WK + wlane, pv_ = lds + L_WV + wlane;
      s16x4 fr[29][8];
      auto rd = [&](auto kk, auto cc) {
        constexpr int ks = kk.value, c = cc.value;
        if constexpr (ks < 28) fr[ks][c] = vtr(((c & 4) ? pv_ : pk_) + ks * 1792 + ((c >> 1) & 1) * 56 + (c & 1) * 112);
      };
      unroll<0, 8>([&](auto cc) { rd(std::integral_constant<int, 0>(), cc); });
      unroll<0, 28>([&](auto kk) {
        constexpr int ks = kk.value;
        waitv<kvn(ks) - ks - 1>(xa[ks]);
        if (ks == 0) STAMP(4);
        unroll<0, 4>([&](auto mm) {
          constexpr int m = mm.value;
          rd(std::integral_constant<int, ks + 1>(), std::integral_constant<int, 2 * m>());
          rd(std::integral_constant<int, ks + 1>(), std::integral_constant<int, 2 * m + 1>());
          if constexpr (m & 1) item(std::integral_constant<int, 8 + 2 * ks + (m >> 1)>());
          const bf16x8 wv = cat(fr[ks][2 * m], fr[ks][2 * m + 1]);
          if constexpr (m < 2) kT[m] = MFMA(wv, xa[ks], kT[m]);
          else vv[m - 2] = MFMA(xa[ks], wv, vv[m - 2]);
          SB();
        });
      });
    }
    STAMP(5);
    {
      f32x16 p[2];
#pragma unroll
      for (int hd = 0; hd < 2; hd++) { p[hd] = MFMA(wf[hd][0], pack8(kT[hd], 0), (f32x16)(0.f)); p[hd] = MFMA(wf[hd][1], pack8(kT[hd], 8), p[hd]); }
      bar();
      {
        const bf16x8 ones = {0x3f80, 0x3f80, 0x3f80, 0x3f80, 0x3f80, 0x3f80, 0x3f80, 0x3f80};
        const ldsp pvx = lds + L_VX + w * 2048 + lane * 16;
#pragma unroll
        for (int hd = 0; hd < 2; hd++)
#pragma unroll
          for (int j = 0; j < 2; j++) { bf16x8 f = pack8(vv[hd], 8 * j); lst(pvx + hd * 8192 + j * 1024, n == 28 ? ones : f); }
      }
      const unsigned pb = (32 * w + n) * 64 + ((h ^ ((n >> 1) & 7)) * 8);
#pragma unroll
      for (int hd = 0; hd < 2; hd++) {
        unsigned ph[2][8];
        features(p[hd], h, ph);
#pragma unroll
        for (int sg = 0; sg < 2; sg++)
#pragma unroll
          for (int g = 0; g < 4; g++) lst(lds + L_PHI + (hd * 2 + sg) * 8192 + (pb ^ (g << 4)), (u32x2){ph[sg][2 * g], ph[sg][2 * g + 1]});
      }
    }
    STAMP(6);
    barv();
    {
      const unsigned sw = 2 * h + (q >> 1);
      const ldsp pl = lds + L_PHI + w * 8192 + (4 * h + q) * 64 + (((4 * blk + pp) ^ sw) * 8);
      const ldsp ph_ = lds + L_PHI + w * 8192 + (8 + 4 * h + q) * 64 + (((4 * blk + pp) ^ sw ^ 4) * 8);
      const ldsp pvx = lds + L_VX + (w >> 1) * 8192 + lane * 16;
      f32x16 kv = {};
#pragma unroll
      for (int jj = 0; jj < 8; jj++) kv = MFMA(cat(vtr(pl + jj * 1024), vtr(ph_ + jj * 1024)), lld<bf16x8>(pvx + jj * 1024), kv);
      const rsrc_t st = mkst(ws + WS_KVP, SZ_KVP);
      const unsigned vo = (2 * pr + (w >> 1)) * 131072 + T * 8192 + (w & 1) * 4096 + lane * 16;
#pragma unroll
      for (int g = 0; g < 4; g++) stc(st, vo + g * 1024, __builtin_bit_cast(u32x4, (f32x4){kv[4 * g], kv[4 * g + 1], kv[4 * g + 2], kv[4 * g + 3]}));
    }
    STAMP(7);
    f32x16 qT[2] = {};
    {
      const ldsp pq_ = lds + L_WQ + wlane;
      s16x4 fr[29][4];
      auto rd = [&](auto kk, auto cc) {
        constexpr int ks = kk.value, c = cc.value;
        if constexpr (ks < 28) fr[ks][c] = vtr(pq_ + ks * 1792 + (c >> 1) * 56 + (c & 1) * 112);
      };
      unroll<0, 4>([&](auto cc) { rd(std::integral_constant<int, 0>(), cc); });
      unroll<0, 28>([&](auto kk) {
        constexpr int ks = kk.value;
        if constexpr (ks == 14) { publish(ws, FL_P + pr * 16 + T, ep); STAMP(8); SB(); }
        unroll<0, 2>([&](auto mm) {
          constexpr int m = mm.value;
          rd(std::integral_constant<int, ks + 1>(), std::integral_constant<int, 2 * m>());
          rd(std::integral_constant<int, ks + 1>(), std::integral_constant<int, 2 * m + 1>());
          qT[m] = MFMA(cat(fr[ks][2 * m], fr[ks][2 * m + 1]), xa[ks], qT[m]);
          SB();
        });
      });
    }
    unsigned phq[2][2][8];
    {
      f32x16 p[2];
#pragma unroll
      for (int hd = 0; hd < 2; hd++) { p[hd] = MFMA(wf[hd][0], pack8(qT[hd], 0), (f32x16)(0.f)); p[hd] = MFMA(wf[hd][1], pack8(qT[hd], 8), p[hd]); }
      bar();
      STAMP(9);
      pre2(0, 4, 6);
      pre2(4, 10, 0);
      features(p[0], h, phq[0]);
      features(p[1], h, phq[1]);
    }
    STAMP(10);
    poll(rsF, (FL_R + 2 * pr + (lane & 1)) * 128, ep);
    STAMP(11);
    {
      const u32x4 rsK = mkrs(ws + WS_KVF, SZ_KVF);
      bf16x8 kf[2][4];
#pragma unroll
      for (int hd = 0; hd < 2; hd++) {
        const unsigned so = (2 * pr + hd) * 4096;
        kf[hd][0] = ldc<0>(rsK, lane * 16, so); kf[hd][1] = ldc<1024>(rsK, lane * 16, so);
        kf[hd][2] = ldc<2048>(rsK, lane * 16, so); kf[hd][3] = ldc<3072>(rsK, lane * 16, so);
      }
      f32x16 o[2] = {};
      waitv<4>(kf[0][0], kf[0][1], kf[0][2], kf[0][3]);
#pragma unroll
      for (int hd = 0; hd < 2; hd++) {
        if (hd) waitv<0>(kf[1][0], kf[1][1], kf[1][2], kf[1][3]);
#pragma unroll
        for (int c = 0; c < 4; c++) {
          const unsigned* s = &phq[hd][c >> 1][4 * (c & 1)];
          o[hd] = MFMA(kf[hd][c], __builtin_bit_cast(bf16x8, (u32x4){s[0], s[1], s[2], s[3]}), o[hd]);
        }
      }
      float da = o[0][12], db = o[1][12];
      swap32(da, db);
      float ia = __builtin_amdgcn_rcpf(db + 1e-6f), ib = ia;
      swap32(ia, ib);
      unsigned a[8], bq[8];
#pragma unroll
      for (int r = 0; r < 8; r++) { a[r] = pk(o[0][2 * r] * ia, o[0][2 * r + 1] * ia); bq[r] = pk(o[1][2 * r] * ib, o[1][2 * r + 1] * ib); }
      const rsrc_t st = mkst(ws + WS_OF, SZ_OF);
      const unsigned tk = (128 * T + 32 * w + n) * 16, jb = 14 * (pr >> 1);
      const unsigned vo = (jb + 6 * (pr & 1) + h) * (S * 16) + tk;
      stc(st, vo, (u32x4){a[0], a[1], a[2], a[3]});
      stc(st, vo + 2 * S * 16, (u32x4){a[4], a[5], bq[0], bq[1]});
      stc(st, vo + 4 * S * 16, (u32x4){bq[2], bq[3], bq[4], bq[5]});
      stc(st, h ? OOB : (jb + 12 + (pr & 1)) * (S * 16) + tk, (u32x4){a[6], a[7], bq[6], bq[7]});
    }
    publish(ws, FL_O + T * 8 + pr, ep);
    STAMP(12);
  }

  if (i >= 4) {
    const unsigned orow = st2 * 64 + 16 * w + (lane >> 3);
    const unsigned vo = orow * (D * 2) + dt2 * 128 + (lane & 7) * 16;
    bf16x8 xr0 = ldp<0>(rsX, vo, 0), xr1 = ldp<0>(rsX, vo, 8 * D * 2);
    const u32x4 rsB = mkrs(b2, S * 2);
    unsigned bs0 = ldu16<0>(rsB, orow * 2, 0), bs1 = ldu16<16>(rsB, orow * 2, 0);
    poll(rsF, (FL_U + dt2 * 14 + ((7 * w) >> 1) + (lane & 3)) * 128, ep);
    tie(xr0); tie(xr1); tie(bs0); tie(bs1);
    STAMP(13);
    const u32x4 rsU = mkrs(ws + WS_UF, SZ_UF), rsO = mkrs(ws + WS_OF, SZ_OF), rsWo = mkrs(wo, D * D * 2);
    const unsigned vu0 = h * (D * 16) + (dt2 * 64 + n) * 16, vu1 = vu0 + 2 * D * 16, sou = w * 56 * (D * 16);
    const unsigned vof = h * (S * 16) + (st2 * 64 + n) * 16, soo = w * 14 * (S * 16);
    const unsigned vfl = (FL_O + (st2 >> 1) * 8 + 2 * w + (lane & 1)) * 128;
    unsigned vwo[4];
#pragma unroll
    for (int j = 0; j < 4; j++) vwo[j] = swz * (D * 2) + swc + j * (8 * D * 2 - 1024);
    const unsigned sowo = w * 112 * (D * 2) + dt2 * 128;
    bf16x8 fa[18][4];
    u32x2 fl;
    auto ldD = [&](auto tt, auto jj) {
      constexpr int t = tt.value, j = jj.value;
      if constexpr (t < 14) fa[t][j] = ldc<(j & 1) * 512>(rsU, (j >> 1) ? vu1 : vu0, sou + t * (4 * D * 16));
      else if constexpr (j < g2ld(t)) fa[t][j] = ldc<(j & 1) * 512>(rsO, vof, soo + (2 * (t - 14) + (j >> 1)) * (2 * S * 16));
    };
    auto dmaS = [&](auto tt, auto jj) {
      constexpr int t = tt.value, j = jj.value;
      if constexpr (t < 14) dma1<j>(rsW2, v2, so2 + t * (32 * S * 2), area + ((t + 6) % 10) * 4096);
      else if constexpr (j < g2dm(t)) dma1<j>(rsWo, vwo, sowo + (t - 14) * (32 * D * 2), area + (t - 14) * 4096);
    };
    unroll<0, G2R>([&](auto tt) { unroll<0, 4>([&](auto jj) { ldD(tt, jj); }); });
    ldsp pb[2], pw[2];
#pragma unroll
    for (int a = 0; a < 2; a++) {
      const unsigned c = q * 128 + (((4 * a + 2 * blk + (pp >> 1)) ^ ((q >> 1) << 2)) * 16) + (pp & 1) * 8;
      pb[a] = lds + w * AREA + h * 1024 + c;
      pw[a] = lds + w * AREA + h * 512 + c;
    }
    f32x16 acc[2][2] = {};
    unroll<0, 18>([&](auto tt) {
      constexpr int t = tt.value, nk = t < 17 ? 2 : 1;
      constexpr int left = g2cnt(t - 3) + g2cnt(t - 2) + g2cnt(t - 1);
      if constexpr (nk == 2) waitv<left>(fa[t][0], fa[t][1], fa[t][2], fa[t][3]); else waitv<left>(fa[t][0], fa[t][1]);
      if constexpr (t == G2POLL + G2R) {
        tie(fl);
        if (!match(fl, ep)) poll(rsF, vfl, ep);
      }
      s16x4 lo[2][2], hi[2][2];
#pragma unroll
      for (int ks = 0; ks < nk; ks++)
#pragma unroll
        for (int a = 0; a < 2; a++) {
          if (t < 14) {
            lo[ks][a] = vtr(pb[a] + ((t + 6) % 10) * 4096 + ks * 2048); hi[ks][a] = vtr(pb[a] + ((t + 6) % 10) * 4096 + ks * 2048 + 512);
          } else {
            const int j = 2 * (t - 14) + ks;
            if (j < 6) {
              const int u = j % 3, rl = 56 * (j / 3) + (u == 0 ? 0 : u == 1 ? 16 : 36), rh = 56 * (j / 3) + (u == 0 ? 8 : u == 1 ? 28 : 44);
              lo[ks][a] = vtr(pw[a] + rl * 128); hi[ks][a] = vtr(pw[a] + rh * 128);
            } else {
              lo[ks][a] = vtr(pw[a] + h * (52 * 128) + 24 * 128); hi[ks][a] = vtr(pw[a] + h * (52 * 128) + 52 * 128);
            }
          }
        }
      SB();
      unroll<0, 4 * nk>([&](auto mm) {
        constexpr int m = mm.value, ks = m >> 2, nh = (m >> 1) & 1, mh = m & 1;
        if constexpr (m < 4) ldD(std::integral_constant<int, t + G2R>(), mm);
        else if constexpr (t >= 1) dmaS(std::integral_constant<int, t + 9>(), std::integral_constant<int, m - 4>());
        if constexpr (t == G2POLL && m == 7) fl = ldc2(rsF, vfl);
        if constexpr (t < 14) acc[nh][mh] = MFMA(fa[t][ks * 2 + mh], cat(lo[ks][nh], hi[ks][nh]), acc[nh][mh]);
        else acc[nh][mh] = MFMA(cat(lo[ks][mh], hi[ks][mh]), fa[t][ks * 2 + nh], acc[nh][mh]);
        SB();
      });
    });
    STAMP(14);
#pragma unroll
    for (int nh = 0; nh < 2; nh++)
#pragma unroll
      for (int mh = 0; mh < 2; mh++)
#pragma unroll
        for (int g = 0; g < 4; g++)
          lst(lds + w * AREA + (32 * nh + n) * 256 + (((8 * mh + 2 * g + h) ^ (n & 15)) * 16),
              (f32x4){acc[nh][mh][4 * g], acc[nh][mh][4 * g + 1], acc[nh][mh][4 * g + 2], acc[nh][mh][4 * g + 3]});
    bar();
    const rsrc_t sto = mkst(out, S * D * 2);
#pragma unroll
    for (int u = 0; u < 2; u++) {
      const int r = 16 * w + 8 * u + (lane >> 3);
      const unsigned c0 = r * 256 + (((2 * (lane & 7)) ^ (r & 15)) * 16), c1 = c0 ^ 16;
      f32x4 a = lld<f32x4>(lds + c0), bq = lld<f32x4>(lds + c1);
#pragma unroll
      for (int s = 1; s < 4; s++) { a += lld<f32x4>(lds + s * AREA + c0); bq += lld<f32x4>(lds + s * AREA + c1); }
      const u32x4 xu = __builtin_bit_cast(u32x4, u ? xr1 : xr0);
      const float bv = blo(u ? bs1 : bs0);
      u32x4 res = {pk(a[0] + bv + blo(xu[0]), a[1] + bv + bhi(xu[0])), pk(a[2] + bv + blo(xu[1]), a[3] + bv + bhi(xu[1])),
                   pk(bq[0] + bv + blo(xu[2]), bq[1] + bv + bhi(xu[2])), pk(bq[2] + bv + blo(xu[3]), bq[3] + bv + bhi(xu[3]))};
      __builtin_amdgcn_raw_buffer_store_b128(res, sto, vo + u * (8 * D * 2), 0, 0);
    }
    if (blockIdx.x == 255 && tid == 0) __builtin_amdgcn_raw_buffer_store_b64((u32x2){ep.lo, ep.hi}, mkst(ws, WS_FLAG_BYTES), 0, 0, 16);
  }
}

template <int M> __global__ __launch_bounds__(256, 1) void probek(const char* a, const char* b, unsigned* ts) {
  const int tid = threadIdx.x, lane = tid & 63, w = __builtin_amdgcn_readfirstlane(tid >> 6);
  asm volatile("s_nop 4" ::"s"(w), "s"(a), "s"(b), "s"(ts) : "memory");
  unsigned s1 = 0, t = 0;
  if (M == 0 || M == 1) {
    const u32x4 rs = mkrs(M == 0 ? a : b, M == 0 ? 7340032 : 1835008);
    unsigned long long c0 = 0;
#pragma unroll 1
    for (int j = 0; j < 9; j++) {
      if (j == 1) c0 = __builtin_amdgcn_s_memtime();
      asm volatile("buffer_load_dword %0, %2, %1, %3 offen\n\ts_waitcnt vmcnt(0)" : "+v"(s1) : "s"(rs), "v"((blockIdx.x * 4 + w) * 128), "s"(j * 131072 + 65536) : "memory");
    }
    t = (unsigned)(__builtin_amdgcn_s_memtime() - c0) / 8;
  } else {
    const u32x4 rs = mkrs(blockIdx.x < 128 ? a : b, 7340032);
    const unsigned so = ((blockIdx.x & 127) * 4 + w) * 14336;
    unsigned long long c0 = __builtin_amdgcn_s_memtime();
    const unsigned v0 = lane * 128, v1 = lane < 48 ? lane * 128 + 8192 : OOB;
    if (M == 2) asm volatile("buffer_load_dword %0, %2, %1, %4 offen\n\tbuffer_load_dword %0, %3, %1, %4 offen\n\ts_waitcnt vmcnt(0)" : "+v"(s1) : "s"(rs), "v"(v0), "v"(v1), "s"(so) : "memory");
    else asm volatile("buffer_load_dword %0, %2, %1, %4 offen nt\n\tbuffer_load_dword %0, %3, %1, %4 offen nt\n\ts_waitcnt vmcnt(0)" : "+v"(s1) : "s"(rs), "v"(v0), "v"(v1), "s"(so) : "memory");
    if (M == 4) {
      while (__builtin_amdgcn_s_memtime() - c0 < 20000) __builtin_amdgcn_s_sleep(2);
      c0 = __builtin_amdgcn_s_memtime();
      asm volatile("buffer_load_dword %0, %2, %1, %4 offen sc1\n\tbuffer_load_dword %0, %3, %1, %4 offen sc1\n\ts_waitcnt vmcnt(0)" : "+v"(s1) : "s"(rs), "v"(v0), "v"(v1), "s"(so) : "memory");
    }
    t = (unsigned)(__builtin_amdgcn_s_memtime() - c0);
  }
  if (lane == 0) ts[blockIdx.x * 4 + w] = t + (s1 == 0x12345678);
}
__global__ void spink(const unsigned* ts, int n, int mult) {
  unsigned long long s = 0;
  for (int i = 0; i < n; i++) s += ts[i];
  s /= n; if (s > 40000) s = 40000; s *= mult;
  unsigned long long r0 = __builtin_amdgcn_s_memrealtime();
  while (__builtin_amdgcn_s_memrealtime() - r0 < s) __builtin_amdgcn_s_sleep(8);
}
extern "C" void kernel_launch(void* const* d_in, const int* in_sizes, int n_in, void* d_out, int out_size, void* d_ws, size_t ws_size,
                              hipStream_t stream) {
  unsigned* ts = (unsigned*)((char*)d_ws + (200 << 20));
  probek<2><<<256, 256, 0, stream>>>((const char*)d_in[5], (const char*)d_in[7], ts);
  fused_kernel<<<256, 256, 0, stream>>>((const short*)d_in[0], (const short*)d_in[1], (const short*)d_in[2], (const short*)d_in[3],
                                        (const short*)d_in[5], (const short*)d_in[6], (const short*)d_in[7], (const short*)d_in[8],
                                        (short*)d_out, (char*)d_ws);
  spink<<<1, 1, 0, stream>>>(ts, 1024, 0);
}
```

```cpp
#include <hip/hip_runtime.h>
#include <stdint.h>

typedef short bf16x8 __attribute__((ext_vector_type(8)));
typedef short s16x4 __attribute__((ext_vector_type(4)));
typedef float f32x2 __attribute__((ext_vector_type(2)));
typedef float f32x4 __attribute__((ext_vector_type(4)));
typedef float f32x16 __attribute__((ext_vector_type(16)));
typedef unsigned u32x2 __attribute__((ext_vector_type(2)));
typedef unsigned u32x4 __attribute__((ext_vector_type(4)));
typedef __bf16 b16x2 __attribute__((ext_vector_type(2)));
typedef __attribute__((address_space(3))) char* ldsp;
typedef __amdgpu_buffer_rsrc_t rsrc_t;

#define DEV __device__ __forceinline__
#define SB() __builtin_amdgcn_sched_barrier(0)
#define MFMA(a, b, c) __builtin_amdgcn_mfma_f32_32x32x16_bf16(a, b, c, 0, 0, 0)

constexpr int S = 2048, D = 448, F = 1792;
constexpr unsigned FL_H = 8, FL_X = FL_H + 128, FL_P = FL_X + 128, FL_R = FL_P + 128, FL_O = FL_R + 16, FL_U = FL_O + 128;
constexpr unsigned WS_FLAG_BYTES = 131072;
constexpr unsigned WS_HF = WS_FLAG_BYTES, SZ_HF = 256 * 448 * 16;
constexpr unsigned WS_XN = WS_HF + SZ_HF, SZ_XN = 56 * 2048 * 16;
constexpr unsigned WS_UF = WS_XN + SZ_XN, SZ_UF = 224 * 448 * 16;
constexpr unsigned WS_OF = WS_UF + SZ_UF, SZ_OF = 56 * 2048 * 16;
constexpr unsigned WS_KVP = WS_OF + SZ_OF, SZ_KVP = 16 * 16 * 2 * 4096;
constexpr unsigned WS_KVF = WS_KVP + SZ_KVP, SZ_KVF = 16 * 4 * 1024;
constexpr unsigned WS_END = WS_KVF + SZ_KVF;
constexpr unsigned AREA = 40960;
constexpr unsigned L_WK = 0, L_WV = 50816, L_WQ = 101632, L_WPHI = 152448, L_PHI = 0, L_VX = 32768;
constexpr unsigned OOB = 0x40000000u;
constexpr int SPIN = 1 << 18;

DEV u32x4 mkrs(const void* p, unsigned bytes) {
  unsigned long long a = (unsigned long long)p;
  u32x4 r = {(unsigned)a, (unsigned)(a >> 32), bytes, 0x00020000u};
  return r;
}
DEV rsrc_t mkst(void* p, unsigned bytes) { return __builtin_amdgcn_make_buffer_rsrc(p, 0, bytes, 0x00020000); }
DEV void stc(rsrc_t rs, unsigned voff, u32x4 v) { __builtin_amdgcn_raw_buffer_store_b128(v, rs, voff, 0, 16); }
template <int OFF> DEV bf16x8 ldp(u32x4 rs, unsigned voff, unsigned soff) {
  bf16x8 r;
  asm volatile("buffer_load_dwordx4 %0, %2, %1, %3 offen offset:%4" : "=v"(r) : "s"(rs), "v"(voff), "s"(soff), "n"(OFF) : "memory");
  return r;
}
template <int OFF> DEV bf16x8 ldc(u32x4 rs, unsigned voff, unsigned soff) {
  bf16x8 r;
  asm volatile("buffer_load_dwordx4 %0, %2, %1, %3 offen offset:%4 sc1" : "=v"(r) : "s"(rs), "v"(voff), "s"(soff), "n"(OFF) : "memory");
  return r;
}
template <int OFF> DEV u32x2 ldp2(u32x4 rs, unsigned voff, unsigned soff) {
  u32x2 r;
  asm volatile("buffer_load_dwordx2 %0, %2, %1, %3 offen offset:%4" : "=v"(r) : "s"(rs), "v"(voff), "s"(soff), "n"(OFF) : "memory");
  return r;
}
DEV u32x2 ldc2(u32x4 rs, unsigned voff) {
  u32x2 r;
  asm volatile("buffer_load_dwordx2 %0, %2, %1, 0 offen sc1" : "=v"(r) : "s"(rs), "v"(voff) : "memory");
  return r;
}
template <int OFF> DEV unsigned ldu16(u32x4 rs, unsigned voff, unsigned soff) {
  unsigned r;
  asm volatile("buffer_load_ushort %0, %2, %1, %3 offen offset:%4" : "=v"(r) : "s"(rs), "v"(voff), "s"(soff), "n"(OFF) : "memory");
  return r;
}
DEV void setm0(unsigned a) { asm volatile("s_mov_b32 m0, %0\n\ts_nop 0" ::"s"(a) : "memory"); }
template <int OFF> DEV void dma(u32x4 rs, unsigned voff, unsigned soff) {
  asm volatile("buffer_load_dwordx4 %1, %0, %2 offen offset:%3 lds" ::"s"(rs), "v"(voff), "s"(soff), "n"(OFF) : "memory");
}
DEV void dma4(u32x4 rs, const unsigned (&v)[4], unsigned soff, unsigned m0) {
  setm0(m0); dma<0>(rs, v[0], soff); dma<1024>(rs, v[1], soff); dma<2048>(rs, v[2], soff); dma<3072>(rs, v[3], soff);
}
template <int J> DEV void dma1(u32x4 rs, const unsigned (&v)[4], unsigned soff, unsigned m0) {
  if (J == 0) setm0(m0);
  dma<J * 1024>(rs, v[J], soff);
}
template <int N> DEV void waitv() { asm volatile("s_waitcnt vmcnt(%0)" ::"n"(N) : "memory"); }
template <int N, class T> DEV void waitv(T& a) { asm volatile("s_waitcnt vmcnt(%1)" : "+v"(a) : "n"(N) : "memory"); }
template <int N, class T> DEV void waitv(T& a, T& b) { asm volatile("s_waitcnt vmcnt(%2)" : "+v"(a), "+v"(b) : "n"(N) : "memory"); }
template <int N, class T> DEV void waitv(T& a, T& b, T& c, T& d) {
  asm volatile("s_waitcnt vmcnt(%4)" : "+v"(a), "+v"(b), "+v"(c), "+v"(d) : "n"(N) : "memory");
}
template <class T> DEV void tie(T& a) { asm volatile("" : "+v"(a)::"memory"); }
DEV void bar() { asm volatile("s_waitcnt lgkmcnt(0)\n\ts_barrier" ::: "memory"); }
DEV void barv() { asm volatile("s_waitcnt vmcnt(0) lgkmcnt(0)\n\ts_barrier" ::: "memory"); }
DEV s16x4 vtr(ldsp p) { return __builtin_amdgcn_ds_read_tr16_b64_v4i16((__attribute__((address_space(3))) s16x4*)p); }
DEV bf16x8 cat(s16x4 a, s16x4 b) { return __builtin_shufflevector(a, b, 0, 1, 2, 3, 4, 5, 6, 7); }
DEV unsigned pk(float a, float b) {
  f32x2 v = {a, b};
  return __builtin_bit_cast(unsigned, __builtin_convertvector(v, b16x2));
}
DEV float blo(unsigned u) { return __builtin_bit_cast(float, u << 16); }
DEV float bhi(unsigned u) { return __builtin_bit_cast(float, u & 0xffff0000u); }
DEV void swap32(float& a, float& b) {
  auto r = __builtin_amdgcn_permlane32_swap(__builtin_bit_cast(unsigned, a), __builtin_bit_cast(unsigned, b), false, false);
  const unsigned r0 = r[0], r1 = r[1];
  a = __builtin_bit_cast(float, r0); b = __builtin_bit_cast(float, r1);
}
DEV void swap16(float& a, float& b) {
  auto r = __builtin_amdgcn_permlane16_swap(__builtin_bit_cast(unsigned, a), __builtin_bit_cast(unsigned, b), false, false);
  const unsigned r0 = r[0], r1 = r[1];
  a = __builtin_bit_cast(float, r0); b = __builtin_bit_cast(float, r1);
}
template <int C> DEV float ror(float z) {
  return __builtin_bit_cast(float, __builtin_amdgcn_update_dpp(0, __builtin_bit_cast(int, z), C, 0xf, 0xf, true));
}
DEV float rowsum(float z) { z += ror<0x128>(z); z += ror<0x124>(z); z += ror<0x122>(z); z += ror<0x121>(z); return z; }
DEV float rdl(float z, int l) { return __builtin_bit_cast(float, __builtin_amdgcn_readlane(__builtin_bit_cast(int, z), l)); }
template <class T> DEV void lst(ldsp p, T v) { *(__attribute__((address_space(3))) T*)p = v; }
template <class T> DEV T lld(ldsp p) { return *(__attribute__((address_space(3))) T*)p; }

struct Epoch { unsigned lo, hi; };
DEV void publish(char* ws, unsigned flag, Epoch e) {
  barv();
  if (threadIdx.x == 0) __builtin_amdgcn_raw_buffer_store_b64((u32x2){e.lo, e.hi}, mkst(ws, WS_FLAG_BYTES), flag * 128, 0, 16);
}
DEV bool match(u32x2 f, Epoch e) { return __all(f.x == e.lo && f.y == e.hi); }
DEV void poll(u32x4 rsF, unsigned voff, Epoch e) {
#pragma unroll 1
  for (int it = 0; it < SPIN; ++it) {
    u32x2 f = ldc2(rsF, voff);
    waitv<0>(f);
    if (match(f, e)) break;
    __builtin_amdgcn_s_sleep(1);
  }
}

template <bool TWICE> DEV void ln_rows(const bf16x8 (&v)[4], float (&xf)[4][8]) {
  float s[4], q[4];
#pragma unroll
  for (int r = 0; r < 4; r++) {
    u32x4 u = __builtin_bit_cast(u32x4, v[r]);
#pragma unroll
    for (int e = 0; e < 4; e++) { xf[r][2 * e] = blo(u[e]); xf[r][2 * e + 1] = bhi(u[e]); }
    s[r] = 0.f; q[r] = 0.f;
#pragma unroll
    for (int e = 0; e < 8; e++) { s[r] += xf[r][e]; q[r] = fmaf(xf[r][e], xf[r][e], q[r]); }
  }
  swap32(s[0], s[1]); swap32(s[2], s[3]); swap32(q[0], q[1]); swap32(q[2], q[3]);
  float sa = s[0] + s[1], sb = s[2] + s[3], qa = q[0] + q[1], qb = q[2] + q[3];
  swap16(sa, sb); swap16(qa, qb);
  float mu = rowsum(sa + sb) * (1.f / D), ms = rowsum(qa + qb) * (1.f / D);
  float var = fmaxf(ms - mu * mu, 0.f);
  float a = __builtin_amdgcn_rsqf(var + 1e-5f);
  if (TWICE) a *= __builtin_amdgcn_rsqf(var * a * a + 1e-5f);
  float c = -mu * a;
#pragma unroll
  for (int r = 0; r < 4; r++) {
    const int l = (r & 1) * 32 + (r >> 1) * 16;
    float ar = rdl(a, l), cr = rdl(c, l);
#pragma unroll
    for (int e = 0; e < 8; e++) xf[r][e] = fmaf(xf[r][e], ar, cr);
  }
}

DEV void features(const f32x16& p, bool h, unsigned (&ph)[2][8]) {
  constexpr float L = 1.4426950408889634f;
  float mx = fmaxf(p[0], p[1]), mn = fminf(p[0], p[1]);
#pragma unroll
  for (int r = 2; r < 12; r += 2) { mx = fmaxf(fmaxf(mx, p[r]), p[r + 1]); mn = fminf(fminf(mn, p[r]), p[r + 1]); }
  float mx2 = fmaxf(fmaxf(fmaxf(fmaxf(mx, p[12]), p[13]), p[14]), p[15]);
  float mn2 = fminf(fminf(fminf(fminf(mn, p[12]), p[13]), p[14]), p[15]);
  mx = h ? mx : mx2; mn = h ? -mn : -mn2;
  swap32(mx, mn);
  float c0 = -L * fmaxf(mx, mn), c1 = c0;
  swap32(c0, c1);
  const float ninf = -__builtin_inff();
  const float d0 = h ? ninf : c0, d1 = h ? ninf : c1;
  float e[2][16], s0 = 0.f, s1 = 0.f;
#pragma unroll
  for (int r = 0; r < 16; r++) {
    e[0][r] = __builtin_amdgcn_exp2f(fmaf(p[r], L, r < 12 ? c0 : d0));
    e[1][r] = __builtin_amdgcn_exp2f(fmaf(p[r], -L, r < 12 ? c1 : d1));
    s0 += e[0][r]; s1 += e[1][r];
  }
  swap32(s0, s1);
  float i0 = __builtin_amdgcn_rcpf(s0 + s1), i1 = i0;
  swap32(i0, i1);
#pragma unroll
  for (int r = 0; r < 8; r++) { ph[0][r] = pk(e[0][2 * r] * i0, e[0][2 * r + 1] * i0); ph[1][r] = pk(e[1][2 * r] * i1, e[1][2 * r + 1] * i1); }
}
DEV bf16x8 pack8(const f32x16& a, int o) {
  u32x4 u = {pk(a[o], a[o + 1]), pk(a[o + 2], a[o + 3]), pk(a[o + 4], a[o + 5]), pk(a[o + 6], a[o + 7])};
  return __builtin_bit_cast(bf16x8, u);
}
DEV float gelu(float a) {
  constexpr float c1 = -2.302208198f, c2 = c1 * 0.044715f;
  return a * __builtin_amdgcn_rcpf(1.f + __builtin_amdgcn_exp2f(a * fmaf(a * a, c2, c1)));
}

#define STAMP(n)

template <int T, int N, class Fn> DEV void unroll(Fn&& f) {
  if constexpr (T < N) { f(std::integral_constant<int, T>()); unroll<T + 1, N>(f); }
}
constexpr int g1cnt(int s) { return s < 0 ? 4 : (s + 3 < 16 ? (s >= 1 ? 12 : 4) : 0); }
constexpr int G2R = 4, G2POLL = 5;
constexpr int g2ld(int t) { return t < 17 ? 4 : (t == 17 ? 2 : 0); }
constexpr int g2dm(int t) { return t < 17 ? 4 : (t == 17 ? 2 : 0); }
constexpr int g2cnt(int s) { return s < 0 ? 4 : g2ld(s + G2R) + (s >= 1 ? g2dm(s + 9) : 0) + (s == G2POLL ? 1 : 0); }
constexpr int kvn(int ks) { return 8 + 2 * ks < 41 ? 8 + 2 * ks : 41; }

__global__ __launch_bounds__(256, 1) void fused_kernel(const short* __restrict__ x, const short* __restrict__ wqkv, const short* __restrict__ wphi,
                                                       const short* __restrict__ wo, const short* __restrict__ w1, const short* __restrict__ b1,
                                                       const short* __restrict__ w2, const short* __restrict__ b2, short* __restrict__ out,
                                                       char* __restrict__ ws) {
  __shared__ __attribute__((aligned(16))) char lds_[163840];
  const ldsp lds = (ldsp)lds_;
  const unsigned lds0 = (unsigned)(uintptr_t)lds;
  const int tid = threadIdx.x, lane = tid & 63, w = __builtin_amdgcn_readfirstlane(tid >> 6);
  asm volatile("s_nop 4" ::"s"(w), "s"(x), "s"(wqkv), "s"(wphi), "s"(wo), "s"(w1), "s"(b1), "s"(w2), "s"(b2), "s"(out), "s"(ws) : "memory");
  const int xc = blockIdx.x & 7, i = blockIdx.x >> 3;
  const int h = lane >> 5, n = lane & 31, q = (lane & 15) >> 2, pp = lane & 3, blk = (lane >> 4) & 1;
  const u32x4 rsF = mkrs(ws, WS_FLAG_BYTES), rsX = mkrs(x, S * D * 2), rsW2 = mkrs(w2, F * S * 2);
  const unsigned area = lds0 + w * AREA;
  const int tile2 = 28 * (xc & 1) + i - 4, dt2 = tile2 >> 3, st2 = 8 * (xc >> 1) + (tile2 & 7);
  const unsigned swz = (lane >> 3), swc = ((lane & 7) ^ (((lane >> 4) & 1) << 2)) * 16;
  unsigned v2[4];
#pragma unroll
  for (int j = 0; j < 4; j++) v2[j] = swz * (S * 2) + swc + j * (8 * S * 2 - 1024);
  const unsigned so2 = (w * 448) * (S * 2) + st2 * 128;
  auto pre2 = [&](int t0, int t1, int slot0) {
#pragma unroll 1
    for (int t = t0; t < t1; t++) dma4(rsW2, v2, so2 + t * (32 * S * 2), area + (slot0 + t - t0) * 4096);
  };

  const bool isA = i >= 16;
  const int T = 4 * (xc >> 1) + (i & 3), pr = 4 * (xc & 1) + ((i - 16) >> 2);
  const int row0 = isA ? 128 * T + 16 * pr : 256 * xc + 16 * i;
  auto ldrows = [&](bf16x8 (&xv)[4], u32x2& gen) {
    const unsigned vo = lane < 56 ? lane * 16 : OOB, so = (row0 + 4 * w) * (D * 2);
    xv[0] = ldp<0>(rsX, vo, so); xv[1] = ldp<896>(rsX, vo, so); xv[2] = ldp<1792>(rsX, vo, so); xv[3] = ldp<2688>(rsX, vo, so);
    gen = ldc2(rsF, 0);
  };
  Epoch ep;

  if (!isA) {
    bf16x8 xv[4];
    u32x2 gen;
    ldrows(xv, gen);
    const int g0 = (49 * xc + 3) >> 2, g1 = (49 * xc + 52) >> 2;
    const bool isG = g0 + i < g1;
    const int ft = (g0 + i) / 7, dt = (g0 + i) % 7;
    const u32x4 rsW1 = mkrs(w1, S * F * 2);
    unsigned v1[4];
#pragma unroll
    for (int j = 0; j < 4; j++) v1[j] = swz * (F * 2) + swc + j * (8 * F * 2 - 1024);
    const unsigned so1 = (w * 512) * (F * 2) + ft * 256;
    if (isG) {
#pragma unroll
      for (int t = 0; t < 2; t++) { dma4(rsW1, v1, so1 + t * (32 * F * 2), area + t * 8192); dma4(rsW1, v1, so1 + t * (32 * F * 2) + 128, area + t * 8192 + 4096); }
    } else {
      pre2(4, 8, 0);
    }
    waitv<16>(xv[0], xv[1], xv[2], xv[3]);
    tie(gen);
    STAMP(0);
    {
      unsigned long long g = (((unsigned long long)__builtin_amdgcn_readfirstlane(gen.y) << 32) | (unsigned)__builtin_amdgcn_readfirstlane(gen.x)) + 1;
      ep.lo = (unsigned)g; ep.hi = (unsigned)(g >> 32);
    }
    {
      float xf[4][8];
      ln_rows<true>(xv, xf);
      if (lane < 56) {
        const ldsp p = lds + (w >> 1) * AREA + 32768 + (w & 1) * 8 + lane * 16;
#pragma unroll
        for (int e = 0; e < 8; e++) lst(p + e * (57 * 16), (u32x2){pk(xf[0][e], xf[1][e]), pk(xf[2][e], xf[3][e])});
      }
      bar();
      const ldsp p = lds + 32768 + ((tid & 7) * 57 + (tid >> 3)) * 16;
      const rsrc_t st = mkst(ws + WS_HF, SZ_HF);
      const unsigned vo = (row0 >> 3) * (D * 16) + tid * 16;
      u32x4 e0 = lld<u32x4>(p), e1 = lld<u32x4>(p + (w < 3 ? 512 : AREA - 384)), e2 = lld<u32x4>(p + AREA + 128);
      stc(st, vo, e0); stc(st, vo + 4096, e1); stc(st, vo + 8192, e2);
      if (w < 2) stc(st, vo + 12288, lld<u32x4>(p + AREA + 640));
    }
    STAMP(1);
    publish(ws, FL_H + (row0 >> 4), ep);
    STAMP(2);

    if (isG) {
#pragma unroll
      for (int t = 2; t < 4; t++) { dma4(rsW1, v1, so1 + t * (32 * F * 2), area + t * 8192); dma4(rsW1, v1, so1 + t * (32 * F * 2) + 128, area + t * 8192 + 4096); }
      u32x2 bb[4];
      {
        const u32x4 rsB = mkrs(b1, F * 2);
        const unsigned so = (ft * 128 + w * 32) * 2;
        bb[0] = ldp2<0>(rsB, h * 8, so); bb[1] = ldp2<16>(rsB, h * 8, so); bb[2] = ldp2<32>(rsB, h * 8, so); bb[3] = ldp2<48>(rsB, h * 8, so);
      }
      poll(rsF, (FL_H + w * 32 + n) * 128, ep);
      STAMP(3);
      const u32x4 rsH = mkrs(ws + WS_HF, SZ_HF);
      const unsigned vh0 = h * (D * 16) + (dt * 64 + n) * 16, vh1 = vh0 + 2 * D * 16, soh = w * 64 * (D * 16);
      bf16x8 fb[16][4];
      auto ldH = [&](auto tt, auto jj) {
        constexpr int t = tt.value, j = jj.value;
        fb[t][j] = ldc<(j & 1) * 512>(rsH, (j >> 1) ? vh1 : vh0, soh + t * (4 * D * 16));
      };
      unroll<0, 3>([&](auto tt) { unroll<0, 4>([&](auto jj) { ldH(tt, jj); }); });
      waitv<12>(bb[0], bb[1], bb[2], bb[3]);
      f32x16 acc[4][2];
#pragma unroll
      for (int g = 0; g < 4; g++) {
        acc[0][0][4 * g] = blo(bb[g].x); acc[0][0][4 * g + 1] = bhi(bb[g].x); acc[0][0][4 * g + 2] = blo(bb[g].y); acc[0][0][4 * g + 3] = bhi(bb[g].y);
      }
      acc[0][1] = acc[0][0];
#pragma unroll
      for (int a = 1; a < 4; a++) { acc[a][0] = (f32x16)(0.f); acc[a][1] = (f32x16)(0.f); }
      ldsp pa[4];
#pragma unroll
      for (int a = 0; a < 4; a++) {
        const int s = a ^ w;
        pa[a] = lds + w * AREA + (s >> 1) * 4096 + (8 * h + q) * 128 + (((4 * (s & 1) + 2 * blk + (pp >> 1)) ^ ((q >> 1) << 2)) * 16) + (pp & 1) * 8;
      }
      unroll<0, 16>([&](auto tt) {
        constexpr int t = tt.value, sl = t & 3;
        waitv<g1cnt(t - 2) + g1cnt(t - 1)>(fb[t][0], fb[t][1], fb[t][2], fb[t][3]);
        s16x4 lo[2][4], hi[2][4];
#pragma unroll
        for (int ks = 0; ks < 2; ks++)
#pragma unroll
          for (int a = 0; a < 4; a++) { lo[ks][a] = vtr(pa[a] + sl * 8192 + ks * 2048); hi[ks][a] = vtr(pa[a] + sl * 8192 + ks * 2048 + 512); }
        SB();
        unroll<0, 16>([&](auto mm) {
          constexpr int m = mm.value, ks = m >> 3, a = (m >> 1) & 3, dh = m & 1;
          if constexpr (t + 3 < 16) {
            if constexpr (m < 4) ldH(std::integral_constant<int, t + 3>(), mm);
            else if constexpr (m < 12 && t >= 1)
              dma1<(m & 3)>(rsW1, v1, so1 + (t + 3) * (32 * F * 2) + (m >= 8 ? 128 : 0), area + ((t + 3) & 3) * 8192 + (m >= 8 ? 4096 : 0));
          }
          acc[a][dh] = MFMA(cat(lo[ks][a], hi[ks][a]), fb[t][ks * 2 + dh], acc[a][dh]);
          SB();
        });
      });
      STAMP(4);
      if (i >= 4) pre2(0, 4, 6);
      {
        const ldsp p = lds + w * AREA + lane * 16;
#pragma unroll
        for (int a = 1; a < 4; a++)
#pragma unroll
          for (int dh = 0; dh < 2; dh++)
#pragma unroll
            for (int g = 0; g < 4; g++)
              lst(p + (a - 1) * 8192 + dh * 4096 + g * 1024, (f32x4){acc[a][dh][4 * g], acc[a][dh][4 * g + 1], acc[a][dh][4 * g + 2], acc[a][dh][4 * g + 3]});
      }
      bar();
      STAMP(5);
      {
        const rsrc_t st = mkst(ws + WS_UF, SZ_UF);
        const unsigned vo = (ft * 16 + w * 4 + h) * (D * 16) + (dt * 64 + n) * 16;
        f32x4 v[2][3][4];
#pragma unroll
        for (int dh = 0; dh < 2; dh++)
#pragma unroll
          for (int a = 1; a < 4; a++)
#pragma unroll
            for (int g = 0; g < 4; g++) v[dh][a - 1][g] = lld<f32x4>(lds + (w ^ a) * AREA + lane * 16 + (a - 1) * 8192 + dh * 4096 + g * 1024);
        SB();
#pragma unroll
        for (int dh = 0; dh < 2; dh++) {
#pragma unroll
          for (int g = 0; g < 4; g += 2) {
            float r[8];
#pragma unroll
            for (int e = 0; e < 8; e++) {
              const int gg = g + (e >> 2), c = e & 3;
              r[e] = gelu((acc[0][dh][4 * gg + c] + v[dh][0][gg][c]) + (v[dh][1][gg][c] + v[dh][2][gg][c]));
            }
            auto r0 = __builtin_amdgcn_permlane32_swap(pk(r[0], r[1]), pk(r[4], r[5]), false, false);
            auto r1 = __builtin_amdgcn_permlane32_swap(pk(r[2], r[3]), pk(r[6], r[7]), false, false);
            stc(st, vo + g * (D * 16) + dh * 512, (u32x4){r0[0], r1[0], r0[1], r1[1]});
          }
        }
      }
      STAMP(6);
      publish(ws, FL_U + dt * 14 + ft, ep);
      STAMP(7);
      if (i >= 4) pre2(4, 10, 0);
    } else {
      pre2(0, 4, 6);
      pre2(4, 10, 0);
      if (i >= 14) {
        const int head = 2 * xc + i - 14;
        poll(rsF, (FL_P + xc * 16 + (lane & 15)) * 128, ep);
        STAMP(3);
        const u32x4 rsP = mkrs(ws + WS_KVP, SZ_KVP);
        const unsigned so = head * 131072 + (w >> 1) * 4096 + (w & 1) * 2048;
        bf16x8 pv[16][2];
        unroll<0, 16>([&](auto tt) { constexpr int t = tt.value; pv[t][0] = ldc<0>(rsP, lane * 16, so + t * 8192); pv[t][1] = ldc<1024>(rsP, lane * 16, so + t * 8192); });
        f32x4 a0 = {0.f, 0.f, 0.f, 0.f}, a1 = a0;
        unroll<0, 16>([&](auto tt) {
          constexpr int t = tt.value;
          waitv<30 - 2 * t>(pv[t][0], pv[t][1]);
          a0 += __builtin_bit_cast(f32x4, pv[t][0]); a1 += __builtin_bit_cast(f32x4, pv[t][1]);
        });
        stc(mkst(ws + WS_KVF, SZ_KVF), head * 4096 + w * 1024 + lane * 16, (u32x4){pk(a0[0], a0[1]), pk(a0[2], a0[3]), pk(a1[0], a1[1]), pk(a1[2], a1[3])});
        STAMP(4);
        publish(ws, FL_R + head, ep);
        STAMP(5);
      }
    }
  } else {
    bf16x8 xv[4];
    u32x2 gen;
    ldrows(xv, gen);
    const u32x4 rsW = mkrs(wqkv, D * 3 * D * 2);
    unsigned vw[4];
    {
      const unsigned r7 = (lane * 37) >> 8;
#pragma unroll
      for (int j = 0; j < 4; j++) vw[j] = r7 * 2688 + (lane - r7 * 7) * 16 + j * (9 * 2688 - 1008);
    }
    const unsigned sow = w * 112 * 2688 + pr * 112, mw = lds0 + w * 112 * 112;
    auto dmaW = [&](auto kk, unsigned col, unsigned base) {
      constexpr int k = kk.value;
      if constexpr ((k & 3) == 0) setm0(mw + base + (k >> 2) * 4032);
      dma<(k & 3) * 1008>(rsW, vw[k & 3], sow + col + (k >> 2) * (36 * 2688));
    };
    setm0(lds0 + L_WPHI + w * 1024);
    dma<0>(mkrs(wphi + pr * 1568, 3136), tid * 16, 0);
    unroll<0, 13>([&](auto kk) { dmaW(kk, 896, L_WK); });
    waitv<14>(xv[0], xv[1], xv[2], xv[3]);
    tie(gen);
    STAMP(0);
    {
      unsigned long long g = (((unsigned long long)__builtin_amdgcn_readfirstlane(gen.y) << 32) | (unsigned)__builtin_amdgcn_readfirstlane(gen.x)) + 1;
      ep.lo = (unsigned)g; ep.hi = (unsigned)(g >> 32);
    }
    {
      float xf[4][8];
      ln_rows<false>(xv, xf);
      if (lane < 56) {
        const ldsp p = lds + L_WQ + (lane >> 1) * 544 + (lane & 1) * 4 + w * 64;
#pragma unroll
        for (int r = 0; r < 4; r++)
#pragma unroll
          for (int e = 0; e < 4; e++) lst(p + r * 16 + (e >> 1) * 272 + (e & 1) * 8, pk(xf[r][e], xf[r][e + 4]));
      }
      bar();
      const ldsp p = lds + L_WQ + (tid >> 4) * 272 + (tid & 15) * 16;
      const rsrc_t st = mkst(ws + WS_XN, SZ_XN);
      const unsigned vo = (tid >> 4) * (S * 16) + (row0 + (tid & 15)) * 16;
      u32x4 e0 = lld<u32x4>(p), e1 = lld<u32x4>(p + 16 * 272), e2 = lld<u32x4>(p + 32 * 272);
      stc(st, vo, e0); stc(st, vo + 16 * S * 16, e1); stc(st, vo + 32 * S * 16, e2);
      if (w < 2) stc(st, vo + 48 * S * 16, lld<u32x4>(p + 48 * 272));
    }
    STAMP(1);
    publish(ws, FL_X + T * 8 + pr, ep);
    STAMP(2);
    unroll<0, 13>([&](auto kk) { dmaW(kk, 1792, L_WV); });
    poll(rsF, (FL_X + T * 8 + 2 * w + (lane & 1)) * 128, ep);
    STAMP(3);
    const u32x4 rsN = mkrs(ws + WS_XN, SZ_XN);
    const unsigned vx = h * (S * 16) + (128 * T + 32 * w + n) * 16;
    bf16x8 xa[28];
    auto item = [&](auto kk) {
      constexpr int k = kk.value;
      if constexpr (k < 28) xa[k] = ldc<0>(rsN, vx, k * (2 * S * 16));
      else if constexpr (k < 41) dmaW(std::integral_constant<int, k - 28>(), 0, L_WQ);
    };
    unroll<0, 8>(item);
    bar();
    bf16x8 wf[2][2];
    {
      const ldsp p = lds + L_WPHI + (4 * h + q) * 56 + 32 * blk + 8 * pp;
#pragma unroll
      for (int hd = 0; hd < 2; hd++) {
        wf[hd][0] = cat(vtr(p + hd * 1568), vtr(p + hd * 1568 + 8 * 56));
        s16x4 z = vtr(p + hd * 1568 + 24 * 56);
        wf[hd][1] = cat(vtr(p + hd * 1568 + 16 * 56), h ? (s16x4){0, 0, 0, 0} : z);
      }
    }
    const unsigned wlane = (2 * h + 4 * q) * 112 + 32 * blk + 8 * pp;
    f32x16 kT[2] = {}, vv[2] = {};
    {
      const ldsp pk_ = lds + L_WK + wlane, pv_ = lds + L_WV + wlane;
      s16x4 fr[29][8];
      auto rd = [&](auto kk, auto cc) {
        constexpr int ks = kk.value, c = cc.value;
        if constexpr (ks < 28) fr[ks][c] = vtr(((c & 4) ? pv_ : pk_) + ks * 1792 + ((c >> 1) & 1) * 56 + (c & 1) * 112);
      };
      unroll<0, 8>([&](auto cc) { rd(std::integral_constant<int, 0>(), cc); });
      unroll<0, 28>([&](auto kk) {
        constexpr int ks = kk.value;
        waitv<kvn(ks) - ks - 1>(xa[ks]);
        if (ks == 0) STAMP(4);
        unroll<0, 4>([&](auto mm) {
          constexpr int m = mm.value;
          rd(std::integral_constant<int, ks + 1>(), std::integral_constant<int, 2 * m>());
          rd(std::integral_constant<int, ks + 1>(), std::integral_constant<int, 2 * m + 1>());
          if constexpr (m & 1) item(std::integral_constant<int, 8 + 2 * ks + (m >> 1)>());
          const bf16x8 wv = cat(fr[ks][2 * m], fr[ks][2 * m + 1]);
          if constexpr (m < 2) kT[m] = MFMA(wv, xa[ks], kT[m]);
          else vv[m - 2] = MFMA(xa[ks], wv, vv[m - 2]);
          SB();
        });
      });
    }
    STAMP(5);
    {
      f32x16 p[2];
#pragma unroll
      for (int hd = 0; hd < 2; hd++) { p[hd] = MFMA(wf[hd][0], pack8(kT[hd], 0), (f32x16)(0.f)); p[hd] = MFMA(wf[hd][1], pack8(kT[hd], 8), p[hd]); }
      bar();
      {
        const bf16x8 ones = {0x3f80, 0x3f80, 0x3f80, 0x3f80, 0x3f80, 0x3f80, 0x3f80, 0x3f80};
        const ldsp pvx = lds + L_VX + w * 2048 + lane * 16;
#pragma unroll
        for (int hd = 0; hd < 2; hd++)
#pragma unroll
          for (int j = 0; j < 2; j++) { bf16x8 f = pack8(vv[hd], 8 * j); lst(pvx + hd * 8192 + j * 1024, n == 28 ? ones : f); }
      }
      const unsigned pb = (32 * w + n) * 64 + ((h ^ ((n >> 1) & 7)) * 8);
#pragma unroll
      for (int hd = 0; hd < 2; hd++) {
        unsigned ph[2][8];
        features(p[hd], h, ph);
#pragma unroll
        for (int sg = 0; sg < 2; sg++)
#pragma unroll
          for (int g = 0; g < 4; g++) lst(lds + L_PHI + (hd * 2 + sg) * 8192 + (pb ^ (g << 4)), (u32x2){ph[sg][2 * g], ph[sg][2 * g + 1]});
      }
    }
    STAMP(6);
    barv();
    {
      const unsigned sw = 2 * h + (q >> 1);
      const ldsp pl = lds + L_PHI + w * 8192 + (4 * h + q) * 64 + (((4 * blk + pp) ^ sw) * 8);
      const ldsp ph_ = lds + L_PHI + w * 8192 + (8 + 4 * h + q) * 64 + (((4 * blk + pp) ^ sw ^ 4) * 8);
      const ldsp pvx = lds + L_VX + (w >> 1) * 8192 + lane * 16;
      f32x16 kv = {};
#pragma unroll
      for (int jj = 0; jj < 8; jj++) kv = MFMA(cat(vtr(pl + jj * 1024), vtr(ph_ + jj * 1024)), lld<bf16x8>(pvx + jj * 1024), kv);
      const rsrc_t st = mkst(ws + WS_KVP, SZ_KVP);
      const unsigned vo = (2 * pr + (w >> 1)) * 131072 + T * 8192 + (w & 1) * 4096 + lane * 16;
#pragma unroll
      for (int g = 0; g < 4; g++) stc(st, vo + g * 1024, __builtin_bit_cast(u32x4, (f32x4){kv[4 * g], kv[4 * g + 1], kv[4 * g + 2], kv[4 * g + 3]}));
    }
    STAMP(7);
    f32x16 qT[2] = {};
    {
      const ldsp pq_ = lds + L_WQ + wlane;
      s16x4 fr[29][4];
      auto rd = [&](auto kk, auto cc) {
        constexpr int ks = kk.value, c = cc.value;
        if constexpr (ks < 28) fr[ks][c] = vtr(pq_ + ks * 1792 + (c >> 1) * 56 + (c & 1) * 112);
      };
      unroll<0, 4>([&](auto cc) { rd(std::integral_constant<int, 0>(), cc); });
      unroll<0, 28>([&](auto kk) {
        constexpr int ks = kk.value;
        if constexpr (ks == 14) { publish(ws, FL_P + pr * 16 + T, ep); STAMP(8); SB(); }
        unroll<0, 2>([&](auto mm) {
          constexpr int m = mm.value;
          rd(std::integral_constant<int, ks + 1>(), std::integral_constant<int, 2 * m>());
          rd(std::integral_constant<int, ks + 1>(), std::integral_constant<int, 2 * m + 1>());
          qT[m] = MFMA(cat(fr[ks][2 * m], fr[ks][2 * m + 1]), xa[ks], qT[m]);
          SB();
        });
      });
    }
    unsigned phq[2][2][8];
    {
      f32x16 p[2];
#pragma unroll
      for (int hd = 0; hd < 2; hd++) { p[hd] = MFMA(wf[hd][0], pack8(qT[hd], 0), (f32x16)(0.f)); p[hd] = MFMA(wf[hd][1], pack8(qT[hd], 8), p[hd]); }
      bar();
      STAMP(9);
      pre2(0, 4, 6);
      pre2(4, 10, 0);
      features(p[0], h, phq[0]);
      features(p[1], h, phq[1]);
    }
    STAMP(10);
    poll(rsF, (FL_R + 2 * pr + (lane & 1)) * 128, ep);
    STAMP(11);
    {
      const u32x4 rsK = mkrs(ws + WS_KVF, SZ_KVF);
      bf16x8 kf[2][4];
#pragma unroll
      for (int hd = 0; hd < 2; hd++) {
        const unsigned so = (2 * pr + hd) * 4096;
        kf[hd][0] = ldc<0>(rsK, lane * 16, so); kf[hd][1] = ldc<1024>(rsK, lane * 16, so);
        kf[hd][2] = ldc<2048>(rsK, lane * 16, so); kf[hd][3] = ldc<3072>(rsK, lane * 16, so);
      }
      f32x16 o[2] = {};
      waitv<4>(kf[0][0], kf[0][1], kf[0][2], kf[0][3]);
#pragma unroll
      for (int hd = 0; hd < 2; hd++) {
        if (hd) waitv<0>(kf[1][0], kf[1][1], kf[1][2], kf[1][3]);
#pragma unroll
        for (int c = 0; c < 4; c++) {
          const unsigned* s = &phq[hd][c >> 1][4 * (c & 1)];
          o[hd] = MFMA(kf[hd][c], __builtin_bit_cast(bf16x8, (u32x4){s[0], s[1], s[2], s[3]}), o[hd]);
        }
      }
      float da = o[0][12], db = o[1][12];
      swap32(da, db);
      float ia = __builtin_amdgcn_rcpf(db + 1e-6f), ib = ia;
      swap32(ia, ib);
      unsigned a[8], bq[8];
#pragma unroll
      for (int r = 0; r < 8; r++) { a[r] = pk(o[0][2 * r] * ia, o[0][2 * r + 1] * ia); bq[r] = pk(o[1][2 * r] * ib, o[1][2 * r + 1] * ib); }
      const rsrc_t st = mkst(ws + WS_OF, SZ_OF);
      const unsigned tk = (128 * T + 32 * w + n) * 16, jb = 14 * (pr >> 1);
      const unsigned vo = (jb + 6 * (pr & 1) + h) * (S * 16) + tk;
      stc(st, vo, (u32x4){a[0], a[1], a[2], a[3]});
      stc(st, vo + 2 * S * 16, (u32x4){a[4], a[5], bq[0], bq[1]});
      stc(st, vo + 4 * S * 16, (u32x4){bq[2], bq[3], bq[4], bq[5]});
      stc(st, h ? OOB : (jb + 12 + (pr & 1)) * (S * 16) + tk, (u32x4){a[6], a[7], bq[6], bq[7]});
    }
    publish(ws, FL_O + T * 8 + pr, ep);
    STAMP(12);
  }

  if (i >= 4) {
    const unsigned orow = st2 * 64 + 16 * w + (lane >> 3);
    const unsigned vo = orow * (D * 2) + dt2 * 128 + (lane & 7) * 16;
    bf16x8 xr0 = ldp<0>(rsX, vo, 0), xr1 = ldp<0>(rsX, vo, 8 * D * 2);
    const u32x4 rsB = mkrs(b2, S * 2);
    unsigned bs0 = ldu16<0>(rsB, orow * 2, 0), bs1 = ldu16<16>(rsB, orow * 2, 0);
    poll(rsF, (FL_U + dt2 * 14 + ((7 * w) >> 1) + (lane & 3)) * 128, ep);
    tie(xr0); tie(xr1); tie(bs0); tie(bs1);
    STAMP(13);
    const u32x4 rsU = mkrs(ws + WS_UF, SZ_UF), rsO = mkrs(ws + WS_OF, SZ_OF), rsWo = mkrs(wo, D * D * 2);
    const unsigned vu0 = h * (D * 16) + (dt2 * 64 + n) * 16, vu1 = vu0 + 2 * D * 16, sou = w * 56 * (D * 16);
    const unsigned vof = h * (S * 16) + (st2 * 64 + n) * 16, soo = w * 14 * (S * 16);
    const unsigned vfl = (FL_O + (st2 >> 1) * 8 + 2 * w + (lane & 1)) * 128;
    unsigned vwo[4];
#pragma unroll
    for (int j = 0; j < 4; j++) vwo[j] = swz * (D * 2) + swc + j * (8 * D * 2 - 1024);
    const unsigned sowo = w * 112 * (D * 2) + dt2 * 128;
    bf16x8 fa[18][4];
    u32x2 fl;
    auto ldD = [&](auto tt, auto jj) {
      constexpr int t = tt.value, j = jj.value;
      if constexpr (t < 14) fa[t][j] = ldc<(j & 1) * 512>(rsU, (j >> 1) ? vu1 : vu0, sou + t * (4 * D * 16));
      else if constexpr (j < g2ld(t)) fa[t][j] = ldc<(j & 1) * 512>(rsO, vof, soo + (2 * (t - 14) + (j >> 1)) * (2 * S * 16));
    };
    auto dmaS = [&](auto tt, auto jj) {
      constexpr int t = tt.value, j = jj.value;
      if constexpr (t < 14) dma1<j>(rsW2, v2, so2 + t * (32 * S * 2), area + ((t + 6) % 10) * 4096);
      else if constexpr (j < g2dm(t)) dma1<j>(rsWo, vwo, sowo + (t - 14) * (32 * D * 2), area + (t - 14) * 4096);
    };
    unroll<0, G2R>([&](auto tt) { unroll<0, 4>([&](auto jj) { ldD(tt, jj); }); });
    ldsp pb[2], pw[2];
#pragma unroll
    for (int a = 0; a < 2; a++) {
      const unsigned c = q * 128 + (((4 * a + 2 * blk + (pp >> 1)) ^ ((q >> 1) << 2)) * 16) + (pp & 1) * 8;
      pb[a] = lds + w * AREA + h * 1024 + c;
      pw[a] = lds + w * AREA + h * 512 + c;
    }
    f32x16 acc[2][2] = {};
    unroll<0, 18>([&](auto tt) {
      constexpr int t = tt.value, nk = t < 17 ? 2 : 1;
      constexpr int left = g2cnt(t - 3) + g2cnt(t - 2) + g2cnt(t - 1);
      if constexpr (nk == 2) waitv<left>(fa[t][0], fa[t][1], fa[t][2], fa[t][3]); else waitv<left>(fa[t][0], fa[t][1]);
      if constexpr (t == G2POLL + G2R) {
        tie(fl);
        if (!match(fl, ep)) poll(rsF, vfl, ep);
      }
      s16x4 lo[2][2], hi[2][2];
#pragma unroll
      for (int ks = 0; ks < nk; ks++)
#pragma unroll
        for (int a = 0; a < 2; a++) {
          if (t < 14) {
            lo[ks][a] = vtr(pb[a] + ((t + 6) % 10) * 4096 + ks * 2048); hi[ks][a] = vtr(pb[a] + ((t + 6) % 10) * 4096 + ks * 2048 + 512);
          } else {
            const int j = 2 * (t - 14) + ks;
            if (j < 6) {
              const int u = j % 3, rl = 56 * (j / 3) + (u == 0 ? 0 : u == 1 ? 16 : 36), rh = 56 * (j / 3) + (u == 0 ? 8 : u == 1 ? 28 : 44);
              lo[ks][a] = vtr(pw[a] + rl * 128); hi[ks][a] = vtr(pw[a] + rh * 128);
            } else {
              lo[ks][a] = vtr(pw[a] + h * (52 * 128) + 24 * 128); hi[ks][a] = vtr(pw[a] + h * (52 * 128) + 52 * 128);
            }
          }
        }
      SB();
      unroll<0, 4 * nk>([&](auto mm) {
        constexpr int m = mm.value, ks = m >> 2, nh = (m >> 1) & 1, mh = m & 1;
        if constexpr (m < 4) ldD(std::integral_constant<int, t + G2R>(), mm);
        else if constexpr (t >= 1) dmaS(std::integral_constant<int, t + 9>(), std::integral_constant<int, m - 4>());
        if constexpr (t == G2POLL && m == 7) fl = ldc2(rsF, vfl);
        if constexpr (t < 14) acc[nh][mh] = MFMA(fa[t][ks * 2 + mh], cat(lo[ks][nh], hi[ks][nh]), acc[nh][mh]);
        else acc[nh][mh] = MFMA(cat(lo[ks][mh], hi[ks][mh]), fa[t][ks * 2 + nh], acc[nh][mh]);
        SB();
      });
    });
    STAMP(14);
#pragma unroll
    for (int nh = 0; nh < 2; nh++)
#pragma unroll
      for (int mh = 0; mh < 2; mh++)
#pragma unroll
        for (int g = 0; g < 4; g++)
          lst(lds + w * AREA + (32 * nh + n) * 256 + (((8 * mh + 2 * g + h) ^ (n & 15)) * 16),
              (f32x4){acc[nh][mh][4 * g], acc[nh][mh][4 * g + 1], acc[nh][mh][4 * g + 2], acc[nh][mh][4 * g + 3]});
    bar();
    const rsrc_t sto = mkst(out, S * D * 2);
#pragma unroll
    for (int u = 0; u < 2; u++) {
      const int r = 16 * w + 8 * u + (lane >> 3);
      const unsigned c0 = r * 256 + (((2 * (lane & 7)) ^ (r & 15)) * 16), c1 = c0 ^ 16;
      f32x4 a = lld<f32x4>(lds + c0), bq = lld<f32x4>(lds + c1);
#pragma unroll
      for (int s = 1; s < 4; s++) { a += lld<f32x4>(lds + s * AREA + c0); bq += lld<f32x4>(lds + s * AREA + c1); }
      const u32x4 xu = __builtin_bit_cast(u32x4, u ? xr1 : xr0);
      const float bv = blo(u ? bs1 : bs0);
      u32x4 res = {pk(a[0] + bv + blo(xu[0]), a[1] + bv + bhi(xu[0])), pk(a[2] + bv + blo(xu[1]), a[3] + bv + bhi(xu[1])),
                   pk(bq[0] + bv + blo(xu[2]), bq[1] + bv + bhi(xu[2])), pk(bq[2] + bv + blo(xu[3]), bq[3] + bv + bhi(xu[3]))};
      __builtin_amdgcn_raw_buffer_store_b128(res, sto, vo + u * (8 * D * 2), 0, 18);
    }
    if (blockIdx.x == 255 && tid == 0) __builtin_amdgcn_raw_buffer_store_b64((u32x2){ep.lo, ep.hi}, mkst(ws, WS_FLAG_BYTES), 0, 0, 16);
  }
}

extern "C" void kernel_launch(void* const* d_in, const int* in_sizes, int n_in, void* d_out, int out_size, void* d_ws, size_t ws_size,
                              hipStream_t stream) {
  fused_kernel<<<256, 256, 0, stream>>>((const short*)d_in[0], (const short*)d_in[1], (const short*)d_in[2], (const short*)d_in[3],
                                        (const short*)d_in[5], (const short*)d_in[6], (const short*)d_in[7], (const short*)d_in[8],
                                        (short*)d_out, (char*)d_ws);
}
```

```cpp
#include <hip/hip_runtime.h>
#include <stdint.h>

typedef short bf16x8 __attribute__((ext_vector_type(8)));
typedef short s16x4 __attribute__((ext_vector_type(4)));
typedef float f32x2 __attribute__((ext_vector_type(2)));
typedef float f32x4 __attribute__((ext_vector_type(4)));
typedef float f32x16 __attribute__((ext_vector_type(16)));
typedef unsigned u32x2 __attribute__((ext_vector_type(2)));
typedef unsigned u32x4 __attribute__((ext_vector_type(4)));
typedef __bf16 b16x2 __attribute__((ext_vector_type(2)));
typedef __attribute__((address_space(3))) char* ldsp;
typedef __amdgpu_buffer_rsrc_t rsrc_t;

#define DEV __device__ __forceinline__
#define SB() __builtin_amdgcn_sched_barrier(0)
#define MFMA(a, b, c) __builtin_amdgcn_mfma_f32_32x32x16_bf16(a, b, c, 0, 0, 0)

constexpr int S = 2048, D = 448, F = 1792;
constexpr unsigned FL_H = 8, FL_X = FL_H + 128, FL_P = FL_X + 128, FL_R = FL_P + 128, FL_O = FL_R + 16, FL_U = FL_O + 128;
constexpr unsigned WS_FLAG_BYTES = 131072;
constexpr unsigned WS_HF = WS_FLAG_BYTES, SZ_HF = 256 * 448 * 16;
constexpr unsigned WS_XN = WS_HF + SZ_HF, SZ_XN = 56 * 2048 * 16;
constexpr unsigned WS_UF = WS_XN + SZ_XN, SZ_UF = 224 * 448 * 16;
constexpr unsigned WS_OF = WS_UF + SZ_UF, SZ_OF = 56 * 2048 * 16;
constexpr unsigned WS_KVP = WS_OF + SZ_OF, SZ_KVP = 16 * 16 * 2 * 4096;
constexpr unsigned WS_KVF = WS_KVP + SZ_KVP, SZ_KVF = 16 * 4 * 1024;
constexpr unsigned WS_END = WS_KVF + SZ_KVF;
constexpr unsigned AREA = 40960;
constexpr unsigned L_WK = 0, L_WV = 50816, L_WQ = 101632, L_WPHI = 152448, L_PHI = 0, L_VX = 32768;
constexpr unsigned OOB = 0x40000000u;
constexpr int SPIN = 1 << 18;

DEV u32x4 mkrs(const void* p, unsigned bytes) {
  unsigned long long a = (unsigned long long)p;
  u32x4 r = {(unsigned)a, (unsigned)(a >> 32), bytes, 0x00020000u};
  return r;
}
DEV rsrc_t mkst(void* p, unsigned bytes) { return __builtin_amdgcn_make_buffer_rsrc(p, 0, bytes, 0x00020000); }
DEV void stc(rsrc_t rs, unsigned voff, u32x4 v) { __builtin_amdgcn_raw_buffer_store_b128(v, rs, voff, 0, 16); }
template <int OFF> DEV bf16x8 ldp(u32x4 rs, unsigned voff, unsigned soff) {
  bf16x8 r;
  asm volatile("buffer_load_dwordx4 %0, %2, %1, %3 offen offset:%4" : "=v"(r) : "s"(rs), "v"(voff), "s"(soff), "n"(OFF) : "memory");
  return r;
}
template <int OFF> DEV bf16x8 ldc(u32x4 rs, unsigned voff, unsigned soff) {
  bf16x8 r;
  asm volatile("buffer_load_dwordx4 %0, %2, %1, %3 offen offset:%4 sc1" : "=v"(r) : "s"(rs), "v"(voff), "s"(soff), "n"(OFF) : "memory");
  return r;
}
template <int OFF> DEV u32x2 ldp2(u32x4 rs, unsigned voff, unsigned soff) {
  u32x2 r;
  asm volatile("buffer_load_dwordx2 %0, %2, %1, %3 offen offset:%4" : "=v"(r) : "s"(rs), "v"(voff), "s"(soff), "n"(OFF) : "memory");
  return r;
}
DEV u32x2 ldc2(u32x4 rs, unsigned voff) {
  u32x2 r;
  asm volatile("buffer_load_dwordx2 %0, %2, %1, 0 offen sc1" : "=v"(r) : "s"(rs), "v"(voff) : "memory");
  return r;
}
template <int OFF> DEV unsigned ldu16(u32x4 rs, unsigned voff, unsigned soff) {
  unsigned r;
  asm volatile("buffer_load_ushort %0, %2, %1, %3 offen offset:%4" : "=v"(r) : "s"(rs), "v"(voff), "s"(soff), "n"(OFF) : "memory");
  return r;
}
DEV void setm0(unsigned a) { asm volatile("s_mov_b32 m0, %0\n\ts_nop 0" ::"s"(a) : "memory"); }
template <int OFF> DEV void dma(u32x4 rs, unsigned voff, unsigned soff) {
  asm volatile("buffer_load_dwordx4 %1, %0, %2 offen offset:%3 lds" ::"s"(rs), "v"(voff), "s"(soff), "n"(OFF) : "memory");
}
DEV void dma4(u32x4 rs, const unsigned (&v)[4], unsigned soff, unsigned m0) {
  setm0(m0); dma<0>(rs, v[0], soff); dma<1024>(rs, v[1], soff); dma<2048>(rs, v[2], soff); dma<3072>(rs, v[3], soff);
}
template <int J> DEV void dma1(u32x4 rs, const unsigned (&v)[4], unsigned soff, unsigned m0) {
  if (J == 0) setm0(m0);
  dma<J * 1024>(rs, v[J], soff);
}
template <int N> DEV void waitv() { asm volatile("s_waitcnt vmcnt(%0)" ::"n"(N) : "memory"); }
template <int N, class T> DEV void waitv(T& a) { asm volatile("s_waitcnt vmcnt(%1)" : "+v"(a) : "n"(N) : "memory"); }
template <int N, class T> DEV void waitv(T& a, T& b) { asm volatile("s_waitcnt vmcnt(%2)" : "+v"(a), "+v"(b) : "n"(N) : "memory"); }
template <int N, class T> DEV void waitv(T& a, T& b, T& c, T& d) {
  asm volatile("s_waitcnt vmcnt(%4)" : "+v"(a), "+v"(b), "+v"(c), "+v"(d) : "n"(N) : "memory");
}
template <class T> DEV void tie(T& a) { asm volatile("" : "+v"(a)::"memory"); }
DEV void bar() { asm volatile("s_waitcnt lgkmcnt(0)\n\ts_barrier" ::: "memory"); }
DEV void barv() { asm volatile("s_waitcnt vmcnt(0) lgkmcnt(0)\n\ts_barrier" ::: "memory"); }
DEV s16x4 vtr(ldsp p) { return __builtin_amdgcn_ds_read_tr16_b64_v4i16((__attribute__((address_space(3))) s16x4*)p); }
DEV bf16x8 cat(s16x4 a, s16x4 b) { return __builtin_shufflevector(a, b, 0, 1, 2, 3, 4, 5, 6, 7); }
DEV unsigned pk(float a, float b) {
  f32x2 v = {a, b};
  return __builtin_bit_cast(unsigned, __builtin_convertvector(v, b16x2));
}
DEV float blo(unsigned u) { return __builtin_bit_cast(float, u << 16); }
DEV float bhi(unsigned u) { return __builtin_bit_cast(float, u & 0xffff0000u); }
DEV void swap32(float& a, float& b) {
  auto r = __builtin_amdgcn_permlane32_swap(__builtin_bit_cast(unsigned, a), __builtin_bit_cast(unsigned, b), false, false);
  const unsigned r0 = r[0], r1 = r[1];
  a = __builtin_bit_cast(float, r0); b = __builtin_bit_cast(float, r1);
}
DEV void swap16(float& a, float& b) {
  auto r = __builtin_amdgcn_permlane16_swap(__builtin_bit_cast(unsigned, a), __builtin_bit_cast(unsigned, b), false, false);
  const unsigned r0 = r[0], r1 = r[1];
  a = __builtin_bit_cast(float, r0); b = __builtin_bit_cast(float, r1);
}
template <int C> DEV float ror(float z) {
  return __builtin_bit_cast(float, __builtin_amdgcn_update_dpp(0, __builtin_bit_cast(int, z), C, 0xf, 0xf, true));
}
DEV float rowsum(float z) { z += ror<0x128>(z); z += ror<0x124>(z); z += ror<0x122>(z); z += ror<0x121>(z); return z; }
DEV float rdl(float z, int l) { return __builtin_bit_cast(float, __builtin_amdgcn_readlane(__builtin_bit_cast(int, z), l)); }
template <class T> DEV void lst(ldsp p, T v) { *(__attribute__((address_space(3))) T*)p = v; }
template <class T> DEV T lld(ldsp p) { return *(__attribute__((address_space(3))) T*)p; }

struct Epoch { unsigned lo, hi; };
DEV void publish(char* ws, unsigned flag, Epoch e) {
  barv();
  if (threadIdx.x == 0) __builtin_amdgcn_raw_buffer_store_b64((u32x2){e.lo, e.hi}, mkst(ws, WS_FLAG_BYTES), flag * 128, 0, 16);
}
DEV bool match(u32x2 f, Epoch e) { return __all(f.x == e.lo && f.y == e.hi); }
DEV void poll(u32x4 rsF, unsigned voff, Epoch e) {
#pragma unroll 1
  for (int it = 0; it < SPIN; ++it) {
    u32x2 f = ldc2(rsF, voff);
    waitv<0>(f);
    if (match(f, e)) break;
    __builtin_amdgcn_s_sleep(1);
  }
}

template <bool TWICE> DEV void ln_rows(const bf16x8 (&v)[4], float (&xf)[4][8]) {
  float s[4], q[4];
#pragma unroll
  for (int r = 0; r < 4; r++) {
    u32x4 u = __builtin_bit_cast(u32x4, v[r]);
#pragma unroll
    for (int e = 0; e < 4; e++) { xf[r][2 * e] = blo(u[e]); xf[r][2 * e + 1] = bhi(u[e]); }
    s[r] = 0.f; q[r] = 0.f;
#pragma unroll
    for (int e = 0; e < 8; e++) { s[r] += xf[r][e]; q[r] = fmaf(xf[r][e], xf[r][e], q[r]); }
  }
  swap32(s[0], s[1]); swap32(s[2], s[3]); swap32(q[0], q[1]); swap32(q[2], q[3]);
  float sa = s[0] + s[1], sb = s[2] + s[3], qa = q[0] + q[1], qb = q[2] + q[3];
  swap16(sa, sb); swap16(qa, qb);
  float mu = rowsum(sa + sb) * (1.f / D), ms = rowsum(qa + qb) * (1.f / D);
  float var = fmaxf(ms - mu * mu, 0.f);
  float a = __builtin_amdgcn_rsqf(var + 1e-5f);
  if (TWICE) a *= __builtin_amdgcn_rsqf(var * a * a + 1e-5f);
  float c = -mu * a;
#pragma unroll
  for (int r = 0; r < 4; r++) {
    const int l = (r & 1) * 32 + (r >> 1) * 16;
    float ar = rdl(a, l), cr = rdl(c, l);
#pragma unroll
    for (int e = 0; e < 8; e++) xf[r][e] = fmaf(xf[r][e], ar, cr);
  }
}

DEV void features(const f32x16& p, bool h, unsigned (&ph)[2][8]) {
  constexpr float L = 1.4426950408889634f;
  float mx = fmaxf(p[0], p[1]), mn = fminf(p[0], p[1]);
#pragma unroll
  for (int r = 2; r < 12; r += 2) { mx = fmaxf(fmaxf(mx, p[r]), p[r + 1]); mn = fminf(fminf(mn, p[r]), p[r + 1]); }
  float mx2 = fmaxf(fmaxf(fmaxf(fmaxf(mx, p[12]), p[13]), p[14]), p[15]);
  float mn2 = fminf(fminf(fminf(fminf(mn, p[12]), p[13]), p[14]), p[15]);
  mx = h ? mx : mx2; mn = h ? -mn : -mn2;
  swap32(mx, mn);
  float c0 = -L * fmaxf(mx, mn), c1 = c0;
  swap32(c0, c1);
  const float ninf = -__builtin_inff();
  const float d0 = h ? ninf : c0, d1 = h ? ninf : c1;
  float e[2][16], s0 = 0.f, s1 = 0.f;
#pragma unroll
  for (int r = 0; r < 16; r++) {
    e[0][r] = __builtin_amdgcn_exp2f(fmaf(p[r], L, r < 12 ? c0 : d0));
    e[1][r] = __builtin_amdgcn_exp2f(fmaf(p[r], -L, r < 12 ? c1 : d1));
    s0 += e[0][r]; s1 += e[1][r];
  }
  swap32(s0, s1);
  float i0 = __builtin_amdgcn_rcpf(s0 + s1), i1 = i0;
  swap32(i0, i1);
#pragma unroll
  for (int r = 0; r < 8; r++) { ph[0][r] = pk(e[0][2 * r] * i0, e[0][2 * r + 1] * i0); ph[1][r] = pk(e[1][2 * r] * i1, e[1][2 * r + 1] * i1); }
}
DEV bf16x8 pack8(const f32x16& a, int o) {
  u32x4 u = {pk(a[o], a[o + 1]), pk(a[o + 2], a[o + 3]), pk(a[o + 4], a[o + 5]), pk(a[o + 6], a[o + 7])};
  return __builtin_bit_cast(bf16x8, u);
}
DEV float gelu(float a) {
  constexpr float c1 = -2.302208198f, c2 = c1 * 0.044715f;
  return a * __builtin_amdgcn_rcpf(1.f + __builtin_amdgcn_exp2f(a * fmaf(a * a, c2, c1)));
}

#define STAMP(n)

template <int T, int N, class Fn> DEV void unroll(Fn&& f) {
  if constexpr (T < N) { f(std::integral_constant<int, T>()); unroll<T + 1, N>(f); }
}
constexpr int g1cnt(int s) { return s < 0 ? 4 : (s + 3 < 16 ? (s >= 1 ? 12 : 4) : 0); }
constexpr int G2R = 4, G2POLL = 5;
constexpr int g2ld(int t) { return t < 17 ? 4 : (t == 17 ? 2 : 0); }
constexpr int g2dm(int t) { return t < 17 ? 4 : (t == 17 ? 2 : 0); }
constexpr int g2cnt(int s) { return s < 0 ? 4 : g2ld(s + G2R) + (s >= 1 ? g2dm(s + 9) : 0) + (s == G2POLL ? 1 : 0); }
constexpr int kvn(int ks) { return 8 + 2 * ks < 41 ? 8 + 2 * ks : 41; }

__global__ __launch_bounds__(256, 1) void fused_kernel(const short* __restrict__ x, const short* __restrict__ wqkv, const short* __restrict__ wphi,
                                                       const short* __restrict__ wo, const short* __restrict__ w1, const short* __restrict__ b1,
                                                       const short* __restrict__ w2, const short* __restrict__ b2, short* __restrict__ out,
                                                       char* __restrict__ ws) {
  __shared__ __attribute__((aligned(16))) char lds_[163840];
  const ldsp lds = (ldsp)lds_;
  const unsigned lds0 = (unsigned)(uintptr_t)lds;
  const int tid = threadIdx.x, lane = tid & 63, w = __builtin_amdgcn_readfirstlane(tid >> 6);
  asm volatile("s_nop 4" ::"s"(w), "s"(x), "s"(wqkv), "s"(wphi), "s"(wo), "s"(w1), "s"(b1), "s"(w2), "s"(b2), "s"(out), "s"(ws) : "memory");
  const int xc = blockIdx.x & 7, i = blockIdx.x >> 3;
  const int h = lane >> 5, n = lane & 31, q = (lane & 15) >> 2, pp = lane & 3, blk = (lane >> 4) & 1;
  const u32x4 rsF = mkrs(ws, WS_FLAG_BYTES), rsX = mkrs(x, S * D * 2), rsW2 = mkrs(w2, F * S * 2);
  const unsigned area = lds0 + w * AREA;
  const int tile2 = 28 * (xc & 1) + i - 4, dt2 = tile2 >> 3, st2 = 8 * (xc >> 1) + (tile2 & 7);
  const unsigned swz = (lane >> 3), swc = ((lane & 7) ^ (((lane >> 4) & 1) << 2)) * 16;
  unsigned v2[4];
#pragma unroll
  for (int j = 0; j < 4; j++) v2[j] = swz * (S * 2) + swc + j * (8 * S * 2 - 1024);
  const unsigned so2 = (w * 448) * (S * 2) + st2 * 128;
  auto pre2 = [&](int t0, int t1, int slot0) {
#pragma unroll 1
    for (int t = t0; t < t1; t++) dma4(rsW2, v2, so2 + t * (32 * S * 2), area + (slot0 + t - t0) * 4096);
  };

  unsigned sink = 0;
  if (i == 31) {
    unsigned long long pc;
    asm volatile("s_getpc_b64 %0" : "=s"(pc));
    const u32x4 rsC = mkrs((const void*)(pc & ~127ull), 41472);
    asm volatile("buffer_load_dword %0, %2, %1, 0 offen" : "+v"(sink) : "s"(rsC), "v"(tid * 128) : "memory");
    asm volatile("buffer_load_dword %0, %2, %1, %3 offen" : "+v"(sink) : "s"(rsC), "v"(tid * 128), "s"(32768) : "memory");
  }
  const bool isA = i >= 16;
  const int T = 4 * (xc >> 1) + (i & 3), pr = 4 * (xc & 1) + ((i - 16) >> 2);
  const int row0 = isA ? 128 * T + 16 * pr : 256 * xc + 16 * i;
  auto ldrows = [&](bf16x8 (&xv)[4], u32x2& gen) {
    const unsigned vo = lane < 56 ? lane * 16 : OOB, so = (row0 + 4 * w) * (D * 2);
    xv[0] = ldp<0>(rsX, vo, so); xv[1] = ldp<896>(rsX, vo, so); xv[2] = ldp<1792>(rsX, vo, so); xv[3] = ldp<2688>(rsX, vo, so);
    gen = ldc2(rsF, 0);
  };
  Epoch ep;

  if (!isA) {
    bf16x8 xv[4];
    u32x2 gen;
    ldrows(xv, gen);
    const int g0 = (49 * xc + 3) >> 2, g1 = (49 * xc + 52) >> 2;
    const bool isG = g0 + i < g1;
    const int ft = (g0 + i) / 7, dt = (g0 + i) % 7;
    const u32x4 rsW1 = mkrs(w1, S * F * 2);
    unsigned v1[4];
#pragma unroll
    for (int j = 0; j < 4; j++) v1[j] = swz * (F * 2) + swc + j * (8 * F * 2 - 1024);
    const unsigned so1 = (w * 512) * (F * 2) + ft * 256;
    if (isG) {
#pragma unroll
      for (int t = 0; t < 2; t++) { dma4(rsW1, v1, so1 + t * (32 * F * 2), area + t * 8192); dma4(rsW1, v1, so1 + t * (32 * F * 2) + 128, area + t * 8192 + 4096); }
    } else {
      pre2(4, 8, 0);
    }
    waitv<16>(xv[0], xv[1], xv[2], xv[3]);
    tie(gen);
    STAMP(0);
    {
      unsigned long long g = (((unsigned long long)__builtin_amdgcn_readfirstlane(gen.y) << 32) | (unsigned)__builtin_amdgcn_readfirstlane(gen.x)) + 1;
      ep.lo = (unsigned)g; ep.hi = (unsigned)(g >> 32);
    }
    {
      float xf[4][8];
      ln_rows<true>(xv, xf);
      if (lane < 56) {
        const ldsp p = lds + (w >> 1) * AREA + 32768 + (w & 1) * 8 + lane * 16;
#pragma unroll
        for (int e = 0; e < 8; e++) lst(p + e * (57 * 16), (u32x2){pk(xf[0][e], xf[1][e]), pk(xf[2][e], xf[3][e])});
      }
      bar();
      const ldsp p = lds + 32768 + ((tid & 7) * 57 + (tid >> 3)) * 16;
      const rsrc_t st = mkst(ws + WS_HF, SZ_HF);
      const unsigned vo = (row0 >> 3) * (D * 16) + tid * 16;
      u32x4 e0 = lld<u32x4>(p), e1 = lld<u32x4>(p + (w < 3 ? 512 : AREA - 384)), e2 = lld<u32x4>(p + AREA + 128);
      stc(st, vo, e0); stc(st, vo + 4096, e1); stc(st, vo + 8192, e2);
      if (w < 2) stc(st, vo + 12288, lld<u32x4>(p + AREA + 640));
    }
    STAMP(1);
    publish(ws, FL_H + (row0 >> 4), ep);
    STAMP(2);

    if (isG) {
#pragma unroll
      for (int t = 2; t < 4; t++) { dma4(rsW1, v1, so1 + t * (32 * F * 2), area + t * 8192); dma4(rsW1, v1, so1 + t * (32 * F * 2) + 128, area + t * 8192 + 4096); }
      u32x2 bb[4];
      {
        const u32x4 rsB = mkrs(b1, F * 2);
        const unsigned so = (ft * 128 + w * 32) * 2;
        bb[0] = ldp2<0>(rsB, h * 8, so); bb[1] = ldp2<16>(rsB, h * 8, so); bb[2] = ldp2<32>(rsB, h * 8, so); bb[3] = ldp2<48>(rsB, h * 8, so);
      }
      poll(rsF, (FL_H + w * 32 + n) * 128, ep);
      STAMP(3);
      const u32x4 rsH = mkrs(ws + WS_HF, SZ_HF);
      const unsigned vh0 = h * (D * 16) + (dt * 64 + n) * 16, vh1 = vh0 + 2 * D * 16, soh = w * 64 * (D * 16);
      bf16x8 fb[16][4];
      auto ldH = [&](auto tt, auto jj) {
        constexpr int t = tt.value, j = jj.value;
        fb[t][j] = ldc<(j & 1) * 512>(rsH, (j >> 1) ? vh1 : vh0, soh + t * (4 * D * 16));
      };
      unroll<0, 3>([&](auto tt) { unroll<0, 4>([&](auto jj) { ldH(tt, jj); }); });
      waitv<12>(bb[0], bb[1], bb[2], bb[3]);
      f32x16 acc[4][2];
#pragma unroll
      for (int g = 0; g < 4; g++) {
        acc[0][0][4 * g] = blo(bb[g].x); acc[0][0][4 * g + 1] = bhi(bb[g].x); acc[0][0][4 * g + 2] = blo(bb[g].y); acc[0][0][4 * g + 3] = bhi(bb[g].y);
      }
      acc[0][1] = acc[0][0];
#pragma unroll
      for (int a = 1; a < 4; a++) { acc[a][0] = (f32x16)(0.f); acc[a][1] = (f32x16)(0.f); }
      ldsp pa[4];
#pragma unroll
      for (int a = 0; a < 4; a++) {
        const int s = a ^ w;
        pa[a] = lds + w * AREA + (s >> 1) * 4096 + (8 * h + q) * 128 + (((4 * (s & 1) + 2 * blk + (pp >> 1)) ^ ((q >> 1) << 2)) * 16) + (pp & 1) * 8;
      }
      unroll<0, 16>([&](auto tt) {
        constexpr int t = tt.value, sl = t & 3;
        waitv<g1cnt(t - 2) + g1cnt(t - 1)>(fb[t][0], fb[t][1], fb[t][2], fb[t][3]);
        s16x4 lo[2][4], hi[2][4];
#pragma unroll
        for (int ks = 0; ks < 2; ks++)
#pragma unroll
          for (int a = 0; a < 4; a++) { lo[ks][a] = vtr(pa[a] + sl * 8192 + ks * 2048); hi[ks][a] = vtr(pa[a] + sl * 8192 + ks * 2048 + 512); }
        SB();
        unroll<0, 16>([&](auto mm) {
          constexpr int m = mm.value, ks = m >> 3, a = (m >> 1) & 3, dh = m & 1;
          if constexpr (t + 3 < 16) {
            if constexpr (m < 4) ldH(std::integral_constant<int, t + 3>(), mm);
            else if constexpr (m < 12 && t >= 1)
              dma1<(m & 3)>(rsW1, v1, so1 + (t + 3) * (32 * F * 2) + (m >= 8 ? 128 : 0), area + ((t + 3) & 3) * 8192 + (m >= 8 ? 4096 : 0));
          }
          acc[a][dh] = MFMA(cat(lo[ks][a], hi[ks][a]), fb[t][ks * 2 + dh], acc[a][dh]);
          SB();
        });
      });
      STAMP(4);
      if (i >= 4) pre2(0, 4, 6);
      {
        const ldsp p = lds + w * AREA + lane * 16;
#pragma unroll
        for (int a = 1; a < 4; a++)
#pragma unroll
          for (int dh = 0; dh < 2; dh++)
#pragma unroll
            for (int g = 0; g < 4; g++)
              lst(p + (a - 1) * 8192 + dh * 4096 + g * 1024, (f32x4){acc[a][dh][4 * g], acc[a][dh][4 * g + 1], acc[a][dh][4 * g + 2], acc[a][dh][4 * g + 3]});
      }
      bar();
      STAMP(5);
      {
        const rsrc_t st = mkst(ws + WS_UF, SZ_UF);
        const unsigned vo = (ft * 16 + w * 4 + h) * (D * 16) + (dt * 64 + n) * 16;
        f32x4 v[2][3][4];
#pragma unroll
        for (int dh = 0; dh < 2; dh++)
#pragma unroll
          for (int a = 1; a < 4; a++)
#pragma unroll
            for (int g = 0; g < 4; g++) v[dh][a - 1][g] = lld<f32x4>(lds + (w ^ a) * AREA + lane * 16 + (a - 1) * 8192 + dh * 4096 + g * 1024);
        SB();
#pragma unroll
        for (int dh = 0; dh < 2; dh++) {
#pragma unroll
          for (int g = 0; g < 4; g += 2) {
            float r[8];
#pragma unroll
            for (int e = 0; e < 8; e++) {
              const int gg = g + (e >> 2), c = e & 3;
              r[e] = gelu((acc[0][dh][4 * gg + c] + v[dh][0][gg][c]) + (v[dh][1][gg][c] + v[dh][2][gg][c]));
            }
            auto r0 = __builtin_amdgcn_permlane32_swap(pk(r[0], r[1]), pk(r[4], r[5]), false, false);
            auto r1 = __builtin_amdgcn_permlane32_swap(pk(r[2], r[3]), pk(r[6], r[7]), false, false);
            stc(st, vo + g * (D * 16) + dh * 512, (u32x4){r0[0], r1[0], r0[1], r1[1]});
          }
        }
      }
      STAMP(6);
      publish(ws, FL_U + dt * 14 + ft, ep);
      STAMP(7);
      if (i >= 4) pre2(4, 10, 0);
    } else {
      pre2(0, 4, 6);
      pre2(4, 10, 0);
      if (i >= 14) {
        const int head = 2 * xc + i - 14;
        poll(rsF, (FL_P + xc * 16 + (lane & 15)) * 128, ep);
        STAMP(3);
        const u32x4 rsP = mkrs(ws + WS_KVP, SZ_KVP);
        const unsigned so = head * 131072 + (w >> 1) * 4096 + (w & 1) * 2048;
        bf16x8 pv[16][2];
        unroll<0, 16>([&](auto tt) { constexpr int t = tt.value; pv[t][0] = ldc<0>(rsP, lane * 16, so + t * 8192); pv[t][1] = ldc<1024>(rsP, lane * 16, so + t * 8192); });
        f32x4 a0 = {0.f, 0.f, 0.f, 0.f}, a1 = a0;
        unroll<0, 16>([&](auto tt) {
          constexpr int t = tt.value;
          waitv<30 - 2 * t>(pv[t][0], pv[t][1]);
          a0 += __builtin_bit_cast(f32x4, pv[t][0]); a1 += __builtin_bit_cast(f32x4, pv[t][1]);
        });
        stc(mkst(ws + WS_KVF, SZ_KVF), head * 4096 + w * 1024 + lane * 16, (u32x4){pk(a0[0], a0[1]), pk(a0[2], a0[3]), pk(a1[0], a1[1]), pk(a1[2], a1[3])});
        STAMP(4);
        publish(ws, FL_R + head, ep);
        STAMP(5);
      }
    }
  } else {
    bf16x8 xv[4];
    u32x2 gen;
    ldrows(xv, gen);
    const u32x4 rsW = mkrs(wqkv, D * 3 * D * 2);
    unsigned vw[4];
    {
      const unsigned r7 = (lane * 37) >> 8;
#pragma unroll
      for (int j = 0; j < 4; j++) vw[j] = r7 * 2688 + (lane - r7 * 7) * 16 + j * (9 * 2688 - 1008);
    }
    const unsigned sow = w * 112 * 2688 + pr * 112, mw = lds0 + w * 112 * 112;
    auto dmaW = [&](auto kk, unsigned col, unsigned base) {
      constexpr int k = kk.value;
      if constexpr ((k & 3) == 0) setm0(mw + base + (k >> 2) * 4032);
      dma<(k & 3) * 1008>(rsW, vw[k & 3], sow + col + (k >> 2) * (36 * 2688));
    };
    setm0(lds0 + L_WPHI + w * 1024);
    dma<0>(mkrs(wphi + pr * 1568, 3136), tid * 16, 0);
    unroll<0, 13>([&](auto kk) { dmaW(kk, 896, L_WK); });
    waitv<14>(xv[0], xv[1], xv[2], xv[3]);
    tie(gen);
    STAMP(0);
    {
      unsigned long long g = (((unsigned long long)__builtin_amdgcn_readfirstlane(gen.y) << 32) | (unsigned)__builtin_amdgcn_readfirstlane(gen.x)) + 1;
      ep.lo = (unsigned)g; ep.hi = (unsigned)(g >> 32);
    }
    {
      float xf[4][8];
      ln_rows<false>(xv, xf);
      if (lane < 56) {
        const ldsp p = lds + L_WQ + (lane >> 1) * 544 + (lane & 1) * 4 + w * 64;
#pragma unroll
        for (int r = 0; r < 4; r++)
#pragma unroll
          for (int e = 0; e < 4; e++) lst(p + r * 16 + (e >> 1) * 272 + (e & 1) * 8, pk(xf[r][e], xf[r][e + 4]));
      }
      bar();
      const ldsp p = lds + L_WQ + (tid >> 4) * 272 + (tid & 15) * 16;
      const rsrc_t st = mkst(ws + WS_XN, SZ_XN);
      const unsigned vo = (tid >> 4) * (S * 16) + (row0 + (tid & 15)) * 16;
      u32x4 e0 = lld<u32x4>(p), e1 = lld<u32x4>(p + 16 * 272), e2 = lld<u32x4>(p + 32 * 272);
      stc(st, vo, e0); stc(st, vo + 16 * S * 16, e1); stc(st, vo + 32 * S * 16, e2);
      if (w < 2) stc(st, vo + 48 * S * 16, lld<u32x4>(p + 48 * 272));
    }
    STAMP(1);
    publish(ws, FL_X + T * 8 + pr, ep);
    STAMP(2);
    unroll<0, 13>([&](auto kk) { dmaW(kk, 1792, L_WV); });
    poll(rsF, (FL_X + T * 8 + 2 * w + (lane & 1)) * 128, ep);
    STAMP(3);
    const u32x4 rsN = mkrs(ws + WS_XN, SZ_XN);
    const unsigned vx = h * (S * 16) + (128 * T + 32 * w + n) * 16;
    bf16x8 xa[28];
    auto item = [&](auto kk) {
      constexpr int k = kk.value;
      if constexpr (k < 28) xa[k] = ldc<0>(rsN, vx, k * (2 * S * 16));
      else if constexpr (k < 41) dmaW(std::integral_constant<int, k - 28>(), 0, L_WQ);
    };
    unroll<0, 8>(item);
    bar();
    bf16x8 wf[2][2];
    {
      const ldsp p = lds + L_WPHI + (4 * h + q) * 56 + 32 * blk + 8 * pp;
#pragma unroll
      for (int hd = 0; hd < 2; hd++) {
        wf[hd][0] = cat(vtr(p + hd * 1568), vtr(p + hd * 1568 + 8 * 56));
        s16x4 z = vtr(p + hd * 1568 + 24 * 56);
        wf[hd][1] = cat(vtr(p + hd * 1568 + 16 * 56), h ? (s16x4){0, 0, 0, 0} : z);
      }
    }
    const unsigned wlane = (2 * h + 4 * q) * 112 + 32 * blk + 8 * pp;
    f32x16 kT[2] = {}, vv[2] = {};
    {
      const ldsp pk_ = lds + L_WK + wlane, pv_ = lds + L_WV + wlane;
      s16x4 fr[29][8];
      auto rd = [&](auto kk, auto cc) {
        constexpr int ks = kk.value, c = cc.value;
        if constexpr (ks < 28) fr[ks][c] = vtr(((c & 4) ? pv_ : pk_) + ks * 1792 + ((c >> 1) & 1) * 56 + (c & 1) * 112);
      };
      unroll<0, 8>([&](auto cc) { rd(std::integral_constant<int, 0>(), cc); });
      unroll<0, 28>([&](auto kk) {
        constexpr int ks = kk.value;
        waitv<kvn(ks) - ks - 1>(xa[ks]);
        if (ks == 0) STAMP(4);
        unroll<0, 4>([&](auto mm) {
          constexpr int m = mm.value;
          rd(std::integral_constant<int, ks + 1>(), std::integral_constant<int, 2 * m>());
          rd(std::integral_constant<int, ks + 1>(), std::integral_constant<int, 2 * m + 1>());
          if constexpr (m & 1) item(std::integral_constant<int, 8 + 2 * ks + (m >> 1)>());
          const bf16x8 wv = cat(fr[ks][2 * m], fr[ks][2 * m + 1]);
          if constexpr (m < 2) kT[m] = MFMA(wv, xa[ks], kT[m]);
          else vv[m - 2] = MFMA(xa[ks], wv, vv[m - 2]);
          SB();
        });
      });
    }
    STAMP(5);
    {
      f32x16 p[2];
#pragma unroll
      for (int hd = 0; hd < 2; hd++) { p[hd] = MFMA(wf[hd][0], pack8(kT[hd], 0), (f32x16)(0.f)); p[hd] = MFMA(wf[hd][1], pack8(kT[hd], 8), p[hd]); }
      bar();
      {
        const bf16x8 ones = {0x3f80, 0x3f80, 0x3f80, 0x3f80, 0x3f80, 0x3f80, 0x3f80, 0x3f80};
        const ldsp pvx = lds + L_VX + w * 2048 + lane * 16;
#pragma unroll
        for (int hd = 0; hd < 2; hd++)
#pragma unroll
          for (int j = 0; j < 2; j++) { bf16x8 f = pack8(vv[hd], 8 * j); lst(pvx + hd * 8192 + j * 1024, n == 28 ? ones : f); }
      }
      const unsigned pb = (32 * w + n) * 64 + ((h ^ ((n >> 1) & 7)) * 8);
#pragma unroll
      for (int hd = 0; hd < 2; hd++) {
        unsigned ph[2][8];
        features(p[hd], h, ph);
#pragma unroll
        for (int sg = 0; sg < 2; sg++)
#pragma unroll
          for (int g = 0; g < 4; g++) lst(lds + L_PHI + (hd * 2 + sg) * 8192 + (pb ^ (g << 4)), (u32x2){ph[sg][2 * g], ph[sg][2 * g + 1]});
      }
    }
    STAMP(6);
    barv();
    {
      const unsigned sw = 2 * h + (q >> 1);
      const ldsp pl = lds + L_PHI + w * 8192 + (4 * h + q) * 64 + (((4 * blk + pp) ^ sw) * 8);
      const ldsp ph_ = lds + L_PHI + w * 8192 + (8 + 4 * h + q) * 64 + (((4 * blk + pp) ^ sw ^ 4) * 8);
      const ldsp pvx = lds + L_VX + (w >> 1) * 8192 + lane * 16;
      f32x16 kv = {};
#pragma unroll
      for (int jj = 0; jj < 8; jj++) kv = MFMA(cat(vtr(pl + jj * 1024), vtr(ph_ + jj * 1024)), lld<bf16x8>(pvx + jj * 1024), kv);
      const rsrc_t st = mkst(ws + WS_KVP, SZ_KVP);
      const unsigned vo = (2 * pr + (w >> 1)) * 131072 + T * 8192 + (w & 1) * 4096 + lane * 16;
#pragma unroll
      for (int g = 0; g < 4; g++) stc(st, vo + g * 1024, __builtin_bit_cast(u32x4, (f32x4){kv[4 * g], kv[4 * g + 1], kv[4 * g + 2], kv[4 * g + 3]}));
    }
    STAMP(7);
    f32x16 qT[2] = {};
    {
      const ldsp pq_ = lds + L_WQ + wlane;
      s16x4 fr[29][4];
      auto rd = [&](auto kk, auto cc) {
        constexpr int ks = kk.value, c = cc.value;
        if constexpr (ks < 28) fr[ks][c] = vtr(pq_ + ks * 1792 + (c >> 1) * 56 + (c & 1) * 112);
      };
      unroll<0, 4>([&](auto cc) { rd(std::integral_constant<int, 0>(), cc); });
      unroll<0, 28>([&](auto kk) {
        constexpr int ks = kk.value;
        if constexpr (ks == 14) { publish(ws, FL_P + pr * 16 + T, ep); STAMP(8); SB(); }
        unroll<0, 2>([&](auto mm) {
          constexpr int m = mm.value;
          rd(std::integral_constant<int, ks + 1>(), std::integral_constant<int, 2 * m>());
          rd(std::integral_constant<int, ks + 1>(), std::integral_constant<int, 2 * m + 1>());
          qT[m] = MFMA(cat(fr[ks][2 * m], fr[ks][2 * m + 1]), xa[ks], qT[m]);
          SB();
        });
      });
    }
    unsigned phq[2][2][8];
    {
      f32x16 p[2];
#pragma unroll
      for (int hd = 0; hd < 2; hd++) { p[hd] = MFMA(wf[hd][0], pack8(qT[hd], 0), (f32x16)(0.f)); p[hd] = MFMA(wf[hd][1], pack8(qT[hd], 8), p[hd]); }
      bar();
      STAMP(9);
      pre2(0, 4, 6);
      pre2(4, 10, 0);
      features(p[0], h, phq[0]);
      features(p[1], h, phq[1]);
    }
    STAMP(10);
    poll(rsF, (FL_R + 2 * pr + (lane & 1)) * 128, ep);
    STAMP(11);
    {
      const u32x4 rsK = mkrs(ws + WS_KVF, SZ_KVF);
      bf16x8 kf[2][4];
#pragma unroll
      for (int hd = 0; hd < 2; hd++) {
        const unsigned so = (2 * pr + hd) * 4096;
        kf[hd][0] = ldc<0>(rsK, lane * 16, so); kf[hd][1] = ldc<1024>(rsK, lane * 16, so);
        kf[hd][2] = ldc<2048>(rsK, lane * 16, so); kf[hd][3] = ldc<3072>(rsK, lane * 16, so);
      }
      f32x16 o[2] = {};
      waitv<4>(kf[0][0], kf[0][1], kf[0][2], kf[0][3]);
#pragma unroll
      for (int hd = 0; hd < 2; hd++) {
        if (hd) waitv<0>(kf[1][0], kf[1][1], kf[1][2], kf[1][3]);
#pragma unroll
        for (int c = 0; c < 4; c++) {
          const unsigned* s = &phq[hd][c >> 1][4 * (c & 1)];
          o[hd] = MFMA(kf[hd][c], __builtin_bit_cast(bf16x8, (u32x4){s[0], s[1], s[2], s[3]}), o[hd]);
        }
      }
      float da = o[0][12], db = o[1][12];
      swap32(da, db);
      float ia = __builtin_amdgcn_rcpf(db + 1e-6f), ib = ia;
      swap32(ia, ib);
      unsigned a[8], bq[8];
#pragma unroll
      for (int r = 0; r < 8; r++) { a[r] = pk(o[0][2 * r] * ia, o[0][2 * r + 1] * ia); bq[r] = pk(o[1][2 * r] * ib, o[1][2 * r + 1] * ib); }
      const rsrc_t st = mkst(ws + WS_OF, SZ_OF);
      const unsigned tk = (128 * T + 32 * w + n) * 16, jb = 14 * (pr >> 1);
      const unsigned vo = (jb + 6 * (pr & 1) + h) * (S * 16) + tk;
      stc(st, vo, (u32x4){a[0], a[1], a[2], a[3]});
      stc(st, vo + 2 * S * 16, (u32x4){a[4], a[5], bq[0], bq[1]});
      stc(st, vo + 4 * S * 16, (u32x4){bq[2], bq[3], bq[4], bq[5]});
      stc(st, h ? OOB : (jb + 12 + (pr & 1)) * (S * 16) + tk, (u32x4){a[6], a[7], bq[6], bq[7]});
    }
    publish(ws, FL_O + T * 8 + pr, ep);
    STAMP(12);
  }

  tie(sink);
  if (i >= 4) {
    const unsigned orow = st2 * 64 + 16 * w + (lane >> 3);
    const unsigned vo = orow * (D * 2) + dt2 * 128 + (lane & 7) * 16;
    bf16x8 xr0 = ldp<0>(rsX, vo, 0), xr1 = ldp<0>(rsX, vo, 8 * D * 2);
    const u32x4 rsB = mkrs(b2, S * 2);
    unsigned bs0 = ldu16<0>(rsB, orow * 2, 0), bs1 = ldu16<16>(rsB, orow * 2, 0);
    poll(rsF, (FL_U + dt2 * 14 + ((7 * w) >> 1) + (lane & 3)) * 128, ep);
    tie(xr0); tie(xr1); tie(bs0); tie(bs1);
    STAMP(13);
    const u32x4 rsU = mkrs(ws + WS_UF, SZ_UF), rsO = mkrs(ws + WS_OF, SZ_OF), rsWo = mkrs(wo, D * D * 2);
    const unsigned vu0 = h * (D * 16) + (dt2 * 64 + n) * 16, vu1 = vu0 + 2 * D * 16, sou = w * 56 * (D * 16);
    const unsigned vof = h * (S * 16) + (st2 * 64 + n) * 16, soo = w * 14 * (S * 16);
    const unsigned vfl = (FL_O + (st2 >> 1) * 8 + 2 * w + (lane & 1)) * 128;
    unsigned vwo[4];
#pragma unroll
    for (int j = 0; j < 4; j++) vwo[j] = swz * (D * 2) + swc + j * (8 * D * 2 - 1024);
    const unsigned sowo = w * 112 * (D * 2) + dt2 * 128;
    bf16x8 fa[18][4];
    u32x2 fl;
    auto ldD = [&](auto tt, auto jj) {
      constexpr int t = tt.value, j = jj.value;
      if constexpr (t < 14) fa[t][j] = ldc<(j & 1) * 512>(rsU, (j >> 1) ? vu1 : vu0, sou + t * (4 * D * 16));
      else if constexpr (j < g2ld(t)) fa[t][j] = ldc<(j & 1) * 512>(rsO, vof, soo + (2 * (t - 14) + (j >> 1)) * (2 * S * 16));
    };
    auto dmaS = [&](auto tt, auto jj) {
      constexpr int t = tt.value, j = jj.value;
      if constexpr (t < 14) dma1<j>(rsW2, v2, so2 + t * (32 * S * 2), area + ((t + 6) % 10) * 4096);
      else if constexpr (j < g2dm(t)) dma1<j>(rsWo, vwo, sowo + (t - 14) * (32 * D * 2), area + (t - 14) * 4096);
    };
    unroll<0, G2R>([&](auto tt) { unroll<0, 4>([&](auto jj) { ldD(tt, jj); }); });
    ldsp pb[2], pw[2];
#pragma unroll
    for (int a = 0; a < 2; a++) {
      const unsigned c = q * 128 + (((4 * a + 2 * blk + (pp >> 1)) ^ ((q >> 1) << 2)) * 16) + (pp & 1) * 8;
      pb[a] = lds + w * AREA + h * 1024 + c;
      pw[a] = lds + w * AREA + h * 512 + c;
    }
    f32x16 acc[2][2] = {};
    unroll<0, 18>([&](auto tt) {
      constexpr int t = tt.value, nk = t < 17 ? 2 : 1;
      constexpr int left = g2cnt(t - 3) + g2cnt(t - 2) + g2cnt(t - 1);
      if constexpr (nk == 2) waitv<left>(fa[t][0], fa[t][1], fa[t][2], fa[t][3]); else waitv<left>(fa[t][0], fa[t][1]);
      if constexpr (t == G2POLL + G2R) {
        tie(fl);
        if (!match(fl, ep)) poll(rsF, vfl, ep);
      }
      s16x4 lo[2][2], hi[2][2];
#pragma unroll
      for (int ks = 0; ks < nk; ks++)
#pragma unroll
        for (int a = 0; a < 2; a++) {
          if (t < 14) {
            lo[ks][a] = vtr(pb[a] + ((t + 6) % 10) * 4096 + ks * 2048); hi[ks][a] = vtr(pb[a] + ((t + 6) % 10) * 4096 + ks * 2048 + 512);
          } else {
            const int j = 2 * (t - 14) + ks;
            if (j < 6) {
              const int u = j % 3, rl = 56 * (j / 3) + (u == 0 ? 0 : u == 1 ? 16 : 36), rh = 56 * (j / 3) + (u == 0 ? 8 : u == 1 ? 28 : 44);
              lo[ks][a] = vtr(pw[a] + rl * 128); hi[ks][a] = vtr(pw[a] + rh * 128);
            } else {
              lo[ks][a] = vtr(pw[a] + h * (52 * 128) + 24 * 128); hi[ks][a] = vtr(pw[a] + h * (52 * 128) + 52 * 128);
            }
          }
        }
      SB();
      unroll<0, 4 * nk>([&](auto mm) {
        constexpr int m = mm.value, ks = m >> 2, nh = (m >> 1) & 1, mh = m & 1;
        if constexpr (m < 4) ldD(std::integral_constant<int, t + G2R>(), mm);
        else if constexpr (t >= 1) dmaS(std::integral_constant<int, t + 9>(), std::integral_constant<int, m - 4>());
        if constexpr (t == G2POLL && m == 7) fl = ldc2(rsF, vfl);
        if constexpr (t < 14) acc[nh][mh] = MFMA(fa[t][ks * 2 + mh], cat(lo[ks][nh], hi[ks][nh]), acc[nh][mh]);
        else acc[nh][mh] = MFMA(cat(lo[ks][mh], hi[ks][mh]), fa[t][ks * 2 + nh], acc[nh][mh]);
        SB();
      });
    });
    STAMP(14);
#pragma unroll
    for (int nh = 0; nh < 2; nh++)
#pragma unroll
      for (int mh = 0; mh < 2; mh++)
#pragma unroll
        for (int g = 0; g < 4; g++)
          lst(lds + w * AREA + (32 * nh + n) * 256 + (((8 * mh + 2 * g + h) ^ (n & 15)) * 16),
              (f32x4){acc[nh][mh][4 * g], acc[nh][mh][4 * g + 1], acc[nh][mh][4 * g + 2], acc[nh][mh][4 * g + 3]});
    bar();
    const rsrc_t sto = mkst(out, S * D * 2);
#pragma unroll
    for (int u = 0; u < 2; u++) {
      const int r = 16 * w + 8 * u + (lane >> 3);
      const unsigned c0 = r * 256 + (((2 * (lane & 7)) ^ (r & 15)) * 16), c1 = c0 ^ 16;
      f32x4 a = lld<f32x4>(lds + c0), bq = lld<f32x4>(lds + c1);
#pragma unroll
      for (int s = 1; s < 4; s++) { a += lld<f32x4>(lds + s * AREA + c0); bq += lld<f32x4>(lds + s * AREA + c1); }
      const u32x4 xu = __builtin_bit_cast(u32x4, u ? xr1 : xr0);
      const float bv = blo(u ? bs1 : bs0);
      u32x4 res = {pk(a[0] + bv + blo(xu[0]), a[1] + bv + bhi(xu[0])), pk(a[2] + bv + blo(xu[1]), a[3] + bv + bhi(xu[1])),
                   pk(bq[0] + bv + blo(xu[2]), bq[1] + bv + bhi(xu[2])), pk(bq[2] + bv + blo(xu[3]), bq[3] + bv + bhi(xu[3]))};
      __builtin_amdgcn_raw_buffer_store_b128(res, sto, vo + u * (8 * D * 2), 0, 16);
    }
    if (blockIdx.x == 255 && tid == 0) __builtin_amdgcn_raw_buffer_store_b64((u32x2){ep.lo, ep.hi}, mkst(ws, WS_FLAG_BYTES), 0, 0, 16);
  }
}

extern "C" void kernel_launch(void* const* d_in, const int* in_sizes, int n_in, void* d_out, int out_size, void* d_ws, size_t ws_size,
                              hipStream_t stream) {
  fused_kernel<<<256, 256, 0, stream>>>((const short*)d_in[0], (const short*)d_in[1], (const short*)d_in[2], (const short*)d_in[3],
                                        (const short*)d_in[5], (const short*)d_in[6], (const short*)d_in[7], (const short*)d_in[8],
                                        (short*)d_out, (char*)d_ws);
}
```

```cpp
#include <hip/hip_runtime.h>
#include <stdint.h>

typedef short bf16x8 __attribute__((ext_vector_type(8)));
typedef short s16x4 __attribute__((ext_vector_type(4)));
typedef float f32x2 __attribute__((ext_vector_type(2)));
typedef float f32x4 __attribute__((ext_vector_type(4)));
typedef float f32x16 __attribute__((ext_vector_type(16)));
typedef unsigned u32x2 __attribute__((ext_vector_type(2)));
typedef unsigned u32x4 __attribute__((ext_vector_type(4)));
typedef __bf16 b16x2 __attribute__((ext_vector_type(2)));
typedef __attribute__((address_space(3))) char* ldsp;
typedef __amdgpu_buffer_rsrc_t rsrc_t;

#define DEV __device__ __forceinline__
#define SB() __builtin_amdgcn_sched_barrier(0)
#define MFMA(a, b, c) __builtin_amdgcn_mfma_f32_32x32x16_bf16(a, b, c, 0, 0, 0)

constexpr int S = 2048, D = 448, F = 1792;
constexpr unsigned FL_P = 8, FL_R = FL_P + 128, FL_O = FL_R + 16, FL_U = FL_O + 128;
constexpr unsigned WS_FLAG_BYTES = 65536;
constexpr unsigned WS_ST = WS_FLAG_BYTES, SZ_ST = S * 16;
constexpr unsigned WS_UF = WS_ST + SZ_ST, SZ_UF = 224 * 448 * 16;
constexpr unsigned WS_OF = WS_UF + SZ_UF, SZ_OF = 56 * 2048 * 16;
constexpr unsigned WS_KVP = WS_OF + SZ_OF, SZ_KVP = 16 * 16 * 2 * 4096;
constexpr unsigned WS_KVF = WS_KVP + SZ_KVP, SZ_KVF = 16 * 4 * 1024;
constexpr unsigned WS_END = WS_KVF + SZ_KVF;
constexpr unsigned AREA = 40960, L_STAT = 36864, SLOT = 12288;
constexpr unsigned L_WK = 0, L_WV = 50176, L_WQ = 100352, L_WPHI = 150528, L_PHI = 0, L_VX = 32768;
constexpr unsigned OOB = 0x40000000u;
constexpr int SPIN = 1 << 18;

DEV u32x4 mkrs(const void* p, unsigned bytes) {
  unsigned long long a = (unsigned long long)p;
  u32x4 r = {(unsigned)a, (unsigned)(a >> 32), bytes, 0x00020000u};
  return r;
}
DEV rsrc_t mkst(void* p, unsigned bytes) { return __builtin_amdgcn_make_buffer_rsrc(p, 0, bytes, 0x00020000); }
DEV void stc(rsrc_t rs, unsigned voff, u32x4 v) { __builtin_amdgcn_raw_buffer_store_b128(v, rs, voff, 0, 16); }
template <int OFF> DEV bf16x8 ldp(u32x4 rs, unsigned voff, unsigned soff) {
  bf16x8 r;
  asm volatile("buffer_load_dwordx4 %0, %2, %1, %3 offen offset:%4" : "=v"(r) : "s"(rs), "v"(voff), "s"(soff), "n"(OFF) : "memory");
  return r;
}
template <int OFF> DEV bf16x8 ldc(u32x4 rs, unsigned voff, unsigned soff) {
  bf16x8 r;
  asm volatile("buffer_load_dwordx4 %0, %2, %1, %3 offen offset:%4 sc1" : "=v"(r) : "s"(rs), "v"(voff), "s"(soff), "n"(OFF) : "memory");
  return r;
}
template <int OFF> DEV u32x2 ldp2(u32x4 rs, unsigned voff, unsigned soff) {
  u32x2 r;
  asm volatile("buffer_load_dwordx2 %0, %2, %1, %3 offen offset:%4" : "=v"(r) : "s"(rs), "v"(voff), "s"(soff), "n"(OFF) : "memory");
  return r;
}
DEV u32x2 ldc2(u32x4 rs, unsigned voff) {
  u32x2 r;
  asm volatile("buffer_load_dwordx2 %0, %2, %1, 0 offen sc1" : "=v"(r) : "s"(rs), "v"(voff) : "memory");
  return r;
}
template <int OFF> DEV unsigned ldu16(u32x4 rs, unsigned voff, unsigned soff) {
  unsigned r;
  asm volatile("buffer_load_ushort %0, %2, %1, %3 offen offset:%4" : "=v"(r) : "s"(rs), "v"(voff), "s"(soff), "n"(OFF) : "memory");
  return r;
}
DEV void setm0(unsigned a) { asm volatile("s_mov_b32 m0, %0\n\ts_nop 0" ::"s"(a) : "memory"); }
template <int OFF> DEV void dma(u32x4 rs, unsigned voff, unsigned soff) {
  asm volatile("buffer_load_dwordx4 %1, %0, %2 offen offset:%3 lds" ::"s"(rs), "v"(voff), "s"(soff), "n"(OFF) : "memory");
}
DEV void dma4(u32x4 rs, const unsigned (&v)[4], unsigned soff, unsigned m0) {
  setm0(m0); dma<0>(rs, v[0], soff); dma<1024>(rs, v[1], soff); dma<2048>(rs, v[2], soff); dma<3072>(rs, v[3], soff);
}
template <int J> DEV void dma1(u32x4 rs, const unsigned (&v)[4], unsigned soff, unsigned m0) {
  if (J == 0) setm0(m0);
  dma<J * 1024>(rs, v[J], soff);
}
template <int N> DEV void waitv() { asm volatile("s_waitcnt vmcnt(%0)" ::"n"(N) : "memory"); }
template <int N, class T> DEV void waitv(T& a) { asm volatile("s_waitcnt vmcnt(%1)" : "+v"(a) : "n"(N) : "memory"); }
template <int N, class T> DEV void waitv(T& a, T& b) { asm volatile("s_waitcnt vmcnt(%2)" : "+v"(a), "+v"(b) : "n"(N) : "memory"); }
template <int N, class T> DEV void waitv(T& a, T& b, T& c, T& d) {
  asm volatile("s_waitcnt vmcnt(%4)" : "+v"(a), "+v"(b), "+v"(c), "+v"(d) : "n"(N) : "memory");
}
template <class T> DEV void tie(T& a) { asm volatile("" : "+v"(a)::"memory"); }
DEV void bar() { asm volatile("s_waitcnt lgkmcnt(0)\n\ts_barrier" ::: "memory"); }
DEV void barv() { asm volatile("s_waitcnt vmcnt(0) lgkmcnt(0)\n\ts_barrier" ::: "memory"); }
DEV s16x4 vtr(ldsp p) { return __builtin_amdgcn_ds_read_tr16_b64_v4i16((__attribute__((address_space(3))) s16x4*)p); }
DEV bf16x8 cat(s16x4 a, s16x4 b) { return __builtin_shufflevector(a, b, 0, 1, 2, 3, 4, 5, 6, 7); }
DEV unsigned pk(float a, float b) {
  f32x2 v = {a, b};
  return __builtin_bit_cast(unsigned, __builtin_convertvector(v, b16x2));
}
DEV float blo(unsigned u) { return __builtin_bit_cast(float, u << 16); }
DEV float bhi(unsigned u) { return __builtin_bit_cast(float, u & 0xffff0000u); }
DEV void swap32(float& a, float& b) {
  auto r = __builtin_amdgcn_permlane32_swap(__builtin_bit_cast(unsigned, a), __builtin_bit_cast(unsigned, b), false, false);
  const unsigned r0 = r[0], r1 = r[1];
  a = __builtin_bit_cast(float, r0); b = __builtin_bit_cast(float, r1);
}
DEV void swap16(float& a, float& b) {
  auto r = __builtin_amdgcn_permlane16_swap(__builtin_bit_cast(unsigned, a), __builtin_bit_cast(unsigned, b), false, false);
  const unsigned r0 = r[0], r1 = r[1];
  a = __builtin_bit_cast(float, r0); b = __builtin_bit_cast(float, r1);
}
template <int C> DEV float ror(float z) {
  return __builtin_bit_cast(float, __builtin_amdgcn_update_dpp(0, __builtin_bit_cast(int, z), C, 0xf, 0xf, true));
}
DEV float rowsum(float z) { z += ror<0x128>(z); z += ror<0x124>(z); z += ror<0x122>(z); z += ror<0x121>(z); return z; }
DEV float rdl(float z, int l) { return __builtin_bit_cast(float, __builtin_amdgcn_readlane(__builtin_bit_cast(int, z), l)); }
template <class T> DEV void lst(ldsp p, T v) { *(__attribute__((address_space(3))) T*)p = v; }
template <class T> DEV T lld(ldsp p) { return *(__attribute__((address_space(3))) T*)p; }

struct Epoch { unsigned lo, hi; };
DEV void publish(char* ws, unsigned flag, Epoch e) {
  barv();
  if (threadIdx.x == 0) __builtin_amdgcn_raw_buffer_store_b64((u32x2){e.lo, e.hi}, mkst(ws, WS_FLAG_BYTES), flag * 128, 0, 16);
}
DEV bool match(u32x2 f, Epoch e) { return __all(f.x == e.lo && f.y == e.hi); }
DEV void poll(u32x4 rsF, unsigned voff, Epoch e) {
#pragma unroll 1
  for (int it = 0; it < SPIN; ++it) {
    u32x2 f = ldc2(rsF, voff);
    waitv<0>(f);
    if (match(f, e)) break;
    __builtin_amdgcn_s_sleep(1);
  }
}

DEV void acc_stats(bf16x8 v, float& s, float& q) {
  const u32x4 u = __builtin_bit_cast(u32x4, v);
#pragma unroll
  for (int e = 0; e < 4; e++) {
    const unsigned t = u[e];
    const b16x2 p = __builtin_bit_cast(b16x2, t);
    s = __builtin_amdgcn_fdot2_f32_bf16(p, __builtin_bit_cast(b16x2, 0x3f803f80u), s, false);
    q = __builtin_amdgcn_fdot2_f32_bf16(p, p, q, false);
  }
}
DEV void affine4(unsigned& u, unsigned& v, float a0, float c0, float a1, float c1, float a2, float c2, float a3, float c3) {
  float t0, t1, t2, t3;
  unsigned r0, r1;
  asm("v_lshlrev_b32 %2, 16, %6\n\tv_and_b32 %3, 0xffff0000, %6\n\tv_lshlrev_b32 %4, 16, %7\n\tv_and_b32 %5, 0xffff0000, %7\n\t"
      "v_fma_f32 %2, %2, %8, %9\n\tv_fma_f32 %3, %3, %10, %11\n\tv_fma_f32 %4, %4, %12, %13\n\tv_fma_f32 %5, %5, %14, %15\n\t"
      "v_cvt_pk_bf16_f32 %0, %2, %3\n\tv_cvt_pk_bf16_f32 %1, %4, %5"
      : "=v"(r0), "=v"(r1), "=&v"(t0), "=&v"(t1), "=&v"(t2), "=&v"(t3)
      : "v"(u), "v"(v), "v"(a0), "v"(c0), "v"(a1), "v"(c1), "v"(a2), "v"(c2), "v"(a3), "v"(c3));
  u = r0; v = r1;
}
DEV void touch(unsigned& sink, u32x4 rs, unsigned voff, unsigned soff) {
  asm volatile("buffer_load_dword %0, %2, %1, %3 offen" : "+v"(sink) : "s"(rs), "v"(voff), "s"(soff) : "memory");
}
DEV void touchl(u32x4 rs, unsigned voff, unsigned soff) {
  asm volatile("buffer_load_dword %1, %0, %2 offen lds" ::"s"(rs), "v"(voff), "s"(soff) : "memory");
}
template <int OFF> DEV void dma56(u32x4 rs, unsigned voff, unsigned soff) {
  asm volatile("s_mov_b32 exec_hi, 0xffffff\n\tbuffer_load_dwordx4 %1, %0, %2 offen offset:%3 lds\n\ts_mov_b32 exec_hi, -1" ::"s"(rs), "v"(voff), "s"(soff), "n"(OFF) : "memory");
}
template <int N> using IC = std::integral_constant<int, N>;

DEV void features(const f32x16& p, bool h, unsigned (&ph)[2][8]) {
  constexpr float L = 1.4426950408889634f;
  float mx = fmaxf(p[0], p[1]), mn = fminf(p[0], p[1]);
#pragma unroll
  for (int r = 2; r < 12; r += 2) { mx = fmaxf(fmaxf(mx, p[r]), p[r + 1]); mn = fminf(fminf(mn, p[r]), p[r + 1]); }
  float mx2 = fmaxf(fmaxf(fmaxf(fmaxf(mx, p[12]), p[13]), p[14]), p[15]);
  float mn2 = fminf(fminf(fminf(fminf(mn, p[12]), p[13]), p[14]), p[15]);
  mx = h ? mx : mx2; mn = h ? -mn : -mn2;
  swap32(mx, mn);
  float c0 = -L * fmaxf(mx, mn), c1 = c0;
  swap32(c0, c1);
  const float ninf = -__builtin_inff();
  const float d0 = h ? ninf : c0, d1 = h ? ninf : c1;
  float e[2][16], s0 = 0.f, s1 = 0.f;
#pragma unroll
  for (int r = 0; r < 16; r++) {
    e[0][r] = __builtin_amdgcn_exp2f(fmaf(p[r], L, r < 12 ? c0 : d0));
    e[1][r] = __builtin_amdgcn_exp2f(fmaf(p[r], -L, r < 12 ? c1 : d1));
    s0 += e[0][r]; s1 += e[1][r];
  }
  swap32(s0, s1);
  float i0 = __builtin_amdgcn_rcpf(s0 + s1), i1 = i0;
  swap32(i0, i1);
#pragma unroll
  for (int r = 0; r < 8; r++) { ph[0][r] = pk(e[0][2 * r] * i0, e[0][2 * r + 1] * i0); ph[1][r] = pk(e[1][2 * r] * i1, e[1][2 * r + 1] * i1); }
}
DEV bf16x8 pack8(const f32x16& a, int o) {
  u32x4 u = {pk(a[o], a[o + 1]), pk(a[o + 2], a[o + 3]), pk(a[o + 4], a[o + 5]), pk(a[o + 6], a[o + 7])};
  return __builtin_bit_cast(bf16x8, u);
}
DEV float gelu(float a) {
  constexpr float c1 = -2.302208198f, c2 = c1 * 0.044715f;
  return a * __builtin_amdgcn_rcpf(1.f + __builtin_amdgcn_exp2f(a * fmaf(a * a, c2, c1)));
}

#define STAMP(n)

template <int T, int N, class Fn> DEV void unroll(Fn&& f) {
  if constexpr (T < N) { f(std::integral_constant<int, T>()); unroll<T + 1, N>(f); }
}
constexpr int G2R = 4, G2POLL = 5;
constexpr int g2ld(int t) { return t < 17 ? 4 : (t == 17 ? 2 : 0); }
constexpr int g2dm(int t) { return t < 17 ? 4 : (t == 17 ? 2 : 0); }
constexpr int g2cnt(int s) { return s < 0 ? 4 : g2ld(s + G2R) + (s >= 1 ? g2dm(s + 9) : 0) + (s == G2POLL ? 1 : 0); }

__global__ __launch_bounds__(256, 1) void fused_kernel(const short* __restrict__ x, const short* __restrict__ wqkv, const short* __restrict__ wphi,
                                                       const short* __restrict__ wo, const short* __restrict__ w1, const short* __restrict__ b1,
                                                       const short* __restrict__ w2, const short* __restrict__ b2, short* __restrict__ out,
                                                       char* __restrict__ ws) {
  __shared__ __attribute__((aligned(16))) char lds_[163840];
  const ldsp lds = (ldsp)lds_;
  const unsigned lds0 = (unsigned)(uintptr_t)lds;
  const int tid = threadIdx.x, lane = tid & 63, w = __builtin_amdgcn_readfirstlane(tid >> 6);
  asm volatile("s_nop 4" ::"s"(w), "s"(x), "s"(wqkv), "s"(wphi), "s"(wo), "s"(w1), "s"(b1), "s"(w2), "s"(b2), "s"(out), "s"(ws) : "memory");
  const int xc = blockIdx.x & 7, i = blockIdx.x >> 3;
  const int h = lane >> 5, n = lane & 31, q = (lane & 15) >> 2, pp = lane & 3, blk = (lane >> 4) & 1;
  const u32x4 rsF = mkrs(ws, WS_FLAG_BYTES), rsX = mkrs(x, S * D * 2), rsW2 = mkrs(w2, F * S * 2);
  const unsigned area = lds0 + w * AREA;
  const int tile2 = 28 * (xc & 1) + i - 4, dt2 = tile2 >> 3, st2 = 8 * (xc >> 1) + (tile2 & 7);
  const unsigned swz = (lane >> 3), swc = ((lane & 7) ^ (((lane >> 4) & 1) << 2)) * 16;
  unsigned v2[4];
#pragma unroll
  for (int j = 0; j < 4; j++) v2[j] = swz * (S * 2) + swc + j * (8 * S * 2 - 1024);
  const unsigned so2 = (w * 448) * (S * 2) + st2 * 128;
  auto pre2 = [&](int t0, int t1, int slot0) {
#pragma unroll 1
    for (int t = t0; t < t1; t++) dma4(rsW2, v2, so2 + t * (32 * S * 2), area + (slot0 + t - t0) * 4096);
  };

  const bool isA = i >= 16;
  const int T = i - 16, pr = xc;
  const u32x4 rsW1 = mkrs(w1, S * F * 2);
  const unsigned vt1 = xc < 7 ? (lane >> 2) * (F * 2) + (lane & 3) * 128 : OOB;
  auto so_t1 = [&](int stage, int wq, int r0) { return (512 * wq + ((64 * xc + 32 * stage + r0) & 511)) * (F * 2) + xc * 512; };
  Epoch ep;
  auto setep = [&](u32x2 gen) {
    const unsigned long long g = (((unsigned long long)__builtin_amdgcn_readfirstlane(gen.y) << 32) | (unsigned)__builtin_amdgcn_readfirstlane(gen.x)) + 1;
    ep.lo = (unsigned)g; ep.hi = (unsigned)(g >> 32);
  };

  if (!isA) {
    const int row0 = 512 * (i >> 2) + 64 * xc + 16 * (i & 3) + 4 * w;
    const bool isG = xc < 7 && i < 14;
    const int ft = 2 * xc + (i >= 7), dt = i >= 7 ? i - 7 : i;
    bf16x8 xv[4];
    u32x2 gen, bb[4];
    {
      const unsigned vo = lane < 56 ? lane * 16 : OOB, so = row0 * (D * 2);
      xv[0] = ldp<0>(rsX, vo, so); xv[1] = ldp<896>(rsX, vo, so); xv[2] = ldp<1792>(rsX, vo, so); xv[3] = ldp<2688>(rsX, vo, so);
      gen = ldc2(rsF, 0);
      setm0(area + L_STAT);
      touchl(rsW1, lane < 32 ? vt1 : OOB, so_t1(i >> 2, i & 3, 8 * w));
      const u32x4 rsB = mkrs(b1, F * 2);
      const unsigned sb = (ft * 128 + w * 32) * 2;
      bb[0] = ldp2<0>(rsB, h * 8, sb); bb[1] = ldp2<16>(rsB, h * 8, sb); bb[2] = ldp2<32>(rsB, h * 8, sb); bb[3] = ldp2<48>(rsB, h * 8, sb);
    }
    unsigned v1[2][4], vx[2][2];
#pragma unroll
    for (int ks = 0; ks < 2; ks++) {
#pragma unroll
      for (int j = 0; j < 4; j++) v1[ks][j] = (16 * ks + 8 * (j & 1) + swz) * (F * 2) + (j >> 1) * 128 + swc - j * 1024;
#pragma unroll
      for (int j = 0; j < 2; j++) vx[ks][j] = (16 * ks + 8 * j + swz) * (D * 2) + swc - j * 1024;
    }
    waitv<5>(xv[0], xv[1], xv[2], xv[3]);
    tie(gen);
    STAMP(0);
    setep(gen);
    const unsigned tagB = ep.lo ^ ep.hi;
    {
      float s[4] = {}, z[4] = {};
#pragma unroll
      for (int r = 0; r < 4; r++) acc_stats(xv[r], s[r], z[r]);
      swap32(s[0], s[1]); swap32(s[2], s[3]); swap32(z[0], z[1]); swap32(z[2], z[3]);
      float sa = s[0] + s[1], sb = s[2] + s[3], za = z[0] + z[1], zb = z[2] + z[3];
      swap16(sa, sb); swap16(za, zb);
      const float mu = rowsum(sa + sb) * (1.f / D), ms = rowsum(za + zb) * (1.f / D);
      const float var = fmaxf(ms - mu * mu, 0.f);
      float a = __builtin_amdgcn_rsqf(var + 1e-5f);
      a *= __builtin_amdgcn_rsqf(var * a * a + 1e-5f);
      const float c = -mu * a;
      stc(mkst(ws + WS_ST, SZ_ST), (lane & 15) ? OOB : (row0 + h + 2 * blk) * 16,
          (u32x4){__builtin_bit_cast(unsigned, a), ep.lo, __builtin_bit_cast(unsigned, c), tagB});
    }
    STAMP(1);

    if (isG) {
      const unsigned b1s = (w * 512) * (F * 2) + ft * 256, bxs = (w * 512) * (D * 2) + dt * 128;
      auto dgrp = [&](auto kk, auto jj, unsigned s1, unsigned sx, unsigned m0) {
        constexpr int ks = kk.value, j = jj.value;
        if constexpr (j == 0) setm0(m0 + ks * 6144);
        if constexpr (j == 4) setm0(m0 + ks * 6144 + 4096);
        if constexpr (j < 4) dma<j * 1024>(rsW1, v1[ks][j], s1);
        else dma<(j - 4) * 1024>(rsX, vx[ks][j - 4], sx);
      };
      auto fill = [&](auto ss) {
        constexpr int t = ss.value >> 1;
        const unsigned tk = (64 * xc + 32 * t) & 511;
        unroll<0, 6>([&](auto jj) { dgrp(IC<(ss.value & 1)>(), jj, b1s + tk * (F * 2), bxs + tk * (D * 2), area + ((t + 1) % 3) * SLOT); });
      };
      const u32x4 rsS = mkrs(ws + WS_ST, SZ_ST);
      auto gather = [&](auto first) {
        bf16x8 g[8];
#pragma unroll
        for (int j = 0; j < 8; j++) g[j] = ldc<0>(rsS, (lane * 16 + (j + xc) * 1024) & 8191, w * 8192);
        if constexpr (first.value) { unroll<3, 6>(fill); waitv<18>(); } else waitv<0>();
        bool ok = true;
#pragma unroll
        for (int j = 0; j < 8; j++) {
          tie(g[j]);
          const u32x4 u = __builtin_bit_cast(u32x4, g[j]);
          ok &= u[1] == ep.lo && u[3] == tagB;
          lst(lds + w * AREA + L_STAT + j * 512 + lane * 8, (u32x2){u[0], u[2]});
        }
        return (bool)__all(ok);
      };
      unroll<0, 3>(fill);
      STAMP(2);
      if (!gather(IC<1>())) {
#pragma unroll 1
        for (int it = 0; it < SPIN; ++it)
          if (gather(IC<0>())) break;
      }
      STAMP(3);
      tie(bb[0]); tie(bb[1]); tie(bb[2]); tie(bb[3]);
      f32x16 acc[4][2];
#pragma unroll
      for (int g = 0; g < 4; g++) {
        acc[0][0][4 * g] = blo(bb[g].x); acc[0][0][4 * g + 1] = bhi(bb[g].x); acc[0][0][4 * g + 2] = blo(bb[g].y); acc[0][0][4 * g + 3] = bhi(bb[g].y);
      }
      acc[0][1] = acc[0][0];
#pragma unroll
      for (int a = 1; a < 4; a++) { acc[a][0] = (f32x16)(0.f); acc[a][1] = (f32x16)(0.f); }
      unsigned pa[4], px[2], ps = area + L_STAT + h * 64;
      {
        const unsigned rowb = area + SLOT + (8 * h + q) * 128 + (pp & 1) * 8, ch = 2 * blk + (pp >> 1), fl = (q >> 1) << 2;
#pragma unroll
        for (int a = 0; a < 4; a++) pa[a] = rowb + ((a ^ w) >> 1) * 2048 + ((4 * ((a ^ w) & 1) + ch) ^ fl) * 16;
        px[0] = rowb + 4096 + (ch ^ fl) * 16; px[1] = rowb + 4096 + ((4 + ch) ^ fl) * 16;
      }
      s16x4 wl[2][4], wh[2][4];
      u32x4 hf[2][2];
      u32x2 xl[2], xh[2];
      f32x4 st[4];
      auto rdX = [&](auto nn, auto dd) {
        constexpr int o = nn.value * 6144, d = dd.value;
        xl[d] = __builtin_bit_cast(u32x2, vtr((ldsp)(uintptr_t)px[d] + o)); xh[d] = __builtin_bit_cast(u32x2, vtr((ldsp)(uintptr_t)px[d] + o + 512));
      };
      auto rdS = [&](auto nn, auto jj) { st[jj.value] = lld<f32x4>((ldsp)(uintptr_t)ps + nn.value * 128 + jj.value * 16); };
      auto rdW = [&](auto nn, auto aa) {
        constexpr int nx = nn.value, a = aa.value;
        wl[nx][a] = vtr((ldsp)(uintptr_t)pa[a] + nx * 6144); wh[nx][a] = vtr((ldsp)(uintptr_t)pa[a] + nx * 6144 + 512);
      };
      auto xf = [&](auto nn, auto dd, auto ee) {
        constexpr int nx = nn.value, d = dd.value, e = 2 * ee.value;
        unsigned u = e ? xh[d][0] : xl[d][0], v = e ? xh[d][1] : xl[d][1];
        affine4(u, v, st[e][0], st[e][1], st[e][2], st[e][3], st[e + 1][0], st[e + 1][1], st[e + 1][2], st[e + 1][3]);
        hf[nx][d][e] = u; hf[nx][d][e + 1] = v;
      };
      auto step = [&](auto kk, auto wn, auto last, auto&& gm) {
        constexpr int ks = kk.value;
        constexpr bool more = !last.value;
        using NX = IC<ks ^ 1>;
        auto mm = [&](auto m_) {
          constexpr int a = m_.value >> 1, dh = m_.value & 1;
          acc[a][dh] = MFMA(cat(wl[ks][a], wh[ks][a]), __builtin_bit_cast(bf16x8, hf[ks][dh]), acc[a][dh]);
          SB();
        };
        if constexpr (more) {
          waitv<wn.value>();
          rdX(NX(), IC<0>()); rdS(NX(), IC<0>()); rdS(NX(), IC<1>()); rdS(NX(), IC<2>()); rdS(NX(), IC<3>()); rdX(NX(), IC<1>());
          SB();
        }
        mm(IC<0>());
        gm(IC<0>());
        if constexpr (more) { rdW(NX(), IC<0>()); rdW(NX(), IC<1>()); SB(); }
        mm(IC<1>());
        if constexpr (more) { rdW(NX(), IC<2>()); rdW(NX(), IC<3>()); SB(); }
        gm(IC<1>());
        mm(IC<2>());
        if constexpr (more) { xf(NX(), IC<0>(), IC<0>()); SB(); }
        gm(IC<2>());
        mm(IC<3>());
        if constexpr (more) { xf(NX(), IC<0>(), IC<1>()); SB(); }
        mm(IC<4>());
        gm(IC<3>());
        mm(IC<5>());
        if constexpr (more) { xf(NX(), IC<1>(), IC<0>()); SB(); }
        gm(IC<4>());
        mm(IC<6>());
        if constexpr (more) { xf(NX(), IC<1>(), IC<1>()); SB(); }
        mm(IC<7>());
        gm(IC<5>());
        asm volatile("s_waitcnt lgkmcnt(0)" ::: "memory");
        SB();
      };
      auto advance = [&](unsigned d) {
#pragma unroll
        for (int a = 0; a < 4; a++) { pa[a] += d; tie(pa[a]); }
        px[0] += d; tie(px[0]); px[1] += d; tie(px[1]); ps += 256; tie(ps);
        SB();
      };
      asm volatile("s_waitcnt lgkmcnt(0)" ::: "memory");
      rdX(IC<0>(), IC<0>()); rdX(IC<0>(), IC<1>());
      unroll<0, 4>([&](auto jj) { rdS(IC<0>(), jj); rdW(IC<0>(), jj); });
      unroll<0, 4>([&](auto jj) { xf(IC<0>(), IC<(jj.value >> 1)>(), IC<(jj.value & 1)>()); });
      asm volatile("s_waitcnt lgkmcnt(0)" ::: "memory");
      SB();
      unsigned sl = SLOT, tk = (64 * xc + 96) & 511;
#pragma unroll 1
      for (int t = 0; t < 13; t++) {
        const unsigned s1 = b1s + tk * (F * 2), sx = bxs + tk * (D * 2), m0 = area + sl, sn = sl == 2 * SLOT ? 0 : sl + SLOT;
        step(IC<0>(), IC<24>(), IC<0>(), [&](auto jj) { dgrp(IC<0>(), jj, s1, sx, m0); SB(); });
        advance(sn - sl);
        step(IC<1>(), IC<24>(), IC<0>(), [&](auto jj) { dgrp(IC<1>(), jj, s1, sx, m0); SB(); });
        sl = sn; tk = (tk + 32) & 511;
      }
      {
        unsigned vp[4];
#pragma unroll
        for (int j = 0; j < 4; j++) vp[j] = i >= 4 ? v2[j] : OOB;
        auto none = [&](auto) {};
        auto pw = [&](auto tt) {
          return [&](auto jj) {
            if constexpr (jj.value < 4) { dma1<jj.value>(rsW2, vp, so2 + decltype(tt)::value * (32 * S * 2), area + (6 + decltype(tt)::value) * 4096); SB(); }
          };
        };
        step(IC<0>(), IC<24>(), IC<0>(), none);
        advance(-2 * SLOT);
        step(IC<1>(), IC<18>(), IC<0>(), pw(IC<0>()));
        step(IC<0>(), IC<16>(), IC<0>(), pw(IC<1>()));
        advance(SLOT);
        step(IC<1>(), IC<14>(), IC<0>(), pw(IC<2>()));
        step(IC<0>(), IC<12>(), IC<0>(), none);
        step(IC<1>(), IC<0>(), IC<1>(), pw(IC<3>()));
      }
      STAMP(4);
      {
        const ldsp p = lds + w * AREA + lane * 16;
#pragma unroll
        for (int a = 1; a < 4; a++)
#pragma unroll
          for (int dh = 0; dh < 2; dh++)
#pragma unroll
            for (int g = 0; g < 4; g++)
              lst(p + (a - 1) * 8192 + dh * 4096 + g * 1024, (f32x4){acc[a][dh][4 * g], acc[a][dh][4 * g + 1], acc[a][dh][4 * g + 2], acc[a][dh][4 * g + 3]});
      }
      bar();
      STAMP(5);
      {
        const rsrc_t st = mkst(ws + WS_UF, SZ_UF);
        const unsigned vo = (ft * 16 + w * 4 + h) * (D * 16) + (dt * 64 + n) * 16;
        f32x4 v[2][3][4];
#pragma unroll
        for (int dh = 0; dh < 2; dh++)
#pragma unroll
          for (int a = 1; a < 4; a++)
#pragma unroll
            for (int g = 0; g < 4; g++) v[dh][a - 1][g] = lld<f32x4>(lds + (w ^ a) * AREA + lane * 16 + (a - 1) * 8192 + dh * 4096 + g * 1024);
        SB();
#pragma unroll
        for (int dh = 0; dh < 2; dh++) {
#pragma unroll
          for (int g = 0; g < 4; g += 2) {
            float r[8];
#pragma unroll
            for (int e = 0; e < 8; e++) {
              const int gg = g + (e >> 2), c = e & 3;
              r[e] = gelu((acc[0][dh][4 * gg + c] + v[dh][0][gg][c]) + (v[dh][1][gg][c] + v[dh][2][gg][c]));
            }
            auto r0 = __builtin_amdgcn_permlane32_swap(pk(r[0], r[1]), pk(r[4], r[5]), false, false);
            auto r1 = __builtin_amdgcn_permlane32_swap(pk(r[2], r[3]), pk(r[6], r[7]), false, false);
            stc(st, vo + g * (D * 16) + dh * 512, (u32x4){r0[0], r1[0], r0[1], r1[1]});
          }
        }
      }
      STAMP(6);
      publish(ws, FL_U + dt * 14 + ft, ep);
      STAMP(7);
      if (i >= 4) pre2(4, 10, 0);
    } else {
      if (xc < 7) {
#pragma unroll 1
        for (int m = 4; m < 8; m++) touchl(rsW1, vt1, so_t1(m, 2 * (i - 14) + (w >> 1), 16 * (w & 1)));
      }
      if (i >= 4) { pre2(0, 4, 6); pre2(4, 10, 0); }
      if (i >= 14) {
        const int head = 2 * xc + i - 14;
        poll(rsF, (FL_P + xc * 16 + (lane & 15)) * 128, ep);
        STAMP(3);
        const u32x4 rsP = mkrs(ws + WS_KVP, SZ_KVP);
        const unsigned so = head * 131072 + (w >> 1) * 4096 + (w & 1) * 2048;
        bf16x8 pv[16][2];
        unroll<0, 16>([&](auto tt) { constexpr int t = tt.value; pv[t][0] = ldc<0>(rsP, lane * 16, so + t * 8192); pv[t][1] = ldc<1024>(rsP, lane * 16, so + t * 8192); });
        f32x4 a0 = {0.f, 0.f, 0.f, 0.f}, a1 = a0;
        unroll<0, 16>([&](auto tt) {
          constexpr int t = tt.value;
          waitv<30 - 2 * t>(pv[t][0], pv[t][1]);
          a0 += __builtin_bit_cast(f32x4, pv[t][0]); a1 += __builtin_bit_cast(f32x4, pv[t][1]);
        });
        stc(mkst(ws + WS_KVF, SZ_KVF), head * 4096 + w * 1024 + lane * 16, (u32x4){pk(a0[0], a0[1]), pk(a0[2], a0[3]), pk(a1[0], a1[1]), pk(a1[2], a1[3])});
        STAMP(4);
        publish(ws, FL_R + head, ep);
        STAMP(5);
      }
    }
  } else {
    const unsigned ring = lds0 + L_WQ + w * 12544, sxa = (128 * T + 32 * w) * (D * 2);
    unsigned vxa[4];
#pragma unroll
    for (int j = 0; j < 4; j++) vxa[j] = (8 * j + swz) * (D * 2) + (((lane & 7) ^ ((4 * j + (lane >> 4)) & 7)) * 16) - j * 1024;
    unsigned sink = 0;
    u32x2 gen = ldc2(rsF, 0);
    dma4(rsX, vxa, sxa, ring);
    const u32x4 rsW = mkrs(wqkv, D * 3 * D * 2);
    {
      const unsigned r6 = (lane * 43) >> 8, c6 = lane - 6 * r6;
      touch(sink, rsW, lane < 42 ? r6 * 2688 + (c6 >> 1) * 896 + (c6 & 1) * 108 : OOB, (28 * T + 7 * w) * 2688 + pr * 112);
    }
    dma4(rsX, vxa, sxa + 128, ring + 4096);
    dma4(rsX, vxa, sxa + 256, ring + 8192);
    unsigned vw[4];
    {
      const unsigned r7 = (lane * 37) >> 8, src = 8 * ((r7 >> 1) & 1) + 4 * (r7 & 1) + (r7 >> 2);
#pragma unroll
      for (int j = 0; j < 4; j++) vw[j] = (src + 16 * (j >> 1) + 2 * (j & 1)) * 2688 + (lane - r7 * 7) * 16 - j * 896;
    }
    auto dmaW = [&](auto jj, unsigned col, unsigned base, int g0) {
      constexpr int j = jj.value;
      if constexpr ((j & 3) == 0) setm0(lds0 + base + (g0 + j) * 896);
      dma56<(j & 3) * 896>(rsW, vw[j & 3], col + pr * 112 + (g0 + (j & ~3)) * (8 * 2688));
    };
    bf16x8 xa[28];
    float sa = 0.f, sq = 0.f;
    {
      unsigned pr_[4];
#pragma unroll
      for (int k = 0; k < 4; k++) pr_[k] = ring + n * 128 + (((2 * k + h) ^ ((n >> 1) & 7)) * 16);
      unroll<0, 7>([&](auto cc) {
        constexpr int cb = cc.value;
        waitv<(cb == 0 ? 9 : cb < 5 ? 8 : cb == 5 ? 12 : 16)>();
        if (cb == 0) STAMP(0);
#pragma unroll
        for (int k = 0; k < 4; k++) xa[4 * cb + k] = lld<bf16x8>((ldsp)(uintptr_t)pr_[k] + (cb % 3) * 4096);
        asm volatile("s_waitcnt lgkmcnt(0)" : "+v"(xa[4 * cb]), "+v"(xa[4 * cb + 1]), "+v"(xa[4 * cb + 2]), "+v"(xa[4 * cb + 3])::"memory");
        if constexpr (cb < 4) dma4(rsX, vxa, sxa + (cb + 3) * 128, ring + (cb % 3) * 4096);
        if constexpr (cb == 4) unroll<0, 8>([&](auto jj) { dmaW(jj, 896, L_WK, 8 * w); });
        if constexpr (cb == 5) unroll<0, 8>([&](auto jj) { dmaW(jj, 1792, L_WV, 8 * w); });
        if constexpr (cb == 6) {
          setm0(lds0 + L_WPHI + w * 1024);
          dma<0>(mkrs(wphi + pr * 1568, 3136), tid * 16, 0);
          unroll<0, 6>([&](auto jj) { dmaW(jj, 896, L_WK, 32 + 6 * w); });
          unroll<0, 6>([&](auto jj) { dmaW(jj, 1792, L_WV, 32 + 6 * w); });
        }
        SB();
#pragma unroll
        for (int k = 0; k < 4; k++) acc_stats(xa[4 * cb + k], sa, sq);
        SB();
      });
    }
    STAMP(1);
    tie(gen);
    setep(gen);
    float la, lc;
    {
      float s2 = sa, q2 = sq;
      swap32(sa, s2); swap32(sq, q2);
      const float mu = (sa + s2) * (1.f / D), var = fmaxf((sq + q2) * (1.f / D) - mu * mu, 0.f);
      la = __builtin_amdgcn_rsqf(var + 1e-5f);
      lc = -mu * la;
    }
    auto norm = [&](auto kk, auto ee) {
      constexpr int ks = kk.value, e = 2 * ee.value;
      if constexpr (ks < 28) {
        u32x4 u = __builtin_bit_cast(u32x4, xa[ks]);
        unsigned t0 = u[e], t1 = u[e + 1];
        affine4(t0, t1, la, lc, la, lc, la, lc, la, lc);
        u[e] = t0; u[e + 1] = t1;
        xa[ks] = __builtin_bit_cast(bf16x8, u);
      }
    };
    norm(IC<0>(), IC<0>()); norm(IC<0>(), IC<1>());
    touch(sink, rsW1, vt1, so_t1(8 + (T >> 1), (2 * T + (w >> 1)) & 3, 16 * (w & 1)));
    waitv<13>();
    STAMP(2);
    bar();
    STAMP(3);
    bf16x8 wf[2][2];
    {
      const ldsp p = lds + L_WPHI + (4 * h + q) * 56 + 32 * blk + 8 * pp;
#pragma unroll
      for (int hd = 0; hd < 2; hd++) {
        wf[hd][0] = cat(vtr(p + hd * 1568), vtr(p + hd * 1568 + 8 * 56));
        s16x4 z = vtr(p + hd * 1568 + 24 * 56);
        wf[hd][1] = cat(vtr(p + hd * 1568 + 16 * 56), h ? (s16x4){0, 0, 0, 0} : z);
      }
    }
    const unsigned vt2 = (lane >> 3) * (S * 2) + (lane & 7) * 128;
    auto touch2 = [&](int m) { touch(sink, rsW2, vt2, (448 * w + 128 * m + 8 * T) * (S * 2) + (xc >> 1) * 1024); };
    const unsigned wlane = (2 * h + 4 * q) * 112 + 32 * blk + 8 * pp;
    f32x16 kT[2] = {}, vv[2] = {};
    {
      const ldsp pk_ = lds + L_WK + wlane, pv_ = lds + L_WV + wlane;
      s16x4 fr[29][8];
      auto rd = [&](auto kk, auto cc) {
        constexpr int ks = kk.value, c = cc.value;
        if constexpr (ks < 28) fr[ks][c] = vtr(((c & 4) ? pv_ : pk_) + ks * 1792 + ((c >> 1) & 1) * 56 + (c & 1) * 112);
      };
      unroll<0, 8>([&](auto cc) { rd(IC<0>(), cc); });
      unroll<0, 28>([&](auto kk) {
        constexpr int ks = kk.value;
        if constexpr (ks == 15) { waitv<15>(); bar(); SB(); }
        unroll<0, 4>([&](auto mm) {
          constexpr int m = mm.value;
          rd(IC<ks + 1>(), IC<2 * m>());
          rd(IC<ks + 1>(), IC<2 * m + 1>());
          if constexpr (m & 1) norm(IC<ks + 1>(), IC<(m >> 1)>());
          if constexpr (m == 0 && ks < 14) dmaW(IC<ks>(), 0, L_WQ, 14 * w);
          if constexpr (m == 0 && ks == 18) touch2(0);
          const bf16x8 wv = cat(fr[ks][2 * m], fr[ks][2 * m + 1]);
          if constexpr (m < 2) kT[m] = MFMA(wv, xa[ks], kT[m]);
          else vv[m - 2] = MFMA(xa[ks], wv, vv[m - 2]);
          SB();
        });
      });
    }
    STAMP(5);
    touch2(1);
    {
      f32x16 p[2];
#pragma unroll
      for (int hd = 0; hd < 2; hd++) { p[hd] = MFMA(wf[hd][0], pack8(kT[hd], 0), (f32x16)(0.f)); p[hd] = MFMA(wf[hd][1], pack8(kT[hd], 8), p[hd]); }
      bar();
      {
        const bf16x8 ones = {0x3f80, 0x3f80, 0x3f80, 0x3f80, 0x3f80, 0x3f80, 0x3f80, 0x3f80};
        const ldsp pvx = lds + L_VX + w * 2048 + lane * 16;
#pragma unroll
        for (int hd = 0; hd < 2; hd++)
#pragma unroll
          for (int j = 0; j < 2; j++) { bf16x8 f = pack8(vv[hd], 8 * j); lst(pvx + hd * 8192 + j * 1024, n == 28 ? ones : f); }
      }
      const unsigned pb = (32 * w + n) * 64 + ((h ^ ((n >> 1) & 7)) * 8);
#pragma unroll
      for (int hd = 0; hd < 2; hd++) {
        unsigned ph[2][8];
        features(p[hd], h, ph);
#pragma unroll
        for (int sg = 0; sg < 2; sg++)
#pragma unroll
          for (int g = 0; g < 4; g++) lst(lds + L_PHI + (hd * 2 + sg) * 8192 + (pb ^ (g << 4)), (u32x2){ph[sg][2 * g], ph[sg][2 * g + 1]});
      }
    }
    STAMP(6);
    barv();
    {
      const unsigned sw = 2 * h + (q >> 1);
      const ldsp pl = lds + L_PHI + w * 8192 + (4 * h + q) * 64 + (((4 * blk + pp) ^ sw) * 8);
      const ldsp ph_ = lds + L_PHI + w * 8192 + (8 + 4 * h + q) * 64 + (((4 * blk + pp) ^ sw ^ 4) * 8);
      const ldsp pvx = lds + L_VX + (w >> 1) * 8192 + lane * 16;
      f32x16 kv = {};
#pragma unroll
      for (int jj = 0; jj < 8; jj++) kv = MFMA(cat(vtr(pl + jj * 1024), vtr(ph_ + jj * 1024)), lld<bf16x8>(pvx + jj * 1024), kv);
      const rsrc_t st = mkst(ws + WS_KVP, SZ_KVP);
      const unsigned vo = (2 * pr + (w >> 1)) * 131072 + T * 8192 + (w & 1) * 4096 + lane * 16;
#pragma unroll
      for (int g = 0; g < 4; g++) stc(st, vo + g * 1024, __builtin_bit_cast(u32x4, (f32x4){kv[4 * g], kv[4 * g + 1], kv[4 * g + 2], kv[4 * g + 3]}));
    }
    STAMP(7);
    f32x16 qT[2] = {};
    {
      const ldsp pq_ = lds + L_WQ + wlane;
      s16x4 fr[29][4];
      auto rd = [&](auto kk, auto cc) {
        constexpr int ks = kk.value, c = cc.value;
        if constexpr (ks < 28) fr[ks][c] = vtr(pq_ + ks * 1792 + (c >> 1) * 56 + (c & 1) * 112);
      };
      unroll<0, 4>([&](auto cc) { rd(std::integral_constant<int, 0>(), cc); });
      unroll<0, 28>([&](auto kk) {
        constexpr int ks = kk.value;
        if constexpr (ks == 14) { publish(ws, FL_P + pr * 16 + T, ep); STAMP(8); touch2(2); touch2(3); SB(); }
        unroll<0, 2>([&](auto mm) {
          constexpr int m = mm.value;
          rd(std::integral_constant<int, ks + 1>(), std::integral_constant<int, 2 * m>());
          rd(std::integral_constant<int, ks + 1>(), std::integral_constant<int, 2 * m + 1>());
          qT[m] = MFMA(cat(fr[ks][2 * m], fr[ks][2 * m + 1]), xa[ks], qT[m]);
          SB();
        });
      });
    }
    unsigned phq[2][2][8];
    {
      f32x16 p[2];
#pragma unroll
      for (int hd = 0; hd < 2; hd++) { p[hd] = MFMA(wf[hd][0], pack8(qT[hd], 0), (f32x16)(0.f)); p[hd] = MFMA(wf[hd][1], pack8(qT[hd], 8), p[hd]); }
      bar();
      STAMP(9);
      pre2(0, 4, 6);
      pre2(4, 10, 0);
      features(p[0], h, phq[0]);
      features(p[1], h, phq[1]);
    }
    STAMP(10);
    poll(rsF, (FL_R + 2 * pr + (lane & 1)) * 128, ep);
    tie(sink);
    STAMP(11);
    {
      const u32x4 rsK = mkrs(ws + WS_KVF, SZ_KVF);
      bf16x8 kf[2][4];
#pragma unroll
      for (int hd = 0; hd < 2; hd++) {
        const unsigned so = (2 * pr + hd) * 4096;
        kf[hd][0] = ldc<0>(rsK, lane * 16, so); kf[hd][1] = ldc<1024>(rsK, lane * 16, so);
        kf[hd][2] = ldc<2048>(rsK, lane * 16, so); kf[hd][3] = ldc<3072>(rsK, lane * 16, so);
      }
      f32x16 o[2] = {};
      waitv<4>(kf[0][0], kf[0][1], kf[0][2], kf[0][3]);
#pragma unroll
      for (int hd = 0; hd < 2; hd++) {
        if (hd) waitv<0>(kf[1][0], kf[1][1], kf[1][2], kf[1][3]);
#pragma unroll
        for (int c = 0; c < 4; c++) {
          const unsigned* s = &phq[hd][c >> 1][4 * (c & 1)];
          o[hd] = MFMA(kf[hd][c], __builtin_bit_cast(bf16x8, (u32x4){s[0], s[1], s[2], s[3]}), o[hd]);
        }
      }
      float da = o[0][12], db = o[1][12];
      swap32(da, db);
      float ia = __builtin_amdgcn_rcpf(db + 1e-6f), ib = ia;
      swap32(ia, ib);
      unsigned a[8], bq[8];
#pragma unroll
      for (int r = 0; r < 8; r++) { a[r] = pk(o[0][2 * r] * ia, o[0][2 * r + 1] * ia); bq[r] = pk(o[1][2 * r] * ib, o[1][2 * r + 1] * ib); }
      const rsrc_t st = mkst(ws + WS_OF, SZ_OF);
      const unsigned tk = (128 * T + 32 * w + n) * 16, jb = 14 * (pr >> 1);
      const unsigned vo = (jb + 6 * (pr & 1) + h) * (S * 16) + tk;
      stc(st, vo, (u32x4){a[0], a[1], a[2], a[3]});
      stc(st, vo + 2 * S * 16, (u32x4){a[4], a[5], bq[0], bq[1]});
      stc(st, vo + 4 * S * 16, (u32x4){bq[2], bq[3], bq[4], bq[5]});
      stc(st, h ? OOB : (jb + 12 + (pr & 1)) * (S * 16) + tk, (u32x4){a[6], a[7], bq[6], bq[7]});
    }
    publish(ws, FL_O + T * 8 + pr, ep);
    STAMP(12);
  }

  if (i >= 4) {
    const unsigned orow = st2 * 64 + 16 * w + (lane >> 3);
    const unsigned vo = orow * (D * 2) + dt2 * 128 + (lane & 7) * 16;
    bf16x8 xr0 = ldp<0>(rsX, vo, 0), xr1 = ldp<0>(rsX, vo, 8 * D * 2);
    const u32x4 rsB = mkrs(b2, S * 2);
    unsigned bs0 = ldu16<0>(rsB, orow * 2, 0), bs1 = ldu16<16>(rsB, orow * 2, 0);
    poll(rsF, (FL_U + dt2 * 14 + ((7 * w) >> 1) + (lane & 3)) * 128, ep);
    tie(xr0); tie(xr1); tie(bs0); tie(bs1);
    STAMP(13);
    const u32x4 rsU = mkrs(ws + WS_UF, SZ_UF), rsO = mkrs(ws + WS_OF, SZ_OF), rsWo = mkrs(wo, D * D * 2);
    const unsigned vu0 = h * (D * 16) + (dt2 * 64 + n) * 16, vu1 = vu0 + 2 * D * 16, sou = w * 56 * (D * 16);
    const unsigned vof = h * (S * 16) + (st2 * 64 + n) * 16, soo = w * 14 * (S * 16);
    const unsigned vfl = (FL_O + (st2 >> 1) * 8 + 2 * w + (lane & 1)) * 128;
    unsigned vwo[4];
#pragma unroll
    for (int j = 0; j < 4; j++) vwo[j] = swz * (D * 2) + swc + j * (8 * D * 2 - 1024);
    const unsigned sowo = w * 112 * (D * 2) + dt2 * 128;
    bf16x8 fa[18][4];
    u32x2 fl;
    auto ldD = [&](auto tt, auto jj) {
      constexpr int t = tt.value, j = jj.value;
      if constexpr (t < 14) fa[t][j] = ldc<(j & 1) * 512>(rsU, (j >> 1) ? vu1 : vu0, sou + t * (4 * D * 16));
      else if constexpr (j < g2ld(t)) fa[t][j] = ldc<(j & 1) * 512>(rsO, vof, soo + (2 * (t - 14) + (j >> 1)) * (2 * S * 16));
    };
    auto dmaS = [&](auto tt, auto jj) {
      constexpr int t = tt.value, j = jj.value;
      if constexpr (t < 14) dma1<j>(rsW2, v2, so2 + t * (32 * S * 2), area + ((t + 6) % 10) * 4096);
      else if constexpr (j < g2dm(t)) dma1<j>(rsWo, vwo, sowo + (t - 14) * (32 * D * 2), area + (t - 14) * 4096);
    };
    unroll<0, G2R>([&](auto tt) { unroll<0, 4>([&](auto jj) { ldD(tt, jj); }); });
    ldsp pb[2], pw[2];
#pragma unroll
    for (int a = 0; a < 2; a++) {
      const unsigned c = q * 128 + (((4 * a + 2 * blk + (pp >> 1)) ^ ((q >> 1) << 2)) * 16) + (pp & 1) * 8;
      pb[a] = lds + w * AREA + h * 1024 + c;
      pw[a] = lds + w * AREA + h * 512 + c;
    }
    f32x16 acc[2][2] = {};
    unroll<0, 18>([&](auto tt) {
      constexpr int t = tt.value, nk = t < 17 ? 2 : 1;
      constexpr int left = g2cnt(t - 3) + g2cnt(t - 2) + g2cnt(t - 1);
      if constexpr (nk == 2) waitv<left>(fa[t][0], fa[t][1], fa[t][2], fa[t][3]); else waitv<left>(fa[t][0], fa[t][1]);
      if constexpr (t == G2POLL + G2R) {
        tie(fl);
        if (!match(fl, ep)) poll(rsF, vfl, ep);
      }
      s16x4 lo[2][2], hi[2][2];
#pragma unroll
      for (int ks = 0; ks < nk; ks++)
#pragma unroll
        for (int a = 0; a < 2; a++) {
          if (t < 14) {
            lo[ks][a] = vtr(pb[a] + ((t + 6) % 10) * 4096 + ks * 2048); hi[ks][a] = vtr(pb[a] + ((t + 6) % 10) * 4096 + ks * 2048 + 512);
          } else {
            const int j = 2 * (t - 14) + ks;
            if (j < 6) {
              const int u = j % 3, rl = 56 * (j / 3) + (u == 0 ? 0 : u == 1 ? 16 : 36), rh = 56 * (j / 3) + (u == 0 ? 8 : u == 1 ? 28 : 44);
              lo[ks][a] = vtr(pw[a] + rl * 128); hi[ks][a] = vtr(pw[a] + rh * 128);
            } else {
              lo[ks][a] = vtr(pw[a] + h * (52 * 128) + 24 * 128); hi[ks][a] = vtr(pw[a] + h * (52 * 128) + 52 * 128);
            }
          }
        }
      SB();
      unroll<0, 4 * nk>([&](auto mm) {
        constexpr int m = mm.value, ks = m >> 2, nh = (m >> 1) & 1, mh = m & 1;
        if constexpr (m < 4) ldD(std::integral_constant<int, t + G2R>(), mm);
        else if constexpr (t >= 1) dmaS(std::integral_constant<int, t + 9>(), std::integral_constant<int, m - 4>());
        if constexpr (t == G2POLL && m == 7) fl = ldc2(rsF, vfl);
        if constexpr (t < 14) acc[nh][mh] = MFMA(fa[t][ks * 2 + mh], cat(lo[ks][nh], hi[ks][nh]), acc[nh][mh]);
        else acc[nh][mh] = MFMA(cat(lo[ks][mh], hi[ks][mh]), fa[t][ks * 2 + nh], acc[nh][mh]);
        SB();
      });
    });
    STAMP(14);
#pragma unroll
    for (int nh = 0; nh < 2; nh++)
#pragma unroll
      for (int mh = 0; mh < 2; mh++)
#pragma unroll
        for (int g = 0; g < 4; g++)
          lst(lds + w * AREA + (32 * nh + n) * 256 + (((8 * mh + 2 * g + h) ^ (n & 15)) * 16),
              (f32x4){acc[nh][mh][4 * g], acc[nh][mh][4 * g + 1], acc[nh][mh][4 * g + 2], acc[nh][mh][4 * g + 3]});
    bar();
    const rsrc_t sto = mkst(out, S * D * 2);
#pragma unroll
    for (int u = 0; u < 2; u++) {
      const int r = 16 * w + 8 * u + (lane >> 3);
      const unsigned c0 = r * 256 + (((2 * (lane & 7)) ^ (r & 15)) * 16), c1 = c0 ^ 16;
      f32x4 a = lld<f32x4>(lds + c0), bq = lld<f32x4>(lds + c1);
#pragma unroll
      for (int s = 1; s < 4; s++) { a += lld<f32x4>(lds + s * AREA + c0); bq += lld<f32x4>(lds + s * AREA + c1); }
      const u32x4 xu = __builtin_bit_cast(u32x4, u ? xr1 : xr0);
      const float bv = blo(u ? bs1 : bs0);
      u32x4 res = {pk(a[0] + bv + blo(xu[0]), a[1] + bv + bhi(xu[0])), pk(a[2] + bv + blo(xu[1]), a[3] + bv + bhi(xu[1])),
                   pk(bq[0] + bv + blo(xu[2]), bq[1] + bv + bhi(xu[2])), pk(bq[2] + bv + blo(xu[3]), bq[3] + bv + bhi(xu[3]))};
      __builtin_amdgcn_raw_buffer_store_b128(res, sto, vo + u * (8 * D * 2), 0, 16);
    }
    if (blockIdx.x == 255 && tid == 0) __builtin_amdgcn_raw_buffer_store_b64((u32x2){ep.lo, ep.hi}, mkst(ws, WS_FLAG_BYTES), 0, 0, 16);
  }
}

extern "C" void kernel_launch(void* const* d_in, const int* in_sizes, int n_in, void* d_out, int out_size, void* d_ws, size_t ws_size,
                              hipStream_t stream) {
  fused_kernel<<<256, 256, 0, stream>>>((const short*)d_in[0], (const short*)d_in[1], (const short*)d_in[2], (const short*)d_in[3],
                                        (const short*)d_in[5], (const short*)d_in[6], (const short*)d_in[7], (const short*)d_in[8],
                                        (short*)d_out, (char*)d_ws);
}
```

```cpp
#include <hip/hip_runtime.h>
#include <stdint.h>

typedef short bf16x8 __attribute__((ext_vector_type(8)));
typedef short s16x4 __attribute__((ext_vector_type(4)));
typedef float f32x2 __attribute__((ext_vector_type(2)));
typedef float f32x4 __attribute__((ext_vector_type(4)));
typedef float f32x16 __attribute__((ext_vector_type(16)));
typedef unsigned u32x2 __attribute__((ext_vector_type(2)));
typedef unsigned u32x4 __attribute__((ext_vector_type(4)));
typedef __bf16 b16x2 __attribute__((ext_vector_type(2)));
typedef __attribute__((address_space(3))) char* ldsp;
typedef __amdgpu_buffer_rsrc_t rsrc_t;

#define DEV __device__ __forceinline__
#define SB() __builtin_amdgcn_sched_barrier(0)
#define MFMA(a, b, c) __builtin_amdgcn_mfma_f32_32x32x16_bf16(a, b, c, 0, 0, 0)

constexpr int S = 2048, D = 448, F = 1792;
constexpr unsigned FL_P = 8, FL_R = FL_P + 128, FL_O = FL_R + 16, FL_U = FL_O + 128;
constexpr unsigned WS_FLAG_BYTES = 65536;
constexpr unsigned WS_ST = WS_FLAG_BYTES, SZ_ST = S * 16;
constexpr unsigned WS_UF = WS_ST + SZ_ST, SZ_UF = 224 * 448 * 16;
constexpr unsigned WS_OF = WS_UF + SZ_UF, SZ_OF = 56 * 2048 * 16;
constexpr unsigned WS_KVP = WS_OF + SZ_OF, SZ_KVP = 16 * 16 * 2 * 4096;
constexpr unsigned WS_KVF = WS_KVP + SZ_KVP, SZ_KVF = 16 * 4 * 1024;
constexpr unsigned WS_END = WS_KVF + SZ_KVF;
constexpr unsigned AREA = 40960, L_STAT = 36864, SLOT = 12288;
constexpr unsigned L_WK = 0, L_WV = 50176, L_WQ = 100352, L_WPHI = 150528, L_PHI = 0, L_VX = 32768;
constexpr unsigned OOB = 0x40000000u;
constexpr int SPIN = 1 << 18;

DEV u32x4 mkrs(const void* p, unsigned bytes) {
  unsigned long long a = (unsigned long long)p;
  u32x4 r = {(unsigned)a, (unsigned)(a >> 32), bytes, 0x00020000u};
  return r;
}
DEV rsrc_t mkst(void* p, unsigned bytes) { return __builtin_amdgcn_make_buffer_rsrc(p, 0, bytes, 0x00020000); }
DEV void stc(rsrc_t rs, unsigned voff, u32x4 v) { __builtin_amdgcn_raw_buffer_store_b128(v, rs, voff, 0, 16); }
template <int OFF> DEV bf16x8 ldp(u32x4 rs, unsigned voff, unsigned soff) {
  bf16x8 r;
  asm volatile("buffer_load_dwordx4 %0, %2, %1, %3 offen offset:%4" : "=v"(r) : "s"(rs), "v"(voff), "s"(soff), "n"(OFF) : "memory");
  return r;
}
template <int OFF> DEV bf16x8 ldc(u32x4 rs, unsigned voff, unsigned soff) {
  bf16x8 r;
  asm volatile("buffer_load_dwordx4 %0, %2, %1, %3 offen offset:%4 sc1" : "=v"(r) : "s"(rs), "v"(voff), "s"(soff), "n"(OFF) : "memory");
  return r;
}
template <int OFF> DEV u32x2 ldp2(u32x4 rs, unsigned voff, unsigned soff) {
  u32x2 r;
  asm volatile("buffer_load_dwordx2 %0, %2, %1, %3 offen offset:%4" : "=v"(r) : "s"(rs), "v"(voff), "s"(soff), "n"(OFF) : "memory");
  return r;
}
DEV u32x2 ldc2(u32x4 rs, unsigned voff) {
  u32x2 r;
  asm volatile("buffer_load_dwordx2 %0, %2, %1, 0 offen sc1" : "=v"(r) : "s"(rs), "v"(voff) : "memory");
  return r;
}
template <int OFF> DEV unsigned ldu16(u32x4 rs, unsigned voff, unsigned soff) {
  unsigned r;
  asm volatile("buffer_load_ushort %0, %2, %1, %3 offen offset:%4" : "=v"(r) : "s"(rs), "v"(voff), "s"(soff), "n"(OFF) : "memory");
  return r;
}
DEV void setm0(unsigned a) { asm volatile("s_mov_b32 m0, %0\n\ts_nop 0" ::"s"(a) : "memory"); }
DEV void setm0n(unsigned a) { asm volatile("s_mov_b32 m0, %0" ::"s"(a) : "memory"); }
template <int OFF> DEV void addm0(unsigned a) { asm volatile("s_add_u32 m0, %0, %1" ::"s"(a), "n"(OFF) : "memory", "scc"); }
template <int OFF> DEV void dma(u32x4 rs, unsigned voff, unsigned soff) {
  asm volatile("buffer_load_dwordx4 %1, %0, %2 offen offset:%3 lds" ::"s"(rs), "v"(voff), "s"(soff), "n"(OFF) : "memory");
}
DEV void dma4(u32x4 rs, const unsigned (&v)[4], unsigned soff, unsigned m0) {
  setm0(m0); dma<0>(rs, v[0], soff); dma<1024>(rs, v[1], soff); dma<2048>(rs, v[2], soff); dma<3072>(rs, v[3], soff);
}
template <int J> DEV void dma1(u32x4 rs, const unsigned (&v)[4], unsigned soff, unsigned m0) {
  if (J == 0) setm0(m0);
  dma<J * 1024>(rs, v[J], soff);
}
template <int N> DEV void waitv() { asm volatile("s_waitcnt vmcnt(%0)" ::"n"(N) : "memory"); }
template <int N, class T> DEV void waitv(T& a) { asm volatile("s_waitcnt vmcnt(%1)" : "+v"(a) : "n"(N) : "memory"); }
template <int N, class T> DEV void waitv(T& a, T& b) { asm volatile("s_waitcnt vmcnt(%2)" : "+v"(a), "+v"(b) : "n"(N) : "memory"); }
template <int N, class T> DEV void waitv(T& a, T& b, T& c, T& d) {
  asm volatile("s_waitcnt vmcnt(%4)" : "+v"(a), "+v"(b), "+v"(c), "+v"(d) : "n"(N) : "memory");
}
template <class T> DEV void tie(T& a) { asm volatile("" : "+v"(a)::"memory"); }
DEV void keep(f32x16 a) { asm volatile("" ::"v"(a)); }
DEV void bar() { asm volatile("s_waitcnt lgkmcnt(0)\n\ts_barrier" ::: "memory"); }
DEV void barv() { asm volatile("s_waitcnt vmcnt(0) lgkmcnt(0)\n\ts_barrier" ::: "memory"); }
DEV s16x4 vtr(ldsp p) { return __builtin_amdgcn_ds_read_tr16_b64_v4i16((__attribute__((address_space(3))) s16x4*)p); }
DEV bf16x8 cat(s16x4 a, s16x4 b) { return __builtin_shufflevector(a, b, 0, 1, 2, 3, 4, 5, 6, 7); }
DEV unsigned pk(float a, float b) {
  f32x2 v = {a, b};
  return __builtin_bit_cast(unsigned, __builtin_convertvector(v, b16x2));
}
DEV float blo(unsigned u) { return __builtin_bit_cast(float, u << 16); }
DEV float bhi(unsigned u) { return __builtin_bit_cast(float, u & 0xffff0000u); }
DEV void swap32(float& a, float& b) {
  auto r = __builtin_amdgcn_permlane32_swap(__builtin_bit_cast(unsigned, a), __builtin_bit_cast(unsigned, b), false, false);
  const unsigned r0 = r[0], r1 = r[1];
  a = __builtin_bit_cast(float, r0); b = __builtin_bit_cast(float, r1);
}
DEV void swap16(float& a, float& b) {
  auto r = __builtin_amdgcn_permlane16_swap(__builtin_bit_cast(unsigned, a), __builtin_bit_cast(unsigned, b), false, false);
  const unsigned r0 = r[0], r1 = r[1];
  a = __builtin_bit_cast(float, r0); b = __builtin_bit_cast(float, r1);
}
template <int C> DEV float ror(float z) {
  return __builtin_bit_cast(float, __builtin_amdgcn_update_dpp(0, __builtin_bit_cast(int, z), C, 0xf, 0xf, true));
}
DEV float rowsum(float z) { z += ror<0x128>(z); z += ror<0x124>(z); z += ror<0x122>(z); z += ror<0x121>(z); return z; }
DEV float rdl(float z, int l) { return __builtin_bit_cast(float, __builtin_amdgcn_readlane(__builtin_bit_cast(int, z), l)); }
template <class T> DEV void lst(ldsp p, T v) { *(__attribute__((address_space(3))) T*)p = v; }
template <class T> DEV T lld(ldsp p) { return *(__attribute__((address_space(3))) T*)p; }

struct Epoch { unsigned lo, hi; };
DEV void publish(char* ws, unsigned flag, Epoch e) {
  barv();
  if (threadIdx.x == 0) __builtin_amdgcn_raw_buffer_store_b64((u32x2){e.lo, e.hi}, mkst(ws, WS_FLAG_BYTES), flag * 128, 0, 16);
}
DEV bool match(u32x2 f, Epoch e) { return __all(f.x == e.lo && f.y == e.hi); }
DEV void poll(u32x4 rsF, unsigned voff, Epoch e) {
#pragma unroll 1
  for (int it = 0; it < SPIN; ++it) {
    u32x2 f = ldc2(rsF, voff);
    waitv<0>(f);
    if (match(f, e)) break;
    __builtin_amdgcn_s_sleep(1);
  }
}

DEV void acc_stats(bf16x8 v, float& s, float& q) {
  const u32x4 u = __builtin_bit_cast(u32x4, v);
#pragma unroll
  for (int e = 0; e < 4; e++) {
    const unsigned t = u[e];
    const b16x2 p = __builtin_bit_cast(b16x2, t);
    s = __builtin_amdgcn_fdot2_f32_bf16(p, __builtin_bit_cast(b16x2, 0x3f803f80u), s, false);
    q = __builtin_amdgcn_fdot2_f32_bf16(p, p, q, false);
  }
}
DEV unsigned affine2(unsigned u, float a, float c) {
  float t0, t1;
  unsigned r;
  asm("v_lshlrev_b32 %1, 16, %3\n\tv_and_b32 %2, 0xffff0000, %3\n\tv_fma_f32 %1, %1, %4, %5\n\tv_fma_f32 %2, %2, %4, %5\n\tv_cvt_pk_bf16_f32 %0, %1, %2"
      : "=v"(r), "=&v"(t0), "=&v"(t1)
      : "v"(u), "v"(a), "v"(c));
  return r;
}
DEV f32x16 mfmav(u32x4 a, bf16x8 b, f32x16 c) {
  f32x16 r;
  asm volatile("v_mfma_f32_32x32x16_bf16 %0, %1, %2, %3" : "=&v"(r) : "v"(a), "v"(b), "v"(c));
  return r;
}
DEV void touch(unsigned& sink, u32x4 rs, unsigned voff, unsigned soff) {
  asm volatile("buffer_load_dword %0, %2, %1, %3 offen" : "+v"(sink) : "s"(rs), "v"(voff), "s"(soff) : "memory");
}
DEV void touchl(u32x4 rs, unsigned voff, unsigned soff) {
  asm volatile("buffer_load_dword %1, %0, %2 offen lds" ::"s"(rs), "v"(voff), "s"(soff) : "memory");
}
template <int N> using IC = std::integral_constant<int, N>;

DEV void features(const f32x16& p, bool h, unsigned (&ph)[2][8]) {
  constexpr float L = 1.4426950408889634f;
  float mx = fmaxf(p[0], p[1]), mn = fminf(p[0], p[1]);
#pragma unroll
  for (int r = 2; r < 12; r += 2) { mx = fmaxf(fmaxf(mx, p[r]), p[r + 1]); mn = fminf(fminf(mn, p[r]), p[r + 1]); }
  float mx2 = fmaxf(fmaxf(fmaxf(fmaxf(mx, p[12]), p[13]), p[14]), p[15]);
  float mn2 = fminf(fminf(fminf(fminf(mn, p[12]), p[13]), p[14]), p[15]);
  mx = h ? mx : mx2; mn = h ? -mn : -mn2;
  swap32(mx, mn);
  float c0 = -L * fmaxf(mx, mn), c1 = c0;
  swap32(c0, c1);
  const float ninf = -__builtin_inff();
  const float d0 = h ? ninf : c0, d1 = h ? ninf : c1;
  float e[2][16], s0 = 0.f, s1 = 0.f;
#pragma unroll
  for (int r = 0; r < 16; r++) {
    e[0][r] = __builtin_amdgcn_exp2f(fmaf(p[r], L, r < 12 ? c0 : d0));
    e[1][r] = __builtin_amdgcn_exp2f(fmaf(p[r], -L, r < 12 ? c1 : d1));
    s0 += e[0][r]; s1 += e[1][r];
  }
  swap32(s0, s1);
  float i0 = __builtin_amdgcn_rcpf(s0 + s1), i1 = i0;
  swap32(i0, i1);
#pragma unroll
  for (int r = 0; r < 8; r++) { ph[0][r] = pk(e[0][2 * r] * i0, e[0][2 * r + 1] * i0); ph[1][r] = pk(e[1][2 * r] * i1, e[1][2 * r + 1] * i1); }
}
DEV bf16x8 pack8(const f32x16& a, int o) {
  u32x4 u = {pk(a[o], a[o + 1]), pk(a[o + 2], a[o + 3]), pk(a[o + 4], a[o + 5]), pk(a[o + 6], a[o + 7])};
  return __builtin_bit_cast(bf16x8, u);
}
DEV float gelu(float a) {
  constexpr float c1 = -2.302208198f, c2 = c1 * 0.044715f;
  return a * __builtin_amdgcn_rcpf(1.f + __builtin_amdgcn_exp2f(a * fmaf(a * a, c2, c1)));
}

#define STAMP(n)

template <int T, int N, class Fn> DEV void unroll(Fn&& f) {
  if constexpr (T < N) { f(std::integral_constant<int, T>()); unroll<T + 1, N>(f); }
}
constexpr int woff(int k) { return (k >> 1) * SLOT + (k & 1) * 8192; }
constexpr int xoff(int k) { return (k >> 1) * SLOT + 4096 + (k & 1) * 2048; }
constexpr int G2R = 4, G2POLL = 5;
constexpr int g2ld(int t) { return t < 17 ? 4 : (t == 17 ? 2 : 0); }
constexpr int g2dm(int t) { return t < 17 ? 4 : (t == 17 ? 2 : 0); }
constexpr int g2cnt(int s) { return s < 0 ? 4 : g2ld(s + G2R) + (s >= 1 ? g2dm(s + 9) : 0) + (s == G2POLL ? 1 : 0); }

__global__ __launch_bounds__(256, 1) void fused_kernel(const short* __restrict__ x, const short* __restrict__ wqkv, const short* __restrict__ wphi,
                                                       const short* __restrict__ wo, const short* __restrict__ w1, const short* __restrict__ b1,
                                                       const short* __restrict__ w2, const short* __restrict__ b2, short* __restrict__ out,
                                                       char* __restrict__ ws) {
  __shared__ __attribute__((aligned(16))) char lds_[163840];
  const ldsp lds = (ldsp)lds_;
  const unsigned lds0 = (unsigned)(uintptr_t)lds;
  const int tid = threadIdx.x, lane = tid & 63, w = __builtin_amdgcn_readfirstlane(tid >> 6);
  asm volatile("s_nop 4" ::"s"(w), "s"(x), "s"(wqkv), "s"(wphi), "s"(wo), "s"(w1), "s"(b1), "s"(w2), "s"(b2), "s"(out), "s"(ws) : "memory");
  const int xc = blockIdx.x & 7, i = blockIdx.x >> 3;
  const int h = lane >> 5, n = lane & 31, q = (lane & 15) >> 2, pp = lane & 3, blk = (lane >> 4) & 1;
  const u32x4 rsF = mkrs(ws, WS_FLAG_BYTES), rsX = mkrs(x, S * D * 2), rsW2 = mkrs(w2, F * S * 2);
  const unsigned area = lds0 + w * AREA;
  const int tile2 = 28 * (xc & 1) + i - 4, dt2 = tile2 >> 3, st2 = 8 * (xc >> 1) + (tile2 & 7);
  const unsigned swz = (lane >> 3), swc = ((lane & 7) ^ (((lane >> 4) & 1) << 2)) * 16;
  unsigned v2[4];
#pragma unroll
  for (int j = 0; j < 4; j++) v2[j] = swz * (S * 2) + swc + j * (8 * S * 2 - 1024);
  const unsigned so2 = (w * 448) * (S * 2) + st2 * 128;
  auto pre2 = [&](int t0, int t1, int slot0) {
#pragma unroll 1
    for (int t = t0; t < t1; t++) dma4(rsW2, v2, so2 + t * (32 * S * 2), area + (slot0 + t - t0) * 4096);
  };

  const bool isA = i >= 16;
  const int T = i - 16, pr = xc;
  const u32x4 rsW1 = mkrs(w1, S * F * 2);
  const unsigned vt1 = xc < 7 ? (lane >> 2) * (F * 2) + (lane & 3) * 128 : OOB;
  auto so_t1 = [&](int stage, int wq, int r0) { return (512 * wq + ((64 * xc + 32 * stage + r0) & 511)) * (F * 2) + xc * 512; };
  Epoch ep;
  auto setep = [&](u32x2 gen) {
    const unsigned long long g = (((unsigned long long)__builtin_amdgcn_readfirstlane(gen.y) << 32) | (unsigned)__builtin_amdgcn_readfirstlane(gen.x)) + 1;
    ep.lo = (unsigned)g; ep.hi = (unsigned)(g >> 32);
  };

  if (!isA) {
    const int row0 = 512 * (i >> 2) + 64 * xc + 16 * (i & 3) + 4 * w;
    const bool isG = xc < 7 && i < 14;
    const int ft = 2 * xc + (i >= 7), dt = i >= 7 ? i - 7 : i;
    bf16x8 xv[4];
    u32x2 gen, bb[4];
    {
      const unsigned vo = lane < 56 ? lane * 16 : OOB, so = row0 * (D * 2);
      xv[0] = ldp<0>(rsX, vo, so); xv[1] = ldp<896>(rsX, vo, so); xv[2] = ldp<1792>(rsX, vo, so); xv[3] = ldp<2688>(rsX, vo, so);
      gen = ldc2(rsF, 0);
      setm0(area + L_STAT);
      touchl(rsW1, lane < 32 ? vt1 : OOB, so_t1(i >> 2, i & 3, 8 * w));
      const u32x4 rsB = mkrs(b1, F * 2);
      const unsigned sb = (ft * 128 + w * 32) * 2;
      bb[0] = ldp2<0>(rsB, h * 8, sb); bb[1] = ldp2<16>(rsB, h * 8, sb); bb[2] = ldp2<32>(rsB, h * 8, sb); bb[3] = ldp2<48>(rsB, h * 8, sb);
    }
    unsigned v1[4][4], vx[4][2];
#pragma unroll
    for (int ks = 0; ks < 4; ks++) {
#pragma unroll
      for (int j = 0; j < 4; j++) v1[ks][j] = (16 * ks + 8 * (j & 1) + swz) * (F * 2) + (j >> 1) * 128 + swc - j * 1024;
#pragma unroll
      for (int j = 0; j < 2; j++) vx[ks][j] = (16 * ks + 8 * j + swz) * (D * 2) + swc - (j + 2 * (ks & 1)) * 1024;
    }
    waitv<5>(xv[0], xv[1], xv[2], xv[3]);
    tie(gen);
    STAMP(0);
    setep(gen);
    const unsigned tagB = ep.lo ^ ep.hi;
    {
      float s[4] = {}, z[4] = {};
#pragma unroll
      for (int r = 0; r < 4; r++) acc_stats(xv[r], s[r], z[r]);
      swap32(s[0], s[1]); swap32(s[2], s[3]); swap32(z[0], z[1]); swap32(z[2], z[3]);
      float sa = s[0] + s[1], sb = s[2] + s[3], za = z[0] + z[1], zb = z[2] + z[3];
      swap16(sa, sb); swap16(za, zb);
      const float mu = rowsum(sa + sb) * (1.f / D), ms = rowsum(za + zb) * (1.f / D);
      const float var = fmaxf(ms - mu * mu, 0.f);
      float a = __builtin_amdgcn_rsqf(var + 1e-5f);
      a *= __builtin_amdgcn_rsqf(var * a * a + 1e-5f);
      const unsigned a2 = pk(a, a);
      const float c = -mu * blo(a2);
      stc(mkst(ws + WS_ST, SZ_ST), (lane & 15) ? OOB : (row0 + h + 2 * blk) * 16, (u32x4){a2, ep.lo, __builtin_bit_cast(unsigned, c), tagB});
    }
    asm volatile("s_barrier" ::: "memory");
    STAMP(1);

    if (isG) {
      const unsigned b1s = (w * 512) * (F * 2) + ft * 256, bxs = (w * 512) * (D * 2) + dt * 128;
      auto mset = [&](auto kk, auto jj) {
        constexpr int ks = kk.value, j = jj.value;
        if constexpr ((ks & 1) ? j == 2 : j == 0) setm0n(area + woff(ks));
        if constexpr (!(ks & 1) && j == 4) setm0n(area + xoff(ks));
      };
      auto dgrp = [&](auto kk, auto jj, unsigned s1, unsigned sx) {
        constexpr int ks = kk.value, j = jj.value, jw = (ks & 1) ? j - 2 : j, jx = (ks & 1) ? j : j - 4;
        if constexpr (jw >= 0 && jw < 4) dma<jw * 1024>(rsW1, v1[ks][jw], s1);
        else dma<(jx + 2 * (ks & 1)) * 1024>(rsX, vx[ks][jx], sx);
      };
      auto fill = [&](auto kk) {
        unroll<0, 6>([&](auto jj) { mset(kk, jj); asm volatile("s_nop 0"); dgrp(kk, jj, b1s + 64 * xc * (F * 2), bxs + 64 * xc * (D * 2)); });
      };
      const u32x4 rsS = mkrs(ws + WS_ST, SZ_ST);
      auto gather = [&]() {
        bf16x8 g[8];
#pragma unroll
        for (int j = 0; j < 8; j++) g[j] = ldc<0>(rsS, (lane * 16 + (j + xc) * 1024) & 8191, w * 8192);
        waitv<0>();
        bool ok = true;
#pragma unroll
        for (int j = 0; j < 8; j++) {
          tie(g[j]);
          const u32x4 u = __builtin_bit_cast(u32x4, g[j]);
          ok &= u[1] == ep.lo && u[3] == tagB;
          const ldsp p = lds + w * AREA + L_STAT + j * 512 + (lane >> 4) * 128 + (lane & 15) * 4;
          lst(p, u[2]); lst(p + 64, u[0]);
        }
        return (bool)__all(ok);
      };
      unroll<0, 4>(fill);
      STAMP(2);
#pragma unroll 1
      for (int it = 0; it < SPIN; ++it)
        if (gather()) break;
      STAMP(3);
      tie(bb[0]); tie(bb[1]); tie(bb[2]); tie(bb[3]);
      f32x16 acc[4][2];
#pragma unroll
      for (int g = 0; g < 4; g++) {
        acc[0][0][4 * g] = blo(bb[g].x); acc[0][0][4 * g + 1] = bhi(bb[g].x); acc[0][0][4 * g + 2] = blo(bb[g].y); acc[0][0][4 * g + 3] = bhi(bb[g].y);
      }
      acc[0][1] = acc[0][0];
#pragma unroll
      for (int a = 1; a < 4; a++) { acc[a][0] = (f32x16)(0.f); acc[a][1] = (f32x16)(0.f); }
      unsigned pa[4], px[2], msk[4], psc = area + L_STAT + h * 32, psa;
      {
        const unsigned rowb = area + (8 * h + q) * 128 + (pp & 1) * 8, ch = 2 * blk + (pp >> 1), fl = (q >> 1) << 2;
#pragma unroll
        for (int a = 0; a < 4; a++) pa[a] = rowb + ((a ^ w) >> 1) * 2048 + ((4 * ((a ^ w) & 1) + ch) ^ fl) * 16;
        px[0] = rowb + (ch ^ fl) * 16; px[1] = rowb + ((4 + ch) ^ fl) * 16;
        const unsigned r = 4 * ((lane >> 3) & 1) + pp, on = (!blk && (q & 1) == h) ? ((r & 1) ? 0xffff0000u : 0xffffu) : 0u;
#pragma unroll
        for (int j = 0; j < 4; j++) { msk[j] = (r >> 1) == j ? on : 0u; tie(msk[j]); }
        psa = area + L_STAT + 64 + (8 * h + r) * 4;
      }
      s16x4 wl[2][4], wh[2][4], xl[2], xh[2];
      u32x4 hf[2][2], dg;
      unsigned av;
      f32x16 hn[2], cc = {};
      tie(cc);
      auto rdX = [&](auto nn, auto dd) {
        constexpr int o = xoff(nn.value & 3), d = dd.value;
        xl[d] = vtr((ldsp)(uintptr_t)px[d] + o); xh[d] = vtr((ldsp)(uintptr_t)px[d] + o + 512);
      };
      auto rdA = [&](auto nn) { av = lld<unsigned>((ldsp)(uintptr_t)psa + nn.value * 128); };
      auto rdC = [&](auto nn) {
        const f32x4 c0 = lld<f32x4>((ldsp)(uintptr_t)psc + nn.value * 128), c1 = lld<f32x4>((ldsp)(uintptr_t)psc + nn.value * 128 + 16);
#pragma unroll
        for (int e = 0; e < 4; e++) { cc[e] = c0[e]; cc[4 + e] = c1[e]; }
      };
      auto rdW = [&](auto nn, auto aa) {
        constexpr int o = woff(nn.value & 3), nx = nn.value & 1, a = aa.value;
        wl[nx][a] = vtr((ldsp)(uintptr_t)pa[a] + o); wh[nx][a] = vtr((ldsp)(uintptr_t)pa[a] + o + 512);
      };
      auto mkD = [&]() { dg = (u32x4){av & msk[0], av & msk[1], av & msk[2], av & msk[3]}; };
      auto nrm = [&](auto dd) { hn[dd.value] = mfmav(dg, cat(xl[dd.value], xh[dd.value]), cc); };
      auto cvt = [&](auto nn, auto dd) {
        constexpr int d = dd.value;
        hf[nn.value & 1][d] = (u32x4){pk(hn[d][0], hn[d][1]), pk(hn[d][2], hn[d][3]), pk(hn[d][4], hn[d][5]), pk(hn[d][6], hn[d][7])};
        SB();
        keep(hn[d]);
      };
      auto step = [&](auto kk, auto wn, auto last, auto&& ms, auto&& gm) {
        constexpr int ks = kk.value;
        constexpr bool more = !last.value;
        using NX = IC<ks + 1>;
        auto mm = [&](auto m_) {
          constexpr int a = m_.value >> 1, dh = m_.value & 1;
          acc[a][dh] = MFMA(cat(wl[ks & 1][a], wh[ks & 1][a]), __builtin_bit_cast(bf16x8, hf[ks & 1][dh]), acc[a][dh]);
          SB();
        };
        __builtin_amdgcn_s_waitcnt(0x70 | wn.value);
        asm volatile("" ::: "memory");
        SB();
        if constexpr (more) { rdX(NX(), IC<0>()); rdA(NX()); }
        ms(IC<0>());
        SB();
        mm(IC<0>());
        gm(IC<0>());
        if constexpr (more) { rdC(NX()); rdX(NX(), IC<1>()); SB(); }
        mm(IC<1>());
        gm(IC<1>());
        ms(IC<2>());
        if constexpr (more) { rdW(NX(), IC<0>()); rdW(NX(), IC<1>()); SB(); }
        mm(IC<2>());
        if constexpr (more) { mkD(); SB(); }
        gm(IC<2>());
        if constexpr (more) { rdW(NX(), IC<2>()); SB(); nrm(IC<0>()); SB(); rdW(NX(), IC<3>()); SB(); }
        mm(IC<3>());
        if constexpr (more) { nrm(IC<1>()); SB(); }
        gm(IC<3>());
        ms(IC<4>());
        mm(IC<4>());
        if constexpr (more) { cvt(NX(), IC<0>()); SB(); }
        mm(IC<5>());
        gm(IC<4>());
        mm(IC<6>());
        if constexpr (more) { cvt(NX(), IC<1>()); SB(); }
        gm(IC<5>());
        mm(IC<7>());
      };
      asm volatile("s_waitcnt lgkmcnt(0)" ::: "memory");
      rdX(IC<0>(), IC<0>()); rdX(IC<0>(), IC<1>()); rdA(IC<0>()); rdC(IC<0>());
      unroll<0, 4>([&](auto jj) { rdW(IC<0>(), jj); });
      mkD();
      SB();
      asm volatile("s_nop 1");
      nrm(IC<0>()); nrm(IC<1>());
      asm volatile("s_nop 7\n\ts_nop 7");
      cvt(IC<0>(), IC<0>()); cvt(IC<0>(), IC<1>());
      SB();
      unsigned tk = 64 * xc;
#pragma unroll 1
      for (int t = 0; t < 7; t++) {
        tk = (tk + 64) & 511;
        const unsigned s1 = b1s + tk * (F * 2), sx = bxs + tk * (D * 2);
        unroll<0, 4>([&](auto kk) {
          step(kk, IC<12>(), IC<0>(), [&](auto jj) { mset(kk, jj); SB(); }, [&](auto jj) { dgrp(kk, jj, s1, sx); SB(); });
        });
        psc += 512; tie(psc); psa += 512; tie(psa);
        SB();
      }
      {
        unsigned vp[4];
#pragma unroll
        for (int j = 0; j < 4; j++) vp[j] = i >= 4 ? v2[j] : OOB;
        unroll<0, 4>([&](auto kk) {
          constexpr int ks = kk.value;
          step(kk, IC<(ks == 3 ? 12 : 12 - 2 * ks)>(), IC<(ks == 3)>(),
               [&](auto jj) { if constexpr (jj.value == 0) { setm0n(area + (6 + ks) * 4096); SB(); } },
               [&](auto jj) { if constexpr (jj.value < 4) { dma<jj.value * 1024>(rsW2, vp[jj.value], so2 + ks * (32 * S * 2)); SB(); } });
        });
      }
      STAMP(4);
      {
        const ldsp p = lds + w * AREA + lane * 16;
#pragma unroll
        for (int a = 1; a < 4; a++)
#pragma unroll
          for (int dh = 0; dh < 2; dh++)
#pragma unroll
            for (int g = 0; g < 4; g++)
              lst(p + (a - 1) * 8192 + dh * 4096 + g * 1024, (f32x4){acc[a][dh][4 * g], acc[a][dh][4 * g + 1], acc[a][dh][4 * g + 2], acc[a][dh][4 * g + 3]});
      }
      bar();
      STAMP(5);
      {
        const rsrc_t st = mkst(ws + WS_UF, SZ_UF);
        const unsigned vo = (ft * 16 + w * 4 + h) * (D * 16) + (dt * 64 + n) * 16;
        f32x4 v[2][3][4];
#pragma unroll
        for (int dh = 0; dh < 2; dh++)
#pragma unroll
          for (int a = 1; a < 4; a++)
#pragma unroll
            for (int g = 0; g < 4; g++) v[dh][a - 1][g] = lld<f32x4>(lds + (w ^ a) * AREA + lane * 16 + (a - 1) * 8192 + dh * 4096 + g * 1024);
        SB();
#pragma unroll
        for (int dh = 0; dh < 2; dh++) {
#pragma unroll
          for (int g = 0; g < 4; g += 2) {
            float r[8];
#pragma unroll
            for (int e = 0; e < 8; e++) {
              const int gg = g + (e >> 2), c = e & 3;
              r[e] = gelu((acc[0][dh][4 * gg + c] + v[dh][0][gg][c]) + (v[dh][1][gg][c] + v[dh][2][gg][c]));
            }
            auto r0 = __builtin_amdgcn_permlane32_swap(pk(r[0], r[1]), pk(r[4], r[5]), false, false);
            auto r1 = __builtin_amdgcn_permlane32_swap(pk(r[2], r[3]), pk(r[6], r[7]), false, false);
            stc(st, vo + g * (D * 16) + dh * 512, (u32x4){r0[0], r1[0], r0[1], r1[1]});
          }
        }
      }
      STAMP(6);
      publish(ws, FL_U + dt * 14 + ft, ep);
      STAMP(7);
      if (i >= 4) pre2(4, 10, 0);
    } else {
      if (xc < 7) {
#pragma unroll 1
        for (int m = 4; m < 9; m++) touchl(rsW1, vt1, so_t1(m, 2 * (i - 14) + (w >> 1), 16 * (w & 1)));
      }
      __builtin_amdgcn_s_sleep(127);
      if (i >= 4) { pre2(0, 4, 6); pre2(4, 10, 0); }
      if (i >= 14) {
        const int head = 2 * xc + i - 14;
        poll(rsF, (FL_P + xc * 16 + (lane & 15)) * 128, ep);
        STAMP(3);
        const u32x4 rsP = mkrs(ws + WS_KVP, SZ_KVP);
        const unsigned so = head * 131072 + (w >> 1) * 4096 + (w & 1) * 2048;
        bf16x8 pv[16][2];
        unroll<0, 16>([&](auto tt) { constexpr int t = tt.value; pv[t][0] = ldc<0>(rsP, lane * 16, so + t * 8192); pv[t][1] = ldc<1024>(rsP, lane * 16, so + t * 8192); });
        f32x4 a0 = {0.f, 0.f, 0.f, 0.f}, a1 = a0;
        unroll<0, 16>([&](auto tt) {
          constexpr int t = tt.value;
          waitv<30 - 2 * t>(pv[t][0], pv[t][1]);
          a0 += __builtin_bit_cast(f32x4, pv[t][0]); a1 += __builtin_bit_cast(f32x4, pv[t][1]);
        });
        stc(mkst(ws + WS_KVF, SZ_KVF), head * 4096 + w * 1024 + lane * 16, (u32x4){pk(a0[0], a0[1]), pk(a0[2], a0[3]), pk(a1[0], a1[1]), pk(a1[2], a1[3])});
        STAMP(4);
        publish(ws, FL_R + head, ep);
        STAMP(5);
      }
    }
  } else {
    const unsigned ring = lds0 + L_WQ + w * 12544, sxa = (128 * T + 32 * w) * (D * 2);
    unsigned vxa[4];
#pragma unroll
    for (int j = 0; j < 4; j++) vxa[j] = (8 * j + swz) * (D * 2) + (((lane & 7) ^ ((4 * j + (lane >> 4)) & 7)) * 16) - j * 1024;
    unsigned sink = 0;
    u32x2 gen = ldc2(rsF, 0);
    dma4(rsX, vxa, sxa, ring);
    const u32x4 rsW = mkrs(wqkv, D * 3 * D * 2);
    {
      const unsigned r6 = (lane * 43) >> 8, c6 = lane - 6 * r6;
      touch(sink, rsW, lane < 42 ? r6 * 2688 + (c6 >> 1) * 896 + (c6 & 1) * 108 : OOB, (28 * T + 7 * w) * 2688 + pr * 112);
    }
    dma4(rsX, vxa, sxa + 128, ring + 4096);
    dma4(rsX, vxa, sxa + 256, ring + 8192);
    unsigned vw[8];
#pragma unroll
    for (int s = 0; s < 8; s++) {
      const unsigned c = (s < 7 ? 256 * s + 64 * w : 3072) + lane, r = (c * 9363) >> 16;
      vw[s] = ((r & ~15u) + 8 * ((r >> 1) & 1) + 4 * (r & 1) + ((r >> 2) & 1) + 2 * ((r >> 3) & 1)) * 2688 + (c - 7 * r) * 16;
    }
    const unsigned mw = lds0 + w * 1024;
    auto mW = [&](auto ss, auto bb) {
      if constexpr (ss.value < 12) addm0<bb.value + ss.value * 4096>(mw); else addm0<bb.value + 49152>(lds0);
    };
    auto dmaW = [&](auto ss, unsigned col) {
      constexpr int s = ss.value;
      dma<0>(rsW, vw[s < 12 ? (s + 14) % 7 : 7], col + pr * 112 + (s >= 7 && s < 12 ? 256 * 2688 : 0));
    };
    bf16x8 xa[28];
    float sa = 0.f, sq = 0.f;
    {
      unsigned pr_[4];
#pragma unroll
      for (int k = 0; k < 4; k++) pr_[k] = ring + n * 128 + (((2 * k + h) ^ ((n >> 1) & 7)) * 16);
      unroll<0, 7>([&](auto cc) {
        constexpr int cb = cc.value;
        waitv<(cb == 0 ? 9 : 8)>();
        if (cb == 0) STAMP(0);
        if (cb == 6) STAMP(4);
#pragma unroll
        for (int k = 0; k < 4; k++) xa[4 * cb + k] = lld<bf16x8>((ldsp)(uintptr_t)pr_[k] + (cb % 3) * 4096);
        asm volatile("s_waitcnt lgkmcnt(0)" : "+v"(xa[4 * cb]), "+v"(xa[4 * cb + 1]), "+v"(xa[4 * cb + 2]), "+v"(xa[4 * cb + 3])::"memory");
        if constexpr (cb < 4) dma4(rsX, vxa, sxa + (cb + 3) * 128, ring + (cb % 3) * 4096);
        if constexpr (cb == 4) unroll<0, 4>([&](auto ss) { mW(ss, IC<L_WK>()); asm volatile("s_nop 0"); dmaW(ss, 896); });
        if constexpr (cb == 5) unroll<0, 4>([&](auto ss) { mW(ss, IC<L_WV>()); asm volatile("s_nop 0"); dmaW(ss, 1792); });
        if constexpr (cb == 6) {
          setm0(lds0 + L_WPHI + w * 1024);
          dma<0>(mkrs(wphi + pr * 1568, 3136), tid * 16, 0);
        }
        SB();
#pragma unroll
        for (int k = 0; k < 4; k++) acc_stats(xa[4 * cb + k], sa, sq);
        SB();
      });
    }
    STAMP(1);
    tie(gen);
    setep(gen);
    float la, lc;
    {
      float s2 = sa, q2 = sq;
      swap32(sa, s2); swap32(sq, q2);
      const float mu = (sa + s2) * (1.f / D), var = fmaxf((sq + q2) * (1.f / D) - mu * mu, 0.f);
      la = __builtin_amdgcn_rsqf(var + 1e-5f);
      lc = -mu * la;
    }
    auto norm = [&](auto kk, auto ee) {
      constexpr int ks = kk.value, e = ee.value;
      if constexpr (ks < 28) {
        u32x4 u = __builtin_bit_cast(u32x4, xa[ks]);
        u[e] = affine2(u[e], la, lc);
        xa[ks] = __builtin_bit_cast(bf16x8, u);
      }
    };
    unroll<0, 4>([&](auto ee) { norm(IC<0>(), ee); });
    tie(xa[0]);
    waitv<1>();
    STAMP(2);
    bar();
    STAMP(3);
    const unsigned vt2 = (lane >> 3) * (S * 2) + (lane & 7) * 128;
    auto touch2 = [&](int m) { touch(sink, rsW2, vt2, (448 * w + 128 * m + 8 * T) * (S * 2) + (xc >> 1) * 1024); };
    const unsigned wlane = (2 * h + 4 * q) * 112 + 32 * blk + 8 * pp;
    f32x16 kT[2] = {}, vv[2] = {};
    {
      const ldsp pk_ = lds + L_WK + wlane, pv_ = lds + L_WV + wlane;
      s16x4 fr[30][8];
      auto rd = [&](auto kk, auto cc) {
        constexpr int ks = kk.value, c = cc.value;
        if constexpr (ks < 28) fr[ks][c] = vtr(((c & 4) ? pv_ : pk_) + ks * 1792 + ((c >> 1) & 1) * 56 + (c & 1) * 112);
      };
      unroll<0, 8>([&](auto cc) { rd(IC<0>(), cc); });
      unroll<0, 8>([&](auto cc) { rd(IC<1>(), cc); });
      SB();
      unroll<0, 28>([&](auto kk) {
        constexpr int ks = kk.value;
        if constexpr (ks == 7) { waitv<6>(); asm volatile("s_barrier" ::: "memory"); }
        if constexpr (ks == 16) { waitv<8>(); asm volatile("s_barrier" ::: "memory"); }
        __builtin_amdgcn_s_waitcnt(ks < 27 ? 0xc87f : 0xc07f);
        SB();
        unroll<0, 4>([&](auto mm) {
          constexpr int m = mm.value;
          const bf16x8 wv = cat(fr[ks][2 * m], fr[ks][2 * m + 1]);
          if constexpr (m < 2) kT[m] = MFMA(wv, xa[ks], kT[m]);
          else vv[m - 2] = MFMA(xa[ks], wv, vv[m - 2]);
          SB();
          if constexpr (ks < 9 && m == 0) mW(IC<4 + ks>(), IC<L_WK>());
          if constexpr (ks < 9 && m == 2) mW(IC<4 + ks>(), IC<L_WV>());
          if constexpr (ks >= 9 && ks < 22 && m == 0) mW(IC<ks - 9>(), IC<L_WQ>());
          norm(IC<ks + 1>(), mm);
          SB();
          if constexpr (ks < 9 && !(m & 1)) dmaW(IC<4 + ks>(), m ? 1792 : 896);
          if constexpr (ks == 9 && m == 0) touch(sink, rsW1, vt1, so_t1(8 + (T >> 1), (2 * T + (w >> 1)) & 3, 16 * (w & 1)));
          if constexpr (ks >= 9 && ks < 22 && m == 0) dmaW(IC<ks - 9>(), 0);
          if constexpr (ks == 22 && m == 0) touch2(0);
          rd(IC<ks + 2>(), IC<2 * m>());
          rd(IC<ks + 2>(), IC<2 * m + 1>());
          if constexpr (ks == 26 && m == 3) asm volatile("s_nop 0");
          SB();
        });
      });
    }
    bf16x8 wf[2][2];
    {
      const ldsp p = lds + L_WPHI + (4 * h + q) * 56 + 32 * blk + 8 * pp;
#pragma unroll
      for (int hd = 0; hd < 2; hd++) {
        wf[hd][0] = cat(vtr(p + hd * 1568), vtr(p + hd * 1568 + 8 * 56));
        s16x4 z = vtr(p + hd * 1568 + 24 * 56);
        wf[hd][1] = cat(vtr(p + hd * 1568 + 16 * 56), h ? (s16x4){0, 0, 0, 0} : z);
      }
    }
    STAMP(5);
    touch2(1);
    {
      f32x16 p[2];
#pragma unroll
      for (int hd = 0; hd < 2; hd++) { p[hd] = MFMA(wf[hd][0], pack8(kT[hd], 0), (f32x16)(0.f)); p[hd] = MFMA(wf[hd][1], pack8(kT[hd], 8), p[hd]); }
      bar();
      {
        const bf16x8 ones = {0x3f80, 0x3f80, 0x3f80, 0x3f80, 0x3f80, 0x3f80, 0x3f80, 0x3f80};
        const ldsp pvx = lds + L_VX + w * 2048 + lane * 16;
#pragma unroll
        for (int hd = 0; hd < 2; hd++)
#pragma unroll
          for (int j = 0; j < 2; j++) { bf16x8 f = pack8(vv[hd], 8 * j); lst(pvx + hd * 8192 + j * 1024, n == 28 ? ones : f); }
      }
      const unsigned pb = (32 * w + n) * 64 + ((h ^ ((n >> 1) & 7)) * 8);
#pragma unroll
      for (int hd = 0; hd < 2; hd++) {
        unsigned ph[2][8];
        features(p[hd], h, ph);
#pragma unroll
        for (int sg = 0; sg < 2; sg++)
#pragma unroll
          for (int g = 0; g < 4; g++) lst(lds + L_PHI + (hd * 2 + sg) * 8192 + (pb ^ (g << 4)), (u32x2){ph[sg][2 * g], ph[sg][2 * g + 1]});
      }
    }
    STAMP(6);
    barv();
    {
      const unsigned sw = 2 * h + (q >> 1);
      const ldsp pl = lds + L_PHI + w * 8192 + (4 * h + q) * 64 + (((4 * blk + pp) ^ sw) * 8);
      const ldsp ph_ = lds + L_PHI + w * 8192 + (8 + 4 * h + q) * 64 + (((4 * blk + pp) ^ sw ^ 4) * 8);
      const ldsp pvx = lds + L_VX + (w >> 1) * 8192 + lane * 16;
      f32x16 kv = {};
#pragma unroll
      for (int jj = 0; jj < 8; jj++) kv = MFMA(cat(vtr(pl + jj * 1024), vtr(ph_ + jj * 1024)), lld<bf16x8>(pvx + jj * 1024), kv);
      const rsrc_t st = mkst(ws + WS_KVP, SZ_KVP);
      const unsigned vo = (2 * pr + (w >> 1)) * 131072 + T * 8192 + (w & 1) * 4096 + lane * 16;
#pragma unroll
      for (int g = 0; g < 4; g++) stc(st, vo + g * 1024, __builtin_bit_cast(u32x4, (f32x4){kv[4 * g], kv[4 * g + 1], kv[4 * g + 2], kv[4 * g + 3]}));
    }
    STAMP(7);
    f32x16 qT[2] = {};
    {
      const ldsp pq_ = lds + L_WQ + wlane;
      s16x4 fr[29][4];
      auto rd = [&](auto kk, auto cc) {
        constexpr int ks = kk.value, c = cc.value;
        if constexpr (ks < 28) fr[ks][c] = vtr(pq_ + ks * 1792 + (c >> 1) * 56 + (c & 1) * 112);
      };
      unroll<0, 4>([&](auto cc) { rd(std::integral_constant<int, 0>(), cc); });
      unroll<0, 28>([&](auto kk) {
        constexpr int ks = kk.value;
        if constexpr (ks == 14) { publish(ws, FL_P + pr * 16 + T, ep); STAMP(8); touch2(2); touch2(3); SB(); }
        unroll<0, 2>([&](auto mm) {
          constexpr int m = mm.value;
          rd(std::integral_constant<int, ks + 1>(), std::integral_constant<int, 2 * m>());
          rd(std::integral_constant<int, ks + 1>(), std::integral_constant<int, 2 * m + 1>());
          qT[m] = MFMA(cat(fr[ks][2 * m], fr[ks][2 * m + 1]), xa[ks], qT[m]);
          SB();
        });
      });
    }
    unsigned phq[2][2][8];
    {
      f32x16 p[2];
#pragma unroll
      for (int hd = 0; hd < 2; hd++) { p[hd] = MFMA(wf[hd][0], pack8(qT[hd], 0), (f32x16)(0.f)); p[hd] = MFMA(wf[hd][1], pack8(qT[hd], 8), p[hd]); }
      bar();
      STAMP(9);
      pre2(0, 4, 6);
      pre2(4, 10, 0);
      features(p[0], h, phq[0]);
      features(p[1], h, phq[1]);
    }
    STAMP(10);
    poll(rsF, (FL_R + 2 * pr + (lane & 1)) * 128, ep);
    tie(sink);
    STAMP(11);
    {
      const u32x4 rsK = mkrs(ws + WS_KVF, SZ_KVF);
      bf16x8 kf[2][4];
#pragma unroll
      for (int hd = 0; hd < 2; hd++) {
        const unsigned so = (2 * pr + hd) * 4096;
        kf[hd][0] = ldc<0>(rsK, lane * 16, so); kf[hd][1] = ldc<1024>(rsK, lane * 16, so);
        kf[hd][2] = ldc<2048>(rsK, lane * 16, so); kf[hd][3] = ldc<3072>(rsK, lane * 16, so);
      }
      f32x16 o[2] = {};
      waitv<4>(kf[0][0], kf[0][1], kf[0][2], kf[0][3]);
#pragma unroll
      for (int hd = 0; hd < 2; hd++) {
        if (hd) waitv<0>(kf[1][0], kf[1][1], kf[1][2], kf[1][3]);
#pragma unroll
        for (int c = 0; c < 4; c++) {
          const unsigned* s = &phq[hd][c >> 1][4 * (c & 1)];
          o[hd] = MFMA(kf[hd][c], __builtin_bit_cast(bf16x8, (u32x4){s[0], s[1], s[2], s[3]}), o[hd]);
        }
      }
      float da = o[0][12], db = o[1][12];
      swap32(da, db);
      float ia = __builtin_amdgcn_rcpf(db + 1e-6f), ib = ia;
      swap32(ia, ib);
      unsigned a[8], bq[8];
#pragma unroll
      for (int r = 0; r < 8; r++) { a[r] = pk(o[0][2 * r] * ia, o[0][2 * r + 1] * ia); bq[r] = pk(o[1][2 * r] * ib, o[1][2 * r + 1] * ib); }
      const rsrc_t st = mkst(ws + WS_OF, SZ_OF);
      const unsigned tk = (128 * T + 32 * w + n) * 16, jb = 14 * (pr >> 1);
      const unsigned vo = (jb + 6 * (pr & 1) + h) * (S * 16) + tk;
      stc(st, vo, (u32x4){a[0], a[1], a[2], a[3]});
      stc(st, vo + 2 * S * 16, (u32x4){a[4], a[5], bq[0], bq[1]});
      stc(st, vo + 4 * S * 16, (u32x4){bq[2], bq[3], bq[4], bq[5]});
      stc(st, h ? OOB : (jb + 12 + (pr & 1)) * (S * 16) + tk, (u32x4){a[6], a[7], bq[6], bq[7]});
    }
    publish(ws, FL_O + T * 8 + pr, ep);
    STAMP(12);
  }

  if (i >= 4) {
    const unsigned orow = st2 * 64 + 16 * w + (lane >> 3);
    const unsigned vo = orow * (D * 2) + dt2 * 128 + (lane & 7) * 16;
    bf16x8 xr0 = ldp<0>(rsX, vo, 0), xr1 = ldp<0>(rsX, vo, 8 * D * 2);
    const u32x4 rsB = mkrs(b2, S * 2);
    unsigned bs0 = ldu16<0>(rsB, orow * 2, 0), bs1 = ldu16<16>(rsB, orow * 2, 0);
    poll(rsF, (FL_U + dt2 * 14 + ((7 * w) >> 1) + (lane & 3)) * 128, ep);
    tie(xr0); tie(xr1); tie(bs0); tie(bs1);
    STAMP(13);
    const u32x4 rsU = mkrs(ws + WS_UF, SZ_UF), rsO = mkrs(ws + WS_OF, SZ_OF), rsWo = mkrs(wo, D * D * 2);
    const unsigned vu0 = h * (D * 16) + (dt2 * 64 + n) * 16, vu1 = vu0 + 2 * D * 16, sou = w * 56 * (D * 16);
    const unsigned vof = h * (S * 16) + (st2 * 64 + n) * 16, soo = w * 14 * (S * 16);
    const unsigned vfl = (FL_O + (st2 >> 1) * 8 + 2 * w + (lane & 1)) * 128;
    unsigned vwo[4];
#pragma unroll
    for (int j = 0; j < 4; j++) vwo[j] = swz * (D * 2) + swc + j * (8 * D * 2 - 1024);
    const unsigned sowo = w * 112 * (D * 2) + dt2 * 128;
    bf16x8 fa[18][4];
    u32x2 fl;
    auto ldD = [&](auto tt, auto jj) {
      constexpr int t = tt.value, j = jj.value;
      if constexpr (t < 14) fa[t][j] = ldc<(j & 1) * 512>(rsU, (j >> 1) ? vu1 : vu0, sou + t * (4 * D * 16));
      else if constexpr (j < g2ld(t)) fa[t][j] = ldc<(j & 1) * 512>(rsO, vof, soo + (2 * (t - 14) + (j >> 1)) * (2 * S * 16));
    };
    auto dmaS = [&](auto tt, auto jj) {
      constexpr int t = tt.value, j = jj.value;
      if constexpr (t < 14) dma1<j>(rsW2, v2, so2 + t * (32 * S * 2), area + ((t + 6) % 10) * 4096);
      else if constexpr (j < g2dm(t)) dma1<j>(rsWo, vwo, sowo + (t - 14) * (32 * D * 2), area + (t - 14) * 4096);
    };
    unroll<0, G2R>([&](auto tt) { unroll<0, 4>([&](auto jj) { ldD(tt, jj); }); });
    ldsp pb[2], pw[2];
#pragma unroll
    for (int a = 0; a < 2; a++) {
      const unsigned c = q * 128 + (((4 * a + 2 * blk + (pp >> 1)) ^ ((q >> 1) << 2)) * 16) + (pp & 1) * 8;
      pb[a] = lds + w * AREA + h * 1024 + c;
      pw[a] = lds + w * AREA + h * 512 + c;
    }
    f32x16 acc[2][2] = {};
    unroll<0, 18>([&](auto tt) {
      constexpr int t = tt.value, nk = t < 17 ? 2 : 1;
      constexpr int left = g2cnt(t - 3) + g2cnt(t - 2) + g2cnt(t - 1);
      if constexpr (nk == 2) waitv<left>(fa[t][0], fa[t][1], fa[t][2], fa[t][3]); else waitv<left>(fa[t][0], fa[t][1]);
      if constexpr (t == G2POLL + G2R) {
        tie(fl);
        if (!match(fl, ep)) poll(rsF, vfl, ep);
      }
      s16x4 lo[2][2], hi[2][2];
#pragma unroll
      for (int ks = 0; ks < nk; ks++)
#pragma unroll
        for (int a = 0; a < 2; a++) {
          if (t < 14) {
            lo[ks][a] = vtr(pb[a] + ((t + 6) % 10) * 4096 + ks * 2048); hi[ks][a] = vtr(pb[a] + ((t + 6) % 10) * 4096 + ks * 2048 + 512);
          } else {
            const int j = 2 * (t - 14) + ks;
            if (j < 6) {
              const int u = j % 3, rl = 56 * (j / 3) + (u == 0 ? 0 : u == 1 ? 16 : 36), rh = 56 * (j / 3) + (u == 0 ? 8 : u == 1 ? 28 : 44);
              lo[ks][a] = vtr(pw[a] + rl * 128); hi[ks][a] = vtr(pw[a] + rh * 128);
            } else {
              lo[ks][a] = vtr(pw[a] + h * (52 * 128) + 24 * 128); hi[ks][a] = vtr(pw[a] + h * (52 * 128) + 52 * 128);
            }
          }
        }
      SB();
      unroll<0, 4 * nk>([&](auto mm) {
        constexpr int m = mm.value, ks = m >> 2, nh = (m >> 1) & 1, mh = m & 1;
        if constexpr (m < 4) ldD(std::integral_constant<int, t + G2R>(), mm);
        else if constexpr (t >= 1) dmaS(std::integral_constant<int, t + 9>(), std::integral_constant<int, m - 4>());
        if constexpr (t == G2POLL && m == 7) fl = ldc2(rsF, vfl);
        if constexpr (t < 14) acc[nh][mh] = MFMA(fa[t][ks * 2 + mh], cat(lo[ks][nh], hi[ks][nh]), acc[nh][mh]);
        else acc[nh][mh] = MFMA(cat(lo[ks][mh], hi[ks][mh]), fa[t][ks * 2 + nh], acc[nh][mh]);
        SB();
      });
    });
    STAMP(14);
#pragma unroll
    for (int nh = 0; nh < 2; nh++)
#pragma unroll
      for (int mh = 0; mh < 2; mh++)
#pragma unroll
        for (int g = 0; g < 4; g++)
          lst(lds + w * AREA + (32 * nh + n) * 256 + (((8 * mh + 2 * g + h) ^ (n & 15)) * 16),
              (f32x4){acc[nh][mh][4 * g], acc[nh][mh][4 * g + 1], acc[nh][mh][4 * g + 2], acc[nh][mh][4 * g + 3]});
    bar();
    const rsrc_t sto = mkst(out, S * D * 2);
#pragma unroll
    for (int u = 0; u < 2; u++) {
      const int r = 16 * w + 8 * u + (lane >> 3);
      const unsigned c0 = r * 256 + (((2 * (lane & 7)) ^ (r & 15)) * 16), c1 = c0 ^ 16;
      f32x4 a = lld<f32x4>(lds + c0), bq = lld<f32x4>(lds + c1);
#pragma unroll
      for (int s = 1; s < 4; s++) { a += lld<f32x4>(lds + s * AREA + c0); bq += lld<f32x4>(lds + s * AREA + c1); }
      const u32x4 xu = __builtin_bit_cast(u32x4, u ? xr1 : xr0);
      const float bv = blo(u ? bs1 : bs0);
      u32x4 res = {pk(a[0] + bv + blo(xu[0]), a[1] + bv + bhi(xu[0])), pk(a[2] + bv + blo(xu[1]), a[3] + bv + bhi(xu[1])),
                   pk(bq[0] + bv + blo(xu[2]), bq[1] + bv + bhi(xu[2])), pk(bq[2] + bv + blo(xu[3]), bq[3] + bv + bhi(xu[3]))};
      __builtin_amdgcn_raw_buffer_store_b128(res, sto, vo + u * (8 * D * 2), 0, 16);
    }
    if (blockIdx.x == 255 && tid == 0) __builtin_amdgcn_raw_buffer_store_b64((u32x2){ep.lo, ep.hi}, mkst(ws, WS_FLAG_BYTES), 0, 0, 16);
  }
}

extern "C" void kernel_launch(void* const* d_in, const int* in_sizes, int n_in, void* d_out, int out_size, void* d_ws, size_t ws_size,
                              hipStream_t stream) {
  fused_kernel<<<256, 256, 0, stream>>>((const short*)d_in[0], (const short*)d_in[1], (const short*)d_in[2], (const short*)d_in[3],
                                        (const short*)d_in[5], (const short*)d_in[6], (const short*)d_in[7], (const short*)d_in[8],
                                        (short*)d_out, (char*)d_ws);
}
```

```cpp
#include <hip/hip_runtime.h>
#include <stdint.h>

typedef short bf16x8 __attribute__((ext_vector_type(8)));
typedef short s16x4 __attribute__((ext_vector_type(4)));
typedef float f32x2 __attribute__((ext_vector_type(2)));
typedef float f32x4 __attribute__((ext_vector_type(4)));
typedef float f32x16 __attribute__((ext_vector_type(16)));
typedef unsigned u32x2 __attribute__((ext_vector_type(2)));
typedef unsigned u32x4 __attribute__((ext_vector_type(4)));
typedef __bf16 b16x2 __attribute__((ext_vector_type(2)));
typedef __attribute__((address_space(3))) char* ldsp;
typedef __amdgpu_buffer_rsrc_t rsrc_t;

#define DEV __device__ __forceinline__
#define SB() __builtin_amdgcn_sched_barrier(0)
#define MFMA(a, b, c) __builtin_amdgcn_mfma_f32_32x32x16_bf16(a, b, c, 0, 0, 0)

constexpr int S = 2048, D = 448, F = 1792;
constexpr unsigned FL_P = 8, FL_R = FL_P + 128, FL_O = FL_R + 16, FL_U = FL_O + 128;
constexpr unsigned WS_FLAG_BYTES = 65536;
constexpr unsigned WS_ST = WS_FLAG_BYTES, SZ_ST = S * 16;
constexpr unsigned WS_UF = WS_ST + SZ_ST, SZ_UF = 224 * 448 * 16;
constexpr unsigned WS_OF = WS_UF + SZ_UF, SZ_OF = 56 * 2048 * 16;
constexpr unsigned WS_KVP = WS_OF + SZ_OF, SZ_KVP = 16 * 16 * 4096;
constexpr unsigned WS_KVF = WS_KVP + SZ_KVP, SZ_KVF = 16 * 8 * 1024;
constexpr unsigned WS_END = WS_KVF + SZ_KVF;
constexpr unsigned AREA = 40960, L_STAT = 36864, SLOT = 12288;
constexpr unsigned L_WK = 0, L_WV = 50176, L_WQ = 100352, L_WPHI = 150528, L_PHI = 0, L_VX = 32768;
constexpr unsigned OOB = 0x40000000u;
constexpr int SPIN = 1 << 18;

DEV u32x4 mkrs(const void* p, unsigned bytes) {
  unsigned long long a = (unsigned long long)p;
  u32x4 r = {(unsigned)a, (unsigned)(a >> 32), bytes, 0x00020000u};
  return r;
}
DEV rsrc_t mkst(void* p, unsigned bytes) { return __builtin_amdgcn_make_buffer_rsrc(p, 0, bytes, 0x00020000); }
DEV void stc(rsrc_t rs, unsigned voff, u32x4 v) { __builtin_amdgcn_raw_buffer_store_b128(v, rs, voff, 0, 16); }
template <int OFF> DEV bf16x8 ldp(u32x4 rs, unsigned voff, unsigned soff) {
  bf16x8 r;
  asm volatile("buffer_load_dwordx4 %0, %2, %1, %3 offen offset:%4" : "=v"(r) : "s"(rs), "v"(voff), "s"(soff), "n"(OFF) : "memory");
  return r;
}
template <int OFF> DEV bf16x8 ldc(u32x4 rs, unsigned voff, unsigned soff) {
  bf16x8 r;
  asm volatile("buffer_load_dwordx4 %0, %2, %1, %3 offen offset:%4 sc1" : "=v"(r) : "s"(rs), "v"(voff), "s"(soff), "n"(OFF) : "memory");
  return r;
}
template <int OFF> DEV u32x2 ldp2(u32x4 rs, unsigned voff, unsigned soff) {
  u32x2 r;
  asm volatile("buffer_load_dwordx2 %0, %2, %1, %3 offen offset:%4" : "=v"(r) : "s"(rs), "v"(voff), "s"(soff), "n"(OFF) : "memory");
  return r;
}
template <int OFF> DEV u32x4 lda(u32x4 rs, unsigned voff, unsigned soff) {
  u32x4 r;
  asm volatile("buffer_load_dwordx4 %0, %2, %1, %3 offen offset:%4" : "=a"(r) : "s"(rs), "v"(voff), "s"(soff), "n"(OFF) : "memory");
  return r;
}
template <int OFF, class T> DEV u32x4 ldap(u32x4 rs, unsigned voff, unsigned soff, T& pin) {
  u32x4 r;
  asm volatile("buffer_load_dwordx4 %0, %3, %2, %4 offen offset:%5" : "=a"(r), "+v"(pin) : "s"(rs), "v"(voff), "s"(soff), "n"(OFF) : "memory");
  return r;
}
template <int OFF> DEV void sta(unsigned addr, u32x4 v) { asm volatile("ds_write_b128 %0, %1 offset:%2" ::"v"(addr), "a"(v), "n"(OFF) : "memory"); }
DEV u32x2 ldc2(u32x4 rs, unsigned voff) {
  u32x2 r;
  asm volatile("buffer_load_dwordx2 %0, %2, %1, 0 offen sc1" : "=v"(r) : "s"(rs), "v"(voff) : "memory");
  return r;
}
template <int OFF> DEV unsigned ldu16(u32x4 rs, unsigned voff, unsigned soff) {
  unsigned r;
  asm volatile("buffer_load_ushort %0, %2, %1, %3 offen offset:%4" : "=v"(r) : "s"(rs), "v"(voff), "s"(soff), "n"(OFF) : "memory");
  return r;
}
DEV void setm0(unsigned a) { asm volatile("s_mov_b32 m0, %0\n\ts_nop 0" ::"s"(a) : "memory"); }
DEV void setm0n(unsigned a) { asm volatile("s_mov_b32 m0, %0" ::"s"(a) : "memory"); }
template <int OFF> DEV void addm0(unsigned a) { asm volatile("s_add_u32 m0, %0, %1" ::"s"(a), "n"(OFF) : "memory", "scc"); }
template <int OFF> DEV void dma(u32x4 rs, unsigned voff, unsigned soff) {
  asm volatile("buffer_load_dwordx4 %1, %0, %2 offen offset:%3 lds" ::"s"(rs), "v"(voff), "s"(soff), "n"(OFF) : "memory");
}
DEV void dma4(u32x4 rs, const unsigned (&v)[4], unsigned soff, unsigned m0) {
  setm0(m0); dma<0>(rs, v[0], soff); dma<1024>(rs, v[1], soff); dma<2048>(rs, v[2], soff); dma<3072>(rs, v[3], soff);
}
template <int J> DEV void dma1(u32x4 rs, const unsigned (&v)[4], unsigned soff, unsigned m0) {
  if (J == 0) setm0(m0);
  dma<J * 1024>(rs, v[J], soff);
}
template <int N> DEV void waitv() { asm volatile("s_waitcnt vmcnt(%0)" ::"n"(N) : "memory"); }
template <int N, class T> DEV void waitv(T& a) { asm volatile("s_waitcnt vmcnt(%1)" : "+v"(a) : "n"(N) : "memory"); }
template <int N, class T> DEV void waitv(T& a, T& b) { asm volatile("s_waitcnt vmcnt(%2)" : "+v"(a), "+v"(b) : "n"(N) : "memory"); }
template <int N, class T> DEV void waitv(T& a, T& b, T& c, T& d) {
  asm volatile("s_waitcnt vmcnt(%4)" : "+v"(a), "+v"(b), "+v"(c), "+v"(d) : "n"(N) : "memory");
}
template <class T> DEV void tie(T& a) { asm volatile("" : "+v"(a)::"memory"); }
template <class T> DEV void hold(T a) { asm volatile("" ::"v"(a)); }
template <class T, class... R> DEV void hold(T a, R... r) { hold(a); hold(r...); }
DEV void keep(f32x16 a) { asm volatile("" ::"v"(a)); }
DEV void bar() { asm volatile("s_waitcnt lgkmcnt(0)\n\ts_barrier" ::: "memory"); }
DEV void barv() { asm volatile("s_waitcnt vmcnt(0) lgkmcnt(0)\n\ts_barrier" ::: "memory"); }
DEV s16x4 vtr(ldsp p) { return __builtin_amdgcn_ds_read_tr16_b64_v4i16((__attribute__((address_space(3))) s16x4*)p); }
DEV bf16x8 cat(s16x4 a, s16x4 b) { return __builtin_shufflevector(a, b, 0, 1, 2, 3, 4, 5, 6, 7); }
DEV unsigned pk(float a, float b) {
  f32x2 v = {a, b};
  return __builtin_bit_cast(unsigned, __builtin_convertvector(v, b16x2));
}
DEV float blo(unsigned u) { return __builtin_bit_cast(float, u << 16); }
DEV float bhi(unsigned u) { return __builtin_bit_cast(float, u & 0xffff0000u); }
DEV void swap32(float& a, float& b) {
  auto r = __builtin_amdgcn_permlane32_swap(__builtin_bit_cast(unsigned, a), __builtin_bit_cast(unsigned, b), false, false);
  const unsigned r0 = r[0], r1 = r[1];
  a = __builtin_bit_cast(float, r0); b = __builtin_bit_cast(float, r1);
}
DEV void swap16(float& a, float& b) {
  auto r = __builtin_amdgcn_permlane16_swap(__builtin_bit_cast(unsigned, a), __builtin_bit_cast(unsigned, b), false, false);
  const unsigned r0 = r[0], r1 = r[1];
  a = __builtin_bit_cast(float, r0); b = __builtin_bit_cast(float, r1);
}
template <int C> DEV float ror(float z) {
  return __builtin_bit_cast(float, __builtin_amdgcn_update_dpp(0, __builtin_bit_cast(int, z), C, 0xf, 0xf, true));
}
DEV float rowsum(float z) { z += ror<0x128>(z); z += ror<0x124>(z); z += ror<0x122>(z); z += ror<0x121>(z); return z; }
DEV float rdl(float z, int l) { return __builtin_bit_cast(float, __builtin_amdgcn_readlane(__builtin_bit_cast(int, z), l)); }
template <class T> DEV void lst(ldsp p, T v) { *(__attribute__((address_space(3))) T*)p = v; }
template <class T> DEV T lld(ldsp p) { return *(__attribute__((address_space(3))) T*)p; }

struct Epoch { unsigned lo, hi; };
template <int N = 0> DEV void publish(char* ws, unsigned flag, Epoch e) {
  asm volatile("s_waitcnt vmcnt(%0) lgkmcnt(0)\n\ts_barrier" ::"n"(N) : "memory");
  if (threadIdx.x == 0) __builtin_amdgcn_raw_buffer_store_b64((u32x2){e.lo, e.hi}, mkst(ws, WS_FLAG_BYTES), flag * 128, 0, 16);
}
DEV bool match(u32x2 f, Epoch e) { return __all(f.x == e.lo && f.y == e.hi); }
DEV void poll(u32x4 rsF, unsigned voff, Epoch e) {
#pragma unroll 1
  for (int it = 0; it < SPIN; ++it) {
    u32x2 f = ldc2(rsF, voff);
    waitv<0>(f);
    if (match(f, e)) break;
    __builtin_amdgcn_s_sleep(1);
  }
}

DEV void pollm(u32x4 rsF, unsigned voff, Epoch e, unsigned need, unsigned& seen) {
  const unsigned long long e64 = ((unsigned long long)e.hi << 32) | e.lo;
  unsigned cnt, t;
  u32x2 f;
  asm volatile(
      "s_mov_b32 %1, %8\n"
      "1:\n\ts_and_b32 %2, %0, %7\n\ts_cmp_eq_u32 %2, %7\n\ts_cbranch_scc1 2f\n\t"
      "buffer_load_dwordx2 %3, %5, %4, 0 offen sc1\n\ts_waitcnt vmcnt(0)\n\t"
      "v_cmp_eq_u64 vcc, %6, %3\n\ts_nop 4\n\ts_mov_b32 %0, vcc_lo\n\ts_sub_u32 %1, %1, 1\n\ts_cbranch_scc0 1b\n"
      "2:"
      : "+s"(seen), "=&s"(cnt), "=&s"(t), "=&v"(f)
      : "s"(rsF), "v"(voff), "s"(e64), "s"(need), "n"(SPIN)
      : "memory", "vcc", "scc");
}

DEV void acc_stats(bf16x8 v, float& s, float& q) {
  const u32x4 u = __builtin_bit_cast(u32x4, v);
#pragma unroll
  for (int e = 0; e < 4; e++) {
    const unsigned t = u[e];
    const b16x2 p = __builtin_bit_cast(b16x2, t);
    s = __builtin_amdgcn_fdot2_f32_bf16(p, __builtin_bit_cast(b16x2, 0x3f803f80u), s, false);
    q = __builtin_amdgcn_fdot2_f32_bf16(p, p, q, false);
  }
}
DEV unsigned affine2(unsigned u, float a, float c) {
  float t0, t1;
  unsigned r;
  asm("v_lshlrev_b32 %1, 16, %3\n\tv_and_b32 %2, 0xffff0000, %3\n\tv_fma_f32 %1, %1, %4, %5\n\tv_fma_f32 %2, %2, %4, %5\n\tv_cvt_pk_bf16_f32 %0, %1, %2"
      : "=v"(r), "=&v"(t0), "=&v"(t1)
      : "v"(u), "v"(a), "v"(c));
  return r;
}
DEV f32x16 mfmav(u32x4 a, bf16x8 b, f32x16 c) {
  f32x16 r;
  asm volatile("v_mfma_f32_32x32x16_bf16 %0, %1, %2, %3" : "=&v"(r) : "v"(a), "v"(b), "v"(c));
  return r;
}
DEV void mfmac(f32x16& c, bf16x8 a, u32x4 b) { asm volatile("v_mfma_f32_32x32x16_bf16 %0, %1, %2, %0" : "+v"(c) : "v"(a), "v"(b)); }
DEV void touch(unsigned& sink, u32x4 rs, unsigned voff, unsigned soff) {
  asm volatile("buffer_load_dword %0, %2, %1, %3 offen" : "+v"(sink) : "s"(rs), "v"(voff), "s"(soff) : "memory");
}
DEV void touchc(unsigned& sink, u32x4 rs, unsigned voff, unsigned soff) {
  asm volatile("buffer_load_dword %0, %2, %1, %3 offen sc1" : "+v"(sink) : "s"(rs), "v"(voff), "s"(soff) : "memory");
}
DEV void touchl(u32x4 rs, unsigned voff, unsigned soff) {
  asm volatile("buffer_load_dword %1, %0, %2 offen lds" ::"s"(rs), "v"(voff), "s"(soff) : "memory");
}
template <int N> using IC = std::integral_constant<int, N>;
template <int T, int N, class Fn> DEV void unroll(Fn&& f) {
  if constexpr (T < N) { f(std::integral_constant<int, T>()); unroll<T + 1, N>(f); }
}
template <int OFF, class T> DEV void dmap(u32x4 rs, unsigned voff, unsigned soff, T& pin) {
  asm volatile("buffer_load_dwordx4 %2, %1, %3 offen offset:%4 lds" : "+v"(pin) : "s"(rs), "v"(voff), "s"(soff), "n"(OFF) : "memory");
}

template <class Fn> DEV void features(const f32x16& p, bool h, unsigned (&ph)[2][8], Fn&& cb) {
  constexpr float L = 1.4426950408889634f;
  float mx = fmaxf(p[0], p[1]), mn = fminf(p[0], p[1]);
#pragma unroll
  for (int r = 2; r < 12; r += 2) { mx = fmaxf(fmaxf(mx, p[r]), p[r + 1]); mn = fminf(fminf(mn, p[r]), p[r + 1]); }
  cb(IC<0>(), mx);
  float mx2 = fmaxf(fmaxf(fmaxf(fmaxf(mx, p[12]), p[13]), p[14]), p[15]);
  float mn2 = fminf(fminf(fminf(fminf(mn, p[12]), p[13]), p[14]), p[15]);
  mx = h ? mx : mx2; mn = h ? -mn : -mn2;
  swap32(mx, mn);
  float c0 = -L * fmaxf(mx, mn), c1 = c0;
  cb(IC<1>(), c0);
  swap32(c0, c1);
  const float ninf = -__builtin_inff();
  const float d0 = h ? ninf : c0, d1 = h ? ninf : c1;
  float e[2][16], s0 = 0.f, s1 = 0.f;
  unroll<0, 16>([&](auto rr) {
    constexpr int r = rr.value;
    float a0 = fmaf(p[r], L, r < 12 ? c0 : d0);
    if constexpr (r & 1) cb(IC<2 + r / 2>(), a0);
    e[0][r] = __builtin_amdgcn_exp2f(a0);
    e[1][r] = __builtin_amdgcn_exp2f(fmaf(p[r], -L, r < 12 ? c1 : d1));
    s0 += e[0][r]; s1 += e[1][r];
  });
  swap32(s0, s1);
  float i0 = __builtin_amdgcn_rcpf(s0 + s1), i1 = i0;
  swap32(i0, i1);
  unroll<0, 8>([&](auto rr) {
    constexpr int r = rr.value;
    ph[0][r] = pk(e[0][2 * r] * i0, e[0][2 * r + 1] * i0); ph[1][r] = pk(e[1][2 * r] * i1, e[1][2 * r + 1] * i1);
    if constexpr ((r & 3) == 1) cb(IC<10 + r / 4>(), ph[1][r]);
  });
}
DEV bf16x8 pack8(const f32x16& a, int o) {
  u32x4 u = {pk(a[o], a[o + 1]), pk(a[o + 2], a[o + 3]), pk(a[o + 4], a[o + 5]), pk(a[o + 6], a[o + 7])};
  return __builtin_bit_cast(bf16x8, u);
}
DEV float gelu(float a) {
  constexpr float c1 = -2.302208198f, c2 = c1 * 0.044715f;
  return a * __builtin_amdgcn_rcpf(1.f + __builtin_amdgcn_exp2f(a * fmaf(a * a, c2, c1)));
}

#define STAMP(n)

constexpr int woff(int k) { return (k >> 1) * SLOT + (k & 1) * 8192; }
constexpr int xoff(int k) { return (k >> 1) * SLOT + 4096 + (k & 1) * 2048; }
constexpr int G2R = 4, G2POLL = 5;
constexpr int g2ld(int t) { return t < 17 ? 4 : (t == 17 ? 2 : 0); }
constexpr int g2cnt(int s) { return s < 0 ? 4 : g2ld(s + G2R) + (s == G2POLL ? 1 : 0) + (s == 14 ? 4 : 0); }

__global__ __launch_bounds__(256, 1) void fused_kernel(const short* __restrict__ x, const short* __restrict__ wqkv, const short* __restrict__ wphi,
                                                       const short* __restrict__ wo, const short* __restrict__ w1, const short* __restrict__ b1,
                                                       const short* __restrict__ w2, const short* __restrict__ b2, short* __restrict__ out,
                                                       char* __restrict__ ws) {
  __shared__ __attribute__((aligned(16))) char lds_[163840];
  const ldsp lds = (ldsp)lds_;
  const unsigned lds0 = (unsigned)(uintptr_t)lds;
  const int tid = threadIdx.x, lane = tid & 63, w = __builtin_amdgcn_readfirstlane(tid >> 6);
  asm volatile("s_nop 4" ::"s"(w), "s"(x), "s"(wqkv), "s"(wphi), "s"(wo), "s"(w1), "s"(b1), "s"(w2), "s"(b2), "s"(out), "s"(ws) : "memory");
  const int xc = blockIdx.x & 7, i = blockIdx.x >> 3;
  const int h = lane >> 5, n = lane & 31, q = (lane & 15) >> 2, pp = lane & 3, blk = (lane >> 4) & 1;
  const u32x4 rsF = mkrs(ws, WS_FLAG_BYTES), rsX = mkrs(x, S * D * 2), rsW2 = mkrs(w2, F * S * 2);
  const unsigned area = lds0 + w * AREA;
  const int tile2 = 28 * (xc & 1) + i - 4, dt2 = tile2 >> 3, st2 = 8 * (xc >> 1) + (tile2 & 7);
  const unsigned swz = (lane >> 3), swc = ((lane & 7) ^ (((lane >> 4) & 1) << 2)) * 16;
  unsigned v2[4];
#pragma unroll
  for (int j = 0; j < 4; j++) v2[j] = i >= 4 ? swz * (S * 2) + swc + j * (8 * S * 2 - 1024) : OOB;
  const unsigned so2 = (w * 448) * (S * 2) + st2 * 128;
  auto pre2 = [&](int t0, int t1, int slot0) {
#pragma unroll 1
    for (int t = t0; t < t1; t++) dma4(rsW2, v2, so2 + t * (32 * S * 2), area + (slot0 + t - t0) * 4096);
  };

  ldsp pb[2];
#pragma unroll
  for (int a = 0; a < 2; a++) pb[a] = lds + w * AREA + h * 1024 + q * 128 + (((4 * a + 2 * blk + (pp >> 1)) ^ ((q >> 1) << 2)) * 16) + (pp & 1) * 8;
  bf16x8 w2r[4][2][2];
  auto pre2w = [&]() {
#pragma unroll
    for (int t = 0; t < 4; t++) asm volatile("s_waitcnt lgkmcnt(0)" : "+v"(w2r[t][0][0]), "+v"(w2r[t][0][1]), "+v"(w2r[t][1][0]), "+v"(w2r[t][1][1])::"memory");
  };
  auto pre2rd = [&]() {
#pragma unroll
    for (int t = 0; t < 4; t++)
#pragma unroll
      for (int ks = 0; ks < 2; ks++)
#pragma unroll
        for (int a = 0; a < 2; a++) w2r[t][ks][a] = cat(vtr(pb[a] + (t + 6) * 4096 + ks * 2048), vtr(pb[a] + (t + 6) * 4096 + ks * 2048 + 512));
  };

  const bool isA = i >= 16;
  const int T = i - 16, pr = xc;
  const u32x4 rsW1 = mkrs(w1, S * F * 2);
  const unsigned vt1 = xc < 7 ? (lane >> 2) * (F * 2) + (lane & 3) * 128 : OOB;
  auto so_t1 = [&](int stage, int wq, int r0) { return (512 * wq + ((64 * xc + 32 * stage + r0) & 511)) * (F * 2) + xc * 512; };
  Epoch ep;
  auto setep = [&](u32x2 gen) {
    const unsigned long long g = (((unsigned long long)__builtin_amdgcn_readfirstlane(gen.y) << 32) | (unsigned)__builtin_amdgcn_readfirstlane(gen.x)) + 1;
    ep.lo = (unsigned)g; ep.hi = (unsigned)(g >> 32);
  };

  if (!isA) {
    const int row0 = 512 * (i >> 2) + 64 * xc + 16 * (i & 3) + 4 * w;
    const bool isG = xc < 7 && i < 14;
    const int ft = 2 * xc + (i >= 7), dt = i >= 7 ? i - 7 : i;
    bf16x8 xv[4];
    u32x2 gen, bb[4];
    unsigned vz = 0;
    tie(vz);
    const unsigned vo = lane < 56 ? lane * 16 : OOB, vt = lane < 32 ? vt1 : OOB, vb = h * 8;
    {
      const unsigned so = row0 * (D * 2);
      xv[0] = ldp<0>(rsX, vo, so); xv[1] = ldp<896>(rsX, vo, so); xv[2] = ldp<1792>(rsX, vo, so); xv[3] = ldp<2688>(rsX, vo, so);
      gen = ldc2(rsF, vz);
      setm0(area + L_STAT);
      touchl(rsW1, vt, so_t1(i >> 2, i & 3, 8 * w));
      const u32x4 rsB = mkrs(b1, F * 2);
      const unsigned sb = (ft * 128 + w * 32) * 2;
      bb[0] = ldp2<0>(rsB, vb, sb); bb[1] = ldp2<16>(rsB, vb, sb); bb[2] = ldp2<32>(rsB, vb, sb); bb[3] = ldp2<48>(rsB, vb, sb);
    }
    unsigned v1[4][4], vx[4][2];
#pragma unroll
    for (int ks = 0; ks < 4; ks++) {
#pragma unroll
      for (int j = 0; j < 4; j++) v1[ks][j] = (16 * ks + 8 * (j & 1) + swz) * (F * 2) + (j >> 1) * 128 + swc - j * 1024;
#pragma unroll
      for (int j = 0; j < 2; j++) vx[ks][j] = (16 * ks + 8 * j + swz) * (D * 2) + swc - (j + 2 * (ks & 1)) * 1024;
    }
    waitv<5>(xv[0], xv[1], xv[2], xv[3]);
    tie(gen);
    STAMP(0);
    setep(gen);
    const unsigned tagB = ep.lo ^ ep.hi, vs = (lane & 15) ? OOB : (row0 + h + 2 * blk) * 16;
    u32x4 rec;
    {
      float s[4] = {}, z[4] = {};
#pragma unroll
      for (int r = 0; r < 4; r++) acc_stats(xv[r], s[r], z[r]);
      swap32(s[0], s[1]); swap32(s[2], s[3]); swap32(z[0], z[1]); swap32(z[2], z[3]);
      float sa = s[0] + s[1], sb = s[2] + s[3], za = z[0] + z[1], zb = z[2] + z[3];
      swap16(sa, sb); swap16(za, zb);
      const float mu = rowsum(sa + sb) * (1.f / D), ms = rowsum(za + zb) * (1.f / D);
      const float var = fmaxf(ms - mu * mu, 0.f);
      float a = __builtin_amdgcn_rsqf(var + 1e-5f);
      a *= __builtin_amdgcn_rsqf(var * a * a + 1e-5f);
      const unsigned a2 = pk(a, a);
      const float c = -mu * blo(a2);
      rec = (u32x4){a2, ep.lo, __builtin_bit_cast(unsigned, c), tagB};
      stc(mkst(ws + WS_ST, SZ_ST), vs, rec);
    }
    asm volatile("s_barrier" ::: "memory");
    hold(vz, vo, vt, vb);
    STAMP(1);

    if (isG) {
      const unsigned b1s = (w * 512) * (F * 2) + ft * 256, bxs = (w * 512) * (D * 2) + dt * 128;
      auto mset = [&](auto kk, auto jj) {
        constexpr int ks = kk.value, j = jj.value;
        if constexpr ((ks & 1) ? j == 2 : j == 0) setm0n(area + woff(ks));
        if constexpr (!(ks & 1) && j == 4) setm0n(area + xoff(ks));
      };
      auto dgrp = [&](auto kk, auto jj, unsigned s1, unsigned sx) {
        constexpr int ks = kk.value, j = jj.value, jw = (ks & 1) ? j - 2 : j, jx = (ks & 1) ? j : j - 4;
        if constexpr (jw >= 0 && jw < 4) dma<jw * 1024>(rsW1, v1[ks][jw], s1);
        else dma<(jx + 2 * (ks & 1)) * 1024>(rsX, vx[ks][jx], sx);
      };
      auto fill = [&](auto kk) {
        unroll<0, 6>([&](auto jj) { mset(kk, jj); asm volatile("s_nop 0"); dgrp(kk, jj, b1s + 64 * xc * (F * 2), bxs + 64 * xc * (D * 2)); });
      };
      const u32x4 rsS = mkrs(ws + WS_ST, SZ_ST);
      auto gather = [&]() {
        bf16x8 g[8];
#pragma unroll
        for (int j = 0; j < 8; j++) g[j] = ldc<0>(rsS, (lane * 16 + (j + xc) * 1024) & 8191, w * 8192);
        waitv<0>();
        bool ok = true;
#pragma unroll
        for (int j = 0; j < 8; j++) {
          tie(g[j]);
          const u32x4 u = __builtin_bit_cast(u32x4, g[j]);
          ok &= u[1] == ep.lo && u[3] == tagB;
          const ldsp p = lds + w * AREA + L_STAT + j * 512 + (lane >> 4) * 128 + (lane & 15) * 4;
          lst(p, u[2]); lst(p + 64, u[0]);
        }
        return (bool)__all(ok);
      };
      unroll<0, 4>(fill);
      hold(vs, rec);
      STAMP(2);
#pragma unroll 1
      for (int it = 0; it < SPIN; ++it)
        if (gather()) break;
      STAMP(3);
      tie(bb[0]); tie(bb[1]); tie(bb[2]); tie(bb[3]);
      f32x16 acc[4][2];
#pragma unroll
      for (int g = 0; g < 4; g++) {
        acc[0][0][4 * g] = blo(bb[g].x); acc[0][0][4 * g + 1] = bhi(bb[g].x); acc[0][0][4 * g + 2] = blo(bb[g].y); acc[0][0][4 * g + 3] = bhi(bb[g].y);
      }
      acc[0][1] = acc[0][0];
#pragma unroll
      for (int a = 1; a < 4; a++) { acc[a][0] = (f32x16)(0.f); acc[a][1] = (f32x16)(0.f); }
      unsigned pa[4], px[2], msk[4], psc = area + L_STAT + h * 32, psa;
      {
        const unsigned rowb = area + (8 * h + q) * 128 + (pp & 1) * 8, ch = 2 * blk + (pp >> 1), fl = (q >> 1) << 2;
#pragma unroll
        for (int a = 0; a < 4; a++) pa[a] = rowb + ((a ^ w) >> 1) * 2048 + ((4 * ((a ^ w) & 1) + ch) ^ fl) * 16;
        px[0] = rowb + (ch ^ fl) * 16; px[1] = rowb + ((4 + ch) ^ fl) * 16;
        const unsigned r = 4 * ((lane >> 3) & 1) + pp, on = (!blk && (q & 1) == h) ? ((r & 1) ? 0xffff0000u : 0xffffu) : 0u;
#pragma unroll
        for (int j = 0; j < 4; j++) { msk[j] = (r >> 1) == j ? on : 0u; tie(msk[j]); }
        psa = area + L_STAT + 64 + (8 * h + r) * 4;
      }
      s16x4 wl[2][4], wh[2][4], xl[2], xh[2];
      u32x4 hf[2][2], dg;
      unsigned av;
      f32x16 hn[2], cc = {};
      tie(cc);
      auto rdX = [&](auto nn, auto dd) {
        constexpr int o = xoff(nn.value & 3), d = dd.value;
        xl[d] = vtr((ldsp)(uintptr_t)px[d] + o); xh[d] = vtr((ldsp)(uintptr_t)px[d] + o + 512);
      };
      auto rdA = [&](auto nn) { av = lld<unsigned>((ldsp)(uintptr_t)psa + nn.value * 128); };
      auto rdC = [&](auto nn) {
        const f32x4 c0 = lld<f32x4>((ldsp)(uintptr_t)psc + nn.value * 128), c1 = lld<f32x4>((ldsp)(uintptr_t)psc + nn.value * 128 + 16);
#pragma unroll
        for (int e = 0; e < 4; e++) { cc[e] = c0[e]; cc[4 + e] = c1[e]; }
      };
      auto rdW = [&](auto nn, auto aa) {
        constexpr int o = woff(nn.value & 3), nx = nn.value & 1, a = aa.value;
        wl[nx][a] = vtr((ldsp)(uintptr_t)pa[a] + o); wh[nx][a] = vtr((ldsp)(uintptr_t)pa[a] + o + 512);
      };
      auto mkD = [&]() { dg = (u32x4){av & msk[0], av & msk[1], av & msk[2], av & msk[3]}; };
      auto nrm = [&](auto dd) { hn[dd.value] = mfmav(dg, cat(xl[dd.value], xh[dd.value]), cc); };
      auto cvt = [&](auto nn, auto dd) {
        constexpr int d = dd.value;
        hf[nn.value & 1][d] = (u32x4){pk(hn[d][0], hn[d][1]), pk(hn[d][2], hn[d][3]), pk(hn[d][4], hn[d][5]), pk(hn[d][6], hn[d][7])};
        SB();
        keep(hn[d]);
      };
      auto step = [&](auto kk, auto wn, auto last, auto&& ms, auto&& gm) {
        constexpr int ks = kk.value;
        constexpr bool more = !last.value;
        using NX = IC<ks + 1>;
        auto mm = [&](auto m_) {
          constexpr int a = more ? m_.value >> 1 : ((m_.value >> 1) + 1) & 3, dh = m_.value & 1;
          if constexpr (a == 0) mfmac(acc[0][dh], cat(wl[ks & 1][0], wh[ks & 1][0]), hf[ks & 1][dh]);
          else acc[a][dh] = MFMA(cat(wl[ks & 1][a], wh[ks & 1][a]), __builtin_bit_cast(bf16x8, hf[ks & 1][dh]), acc[a][dh]);
          SB();
          if constexpr (!more && m_.value >= 2) {
            constexpr int c = m_.value >> 1;
#pragma unroll
            for (int g = 0; g < 4; g++)
              lst(lds + w * AREA + lane * 16 + (c - 1) * 8192 + dh * 4096 + g * 1024, (f32x4){acc[c][dh][4 * g], acc[c][dh][4 * g + 1], acc[c][dh][4 * g + 2], acc[c][dh][4 * g + 3]});
            SB();
          }
        };
        __builtin_amdgcn_s_waitcnt(0x70 | wn.value);
        asm volatile("" ::: "memory");
        SB();
        if constexpr (more) { rdX(NX(), IC<0>()); rdA(NX()); }
        ms(IC<0>());
        SB();
        mm(IC<0>());
        gm(IC<0>());
        if constexpr (more) { rdC(NX()); rdX(NX(), IC<1>()); SB(); }
        mm(IC<1>());
        gm(IC<1>());
        ms(IC<2>());
        if constexpr (more) { rdW(NX(), IC<0>()); rdW(NX(), IC<1>()); SB(); }
        mm(IC<2>());
        if constexpr (more) { mkD(); SB(); }
        gm(IC<2>());
        if constexpr (more) { rdW(NX(), IC<2>()); SB(); nrm(IC<0>()); SB(); rdW(NX(), IC<3>()); SB(); }
        mm(IC<3>());
        if constexpr (more) { nrm(IC<1>()); SB(); }
        gm(IC<3>());
        ms(IC<4>());
        mm(IC<4>());
        if constexpr (more) { cvt(NX(), IC<0>()); SB(); }
        mm(IC<5>());
        gm(IC<4>());
        mm(IC<6>());
        if constexpr (more) { cvt(NX(), IC<1>()); SB(); }
        gm(IC<5>());
        mm(IC<7>());
      };
      asm volatile("s_waitcnt lgkmcnt(0)" ::: "memory");
      rdX(IC<0>(), IC<0>()); rdX(IC<0>(), IC<1>()); rdA(IC<0>()); rdC(IC<0>());
      unroll<0, 4>([&](auto jj) { rdW(IC<0>(), jj); });
      mkD();
      SB();
      asm volatile("s_nop 1");
      nrm(IC<0>()); nrm(IC<1>());
      asm volatile("s_nop 7\n\ts_nop 7");
      cvt(IC<0>(), IC<0>()); cvt(IC<0>(), IC<1>());
      SB();
      unsigned tk = 64 * xc;
#pragma unroll 1
      for (int t = 0; t < 7; t++) {
        tk = (tk + 64) & 511;
        const unsigned s1 = b1s + tk * (F * 2), sx = bxs + tk * (D * 2);
        unroll<0, 4>([&](auto kk) {
          step(kk, IC<12>(), IC<0>(), [&](auto jj) { mset(kk, jj); SB(); }, [&](auto jj) { dgrp(kk, jj, s1, sx); SB(); });
        });
        psc += 512; tie(psc); psa += 512; tie(psa);
        SB();
      }
      {
        unroll<0, 4>([&](auto kk) {
          constexpr int ks = kk.value;
          step(kk, IC<(ks == 3 ? 12 : 12 - 2 * ks)>(), IC<(ks == 3)>(),
               [&](auto jj) { if constexpr (jj.value == 0) { setm0n(area + (6 + ks) * 4096); SB(); } },
               [&](auto jj) { if constexpr (jj.value < 4) { dma<jj.value * 1024>(rsW2, v2[jj.value], so2 + ks * (32 * S * 2)); SB(); } });
        });
      }
      STAMP(4);
      bar();
      STAMP(5);
      {
        const rsrc_t st = mkst(ws + WS_UF, SZ_UF);
        const unsigned vo = (ft * 16 + w * 4 + h) * (D * 16) + (dt * 64 + n) * 16;
        f32x4 v[2][3][4];
        u32x4 res[4];
#pragma unroll
        for (int dh = 0; dh < 2; dh++)
#pragma unroll
          for (int a = 1; a < 4; a++)
#pragma unroll
            for (int g = 0; g < 4; g++) v[dh][a - 1][g] = lld<f32x4>(lds + (w ^ a) * AREA + lane * 16 + (a - 1) * 8192 + dh * 4096 + g * 1024);
        SB();
#pragma unroll
        for (int dh = 0; dh < 2; dh++) {
#pragma unroll
          for (int g = 0; g < 4; g += 2) {
            float r[8];
#pragma unroll
            for (int e = 0; e < 8; e++) {
              const int gg = g + (e >> 2), c = e & 3;
              r[e] = gelu((acc[0][dh][4 * gg + c] + v[dh][0][gg][c]) + (v[dh][1][gg][c] + v[dh][2][gg][c]));
            }
            auto r0 = __builtin_amdgcn_permlane32_swap(pk(r[0], r[1]), pk(r[4], r[5]), false, false);
            auto r1 = __builtin_amdgcn_permlane32_swap(pk(r[2], r[3]), pk(r[6], r[7]), false, false);
            res[2 * dh + g / 2] = (u32x4){r0[0], r1[0], r0[1], r1[1]};
            stc(st, vo + g * (D * 16) + dh * 512, res[2 * dh + g / 2]);
          }
        }
        STAMP(8);
        waitv<4>();
        pre2rd();
        pre2w();
        hold(res[0], res[1], res[2], res[3], vo);
      }
      dma4(rsW2, v2, so2 + 10 * (32 * S * 2), area + 6 * 4096);
      STAMP(6);
      publish<4>(ws, FL_U + dt * 14 + ft, ep);
      STAMP(7);
      pre2(4, 10, 0);
      pre2(11, 14, 7);
    } else {
      if (xc < 7) {
#pragma unroll 1
        for (int m = 4; m < 9; m++) touchl(rsW1, vt1, so_t1(m, 2 * (i - 14) + (w >> 1), 16 * (w & 1)));
      }
      if (i >= 14) {
#pragma unroll 1
        for (int m = 0; m < 4; m++) touchl(mkrs(wo, D * D * 2), (lane >> 2) * (D * 2) + (lane & 3) * 128, (128 * m + 64 * (i - 14) + 16 * w) * (D * 2) + (xc & 1) * 384);
        const int head = 2 * xc + i - 14;
        const u32x4 rsP = mkrs(ws + WS_KVP, SZ_KVP);
        const unsigned so = head * 65536 + w * 1024, vf = (FL_P + xc * 16 + (lane & 15)) * 128;
        bf16x8 pv[16];
        float a[8] = {};
        unroll<0, 5>([&](auto gg) {
          constexpr int t = 4 * gg.value;
          if constexpr (t < 16) {
            if constexpr (t == 0) poll(rsF, vf, ep);
            if constexpr (t == 12) STAMP(3);
            unroll<t, t + 4>([&](auto tt) { pv[tt.value] = ldc<0>(rsP, lane * 16, so + tt.value * 4096); });
          }
          if constexpr (t > 0) {
            waitv<(t < 16 ? 4 : 0)>(pv[t - 4], pv[t - 3], pv[t - 2], pv[t - 1]);
#pragma unroll
            for (int j = t - 4; j < t; j++) {
              const u32x4 u = __builtin_bit_cast(u32x4, pv[j]);
#pragma unroll
              for (int e = 0; e < 4; e++) { const unsigned v = u[e]; a[2 * e] += blo(v); a[2 * e + 1] += bhi(v); }
            }
            SB();
          }
        });
        const rsrc_t st = mkst(ws + WS_KVF, SZ_KVF);
        const u32x4 r0 = {pk(a[0], a[1]), ep.lo, pk(a[2], a[3]), tagB}, r1 = {pk(a[4], a[5]), ep.lo, pk(a[6], a[7]), tagB};
        stc(st, head * 8192 + w * 2048 + lane * 16, r0);
        stc(st, head * 8192 + w * 2048 + 1024 + lane * 16, r1);
        hold(r0, r1);
        STAMP(4);
      } else {
        __builtin_amdgcn_s_sleep(127);
      }
      pre2(0, 4, 6); pre2(4, 10, 0);
      waitv<24>();
      pre2rd();
      pre2w();
      pre2(10, 14, 6);
    }
  } else {
    const unsigned ring = lds0 + L_WQ + w * 12544, sxa = (128 * T + 32 * w) * (D * 2);
    unsigned vxa[4];
#pragma unroll
    for (int j = 0; j < 4; j++) vxa[j] = (8 * j + swz) * (D * 2) + (((lane & 7) ^ ((4 * j + (lane >> 4)) & 7)) * 16) - j * 1024;
    unsigned sink = 0;
    u32x2 gen = ldc2(rsF, 0);
    dma4(rsX, vxa, sxa, ring);
    const u32x4 rsW = mkrs(wqkv, D * 3 * D * 2);
    {
      const unsigned r6 = (lane * 43) >> 8, c6 = lane - 6 * r6;
      touch(sink, rsW, lane < 42 ? r6 * 2688 + (c6 >> 1) * 896 + (c6 & 1) * 108 : OOB, (28 * T + 7 * w) * 2688 + pr * 112);
    }
    dma4(rsX, vxa, sxa + 128, ring + 4096);
    dma4(rsX, vxa, sxa + 256, ring + 8192);
    unsigned vw[8];
#pragma unroll
    for (int s = 0; s < 8; s++) {
      const unsigned c = (s < 7 ? 256 * s + 64 * w : 3072) + lane, r = (c * 9363) >> 16;
      vw[s] = ((r & ~15u) + 8 * ((r >> 1) & 1) + 4 * (r & 1) + ((r >> 2) & 1) + 2 * ((r >> 3) & 1)) * 2688 + (c - 7 * r) * 16;
    }
    const unsigned mw = lds0 + w * 1024;
    auto mW = [&](auto ss, auto bb) {
      if constexpr (ss.value < 12) addm0<bb.value + ss.value * 4096>(mw); else addm0<bb.value + 49152>(lds0);
    };
    auto dmaW = [&](auto ss, unsigned col) {
      constexpr int s = ss.value;
      dma<0>(rsW, vw[s < 12 ? (s + 14) % 7 : 7], col + pr * 112 + (s >= 7 && s < 12 ? 256 * 2688 : 0));
    };
    bf16x8 xa[28];
    float sa = 0.f, sq = 0.f;
    {
      unsigned pr_[4];
#pragma unroll
      for (int k = 0; k < 4; k++) pr_[k] = ring + n * 128 + (((2 * k + h) ^ ((n >> 1) & 7)) * 16);
      unroll<0, 7>([&](auto cc) {
        constexpr int cb = cc.value;
        waitv<(cb == 0 ? 9 : 8)>();
#pragma unroll
        for (int k = 0; k < 4; k++) xa[4 * cb + k] = lld<bf16x8>((ldsp)(uintptr_t)pr_[k] + (cb % 3) * 4096);
        asm volatile("s_waitcnt lgkmcnt(0)" : "+v"(xa[4 * cb]), "+v"(xa[4 * cb + 1]), "+v"(xa[4 * cb + 2]), "+v"(xa[4 * cb + 3])::"memory");
        if constexpr (cb < 4) dma4(rsX, vxa, sxa + (cb + 3) * 128, ring + (cb % 3) * 4096);
        if constexpr (cb == 4) unroll<0, 4>([&](auto ss) { mW(ss, IC<L_WK>()); asm volatile("s_nop 0"); dmaW(ss, 896); });
        if constexpr (cb == 5) unroll<0, 4>([&](auto ss) { mW(ss, IC<L_WV>()); asm volatile("s_nop 0"); dmaW(ss, 1792); });
        if constexpr (cb == 6) {
          setm0(lds0 + L_WPHI + w * 1024);
          dma<0>(mkrs(wphi + pr * 1568, 3136), tid * 16, 0);
        }
        SB();
#pragma unroll
        for (int k = 0; k < 4; k++) acc_stats(xa[4 * cb + k], sa, sq);
        SB();
      });
    }
    STAMP(1);
    hold(vxa[0], vxa[1], vxa[2], vxa[3]);
    tie(gen);
    setep(gen);
    float la, lc;
    {
      float s2 = sa, q2 = sq;
      swap32(sa, s2); swap32(sq, q2);
      const float mu = (sa + s2) * (1.f / D), var = fmaxf((sq + q2) * (1.f / D) - mu * mu, 0.f);
      la = __builtin_amdgcn_rsqf(var + 1e-5f);
      lc = -mu * la;
    }
    auto norm = [&](auto kk, auto ee) {
      constexpr int ks = kk.value, e = ee.value;
      if constexpr (ks < 28) {
        u32x4 u = __builtin_bit_cast(u32x4, xa[ks]);
        u[e] = affine2(u[e], la, lc);
        xa[ks] = __builtin_bit_cast(bf16x8, u);
      }
    };
    unroll<0, 4>([&](auto ee) { norm(IC<0>(), ee); });
    norm(IC<1>(), IC<0>());
    tie(xa[0]); tie(xa[1]);
    waitv<1>();
    bar();
    STAMP(3);
    const unsigned vt2 = (lane >> 3) * (S * 2) + (lane & 7) * 128;
    auto touch2 = [&](int m) { touch(sink, rsW2, vt2, (448 * w + 128 * m + 8 * T) * (S * 2) + (xc >> 1) * 1024); };
    const unsigned wlane = (2 * h + 4 * q) * 112 + 32 * blk + 8 * pp;
    f32x16 kT[2] = {}, vv[2] = {};
    {
      const ldsp pk_ = lds + L_WK + wlane, pv_ = lds + L_WV + wlane;
      s16x4 fr[30][8];
      auto rd = [&](auto kk, auto cc) {
        constexpr int ks = kk.value, c = cc.value;
        if constexpr (ks < 28) fr[ks][c] = vtr(((c & 4) ? pv_ : pk_) + ks * 1792 + ((c >> 1) & 1) * 56 + (c & 1) * 112);
      };
      unroll<0, 8>([&](auto cc) { rd(IC<0>(), cc); });
      unroll<0, 8>([&](auto cc) { rd(IC<1>(), cc); });
      SB();
      unroll<0, 28>([&](auto kk) {
        constexpr int ks = kk.value;
        if constexpr (ks == 7) { waitv<6>(); asm volatile("s_barrier" ::: "memory"); }
        if constexpr (ks == 16) { waitv<1>(); asm volatile("s_barrier" ::: "memory"); }
        __builtin_amdgcn_s_waitcnt(ks < 27 ? 0xc87f : 0xc07f);
        SB();
        unroll<0, 4>([&](auto mm) {
          constexpr int m = mm.value;
          const bf16x8 wv = cat(fr[ks][2 * m], fr[ks][2 * m + 1]);
          if constexpr (m < 2) kT[m] = MFMA(wv, xa[ks], kT[m]);
          else vv[m - 2] = MFMA(xa[ks], wv, vv[m - 2]);
          SB();
          if constexpr (ks < 9 && m == 0) mW(IC<4 + ks>(), IC<L_WK>());
          if constexpr (ks < 9 && m == 2) mW(IC<4 + ks>(), IC<L_WV>());
          rd(IC<ks + 2>(), IC<2 * m>());
          rd(IC<ks + 2>(), IC<2 * m + 1>());
          SB();
          if constexpr (ks < 9 && !(m & 1)) dmaW(IC<4 + ks>(), m ? 1792 : 896);
          if constexpr (ks == 9 && m == 0) touch(sink, rsW1, vt1, so_t1(8 + (T >> 1), (2 * T + (w >> 1)) & 3, 16 * (w & 1)));
          if constexpr (ks == 22 && m == 0) touch2(0);
          SB();
          norm(IC<ks + 1 + (m == 3)>(), IC<(m + 1) & 3>());
          SB();
        });
      });
    }
    bf16x8 wf[2][2];
    {
      const ldsp p = lds + L_WPHI + (4 * h + q) * 56 + 32 * blk + 8 * pp;
#pragma unroll
      for (int hd = 0; hd < 2; hd++) {
        wf[hd][0] = cat(vtr(p + hd * 1568), vtr(p + hd * 1568 + 8 * 56));
        s16x4 z = vtr(p + hd * 1568 + 24 * 56);
        wf[hd][1] = cat(vtr(p + hd * 1568 + 16 * 56), h ? (s16x4){0, 0, 0, 0} : z);
      }
    }
    STAMP(5);
    {
      f32x16 p[2];
#pragma unroll
      for (int hd = 0; hd < 2; hd++) { p[hd] = MFMA(wf[hd][0], pack8(kT[hd], 0), (f32x16)(0.f)); p[hd] = MFMA(wf[hd][1], pack8(kT[hd], 8), p[hd]); }
      bar();
      {
        const bf16x8 ones = {0x3f80, 0x3f80, 0x3f80, 0x3f80, 0x3f80, 0x3f80, 0x3f80, 0x3f80};
        const ldsp pvx = lds + L_VX + w * 2048 + lane * 16;
#pragma unroll
        for (int hd = 0; hd < 2; hd++)
#pragma unroll
          for (int j = 0; j < 2; j++) { bf16x8 f = pack8(vv[hd], 8 * j); lst(pvx + hd * 8192 + j * 1024, n == 28 ? ones : f); }
      }
      const unsigned pb = (32 * w + n) * 64 + ((h ^ ((n >> 1) & 7)) * 8);
      unroll<0, 2>([&](auto hh) {
        constexpr int hd = hh.value;
        unsigned ph[2][8];
        features(p[hd], h, ph, [&](auto kk, auto& pin) {
          constexpr int s = 12 * hd + kk.value;
          if constexpr (s < 13) {
            mW(IC<s>(), IC<L_WQ>());
            asm volatile("s_nop 0");
            dmap<0>(rsW, vw[s < 12 ? s % 7 : 7], pr * 112 + (s >= 7 && s < 12 ? 256 * 2688 : 0), pin);
          }
          if constexpr (s == 14) touch2(1);
        });
#pragma unroll
        for (int sg = 0; sg < 2; sg++)
#pragma unroll
          for (int g = 0; g < 4; g++) lst(lds + L_PHI + (hd * 2 + sg) * 8192 + (pb ^ (g << 4)), (u32x2){ph[sg][2 * g], ph[sg][2 * g + 1]});
      });
    }
    STAMP(6);
    hold(vw[0], vw[1], vw[2], vw[3], vw[4], vw[5], vw[6], vw[7], vt1);
    waitv<1>();
    bar();
    u32x4 kvp[2];
    const unsigned vkv = (2 * pr + (w >> 1)) * 65536 + T * 4096 + (w & 1) * 2048 + lane * 16;
    {
      const unsigned sw = 2 * h + (q >> 1);
      const ldsp pl = lds + L_PHI + w * 8192 + (4 * h + q) * 64 + (((4 * blk + pp) ^ sw) * 8);
      const ldsp ph_ = lds + L_PHI + w * 8192 + (8 + 4 * h + q) * 64 + (((4 * blk + pp) ^ sw ^ 4) * 8);
      const ldsp pvx = lds + L_VX + (w >> 1) * 8192 + lane * 16;
      f32x16 kv = {};
#pragma unroll
      for (int jj = 0; jj < 8; jj++) kv = MFMA(cat(vtr(pl + jj * 1024), vtr(ph_ + jj * 1024)), lld<bf16x8>(pvx + jj * 1024), kv);
      const rsrc_t st = mkst(ws + WS_KVP, SZ_KVP);
      kvp[0] = __builtin_bit_cast(u32x4, pack8(kv, 0)); kvp[1] = __builtin_bit_cast(u32x4, pack8(kv, 8));
      stc(st, vkv, kvp[0]);
      stc(st, vkv + 1024, kvp[1]);
    }
    STAMP(7);
    f32x16 qT[2] = {};
    {
      const ldsp pq_ = lds + L_WQ + wlane;
      s16x4 fr[29][4];
      auto rd = [&](auto kk, auto cc) {
        constexpr int ks = kk.value, c = cc.value;
        if constexpr (ks < 28) fr[ks][c] = vtr(pq_ + ks * 1792 + (c >> 1) * 56 + (c & 1) * 112);
      };
      unroll<0, 4>([&](auto cc) { rd(std::integral_constant<int, 0>(), cc); });
      unroll<0, 28>([&](auto kk) {
        constexpr int ks = kk.value;
        if constexpr (ks == 14) { hold(kvp[0], kvp[1], vkv); publish(ws, FL_P + pr * 16 + T, ep); STAMP(8); touch2(2); touch2(3); SB(); }
        unroll<0, 2>([&](auto mm) {
          constexpr int m = mm.value;
          rd(std::integral_constant<int, ks + 1>(), std::integral_constant<int, 2 * m>());
          rd(std::integral_constant<int, ks + 1>(), std::integral_constant<int, 2 * m + 1>());
          qT[m] = MFMA(cat(fr[ks][2 * m], fr[ks][2 * m + 1]), xa[ks], qT[m]);
          SB();
        });
      });
    }
    unsigned phq[2][2][8];
    {
      f32x16 p[2];
#pragma unroll
      for (int hd = 0; hd < 2; hd++) { p[hd] = MFMA(wf[hd][0], pack8(qT[hd], 0), (f32x16)(0.f)); p[hd] = MFMA(wf[hd][1], pack8(qT[hd], 8), p[hd]); }
      bar();
      STAMP(9);
      unroll<0, 2>([&](auto hh) {
        features(p[hh.value], h, phq[hh.value], [&](auto kk, auto& pin) {
          constexpr int k = 12 * hh.value + kk.value, t = k >> 2;
          if constexpr ((k & 3) == 0) setm0(area + ((t + 6) % 10) * 4096);
          dmap<(k & 3) * 1024>(rsW2, v2[k & 3], so2 + t * (32 * S * 2), pin);
        });
      });
    }
    STAMP(10);
    pre2(6, 9, 2);
    {
      const u32x4 rsK = mkrs(ws + WS_KVF, SZ_KVF);
      const unsigned so = pr * 16384 + w * 2048, tagB = ep.lo ^ ep.hi;
      const ldsp px = lds + 5 * 4096 + lane * 16;
#pragma unroll 1
      for (int it = 0; it < SPIN; ++it) {
        bf16x8 g0 = ldc<0>(rsK, lane * 16, so), g1 = ldc<1024>(rsK, lane * 16, so), g2 = ldc<0>(rsK, lane * 16, so + 8192), g3 = ldc<1024>(rsK, lane * 16, so + 8192);
        waitv<0>(g0, g1, g2, g3);
        const u32x4 u0 = __builtin_bit_cast(u32x4, g0), u1 = __builtin_bit_cast(u32x4, g1), u2 = __builtin_bit_cast(u32x4, g2), u3 = __builtin_bit_cast(u32x4, g3);
        lst(px + w * AREA, (u32x4){u0[0], u0[2], u1[0], u1[2]});
        lst(px + w * AREA + 1024, (u32x4){u2[0], u2[2], u3[0], u3[2]});
        const unsigned bad = ((u0[1] ^ ep.lo) | (u1[1] ^ ep.lo) | (u2[1] ^ ep.lo) | (u3[1] ^ ep.lo)) | ((u0[3] ^ tagB) | (u1[3] ^ tagB) | (u2[3] ^ tagB) | (u3[3] ^ tagB));
        if (__all(bad == 0)) break;
      }
      STAMP(2);
      tie(sink);
      bar();
      STAMP(11);
      bf16x8 kf[2][4];
#pragma unroll
      for (int hd = 0; hd < 2; hd++)
#pragma unroll
        for (int c = 0; c < 4; c++) kf[hd][c] = lld<bf16x8>(px + c * AREA + hd * 1024);
      f32x16 o[2] = {};
#pragma unroll
      for (int hd = 0; hd < 2; hd++) {
#pragma unroll
        for (int c = 0; c < 4; c++) {
          const unsigned* s = &phq[hd][c >> 1][4 * (c & 1)];
          o[hd] = MFMA(kf[hd][c], __builtin_bit_cast(bf16x8, (u32x4){s[0], s[1], s[2], s[3]}), o[hd]);
        }
      }
      float da = o[0][12], db = o[1][12];
      swap32(da, db);
      float ia = __builtin_amdgcn_rcpf(db + 1e-6f), ib = ia;
      swap32(ia, ib);
      unsigned a[8], bq[8];
#pragma unroll
      for (int r = 0; r < 8; r++) { a[r] = pk(o[0][2 * r] * ia, o[0][2 * r + 1] * ia); bq[r] = pk(o[1][2 * r] * ib, o[1][2 * r + 1] * ib); }
      const rsrc_t st = mkst(ws + WS_OF, SZ_OF);
      const unsigned tk = (128 * T + 32 * w + n) * 16, jb = 14 * (pr >> 1);
      const unsigned vo = (jb + 6 * (pr & 1) + h) * (S * 16) + tk;
      const unsigned v1 = vo + 2 * S * 16, v2_ = vo + 4 * S * 16, v3 = h ? OOB : (jb + 12 + (pr & 1)) * (S * 16) + tk;
      const u32x4 r0 = {a[0], a[1], a[2], a[3]}, r1 = {a[4], a[5], bq[0], bq[1]}, r2 = {bq[2], bq[3], bq[4], bq[5]}, r3 = {a[6], a[7], bq[6], bq[7]};
      stc(st, vo, r0); stc(st, v1, r1); stc(st, v2_, r2); stc(st, v3, r3);
      pre2rd();
      pre2w();
      hold(vo, v1, v2_, v3, r0, r1, r2, r3);
    }
    dma4(rsW2, v2, so2 + 10 * (32 * S * 2), area + 6 * 4096);
    publish<4>(ws, FL_O + T * 8 + pr, ep);
    STAMP(12);
    pre2(9, 10, 5);
    pre2(11, 14, 7);
  }

  if (i >= 4) {
    const unsigned orow = st2 * 64 + 16 * w + (lane >> 3);
    const unsigned vo = orow * (D * 2) + dt2 * 128 + (lane & 7) * 16;
    const u32x4 rsB = mkrs(b2, S * 2), rsU = mkrs(ws + WS_UF, SZ_UF), rsO = mkrs(ws + WS_OF, SZ_OF), rsWo = mkrs(wo, D * D * 2);
    u32x4 wor[14];
    unsigned vwo[4];
#pragma unroll
    for (int j = 0; j < 4; j++) vwo[j] = swz * (D * 2) + swc + j * (8 * D * 2 - 1024);
    unroll<0, 14>([&](auto jj) {
      constexpr int j = jj.value & 3;
      wor[jj.value] = lda<j * 1024>(rsWo, vwo[j], w * 112 * (D * 2) + dt2 * 128 + (jj.value >> 2) * (32 * D * 2));
    });
    poll(rsF, (FL_U + dt2 * 14 + ((7 * w) >> 1) + (lane & 3)) * 128, ep);
    hold(vwo[0], vwo[1], vwo[2], vwo[3]);
    STAMP(13);
    const unsigned vu0 = h * (D * 16) + (dt2 * 64 + n) * 16, vu1 = vu0 + 2 * D * 16, sou = w * 56 * (D * 16);
    const unsigned vof = h * (S * 16) + (st2 * 64 + n) * 16, soo = w * 14 * (S * 16);
    const unsigned vfl = (FL_O + (st2 >> 1) * 8 + 2 * w + (lane & 1)) * 128;
    bf16x8 fa[18][4], xr0, xr1;
    unsigned bs0, bs1;
    const unsigned vb2 = orow * 2;
    u32x2 fl;
    auto ldD = [&](auto tt, auto jj) {
      constexpr int t = tt.value, j = jj.value;
      if constexpr (t < 14) fa[t][j] = ldc<(j & 1) * 512>(rsU, (j >> 1) ? vu1 : vu0, sou + t * (4 * D * 16));
      else if constexpr (j < g2ld(t)) fa[t][j] = ldc<(j & 1) * 512>(rsO, vof, soo + (2 * (t - 14) + (j >> 1)) * (2 * S * 16));
    };
    unroll<0, G2R>([&](auto tt) { unroll<0, 4>([&](auto jj) { ldD(tt, jj); }); });
    const ldsp pw[2] = {pb[0] - h * 512, pb[1] - h * 512};
    f32x16 acc[2][2] = {};
    bf16x8 wb[19][2][2];
    auto rdB = [&](auto tt, auto kk, auto aa) {
      constexpr int t = tt.value, ks = kk.value, a = aa.value, j = 2 * (t - 14) + ks;
      if constexpr (t < 4) wb[t][ks][a] = w2r[t][ks][a];
      else if constexpr (t < 14) wb[t][ks][a] = cat(vtr(pb[a] + (t - 4) * 4096 + ks * 2048), vtr(pb[a] + (t - 4) * 4096 + ks * 2048 + 512));
      else if constexpr (j < 6) {
        constexpr int u = j % 3, rl = 56 * (j / 3) + (u == 0 ? 0 : u == 1 ? 16 : 36), rh = 56 * (j / 3) + (u == 0 ? 8 : u == 1 ? 28 : 44);
        wb[t][ks][a] = cat(vtr(pw[a] + rl * 128), vtr(pw[a] + rh * 128));
      } else if constexpr (j == 6) wb[t][ks][a] = cat(vtr(pw[a] + h * (52 * 128) + 24 * 128), vtr(pw[a] + h * (52 * 128) + 52 * 128));
    };
    unroll<0, 4>([&](auto cc) { rdB(IC<0>(), IC<(cc.value >> 1)>(), IC<(cc.value & 1)>()); });
    unroll<0, 18>([&](auto tt) {
      constexpr int t = tt.value, nk = t < 17 ? 2 : 1;
      constexpr int left = g2cnt(t - 3) + g2cnt(t - 2) + g2cnt(t - 1);
      if constexpr (nk == 2) waitv<left>(fa[t][0], fa[t][1], fa[t][2], fa[t][3]); else waitv<left>(fa[t][0], fa[t][1]);
      if constexpr (t == G2POLL + G2R) {
        tie(fl);
        if (!match(fl, ep)) poll(rsF, vfl, ep);
      }
      SB();
      unroll<0, 4 * nk>([&](auto mm) {
        constexpr int m = mm.value, ks = m >> 2, nh = (m >> 1) & 1, mh = m & 1;
        if constexpr (t < 14) acc[nh][mh] = MFMA(fa[t][ks * 2 + mh], wb[t][ks][nh], acc[nh][mh]);
        else acc[nh][mh] = MFMA(wb[t][ks][mh], fa[t][ks * 2 + nh], acc[nh][mh]);
        SB();
        if constexpr (m & 1) rdB(IC<t + 1>(), IC<(m >> 2)>(), IC<((m >> 1) & 1)>());
        else if constexpr (t >= 5 && 4 * (t - 5) + m / 2 < 14) sta<(4 * (t - 5) + m / 2) * 1024>(area + lane * 16, wor[4 * (t - 5) + m / 2]);
        SB();
        if constexpr (!(m & 1)) ldD(IC<t + G2R>(), IC<m / 2>());
        if constexpr (t == G2POLL && m == 7) fl = ldc2(rsF, vfl);
        if constexpr (t == 14 && m == 0) xr0 = ldp<0>(rsX, vo, 0);
        if constexpr (t == 14 && m == 2) xr1 = ldp<0>(rsX, vo, 8 * D * 2);
        if constexpr (t == 14 && m == 4) bs0 = ldu16<0>(rsB, vb2, 0);
        if constexpr (t == 14 && m == 6) bs1 = ldu16<16>(rsB, vb2, 0);
        SB();
      });
    });
    STAMP(14);
#pragma unroll
    for (int nh = 0; nh < 2; nh++)
#pragma unroll
      for (int mh = 0; mh < 2; mh++)
#pragma unroll
        for (int g = 0; g < 4; g++)
          lst(lds + w * AREA + (32 * nh + n) * 256 + (((8 * mh + 2 * g + h) ^ (n & 15)) * 16),
              (f32x4){acc[nh][mh][4 * g], acc[nh][mh][4 * g + 1], acc[nh][mh][4 * g + 2], acc[nh][mh][4 * g + 3]});
    barv();
    tie(xr0); tie(xr1); tie(bs0); tie(bs1);
    hold(vb2);
    u32x4 res[2];
    const rsrc_t sto = mkst(out, S * D * 2);
#pragma unroll
    for (int u = 0; u < 2; u++) {
      const int r = 16 * w + 8 * u + (lane >> 3);
      const unsigned c0 = r * 256 + (((2 * (lane & 7)) ^ (r & 15)) * 16), c1 = c0 ^ 16;
      f32x4 a = lld<f32x4>(lds + c0), bq = lld<f32x4>(lds + c1);
#pragma unroll
      for (int s = 1; s < 4; s++) { a += lld<f32x4>(lds + s * AREA + c0); bq += lld<f32x4>(lds + s * AREA + c1); }
      const u32x4 xu = __builtin_bit_cast(u32x4, u ? xr1 : xr0);
      const float bv = blo(u ? bs1 : bs0);
      res[u] = (u32x4){pk(a[0] + bv + blo(xu[0]), a[1] + bv + bhi(xu[0])), pk(a[2] + bv + blo(xu[1]), a[3] + bv + bhi(xu[1])),
                   pk(bq[0] + bv + blo(xu[2]), bq[1] + bv + bhi(xu[2])), pk(bq[2] + bv + blo(xu[3]), bq[3] + bv + bhi(xu[3]))};
      __builtin_amdgcn_raw_buffer_store_b128(res[u], sto, vo + u * (8 * D * 2), 0, 16);
    }
    hold(res[0], res[1]);
    if (blockIdx.x == 255 && tid == 0) __builtin_amdgcn_raw_buffer_store_b64((u32x2){ep.lo, ep.hi}, mkst(ws, WS_FLAG_BYTES), 0, 0, 16);
  }
}

extern "C" void kernel_launch(void* const* d_in, const int* in_sizes, int n_in, void* d_out, int out_size, void* d_ws, size_t ws_size,
                              hipStream_t stream) {
  fused_kernel<<<256, 256, 0, stream>>>((const short*)d_in[0], (const short*)d_in[1], (const short*)d_in[2], (const short*)d_in[3],
                                        (const short*)d_in[5], (const short*)d_in[6], (const short*)d_in[7], (const short*)d_in[8],
                                        (short*)d_out, (char*)d_ws);
}
```

```cpp
#include <hip/hip_runtime.h>
#include <stdint.h>

typedef short bf16x8 __attribute__((ext_vector_type(8)));
typedef short s16x4 __attribute__((ext_vector_type(4)));
typedef float f32x2 __attribute__((ext_vector_type(2)));
typedef float f32x4 __attribute__((ext_vector_type(4)));
typedef float f32x16 __attribute__((ext_vector_type(16)));
typedef unsigned u32x2 __attribute__((ext_vector_type(2)));
typedef unsigned u32x4 __attribute__((ext_vector_type(4)));
typedef __bf16 b16x2 __attribute__((ext_vector_type(2)));
typedef __attribute__((address_space(3))) char* ldsp;
typedef __amdgpu_buffer_rsrc_t rsrc_t;

#define DEV __device__ __forceinline__
#define SB() __builtin_amdgcn_sched_barrier(0)
#define MFMA(a, b, c) __builtin_amdgcn_mfma_f32_32x32x16_bf16(a, b, c, 0, 0, 0)

constexpr int S = 2048, D = 448, F = 1792;
constexpr unsigned FL_P = 8, FL_R = FL_P + 128, FL_O = FL_R + 16, FL_U = FL_O + 128;
constexpr unsigned WS_FLAG_BYTES = 65536;
constexpr unsigned WS_ST = WS_FLAG_BYTES, SZ_ST = S * 16;
constexpr unsigned WS_UF = WS_ST + SZ_ST, SZ_UF = 224 * 448 * 16;
constexpr unsigned WS_OF = WS_UF + SZ_UF, SZ_OF = 56 * 2048 * 16;
constexpr unsigned WS_KVP = WS_OF + SZ_OF, SZ_KVP = 16 * 16 * 4096;
constexpr unsigned WS_KVF = WS_KVP + SZ_KVP, SZ_KVF = 16 * 8 * 1024;
constexpr unsigned WS_END = WS_KVF + SZ_KVF;
constexpr unsigned AREA = 40960, L_STAT = 36864, SLOT = 12288;
constexpr unsigned L_WK = 0, L_WV = 50176, L_WQ = 100352, L_WPHI = 150528, L_PHI = 0, L_VX = 32768;
constexpr unsigned OOB = 0x40000000u;
constexpr int SPIN = 1 << 18;

DEV u32x4 mkrs(const void* p, unsigned bytes) {
  unsigned long long a = (unsigned long long)p;
  u32x4 r = {(unsigned)a, (unsigned)(a >> 32), bytes, 0x00020000u};
  return r;
}
DEV rsrc_t mkst(void* p, unsigned bytes) { return __builtin_amdgcn_make_buffer_rsrc(p, 0, bytes, 0x00020000); }
DEV void stc(rsrc_t rs, unsigned voff, u32x4 v) { __builtin_amdgcn_raw_buffer_store_b128(v, rs, voff, 0, 16); }
template <int OFF> DEV bf16x8 ldp(u32x4 rs, unsigned voff, unsigned soff) {
  bf16x8 r;
  asm volatile("buffer_load_dwordx4 %0, %2, %1, %3 offen offset:%4" : "=v"(r) : "s"(rs), "v"(voff), "s"(soff), "n"(OFF) : "memory");
  return r;
}
template <int OFF> DEV bf16x8 ldc(u32x4 rs, unsigned voff, unsigned soff) {
  bf16x8 r;
  asm volatile("buffer_load_dwordx4 %0, %2, %1, %3 offen offset:%4 sc1" : "=v"(r) : "s"(rs), "v"(voff), "s"(soff), "n"(OFF) : "memory");
  return r;
}
template <int OFF> DEV u32x2 ldp2(u32x4 rs, unsigned voff, unsigned soff) {
  u32x2 r;
  asm volatile("buffer_load_dwordx2 %0, %2, %1, %3 offen offset:%4" : "=v"(r) : "s"(rs), "v"(voff), "s"(soff), "n"(OFF) : "memory");
  return r;
}
template <int OFF> DEV u32x4 lda(u32x4 rs, unsigned voff, unsigned soff) {
  u32x4 r;
  asm volatile("buffer_load_dwordx4 %0, %2, %1, %3 offen offset:%4" : "=a"(r) : "s"(rs), "v"(voff), "s"(soff), "n"(OFF) : "memory");
  return r;
}
template <int OFF, class T> DEV u32x4 ldap(u32x4 rs, unsigned voff, unsigned soff, T& pin) {
  u32x4 r;
  asm volatile("buffer_load_dwordx4 %0, %3, %2, %4 offen offset:%5" : "=a"(r), "+v"(pin) : "s"(rs), "v"(voff), "s"(soff), "n"(OFF) : "memory");
  return r;
}
template <int OFF> DEV void sta(unsigned addr, u32x4 v) { asm volatile("ds_write_b128 %0, %1 offset:%2" ::"v"(addr), "a"(v), "n"(OFF) : "memory"); }
DEV u32x2 ldc2(u32x4 rs, unsigned voff) {
  u32x2 r;
  asm volatile("buffer_load_dwordx2 %0, %2, %1, 0 offen sc1" : "=v"(r) : "s"(rs), "v"(voff) : "memory");
  return r;
}
template <int OFF> DEV unsigned ldu16(u32x4 rs, unsigned voff, unsigned soff) {
  unsigned r;
  asm volatile("buffer_load_ushort %0, %2, %1, %3 offen offset:%4" : "=v"(r) : "s"(rs), "v"(voff), "s"(soff), "n"(OFF) : "memory");
  return r;
}
DEV void setm0(unsigned a) { asm volatile("s_mov_b32 m0, %0\n\ts_nop 0" ::"s"(a) : "memory"); }
DEV void setm0n(unsigned a) { asm volatile("s_mov_b32 m0, %0" ::"s"(a) : "memory"); }
template <int OFF> DEV void addm0(unsigned a) { asm volatile("s_add_u32 m0, %0, %1" ::"s"(a), "n"(OFF) : "memory", "scc"); }
template <int OFF> DEV void dma(u32x4 rs, unsigned voff, unsigned soff) {
  asm volatile("buffer_load_dwordx4 %1, %0, %2 offen offset:%3 lds" ::"s"(rs), "v"(voff), "s"(soff), "n"(OFF) : "memory");
}
DEV void dma4(u32x4 rs, const unsigned (&v)[4], unsigned soff, unsigned m0) {
  setm0(m0); dma<0>(rs, v[0], soff); dma<1024>(rs, v[1], soff); dma<2048>(rs, v[2], soff); dma<3072>(rs, v[3], soff);
}
template <int J> DEV void dma1(u32x4 rs, const unsigned (&v)[4], unsigned soff, unsigned m0) {
  if (J == 0) setm0(m0);
  dma<J * 1024>(rs, v[J], soff);
}
template <int N> DEV void waitv() { asm volatile("s_waitcnt vmcnt(%0)" ::"n"(N) : "memory"); }
template <int N, class T> DEV void waitv(T& a) { asm volatile("s_waitcnt vmcnt(%1)" : "+v"(a) : "n"(N) : "memory"); }
template <int N, class T> DEV void waitv(T& a, T& b) { asm volatile("s_waitcnt vmcnt(%2)" : "+v"(a), "+v"(b) : "n"(N) : "memory"); }
template <int N, class T> DEV void waitv(T& a, T& b, T& c, T& d) {
  asm volatile("s_waitcnt vmcnt(%4)" : "+v"(a), "+v"(b), "+v"(c), "+v"(d) : "n"(N) : "memory");
}
template <class T> DEV void tie(T& a) { asm volatile("" : "+v"(a)::"memory"); }
template <class T> DEV void hold(T a) { asm volatile("" ::"v"(a)); }
template <class T, class... R> DEV void hold(T a, R... r) { hold(a); hold(r...); }
DEV void keep(f32x16 a) { asm volatile("" ::"v"(a)); }
DEV void bar() { asm volatile("s_waitcnt lgkmcnt(0)\n\ts_barrier" ::: "memory"); }
DEV void barv() { asm volatile("s_waitcnt vmcnt(0) lgkmcnt(0)\n\ts_barrier" ::: "memory"); }
DEV s16x4 vtr(ldsp p) { return __builtin_amdgcn_ds_read_tr16_b64_v4i16((__attribute__((address_space(3))) s16x4*)p); }
DEV bf16x8 cat(s16x4 a, s16x4 b) { return __builtin_shufflevector(a, b, 0, 1, 2, 3, 4, 5, 6, 7); }
DEV unsigned pk(float a, float b) {
  f32x2 v = {a, b};
  return __builtin_bit_cast(unsigned, __builtin_convertvector(v, b16x2));
}
DEV float blo(unsigned u) { return __builtin_bit_cast(float, u << 16); }
DEV float bhi(unsigned u) { return __builtin_bit_cast(float, u & 0xffff0000u); }
DEV void swap32(float& a, float& b) {
  auto r = __builtin_amdgcn_permlane32_swap(__builtin_bit_cast(unsigned, a), __builtin_bit_cast(unsigned, b), false, false);
  const unsigned r0 = r[0], r1 = r[1];
  a = __builtin_bit_cast(float, r0); b = __builtin_bit_cast(float, r1);
}
DEV void swap16(float& a, float& b) {
  auto r = __builtin_amdgcn_permlane16_swap(__builtin_bit_cast(unsigned, a), __builtin_bit_cast(unsigned, b), false, false);
  const unsigned r0 = r[0], r1 = r[1];
  a = __builtin_bit_cast(float, r0); b = __builtin_bit_cast(float, r1);
}
template <int C> DEV float ror(float z) {
  return __builtin_bit_cast(float, __builtin_amdgcn_update_dpp(0, __builtin_bit_cast(int, z), C, 0xf, 0xf, true));
}
DEV float rowsum(float z) { z += ror<0x128>(z); z += ror<0x124>(z); z += ror<0x122>(z); z += ror<0x121>(z); return z; }
DEV float rdl(float z, int l) { return __builtin_bit_cast(float, __builtin_amdgcn_readlane(__builtin_bit_cast(int, z), l)); }
template <class T> DEV void lst(ldsp p, T v) { *(__attribute__((address_space(3))) T*)p = v; }
template <class T> DEV T lld(ldsp p) { return *(__attribute__((address_space(3))) T*)p; }

struct Epoch { unsigned lo, hi; };
template <int N = 0> DEV void publish(char* ws, unsigned flag, Epoch e) {
  asm volatile("s_waitcnt vmcnt(%0) lgkmcnt(0)\n\ts_barrier" ::"n"(N) : "memory");
  if (threadIdx.x == 0) __builtin_amdgcn_raw_buffer_store_b64((u32x2){e.lo, e.hi}, mkst(ws, WS_FLAG_BYTES), flag * 128, 0, 16);
}
DEV bool match(u32x2 f, Epoch e) { return __all(f.x == e.lo && f.y == e.hi); }
DEV void poll(u32x4 rsF, unsigned voff, Epoch e) {
#pragma unroll 1
  for (int it = 0; it < SPIN; ++it) {
    u32x2 f = ldc2(rsF, voff);
    waitv<0>(f);
    if (match(f, e)) break;
    __builtin_amdgcn_s_sleep(1);
  }
}

DEV void pollm(u32x4 rsF, unsigned voff, Epoch e, unsigned need, unsigned& seen) {
  const unsigned long long e64 = ((unsigned long long)e.hi << 32) | e.lo;
  unsigned cnt, t;
  u32x2 f;
  asm volatile(
      "s_mov_b32 %1, %8\n"
      "1:\n\ts_and_b32 %2, %0, %7\n\ts_cmp_eq_u32 %2, %7\n\ts_cbranch_scc1 2f\n\t"
      "buffer_load_dwordx2 %3, %5, %4, 0 offen sc1\n\ts_waitcnt vmcnt(0)\n\t"
      "v_cmp_eq_u64 vcc, %6, %3\n\ts_nop 4\n\ts_mov_b32 %0, vcc_lo\n\ts_sub_u32 %1, %1, 1\n\ts_cbranch_scc0 1b\n"
      "2:"
      : "+s"(seen), "=&s"(cnt), "=&s"(t), "=&v"(f)
      : "s"(rsF), "v"(voff), "s"(e64), "s"(need), "n"(SPIN)
      : "memory", "vcc", "scc");
}

DEV void acc_stats(bf16x8 v, float& s, float& q) {
  const u32x4 u = __builtin_bit_cast(u32x4, v);
#pragma unroll
  for (int e = 0; e < 4; e++) {
    const unsigned t = u[e];
    const b16x2 p = __builtin_bit_cast(b16x2, t);
    s = __builtin_amdgcn_fdot2_f32_bf16(p, __builtin_bit_cast(b16x2, 0x3f803f80u), s, false);
    q = __builtin_amdgcn_fdot2_f32_bf16(p, p, q, false);
  }
}
DEV unsigned affine2(unsigned u, float a, float c) {
  float t0, t1;
  unsigned r;
  asm("v_lshlrev_b32 %1, 16, %3\n\tv_and_b32 %2, 0xffff0000, %3\n\tv_fma_f32 %1, %1, %4, %5\n\tv_fma_f32 %2, %2, %4, %5\n\tv_cvt_pk_bf16_f32 %0, %1, %2"
      : "=v"(r), "=&v"(t0), "=&v"(t1)
      : "v"(u), "v"(a), "v"(c));
  return r;
}
DEV f32x16 mfmav(u32x4 a, bf16x8 b, f32x16 c) {
  f32x16 r;
  asm volatile("v_mfma_f32_32x32x16_bf16 %0, %1, %2, %3" : "=&v"(r) : "v"(a), "v"(b), "v"(c));
  return r;
}
DEV void mfmac(f32x16& c, bf16x8 a, u32x4 b) { asm volatile("v_mfma_f32_32x32x16_bf16 %0, %1, %2, %0" : "+v"(c) : "v"(a), "v"(b)); }
DEV void touch(unsigned& sink, u32x4 rs, unsigned voff, unsigned soff) {
  asm volatile("buffer_load_dword %0, %2, %1, %3 offen" : "+v"(sink) : "s"(rs), "v"(voff), "s"(soff) : "memory");
}
DEV void touchc(unsigned& sink, u32x4 rs, unsigned voff, unsigned soff) {
  asm volatile("buffer_load_dword %0, %2, %1, %3 offen sc1" : "+v"(sink) : "s"(rs), "v"(voff), "s"(soff) : "memory");
}
DEV void touchl(u32x4 rs, unsigned voff, unsigned soff) {
  asm volatile("buffer_load_dword %1, %0, %2 offen lds" ::"s"(rs), "v"(voff), "s"(soff) : "memory");
}
template <int N> using IC = std::integral_constant<int, N>;
template <int T, int N, class Fn> DEV void unroll(Fn&& f) {
  if constexpr (T < N) { f(std::integral_constant<int, T>()); unroll<T + 1, N>(f); }
}
template <int OFF, class T> DEV void dmap(u32x4 rs, unsigned voff, unsigned soff, T& pin) {
  asm volatile("buffer_load_dwordx4 %2, %1, %3 offen offset:%4 lds" : "+v"(pin) : "s"(rs), "v"(voff), "s"(soff), "n"(OFF) : "memory");
}

template <class Fn> DEV void features(const f32x16& p, bool h, unsigned (&ph)[2][8], Fn&& cb) {
  constexpr float L = 1.4426950408889634f;
  float mx = fmaxf(p[0], p[1]), mn = fminf(p[0], p[1]);
#pragma unroll
  for (int r = 2; r < 12; r += 2) { mx = fmaxf(fmaxf(mx, p[r]), p[r + 1]); mn = fminf(fminf(mn, p[r]), p[r + 1]); }
  cb(IC<0>(), mx);
  float mx2 = fmaxf(fmaxf(fmaxf(fmaxf(mx, p[12]), p[13]), p[14]), p[15]);
  float mn2 = fminf(fminf(fminf(fminf(mn, p[12]), p[13]), p[14]), p[15]);
  mx = h ? mx : mx2; mn = h ? -mn : -mn2;
  swap32(mx, mn);
  float c0 = -L * fmaxf(mx, mn), c1 = c0;
  cb(IC<1>(), c0);
  swap32(c0, c1);
  const float ninf = -__builtin_inff();
  const float d0 = h ? ninf : c0, d1 = h ? ninf : c1;
  float e[2][16], s0 = 0.f, s1 = 0.f;
  unroll<0, 16>([&](auto rr) {
    constexpr int r = rr.value;
    float a0 = fmaf(p[r], L, r < 12 ? c0 : d0);
    if constexpr (r & 1) cb(IC<2 + r / 2>(), a0);
    e[0][r] = __builtin_amdgcn_exp2f(a0);
    e[1][r] = __builtin_amdgcn_exp2f(fmaf(p[r], -L, r < 12 ? c1 : d1));
    s0 += e[0][r]; s1 += e[1][r];
  });
  swap32(s0, s1);
  float i0 = __builtin_amdgcn_rcpf(s0 + s1), i1 = i0;
  swap32(i0, i1);
  unroll<0, 8>([&](auto rr) {
    constexpr int r = rr.value;
    ph[0][r] = pk(e[0][2 * r] * i0, e[0][2 * r + 1] * i0); ph[1][r] = pk(e[1][2 * r] * i1, e[1][2 * r + 1] * i1);
    if constexpr ((r & 3) == 1) cb(IC<10 + r / 4>(), ph[1][r]);
  });
}
DEV bf16x8 pack8(const f32x16& a, int o) {
  u32x4 u = {pk(a[o], a[o + 1]), pk(a[o + 2], a[o + 3]), pk(a[o + 4], a[o + 5]), pk(a[o + 6], a[o + 7])};
  return __builtin_bit_cast(bf16x8, u);
}
DEV float gelu(float a) {
  constexpr float c1 = -2.302208198f, c2 = c1 * 0.044715f;
  return a * __builtin_amdgcn_rcpf(1.f + __builtin_amdgcn_exp2f(a * fmaf(a * a, c2, c1)));
}

#define STAMP(n)

constexpr int woff(int k) { return (k >> 1) * SLOT + (k & 1) * 8192; }
constexpr int xoff(int k) { return (k >> 1) * SLOT + 4096 + (k & 1) * 2048; }
constexpr int G2R = 4, G2POLL = 5;
constexpr int g2ld(int t) { return t < 17 ? 4 : (t == 17 ? 2 : 0); }
constexpr int g2cnt(int s) { return s < 0 ? 4 : g2ld(s + G2R) + (s == G2POLL ? 1 : 0) + (s == 14 ? 4 : 0); }

__global__ __launch_bounds__(256, 1) void fused_kernel(const short* __restrict__ x, const short* __restrict__ wqkv, const short* __restrict__ wphi,
                                                       const short* __restrict__ wo, const short* __restrict__ w1, const short* __restrict__ b1,
                                                       const short* __restrict__ w2, const short* __restrict__ b2, short* __restrict__ out,
                                                       char* __restrict__ ws) {
  __shared__ __attribute__((aligned(16))) char lds_[163840];
  const ldsp lds = (ldsp)lds_;
  const unsigned lds0 = (unsigned)(uintptr_t)lds;
  const unsigned long long trs = __builtin_amdgcn_s_memrealtime();
  const int tid = threadIdx.x, lane = tid & 63, w = __builtin_amdgcn_readfirstlane(tid >> 6);
  asm volatile("s_nop 4" ::"s"(w), "s"(x), "s"(wqkv), "s"(wphi), "s"(wo), "s"(w1), "s"(b1), "s"(w2), "s"(b2), "s"(out), "s"(ws) : "memory");
  const int xc = blockIdx.x & 7, i = blockIdx.x >> 3;
  const int h = lane >> 5, n = lane & 31, q = (lane & 15) >> 2, pp = lane & 3, blk = (lane >> 4) & 1;
  const u32x4 rsF = mkrs(ws, WS_FLAG_BYTES), rsX = mkrs(x, S * D * 2), rsW2 = mkrs(w2, F * S * 2);
  const unsigned area = lds0 + w * AREA;
  const int tile2 = 28 * (xc & 1) + i - 4, dt2 = tile2 >> 3, st2 = 8 * (xc >> 1) + (tile2 & 7);
  const unsigned swz = (lane >> 3), swc = ((lane & 7) ^ (((lane >> 4) & 1) << 2)) * 16;
  unsigned v2[4];
#pragma unroll
  for (int j = 0; j < 4; j++) v2[j] = i >= 4 ? swz * (S * 2) + swc + j * (8 * S * 2 - 1024) : OOB;
  const unsigned so2 = (w * 448) * (S * 2) + st2 * 128;
  auto pre2 = [&](int t0, int t1, int slot0) {
#pragma unroll 1
    for (int t = t0; t < t1; t++) dma4(rsW2, v2, so2 + t * (32 * S * 2), area + (slot0 + t - t0) * 4096);
  };

  ldsp pb[2];
#pragma unroll
  for (int a = 0; a < 2; a++) pb[a] = lds + w * AREA + h * 1024 + q * 128 + (((4 * a + 2 * blk + (pp >> 1)) ^ ((q >> 1) << 2)) * 16) + (pp & 1) * 8;
  bf16x8 w2r[4][2][2];
  auto pre2w = [&]() {
#pragma unroll
    for (int t = 0; t < 4; t++) asm volatile("s_waitcnt lgkmcnt(0)" : "+v"(w2r[t][0][0]), "+v"(w2r[t][0][1]), "+v"(w2r[t][1][0]), "+v"(w2r[t][1][1])::"memory");
  };
  auto pre2rd = [&]() {
#pragma unroll
    for (int t = 0; t < 4; t++)
#pragma unroll
      for (int ks = 0; ks < 2; ks++)
#pragma unroll
        for (int a = 0; a < 2; a++) w2r[t][ks][a] = cat(vtr(pb[a] + (t + 6) * 4096 + ks * 2048), vtr(pb[a] + (t + 6) * 4096 + ks * 2048 + 512));
  };

  const bool isA = i >= 16;
  const int T = i - 16, pr = xc;
  const u32x4 rsW1 = mkrs(w1, S * F * 2);
  const unsigned vt1 = xc < 7 ? (lane >> 2) * (F * 2) + (lane & 3) * 128 : OOB;
  auto so_t1 = [&](int stage, int wq, int r0) { return (512 * wq + ((64 * xc + 32 * stage + r0) & 511)) * (F * 2) + xc * 512; };
  Epoch ep;
  auto setep = [&](u32x2 gen) {
    const unsigned long long g = (((unsigned long long)__builtin_amdgcn_readfirstlane(gen.y) << 32) | (unsigned)__builtin_amdgcn_readfirstlane(gen.x)) + 1;
    ep.lo = (unsigned)g; ep.hi = (unsigned)(g >> 32);
  };

  if (!isA) {
    const int row0 = 512 * (i >> 2) + 64 * xc + 16 * (i & 3) + 4 * w;
    const bool isG = xc < 7 && i < 14;
    const int ft = 2 * xc + (i >= 7), dt = i >= 7 ? i - 7 : i;
    bf16x8 xv[4];
    u32x2 gen, bb[4];
    unsigned vz = 0;
    tie(vz);
    const unsigned vo = lane < 56 ? lane * 16 : OOB, vt = lane < 32 ? vt1 : OOB, vb = h * 8;
    {
      const unsigned so = row0 * (D * 2);
      xv[0] = ldp<0>(rsX, vo, so); xv[1] = ldp<896>(rsX, vo, so); xv[2] = ldp<1792>(rsX, vo, so); xv[3] = ldp<2688>(rsX, vo, so);
      gen = ldc2(rsF, vz);
      setm0(area + L_STAT);
      touchl(rsW1, vt, so_t1(i >> 2, i & 3, 8 * w));
      const u32x4 rsB = mkrs(b1, F * 2);
      const unsigned sb = (ft * 128 + w * 32) * 2;
      bb[0] = ldp2<0>(rsB, vb, sb); bb[1] = ldp2<16>(rsB, vb, sb); bb[2] = ldp2<32>(rsB, vb, sb); bb[3] = ldp2<48>(rsB, vb, sb);
    }
    unsigned v1[4][4], vx[4][2];
#pragma unroll
    for (int ks = 0; ks < 4; ks++) {
#pragma unroll
      for (int j = 0; j < 4; j++) v1[ks][j] = (16 * ks + 8 * (j & 1) + swz) * (F * 2) + (j >> 1) * 128 + swc - j * 1024;
#pragma unroll
      for (int j = 0; j < 2; j++) vx[ks][j] = (16 * ks + 8 * j + swz) * (D * 2) + swc - (j + 2 * (ks & 1)) * 1024;
    }
    waitv<5>(xv[0], xv[1], xv[2], xv[3]);
    tie(gen);
    STAMP(0);
    setep(gen);
    const unsigned tagB = ep.lo ^ ep.hi, vs = (lane & 15) ? OOB : (row0 + h + 2 * blk) * 16;
    u32x4 rec;
    {
      float s[4] = {}, z[4] = {};
#pragma unroll
      for (int r = 0; r < 4; r++) acc_stats(xv[r], s[r], z[r]);
      swap32(s[0], s[1]); swap32(s[2], s[3]); swap32(z[0], z[1]); swap32(z[2], z[3]);
      float sa = s[0] + s[1], sb = s[2] + s[3], za = z[0] + z[1], zb = z[2] + z[3];
      swap16(sa, sb); swap16(za, zb);
      const float mu = rowsum(sa + sb) * (1.f / D), ms = rowsum(za + zb) * (1.f / D);
      const float var = fmaxf(ms - mu * mu, 0.f);
      float a = __builtin_amdgcn_rsqf(var + 1e-5f);
      a *= __builtin_amdgcn_rsqf(var * a * a + 1e-5f);
      const unsigned a2 = pk(a, a);
      const float c = -mu * blo(a2);
      rec = (u32x4){a2, ep.lo, __builtin_bit_cast(unsigned, c), tagB};
      stc(mkst(ws + WS_ST, SZ_ST), vs, rec);
    }
    asm volatile("s_barrier" ::: "memory");
    hold(vz, vo, vt, vb);
    STAMP(1);

    if (isG) {
      const unsigned b1s = (w * 512) * (F * 2) + ft * 256, bxs = (w * 512) * (D * 2) + dt * 128;
      auto mset = [&](auto kk, auto jj) {
        constexpr int ks = kk.value, j = jj.value;
        if constexpr ((ks & 1) ? j == 2 : j == 0) setm0n(area + woff(ks));
        if constexpr (!(ks & 1) && j == 4) setm0n(area + xoff(ks));
      };
      auto dgrp = [&](auto kk, auto jj, unsigned s1, unsigned sx) {
        constexpr int ks = kk.value, j = jj.value, jw = (ks & 1) ? j - 2 : j, jx = (ks & 1) ? j : j - 4;
        if constexpr (jw >= 0 && jw < 4) dma<jw * 1024>(rsW1, v1[ks][jw], s1);
        else dma<(jx + 2 * (ks & 1)) * 1024>(rsX, vx[ks][jx], sx);
      };
      auto fill = [&](auto kk) {
        unroll<0, 6>([&](auto jj) { mset(kk, jj); asm volatile("s_nop 0"); dgrp(kk, jj, b1s + 64 * xc * (F * 2), bxs + 64 * xc * (D * 2)); });
      };
      const u32x4 rsS = mkrs(ws + WS_ST, SZ_ST);
      auto gather = [&]() {
        bf16x8 g[8];
#pragma unroll
        for (int j = 0; j < 8; j++) g[j] = ldc<0>(rsS, (lane * 16 + (j + xc) * 1024) & 8191, w * 8192);
        waitv<0>();
        bool ok = true;
#pragma unroll
        for (int j = 0; j < 8; j++) {
          tie(g[j]);
          const u32x4 u = __builtin_bit_cast(u32x4, g[j]);
          ok &= u[1] == ep.lo && u[3] == tagB;
          const ldsp p = lds + w * AREA + L_STAT + j * 512 + (lane >> 4) * 128 + (lane & 15) * 4;
          lst(p, u[2]); lst(p + 64, u[0]);
        }
        return (bool)__all(ok);
      };
      unroll<0, 4>(fill);
      hold(vs, rec);
      STAMP(2);
#pragma unroll 1
      for (int it = 0; it < SPIN; ++it)
        if (gather()) break;
      STAMP(3);
      tie(bb[0]); tie(bb[1]); tie(bb[2]); tie(bb[3]);
      f32x16 acc[4][2];
#pragma unroll
      for (int g = 0; g < 4; g++) {
        acc[0][0][4 * g] = blo(bb[g].x); acc[0][0][4 * g + 1] = bhi(bb[g].x); acc[0][0][4 * g + 2] = blo(bb[g].y); acc[0][0][4 * g + 3] = bhi(bb[g].y);
      }
      acc[0][1] = acc[0][0];
#pragma unroll
      for (int a = 1; a < 4; a++) { acc[a][0] = (f32x16)(0.f); acc[a][1] = (f32x16)(0.f); }
      unsigned pa[4], px[2], msk[4], psc = area + L_STAT + h * 32, psa;
      {
        const unsigned rowb = area + (8 * h + q) * 128 + (pp & 1) * 8, ch = 2 * blk + (pp >> 1), fl = (q >> 1) << 2;
#pragma unroll
        for (int a = 0; a < 4; a++) pa[a] = rowb + ((a ^ w) >> 1) * 2048 + ((4 * ((a ^ w) & 1) + ch) ^ fl) * 16;
        px[0] = rowb + (ch ^ fl) * 16; px[1] = rowb + ((4 + ch) ^ fl) * 16;
        const unsigned r = 4 * ((lane >> 3) & 1) + pp, on = (!blk && (q & 1) == h) ? ((r & 1) ? 0xffff0000u : 0xffffu) : 0u;
#pragma unroll
        for (int j = 0; j < 4; j++) { msk[j] = (r >> 1) == j ? on : 0u; tie(msk[j]); }
        psa = area + L_STAT + 64 + (8 * h + r) * 4;
      }
      s16x4 wl[2][4], wh[2][4], xl[2], xh[2];
      u32x4 hf[2][2], dg;
      unsigned av;
      f32x16 hn[2], cc = {};
      tie(cc);
      auto rdX = [&](auto nn, auto dd) {
        constexpr int o = xoff(nn.value & 3), d = dd.value;
        xl[d] = vtr((ldsp)(uintptr_t)px[d] + o); xh[d] = vtr((ldsp)(uintptr_t)px[d] + o + 512);
      };
      auto rdA = [&](auto nn) { av = lld<unsigned>((ldsp)(uintptr_t)psa + nn.value * 128); };
      auto rdC = [&](auto nn) {
        const f32x4 c0 = lld<f32x4>((ldsp)(uintptr_t)psc + nn.value * 128), c1 = lld<f32x4>((ldsp)(uintptr_t)psc + nn.value * 128 + 16);
#pragma unroll
        for (int e = 0; e < 4; e++) { cc[e] = c0[e]; cc[4 + e] = c1[e]; }
      };
      auto rdW = [&](auto nn, auto aa) {
        constexpr int o = woff(nn.value & 3), nx = nn.value & 1, a = aa.value;
        wl[nx][a] = vtr((ldsp)(uintptr_t)pa[a] + o); wh[nx][a] = vtr((ldsp)(uintptr_t)pa[a] + o + 512);
      };
      auto mkD = [&]() { dg = (u32x4){av & msk[0], av & msk[1], av & msk[2], av & msk[3]}; };
      auto nrm = [&](auto dd) { hn[dd.value] = mfmav(dg, cat(xl[dd.value], xh[dd.value]), cc); };
      auto cvt = [&](auto nn, auto dd) {
        constexpr int d = dd.value;
        hf[nn.value & 1][d] = (u32x4){pk(hn[d][0], hn[d][1]), pk(hn[d][2], hn[d][3]), pk(hn[d][4], hn[d][5]), pk(hn[d][6], hn[d][7])};
        SB();
        keep(hn[d]);
      };
      auto step = [&](auto kk, auto wn, auto last, auto&& ms, auto&& gm) {
        constexpr int ks = kk.value;
        constexpr bool more = !last.value;
        using NX = IC<ks + 1>;
        auto mm = [&](auto m_) {
          constexpr int a = more ? m_.value >> 1 : ((m_.value >> 1) + 1) & 3, dh = m_.value & 1;
          if constexpr (a == 0) mfmac(acc[0][dh], cat(wl[ks & 1][0], wh[ks & 1][0]), hf[ks & 1][dh]);
          else acc[a][dh] = MFMA(cat(wl[ks & 1][a], wh[ks & 1][a]), __builtin_bit_cast(bf16x8, hf[ks & 1][dh]), acc[a][dh]);
          SB();
          if constexpr (!more && m_.value >= 2) {
            constexpr int c = m_.value >> 1;
#pragma unroll
            for (int g = 0; g < 4; g++)
              lst(lds + w * AREA + lane * 16 + (c - 1) * 8192 + dh * 4096 + g * 1024, (f32x4){acc[c][dh][4 * g], acc[c][dh][4 * g + 1], acc[c][dh][4 * g + 2], acc[c][dh][4 * g + 3]});
            SB();
          }
        };
        __builtin_amdgcn_s_waitcnt(0x70 | wn.value);
        asm volatile("" ::: "memory");
        SB();
        if constexpr (more) { rdX(NX(), IC<0>()); rdA(NX()); }
        ms(IC<0>());
        SB();
        mm(IC<0>());
        gm(IC<0>());
        if constexpr (more) { rdC(NX()); rdX(NX(), IC<1>()); SB(); }
        mm(IC<1>());
        gm(IC<1>());
        ms(IC<2>());
        if constexpr (more) { rdW(NX(), IC<0>()); rdW(NX(), IC<1>()); SB(); }
        mm(IC<2>());
        if constexpr (more) { mkD(); SB(); }
        gm(IC<2>());
        if constexpr (more) { rdW(NX(), IC<2>()); SB(); nrm(IC<0>()); SB(); rdW(NX(), IC<3>()); SB(); }
        mm(IC<3>());
        if constexpr (more) { nrm(IC<1>()); SB(); }
        gm(IC<3>());
        ms(IC<4>());
        mm(IC<4>());
        if constexpr (more) { cvt(NX(), IC<0>()); SB(); }
        mm(IC<5>());
        gm(IC<4>());
        mm(IC<6>());
        if constexpr (more) { cvt(NX(), IC<1>()); SB(); }
        gm(IC<5>());
        mm(IC<7>());
      };
      asm volatile("s_waitcnt lgkmcnt(0)" ::: "memory");
      rdX(IC<0>(), IC<0>()); rdX(IC<0>(), IC<1>()); rdA(IC<0>()); rdC(IC<0>());
      unroll<0, 4>([&](auto jj) { rdW(IC<0>(), jj); });
      mkD();
      SB();
      asm volatile("s_nop 1");
      nrm(IC<0>()); nrm(IC<1>());
      asm volatile("s_nop 7\n\ts_nop 7");
      cvt(IC<0>(), IC<0>()); cvt(IC<0>(), IC<1>());
      SB();
      unsigned tk = 64 * xc;
#pragma unroll 1
      for (int t = 0; t < 7; t++) {
        tk = (tk + 64) & 511;
        const unsigned s1 = b1s + tk * (F * 2), sx = bxs + tk * (D * 2);
        unroll<0, 4>([&](auto kk) {
          step(kk, IC<12>(), IC<0>(), [&](auto jj) { mset(kk, jj); SB(); }, [&](auto jj) { dgrp(kk, jj, s1, sx); SB(); });
        });
        psc += 512; tie(psc); psa += 512; tie(psa);
        SB();
      }
      {
        unroll<0, 4>([&](auto kk) {
          constexpr int ks = kk.value;
          step(kk, IC<(ks == 3 ? 12 : 12 - 2 * ks)>(), IC<(ks == 3)>(),
               [&](auto jj) { if constexpr (jj.value == 0) { setm0n(area + (6 + ks) * 4096); SB(); } },
               [&](auto jj) { if constexpr (jj.value < 4) { dma<jj.value * 1024>(rsW2, v2[jj.value], so2 + ks * (32 * S * 2)); SB(); } });
        });
      }
      STAMP(4);
      bar();
      STAMP(5);
      {
        const rsrc_t st = mkst(ws + WS_UF, SZ_UF);
        const unsigned vo = (ft * 16 + w * 4 + h) * (D * 16) + (dt * 64 + n) * 16;
        f32x4 v[2][3][4];
        u32x4 res[4];
#pragma unroll
        for (int dh = 0; dh < 2; dh++)
#pragma unroll
          for (int a = 1; a < 4; a++)
#pragma unroll
            for (int g = 0; g < 4; g++) v[dh][a - 1][g] = lld<f32x4>(lds + (w ^ a) * AREA + lane * 16 + (a - 1) * 8192 + dh * 4096 + g * 1024);
        SB();
#pragma unroll
        for (int dh = 0; dh < 2; dh++) {
#pragma unroll
          for (int g = 0; g < 4; g += 2) {
            float r[8];
#pragma unroll
            for (int e = 0; e < 8; e++) {
              const int gg = g + (e >> 2), c = e & 3;
              r[e] = gelu((acc[0][dh][4 * gg + c] + v[dh][0][gg][c]) + (v[dh][1][gg][c] + v[dh][2][gg][c]));
            }
            auto r0 = __builtin_amdgcn_permlane32_swap(pk(r[0], r[1]), pk(r[4], r[5]), false, false);
            auto r1 = __builtin_amdgcn_permlane32_swap(pk(r[2], r[3]), pk(r[6], r[7]), false, false);
            res[2 * dh + g / 2] = (u32x4){r0[0], r1[0], r0[1], r1[1]};
            stc(st, vo + g * (D * 16) + dh * 512, res[2 * dh + g / 2]);
          }
        }
        STAMP(8);
        waitv<4>();
        pre2rd();
        pre2w();
        hold(res[0], res[1], res[2], res[3], vo);
      }
      dma4(rsW2, v2, so2 + 10 * (32 * S * 2), area + 6 * 4096);
      STAMP(6);
      publish<4>(ws, FL_U + dt * 14 + ft, ep);
      STAMP(7);
      pre2(4, 10, 0);
      pre2(11, 14, 7);
    } else {
      if (xc < 7) {
#pragma unroll 1
        for (int m = 4; m < 9; m++) touchl(rsW1, vt1, so_t1(m, 2 * (i - 14) + (w >> 1), 16 * (w & 1)));
      }
      if (i >= 14) {
#pragma unroll 1
        for (int m = 0; m < 4; m++) touchl(mkrs(wo, D * D * 2), (lane >> 2) * (D * 2) + (lane & 3) * 128, (128 * m + 64 * (i - 14) + 16 * w) * (D * 2) + (xc & 1) * 384);
        const int head = 2 * xc + i - 14;
        const u32x4 rsP = mkrs(ws + WS_KVP, SZ_KVP);
        const unsigned so = head * 65536 + w * 1024, vf = (FL_P + xc * 16 + (lane & 15)) * 128;
        bf16x8 pv[16];
        float a[8] = {};
        unroll<0, 5>([&](auto gg) {
          constexpr int t = 4 * gg.value;
          if constexpr (t < 16) {
            if constexpr (t == 0) poll(rsF, vf, ep);
            if constexpr (t == 12) STAMP(3);
            unroll<t, t + 4>([&](auto tt) { pv[tt.value] = ldc<0>(rsP, lane * 16, so + tt.value * 4096); });
          }
          if constexpr (t > 0) {
            waitv<(t < 16 ? 4 : 0)>(pv[t - 4], pv[t - 3], pv[t - 2], pv[t - 1]);
#pragma unroll
            for (int j = t - 4; j < t; j++) {
              const u32x4 u = __builtin_bit_cast(u32x4, pv[j]);
#pragma unroll
              for (int e = 0; e < 4; e++) { const unsigned v = u[e]; a[2 * e] += blo(v); a[2 * e + 1] += bhi(v); }
            }
            SB();
          }
        });
        const rsrc_t st = mkst(ws + WS_KVF, SZ_KVF);
        const u32x4 r0 = {pk(a[0], a[1]), ep.lo, pk(a[2], a[3]), tagB}, r1 = {pk(a[4], a[5]), ep.lo, pk(a[6], a[7]), tagB};
        stc(st, head * 8192 + w * 2048 + lane * 16, r0);
        stc(st, head * 8192 + w * 2048 + 1024 + lane * 16, r1);
        hold(r0, r1);
        STAMP(4);
      } else {
        __builtin_amdgcn_s_sleep(127);
      }
      pre2(0, 4, 6); pre2(4, 10, 0);
      waitv<24>();
      pre2rd();
      pre2w();
      pre2(10, 14, 6);
    }
  } else {
    const unsigned ring = lds0 + L_WQ + w * 12544, sxa = (128 * T + 32 * w) * (D * 2);
    unsigned vxa[4];
#pragma unroll
    for (int j = 0; j < 4; j++) vxa[j] = (8 * j + swz) * (D * 2) + (((lane & 7) ^ ((4 * j + (lane >> 4)) & 7)) * 16) - j * 1024;
    unsigned sink = 0;
    u32x2 gen = ldc2(rsF, 0);
    dma4(rsX, vxa, sxa, ring);
    const u32x4 rsW = mkrs(wqkv, D * 3 * D * 2);
    {
      const unsigned r6 = (lane * 43) >> 8, c6 = lane - 6 * r6;
      touch(sink, rsW, lane < 42 ? r6 * 2688 + (c6 >> 1) * 896 + (c6 & 1) * 108 : OOB, (28 * T + 7 * w) * 2688 + pr * 112);
    }
    dma4(rsX, vxa, sxa + 128, ring + 4096);
    dma4(rsX, vxa, sxa + 256, ring + 8192);
    unsigned vw[8];
#pragma unroll
    for (int s = 0; s < 8; s++) {
      const unsigned c = (s < 7 ? 256 * s + 64 * w : 3072) + lane, r = (c * 9363) >> 16;
      vw[s] = ((r & ~15u) + 8 * ((r >> 1) & 1) + 4 * (r & 1) + ((r >> 2) & 1) + 2 * ((r >> 3) & 1)) * 2688 + (c - 7 * r) * 16;
    }
    const unsigned mw = lds0 + w * 1024;
    auto mW = [&](auto ss, auto bb) {
      if constexpr (ss.value < 12) addm0<bb.value + ss.value * 4096>(mw); else addm0<bb.value + 49152>(lds0);
    };
    auto dmaW = [&](auto ss, unsigned col) {
      constexpr int s = ss.value;
      dma<0>(rsW, vw[s < 12 ? (s + 14) % 7 : 7], col + pr * 112 + (s >= 7 && s < 12 ? 256 * 2688 : 0));
    };
    bf16x8 xa[28];
    float sa = 0.f, sq = 0.f;
    {
      unsigned pr_[4];
#pragma unroll
      for (int k = 0; k < 4; k++) pr_[k] = ring + n * 128 + (((2 * k + h) ^ ((n >> 1) & 7)) * 16);
      unroll<0, 7>([&](auto cc) {
        constexpr int cb = cc.value;
        waitv<(cb == 0 ? 9 : 8)>();
#pragma unroll
        for (int k = 0; k < 4; k++) xa[4 * cb + k] = lld<bf16x8>((ldsp)(uintptr_t)pr_[k] + (cb % 3) * 4096);
        asm volatile("s_waitcnt lgkmcnt(0)" : "+v"(xa[4 * cb]), "+v"(xa[4 * cb + 1]), "+v"(xa[4 * cb + 2]), "+v"(xa[4 * cb + 3])::"memory");
        if constexpr (cb < 4) dma4(rsX, vxa, sxa + (cb + 3) * 128, ring + (cb % 3) * 4096);
        if constexpr (cb == 4) unroll<0, 4>([&](auto ss) { mW(ss, IC<L_WK>()); asm volatile("s_nop 0"); dmaW(ss, 896); });
        if constexpr (cb == 5) unroll<0, 4>([&](auto ss) { mW(ss, IC<L_WV>()); asm volatile("s_nop 0"); dmaW(ss, 1792); });
        if constexpr (cb == 6) {
          setm0(lds0 + L_WPHI + w * 1024);
          dma<0>(mkrs(wphi + pr * 1568, 3136), tid * 16, 0);
        }
        SB();
#pragma unroll
        for (int k = 0; k < 4; k++) acc_stats(xa[4 * cb + k], sa, sq);
        SB();
      });
    }
    STAMP(1);
    hold(vxa[0], vxa[1], vxa[2], vxa[3]);
    tie(gen);
    setep(gen);
    float la, lc;
    {
      float s2 = sa, q2 = sq;
      swap32(sa, s2); swap32(sq, q2);
      const float mu = (sa + s2) * (1.f / D), var = fmaxf((sq + q2) * (1.f / D) - mu * mu, 0.f);
      la = __builtin_amdgcn_rsqf(var + 1e-5f);
      lc = -mu * la;
    }
    auto norm = [&](auto kk, auto ee) {
      constexpr int ks = kk.value, e = ee.value;
      if constexpr (ks < 28) {
        u32x4 u = __builtin_bit_cast(u32x4, xa[ks]);
        u[e] = affine2(u[e], la, lc);
        xa[ks] = __builtin_bit_cast(bf16x8, u);
      }
    };
    unroll<0, 4>([&](auto ee) { norm(IC<0>(), ee); });
    norm(IC<1>(), IC<0>());
    tie(xa[0]); tie(xa[1]);
    waitv<1>();
    bar();
    STAMP(3);
    const unsigned vt2 = (lane >> 3) * (S * 2) + (lane & 7) * 128;
    auto touch2 = [&](int m) { touch(sink, rsW2, vt2, (448 * w + 128 * m + 8 * T) * (S * 2) + (xc >> 1) * 1024); };
    const unsigned wlane = (2 * h + 4 * q) * 112 + 32 * blk + 8 * pp;
    f32x16 kT[2] = {}, vv[2] = {};
    {
      const ldsp pk_ = lds + L_WK + wlane, pv_ = lds + L_WV + wlane;
      s16x4 fr[30][8];
      auto rd = [&](auto kk, auto cc) {
        constexpr int ks = kk.value, c = cc.value;
        if constexpr (ks < 28) fr[ks][c] = vtr(((c & 4) ? pv_ : pk_) + ks * 1792 + ((c >> 1) & 1) * 56 + (c & 1) * 112);
      };
      unroll<0, 8>([&](auto cc) { rd(IC<0>(), cc); });
      unroll<0, 8>([&](auto cc) { rd(IC<1>(), cc); });
      SB();
      unroll<0, 28>([&](auto kk) {
        constexpr int ks = kk.value;
        if constexpr (ks == 7) { waitv<6>(); asm volatile("s_barrier" ::: "memory"); }
        if constexpr (ks == 16) { waitv<1>(); asm volatile("s_barrier" ::: "memory"); }
        __builtin_amdgcn_s_waitcnt(ks < 27 ? 0xc87f : 0xc07f);
        SB();
        unroll<0, 4>([&](auto mm) {
          constexpr int m = mm.value;
          const bf16x8 wv = cat(fr[ks][2 * m], fr[ks][2 * m + 1]);
          if constexpr (m < 2) kT[m] = MFMA(wv, xa[ks], kT[m]);
          else vv[m - 2] = MFMA(xa[ks], wv, vv[m - 2]);
          SB();
          if constexpr (ks < 9 && m == 0) mW(IC<4 + ks>(), IC<L_WK>());
          if constexpr (ks < 9 && m == 2) mW(IC<4 + ks>(), IC<L_WV>());
          rd(IC<ks + 2>(), IC<2 * m>());
          rd(IC<ks + 2>(), IC<2 * m + 1>());
          SB();
          if constexpr (ks < 9 && !(m & 1)) dmaW(IC<4 + ks>(), m ? 1792 : 896);
          if constexpr (ks == 9 && m == 0) touch(sink, rsW1, vt1, so_t1(8 + (T >> 1), (2 * T + (w >> 1)) & 3, 16 * (w & 1)));
          if constexpr (ks == 22 && m == 0) touch2(0);
          SB();
          norm(IC<ks + 1 + (m == 3)>(), IC<(m + 1) & 3>());
          SB();
        });
      });
    }
    bf16x8 wf[2][2];
    {
      const ldsp p = lds + L_WPHI + (4 * h + q) * 56 + 32 * blk + 8 * pp;
#pragma unroll
      for (int hd = 0; hd < 2; hd++) {
        wf[hd][0] = cat(vtr(p + hd * 1568), vtr(p + hd * 1568 + 8 * 56));
        s16x4 z = vtr(p + hd * 1568 + 24 * 56);
        wf[hd][1] = cat(vtr(p + hd * 1568 + 16 * 56), h ? (s16x4){0, 0, 0, 0} : z);
      }
    }
    STAMP(5);
    {
      f32x16 p[2];
#pragma unroll
      for (int hd = 0; hd < 2; hd++) { p[hd] = MFMA(wf[hd][0], pack8(kT[hd], 0), (f32x16)(0.f)); p[hd] = MFMA(wf[hd][1], pack8(kT[hd], 8), p[hd]); }
      bar();
      {
        const bf16x8 ones = {0x3f80, 0x3f80, 0x3f80, 0x3f80, 0x3f80, 0x3f80, 0x3f80, 0x3f80};
        const ldsp pvx = lds + L_VX + w * 2048 + lane * 16;
#pragma unroll
        for (int hd = 0; hd < 2; hd++)
#pragma unroll
          for (int j = 0; j < 2; j++) { bf16x8 f = pack8(vv[hd], 8 * j); lst(pvx + hd * 8192 + j * 1024, n == 28 ? ones : f); }
      }
      const unsigned pb = (32 * w + n) * 64 + ((h ^ ((n >> 1) & 7)) * 8);
      unroll<0, 2>([&](auto hh) {
        constexpr int hd = hh.value;
        unsigned ph[2][8];
        features(p[hd], h, ph, [&](auto kk, auto& pin) {
          constexpr int s = 12 * hd + kk.value;
          if constexpr (s < 13) {
            mW(IC<s>(), IC<L_WQ>());
            asm volatile("s_nop 0");
            dmap<0>(rsW, vw[s < 12 ? s % 7 : 7], pr * 112 + (s >= 7 && s < 12 ? 256 * 2688 : 0), pin);
          }
          if constexpr (s == 14) touch2(1);
        });
#pragma unroll
        for (int sg = 0; sg < 2; sg++)
#pragma unroll
          for (int g = 0; g < 4; g++) lst(lds + L_PHI + (hd * 2 + sg) * 8192 + (pb ^ (g << 4)), (u32x2){ph[sg][2 * g], ph[sg][2 * g + 1]});
      });
    }
    STAMP(6);
    hold(vw[0], vw[1], vw[2], vw[3], vw[4], vw[5], vw[6], vw[7], vt1);
    waitv<1>();
    bar();
    u32x4 kvp[2];
    const unsigned vkv = (2 * pr + (w >> 1)) * 65536 + T * 4096 + (w & 1) * 2048 + lane * 16;
    {
      const unsigned sw = 2 * h + (q >> 1);
      const ldsp pl = lds + L_PHI + w * 8192 + (4 * h + q) * 64 + (((4 * blk + pp) ^ sw) * 8);
      const ldsp ph_ = lds + L_PHI + w * 8192 + (8 + 4 * h + q) * 64 + (((4 * blk + pp) ^ sw ^ 4) * 8);
      const ldsp pvx = lds + L_VX + (w >> 1) * 8192 + lane * 16;
      f32x16 kv = {};
#pragma unroll
      for (int jj = 0; jj < 8; jj++) kv = MFMA(cat(vtr(pl + jj * 1024), vtr(ph_ + jj * 1024)), lld<bf16x8>(pvx + jj * 1024), kv);
      const rsrc_t st = mkst(ws + WS_KVP, SZ_KVP);
      kvp[0] = __builtin_bit_cast(u32x4, pack8(kv, 0)); kvp[1] = __builtin_bit_cast(u32x4, pack8(kv, 8));
      stc(st, vkv, kvp[0]);
      stc(st, vkv + 1024, kvp[1]);
    }
    STAMP(7);
    f32x16 qT[2] = {};
    {
      const ldsp pq_ = lds + L_WQ + wlane;
      s16x4 fr[29][4];
      auto rd = [&](auto kk, auto cc) {
        constexpr int ks = kk.value, c = cc.value;
        if constexpr (ks < 28) fr[ks][c] = vtr(pq_ + ks * 1792 + (c >> 1) * 56 + (c & 1) * 112);
      };
      unroll<0, 4>([&](auto cc) { rd(std::integral_constant<int, 0>(), cc); });
      unroll<0, 28>([&](auto kk) {
        constexpr int ks = kk.value;
        if constexpr (ks == 14) { hold(kvp[0], kvp[1], vkv); publish(ws, FL_P + pr * 16 + T, ep); STAMP(8); touch2(2); touch2(3); SB(); }
        unroll<0, 2>([&](auto mm) {
          constexpr int m = mm.value;
          rd(std::integral_constant<int, ks + 1>(), std::integral_constant<int, 2 * m>());
          rd(std::integral_constant<int, ks + 1>(), std::integral_constant<int, 2 * m + 1>());
          qT[m] = MFMA(cat(fr[ks][2 * m], fr[ks][2 * m + 1]), xa[ks], qT[m]);
          SB();
        });
      });
    }
    unsigned phq[2][2][8];
    {
      f32x16 p[2];
#pragma unroll
      for (int hd = 0; hd < 2; hd++) { p[hd] = MFMA(wf[hd][0], pack8(qT[hd], 0), (f32x16)(0.f)); p[hd] = MFMA(wf[hd][1], pack8(qT[hd], 8), p[hd]); }
      bar();
      STAMP(9);
      unroll<0, 2>([&](auto hh) {
        features(p[hh.value], h, phq[hh.value], [&](auto kk, auto& pin) {
          constexpr int k = 12 * hh.value + kk.value, t = k >> 2;
          if constexpr ((k & 3) == 0) setm0(area + ((t + 6) % 10) * 4096);
          dmap<(k & 3) * 1024>(rsW2, v2[k & 3], so2 + t * (32 * S * 2), pin);
        });
      });
    }
    STAMP(10);
    pre2(6, 9, 2);
    {
      const u32x4 rsK = mkrs(ws + WS_KVF, SZ_KVF);
      const unsigned so = pr * 16384 + w * 2048, tagB = ep.lo ^ ep.hi;
      const ldsp px = lds + 5 * 4096 + lane * 16;
#pragma unroll 1
      for (int it = 0; it < SPIN; ++it) {
        bf16x8 g0 = ldc<0>(rsK, lane * 16, so), g1 = ldc<1024>(rsK, lane * 16, so), g2 = ldc<0>(rsK, lane * 16, so + 8192), g3 = ldc<1024>(rsK, lane * 16, so + 8192);
        waitv<0>(g0, g1, g2, g3);
        const u32x4 u0 = __builtin_bit_cast(u32x4, g0), u1 = __builtin_bit_cast(u32x4, g1), u2 = __builtin_bit_cast(u32x4, g2), u3 = __builtin_bit_cast(u32x4, g3);
        lst(px + w * AREA, (u32x4){u0[0], u0[2], u1[0], u1[2]});
        lst(px + w * AREA + 1024, (u32x4){u2[0], u2[2], u3[0], u3[2]});
        const unsigned bad = ((u0[1] ^ ep.lo) | (u1[1] ^ ep.lo) | (u2[1] ^ ep.lo) | (u3[1] ^ ep.lo)) | ((u0[3] ^ tagB) | (u1[3] ^ tagB) | (u2[3] ^ tagB) | (u3[3] ^ tagB));
        if (__all(bad == 0)) break;
      }
      STAMP(2);
      tie(sink);
      bar();
      STAMP(11);
      bf16x8 kf[2][4];
#pragma unroll
      for (int hd = 0; hd < 2; hd++)
#pragma unroll
        for (int c = 0; c < 4; c++) kf[hd][c] = lld<bf16x8>(px + c * AREA + hd * 1024);
      f32x16 o[2] = {};
#pragma unroll
      for (int hd = 0; hd < 2; hd++) {
#pragma unroll
        for (int c = 0; c < 4; c++) {
          const unsigned* s = &phq[hd][c >> 1][4 * (c & 1)];
          o[hd] = MFMA(kf[hd][c], __builtin_bit_cast(bf16x8, (u32x4){s[0], s[1], s[2], s[3]}), o[hd]);
        }
      }
      float da = o[0][12], db = o[1][12];
      swap32(da, db);
      float ia = __builtin_amdgcn_rcpf(db + 1e-6f), ib = ia;
      swap32(ia, ib);
      unsigned a[8], bq[8];
#pragma unroll
      for (int r = 0; r < 8; r++) { a[r] = pk(o[0][2 * r] * ia, o[0][2 * r + 1] * ia); bq[r] = pk(o[1][2 * r] * ib, o[1][2 * r + 1] * ib); }
      const rsrc_t st = mkst(ws + WS_OF, SZ_OF);
      const unsigned tk = (128 * T + 32 * w + n) * 16, jb = 14 * (pr >> 1);
      const unsigned vo = (jb + 6 * (pr & 1) + h) * (S * 16) + tk;
      const unsigned v1 = vo + 2 * S * 16, v2_ = vo + 4 * S * 16, v3 = h ? OOB : (jb + 12 + (pr & 1)) * (S * 16) + tk;
      const u32x4 r0 = {a[0], a[1], a[2], a[3]}, r1 = {a[4], a[5], bq[0], bq[1]}, r2 = {bq[2], bq[3], bq[4], bq[5]}, r3 = {a[6], a[7], bq[6], bq[7]};
      stc(st, vo, r0); stc(st, v1, r1); stc(st, v2_, r2); stc(st, v3, r3);
      pre2rd();
      pre2w();
      hold(vo, v1, v2_, v3, r0, r1, r2, r3);
    }
    dma4(rsW2, v2, so2 + 10 * (32 * S * 2), area + 6 * 4096);
    publish<4>(ws, FL_O + T * 8 + pr, ep);
    STAMP(12);
    pre2(9, 10, 5);
    pre2(11, 14, 7);
  }

  if (i >= 4) {
    const unsigned orow = st2 * 64 + 16 * w + (lane >> 3);
    const unsigned vo = orow * (D * 2) + dt2 * 128 + (lane & 7) * 16;
    const u32x4 rsB = mkrs(b2, S * 2), rsU = mkrs(ws + WS_UF, SZ_UF), rsO = mkrs(ws + WS_OF, SZ_OF), rsWo = mkrs(wo, D * D * 2);
    u32x4 wor[14];
    unsigned vwo[4];
#pragma unroll
    for (int j = 0; j < 4; j++) vwo[j] = swz * (D * 2) + swc + j * (8 * D * 2 - 1024);
    unroll<0, 14>([&](auto jj) {
      constexpr int j = jj.value & 3;
      wor[jj.value] = lda<j * 1024>(rsWo, vwo[j], w * 112 * (D * 2) + dt2 * 128 + (jj.value >> 2) * (32 * D * 2));
    });
    poll(rsF, (FL_U + dt2 * 14 + ((7 * w) >> 1) + (lane & 3)) * 128, ep);
    hold(vwo[0], vwo[1], vwo[2], vwo[3]);
    STAMP(13);
    const unsigned vu0 = h * (D * 16) + (dt2 * 64 + n) * 16, vu1 = vu0 + 2 * D * 16, sou = w * 56 * (D * 16);
    const unsigned vof = h * (S * 16) + (st2 * 64 + n) * 16, soo = w * 14 * (S * 16);
    const unsigned vfl = (FL_O + (st2 >> 1) * 8 + 2 * w + (lane & 1)) * 128;
    bf16x8 fa[18][4], xr0, xr1;
    unsigned bs0, bs1;
    const unsigned vb2 = orow * 2;
    u32x2 fl;
    auto ldD = [&](auto tt, auto jj) {
      constexpr int t = tt.value, j = jj.value;
      if constexpr (t < 14) fa[t][j] = ldc<(j & 1) * 512>(rsU, (j >> 1) ? vu1 : vu0, sou + t * (4 * D * 16));
      else if constexpr (j < g2ld(t)) fa[t][j] = ldc<(j & 1) * 512>(rsO, vof, soo + (2 * (t - 14) + (j >> 1)) * (2 * S * 16));
    };
    unroll<0, G2R>([&](auto tt) { unroll<0, 4>([&](auto jj) { ldD(tt, jj); }); });
    const ldsp pw[2] = {pb[0] - h * 512, pb[1] - h * 512};
    f32x16 acc[2][2] = {};
    bf16x8 wb[19][2][2];
    auto rdB = [&](auto tt, auto kk, auto aa) {
      constexpr int t = tt.value, ks = kk.value, a = aa.value, j = 2 * (t - 14) + ks;
      if constexpr (t < 4) wb[t][ks][a] = w2r[t][ks][a];
      else if constexpr (t < 14) wb[t][ks][a] = cat(vtr(pb[a] + (t - 4) * 4096 + ks * 2048), vtr(pb[a] + (t - 4) * 4096 + ks * 2048 + 512));
      else if constexpr (j < 6) {
        constexpr int u = j % 3, rl = 56 * (j / 3) + (u == 0 ? 0 : u == 1 ? 16 : 36), rh = 56 * (j / 3) + (u == 0 ? 8 : u == 1 ? 28 : 44);
        wb[t][ks][a] = cat(vtr(pw[a] + rl * 128), vtr(pw[a] + rh * 128));
      } else if constexpr (j == 6) wb[t][ks][a] = cat(vtr(pw[a] + h * (52 * 128) + 24 * 128), vtr(pw[a] + h * (52 * 128) + 52 * 128));
    };
    unroll<0, 4>([&](auto cc) { rdB(IC<0>(), IC<(cc.value >> 1)>(), IC<(cc.value & 1)>()); });
    unroll<0, 18>([&](auto tt) {
      constexpr int t = tt.value, nk = t < 17 ? 2 : 1;
      constexpr int left = g2cnt(t - 3) + g2cnt(t - 2) + g2cnt(t - 1);
      if constexpr (nk == 2) waitv<left>(fa[t][0], fa[t][1], fa[t][2], fa[t][3]); else waitv<left>(fa[t][0], fa[t][1]);
      if constexpr (t == G2POLL + G2R) {
        tie(fl);
        if (!match(fl, ep)) poll(rsF, vfl, ep);
      }
      SB();
      unroll<0, 4 * nk>([&](auto mm) {
        constexpr int m = mm.value, ks = m >> 2, nh = (m >> 1) & 1, mh = m & 1;
        if constexpr (t < 14) acc[nh][mh] = MFMA(fa[t][ks * 2 + mh], wb[t][ks][nh], acc[nh][mh]);
        else acc[nh][mh] = MFMA(wb[t][ks][mh], fa[t][ks * 2 + nh], acc[nh][mh]);
        SB();
        if constexpr (m & 1) rdB(IC<t + 1>(), IC<(m >> 2)>(), IC<((m >> 1) & 1)>());
        else if constexpr (t >= 5 && 4 * (t - 5) + m / 2 < 14) sta<(4 * (t - 5) + m / 2) * 1024>(area + lane * 16, wor[4 * (t - 5) + m / 2]);
        SB();
        if constexpr (!(m & 1)) ldD(IC<t + G2R>(), IC<m / 2>());
        if constexpr (t == G2POLL && m == 7) fl = ldc2(rsF, vfl);
        if constexpr (t == 14 && m == 0) xr0 = ldp<0>(rsX, vo, 0);
        if constexpr (t == 14 && m == 2) xr1 = ldp<0>(rsX, vo, 8 * D * 2);
        if constexpr (t == 14 && m == 4) bs0 = ldu16<0>(rsB, vb2, 0);
        if constexpr (t == 14 && m == 6) bs1 = ldu16<16>(rsB, vb2, 0);
        SB();
      });
    });
    STAMP(14);
#pragma unroll
    for (int nh = 0; nh < 2; nh++)
#pragma unroll
      for (int mh = 0; mh < 2; mh++)
#pragma unroll
        for (int g = 0; g < 4; g++)
          lst(lds + w * AREA + (32 * nh + n) * 256 + (((8 * mh + 2 * g + h) ^ (n & 15)) * 16),
              (f32x4){acc[nh][mh][4 * g], acc[nh][mh][4 * g + 1], acc[nh][mh][4 * g + 2], acc[nh][mh][4 * g + 3]});
    barv();
    tie(xr0); tie(xr1); tie(bs0); tie(bs1);
    hold(vb2);
    u32x4 res[2];
    const rsrc_t sto = mkst(out, S * D * 2);
#pragma unroll
    for (int u = 0; u < 2; u++) {
      const int r = 16 * w + 8 * u + (lane >> 3);
      const unsigned c0 = r * 256 + (((2 * (lane & 7)) ^ (r & 15)) * 16), c1 = c0 ^ 16;
      f32x4 a = lld<f32x4>(lds + c0), bq = lld<f32x4>(lds + c1);
#pragma unroll
      for (int s = 1; s < 4; s++) { a += lld<f32x4>(lds + s * AREA + c0); bq += lld<f32x4>(lds + s * AREA + c1); }
      const u32x4 xu = __builtin_bit_cast(u32x4, u ? xr1 : xr0);
      const float bv = blo(u ? bs1 : bs0);
      res[u] = (u32x4){pk(a[0] + bv + blo(xu[0]), a[1] + bv + bhi(xu[0])), pk(a[2] + bv + blo(xu[1]), a[3] + bv + bhi(xu[1])),
                   pk(bq[0] + bv + blo(xu[2]), bq[1] + bv + bhi(xu[2])), pk(bq[2] + bv + blo(xu[3]), bq[3] + bv + bhi(xu[3]))};
      __builtin_amdgcn_raw_buffer_store_b128(res[u], sto, vo + u * (8 * D * 2), 0, 16);
    }
    hold(res[0], res[1]);
    if (blockIdx.x == 255 && tid == 0) __builtin_amdgcn_raw_buffer_store_b64((u32x2){ep.lo, ep.hi}, mkst(ws, WS_FLAG_BYTES), 0, 0, 16);
  }
}

__global__ void tinyA(char* ws) { if (threadIdx.x == 0) *(volatile unsigned long long*)(ws + (200u << 20)) = __builtin_amdgcn_s_memrealtime(); }
__global__ void tinyZ(char* ws) {
  const unsigned long long t0 = *(volatile unsigned long long*)(ws + (200u << 20));
  for (int k = 0; k < 100000 && __builtin_amdgcn_s_memrealtime() - t0 < 6000; k++) __builtin_amdgcn_s_sleep(1);
}
extern "C" void kernel_launch(void* const* d_in, const int* in_sizes, int n_in, void* d_out, int out_size, void* d_ws, size_t ws_size,
                              hipStream_t stream) {
  tinyA<<<1, 64, 0, stream>>>((char*)d_ws);
  fused_kernel<<<256, 256, 0, stream>>>((const short*)d_in[0], (const short*)d_in[1], (const short*)d_in[2], (const short*)d_in[3],
                                        (const short*)d_in[5], (const short*)d_in[6], (const short*)d_in[7], (const short*)d_in[8],
                                        (short*)d_out, (char*)d_ws);
  tinyZ<<<1, 64, 0, stream>>>((char*)d_ws);
}
```

```cpp
#include <hip/hip_runtime.h>
#include <stdint.h>

typedef short bf16x8 __attribute__((ext_vector_type(8)));
typedef short s16x4 __attribute__((ext_vector_type(4)));
typedef float f32x2 __attribute__((ext_vector_type(2)));
typedef float f32x4 __attribute__((ext_vector_type(4)));
typedef float f32x16 __attribute__((ext_vector_type(16)));
typedef unsigned u32x2 __attribute__((ext_vector_type(2)));
typedef unsigned u32x4 __attribute__((ext_vector_type(4)));
typedef __bf16 b16x2 __attribute__((ext_vector_type(2)));
typedef __attribute__((address_space(3))) char* ldsp;
typedef __amdgpu_buffer_rsrc_t rsrc_t;

#define DEV __device__ __forceinline__
#define SB() __builtin_amdgcn_sched_barrier(0)
#define MFMA(a, b, c) __builtin_amdgcn_mfma_f32_32x32x16_bf16(a, b, c, 0, 0, 0)

constexpr int S = 2048, D = 448, F = 1792;
constexpr unsigned FL_P = 8, FL_R = FL_P + 128, FL_O = FL_R + 16, FL_U = FL_O + 128;
constexpr unsigned WS_FLAG_BYTES = 65536;
constexpr unsigned WS_ST = WS_FLAG_BYTES, SZ_ST = S * 16;
constexpr unsigned WS_UF = WS_ST + SZ_ST, SZ_UF = 224 * 448 * 16;
constexpr unsigned WS_OF = WS_UF + SZ_UF, SZ_OF = 56 * 2048 * 16;
constexpr unsigned WS_KVP = WS_OF + SZ_OF, SZ_KVP = 16 * 16 * 4096;
constexpr unsigned WS_KVF = WS_KVP + SZ_KVP, SZ_KVF = 16 * 8 * 1024;
constexpr unsigned WS_END = WS_KVF + SZ_KVF;
constexpr unsigned AREA = 40960, L_STAT = 36864, SLOT = 12288;
constexpr unsigned L_WK = 0, L_WV = 50176, L_WQ = 100352, L_WPHI = 150528, L_PHI = 0, L_VX = 32768;
constexpr unsigned OOB = 0x40000000u;
constexpr int SPIN = 1 << 18;

DEV u32x4 mkrs(const void* p, unsigned bytes) {
  unsigned long long a = (unsigned long long)p;
  u32x4 r = {(unsigned)a, (unsigned)(a >> 32), bytes, 0x00020000u};
  return r;
}
DEV rsrc_t mkst(void* p, unsigned bytes) { return __builtin_amdgcn_make_buffer_rsrc(p, 0, bytes, 0x00020000); }
DEV void stc(rsrc_t rs, unsigned voff, u32x4 v) { __builtin_amdgcn_raw_buffer_store_b128(v, rs, voff, 0, 16); }
template <int OFF> DEV bf16x8 ldp(u32x4 rs, unsigned voff, unsigned soff) {
  bf16x8 r;
  asm volatile("buffer_load_dwordx4 %0, %2, %1, %3 offen offset:%4" : "=v"(r) : "s"(rs), "v"(voff), "s"(soff), "n"(OFF) : "memory");
  return r;
}
template <int OFF> DEV bf16x8 ldc(u32x4 rs, unsigned voff, unsigned soff) {
  bf16x8 r;
  asm volatile("buffer_load_dwordx4 %0, %2, %1, %3 offen offset:%4 sc1" : "=v"(r) : "s"(rs), "v"(voff), "s"(soff), "n"(OFF) : "memory");
  return r;
}
template <int OFF> DEV u32x2 ldp2(u32x4 rs, unsigned voff, unsigned soff) {
  u32x2 r;
  asm volatile("buffer_load_dwordx2 %0, %2, %1, %3 offen offset:%4" : "=v"(r) : "s"(rs), "v"(voff), "s"(soff), "n"(OFF) : "memory");
  return r;
}
template <int OFF> DEV u32x4 lda(u32x4 rs, unsigned voff, unsigned soff) {
  u32x4 r;
  asm volatile("buffer_load_dwordx4 %0, %2, %1, %3 offen offset:%4" : "=a"(r) : "s"(rs), "v"(voff), "s"(soff), "n"(OFF) : "memory");
  return r;
}
template <int OFF, class T> DEV u32x4 ldap(u32x4 rs, unsigned voff, unsigned soff, T& pin) {
  u32x4 r;
  asm volatile("buffer_load_dwordx4 %0, %3, %2, %4 offen offset:%5" : "=a"(r), "+v"(pin) : "s"(rs), "v"(voff), "s"(soff), "n"(OFF) : "memory");
  return r;
}
template <int OFF> DEV void sta(unsigned addr, u32x4 v) { asm volatile("ds_write_b128 %0, %1 offset:%2" ::"v"(addr), "a"(v), "n"(OFF) : "memory"); }
DEV u32x2 ldc2(u32x4 rs, unsigned voff) {
  u32x2 r;
  asm volatile("buffer_load_dwordx2 %0, %2, %1, 0 offen sc1" : "=v"(r) : "s"(rs), "v"(voff) : "memory");
  return r;
}
template <int OFF> DEV unsigned ldu16(u32x4 rs, unsigned voff, unsigned soff) {
  unsigned r;
  asm volatile("buffer_load_ushort %0, %2, %1, %3 offen offset:%4" : "=v"(r) : "s"(rs), "v"(voff), "s"(soff), "n"(OFF) : "memory");
  return r;
}
DEV void setm0(unsigned a) { asm volatile("s_mov_b32 m0, %0\n\ts_nop 0" ::"s"(a) : "memory"); }
DEV void setm0n(unsigned a) { asm volatile("s_mov_b32 m0, %0" ::"s"(a) : "memory"); }
template <int OFF> DEV void addm0(unsigned a) { asm volatile("s_add_u32 m0, %0, %1" ::"s"(a), "n"(OFF) : "memory", "scc"); }
template <int OFF> DEV void dma(u32x4 rs, unsigned voff, unsigned soff) {
  asm volatile("buffer_load_dwordx4 %1, %0, %2 offen offset:%3 lds" ::"s"(rs), "v"(voff), "s"(soff), "n"(OFF) : "memory");
}
DEV void dma4(u32x4 rs, const unsigned (&v)[4], unsigned soff, unsigned m0) {
  setm0(m0); dma<0>(rs, v[0], soff); dma<1024>(rs, v[1], soff); dma<2048>(rs, v[2], soff); dma<3072>(rs, v[3], soff);
}
template <int J> DEV void dma1(u32x4 rs, const unsigned (&v)[4], unsigned soff, unsigned m0) {
  if (J == 0) setm0(m0);
  dma<J * 1024>(rs, v[J], soff);
}
template <int N> DEV void waitv() { asm volatile("s_waitcnt vmcnt(%0)" ::"n"(N) : "memory"); }
template <int N, class T> DEV void waitv(T& a) { asm volatile("s_waitcnt vmcnt(%1)" : "+v"(a) : "n"(N) : "memory"); }
template <int N, class T> DEV void waitv(T& a, T& b) { asm volatile("s_waitcnt vmcnt(%2)" : "+v"(a), "+v"(b) : "n"(N) : "memory"); }
template <int N, class T> DEV void waitv(T& a, T& b, T& c, T& d) {
  asm volatile("s_waitcnt vmcnt(%4)" : "+v"(a), "+v"(b), "+v"(c), "+v"(d) : "n"(N) : "memory");
}
template <class T> DEV void tie(T& a) { asm volatile("" : "+v"(a)::"memory"); }
template <class T> DEV void hold(T a) { asm volatile("" ::"v"(a)); }
template <class T, class... R> DEV void hold(T a, R... r) { hold(a); hold(r...); }
DEV void keep(f32x16 a) { asm volatile("" ::"v"(a)); }
DEV void bar() { asm volatile("s_waitcnt lgkmcnt(0)\n\ts_barrier" ::: "memory"); }
DEV void barv() { asm volatile("s_waitcnt vmcnt(0) lgkmcnt(0)\n\ts_barrier" ::: "memory"); }
DEV s16x4 vtr(ldsp p) { return __builtin_amdgcn_ds_read_tr16_b64_v4i16((__attribute__((address_space(3))) s16x4*)p); }
DEV bf16x8 cat(s16x4 a, s16x4 b) { return __builtin_shufflevector(a, b, 0, 1, 2, 3, 4, 5, 6, 7); }
DEV unsigned pk(float a, float b) {
  f32x2 v = {a, b};
  return __builtin_bit_cast(unsigned, __builtin_convertvector(v, b16x2));
}
DEV float blo(unsigned u) { return __builtin_bit_cast(float, u << 16); }
DEV float bhi(unsigned u) { return __builtin_bit_cast(float, u & 0xffff0000u); }
DEV void swap32(float& a, float& b) {
  auto r = __builtin_amdgcn_permlane32_swap(__builtin_bit_cast(unsigned, a), __builtin_bit_cast(unsigned, b), false, false);
  const unsigned r0 = r[0], r1 = r[1];
  a = __builtin_bit_cast(float, r0); b = __builtin_bit_cast(float, r1);
}
DEV void swap16(float& a, float& b) {
  auto r = __builtin_amdgcn_permlane16_swap(__builtin_bit_cast(unsigned, a), __builtin_bit_cast(unsigned, b), false, false);
  const unsigned r0 = r[0], r1 = r[1];
  a = __builtin_bit_cast(float, r0); b = __builtin_bit_cast(float, r1);
}
template <int C> DEV float ror(float z) {
  return __builtin_bit_cast(float, __builtin_amdgcn_update_dpp(0, __builtin_bit_cast(int, z), C, 0xf, 0xf, true));
}
DEV float rowsum(float z) { z += ror<0x128>(z); z += ror<0x124>(z); z += ror<0x122>(z); z += ror<0x121>(z); return z; }
DEV float rdl(float z, int l) { return __builtin_bit_cast(float, __builtin_amdgcn_readlane(__builtin_bit_cast(int, z), l)); }
template <class T> DEV void lst(ldsp p, T v) { *(__attribute__((address_space(3))) T*)p = v; }
template <class T> DEV T lld(ldsp p) { return *(__attribute__((address_space(3))) T*)p; }

struct Epoch { unsigned lo, hi; };
template <int N = 0> DEV void publish(char* ws, unsigned flag, Epoch e) {
  asm volatile("s_waitcnt vmcnt(%0) lgkmcnt(0)\n\ts_barrier" ::"n"(N) : "memory");
  if (threadIdx.x == 0) __builtin_amdgcn_raw_buffer_store_b64((u32x2){e.lo, e.hi}, mkst(ws, WS_FLAG_BYTES), flag * 128, 0, 16);
}
DEV bool match(u32x2 f, Epoch e) { return __all(f.x == e.lo && f.y == e.hi); }
DEV void poll(u32x4 rsF, unsigned voff, Epoch e) {
#pragma unroll 1
  for (int it = 0; it < SPIN; ++it) {
    u32x2 f = ldc2(rsF, voff);
    waitv<0>(f);
    if (match(f, e)) break;
    __builtin_amdgcn_s_sleep(1);
  }
}

DEV void pollm(u32x4 rsF, unsigned voff, Epoch e, unsigned need, unsigned& seen) {
  const unsigned long long e64 = ((unsigned long long)e.hi << 32) | e.lo;
  unsigned cnt, t;
  u32x2 f;
  asm volatile(
      "s_mov_b32 %1, %8\n"
      "1:\n\ts_and_b32 %2, %0, %7\n\ts_cmp_eq_u32 %2, %7\n\ts_cbranch_scc1 2f\n\t"
      "buffer_load_dwordx2 %3, %5, %4, 0 offen sc1\n\ts_waitcnt vmcnt(0)\n\t"
      "v_cmp_eq_u64 vcc, %6, %3\n\ts_nop 4\n\ts_mov_b32 %0, vcc_lo\n\ts_sub_u32 %1, %1, 1\n\ts_cbranch_scc0 1b\n"
      "2:"
      : "+s"(seen), "=&s"(cnt), "=&s"(t), "=&v"(f)
      : "s"(rsF), "v"(voff), "s"(e64), "s"(need), "n"(SPIN)
      : "memory", "vcc", "scc");
}

DEV void acc_stats(bf16x8 v, float& s, float& q) {
  const u32x4 u = __builtin_bit_cast(u32x4, v);
#pragma unroll
  for (int e = 0; e < 4; e++) {
    const unsigned t = u[e];
    const b16x2 p = __builtin_bit_cast(b16x2, t);
    s = __builtin_amdgcn_fdot2_f32_bf16(p, __builtin_bit_cast(b16x2, 0x3f803f80u), s, false);
    q = __builtin_amdgcn_fdot2_f32_bf16(p, p, q, false);
  }
}
DEV unsigned affine2(unsigned u, float a, float c) {
  float t0, t1;
  unsigned r;
  asm("v_lshlrev_b32 %1, 16, %3\n\tv_and_b32 %2, 0xffff0000, %3\n\tv_fma_f32 %1, %1, %4, %5\n\tv_fma_f32 %2, %2, %4, %5\n\tv_cvt_pk_bf16_f32 %0, %1, %2"
      : "=v"(r), "=&v"(t0), "=&v"(t1)
      : "v"(u), "v"(a), "v"(c));
  return r;
}
DEV f32x16 mfmav(u32x4 a, bf16x8 b, f32x16 c) {
  f32x16 r;
  asm volatile("v_mfma_f32_32x32x16_bf16 %0, %1, %2, %3" : "=&v"(r) : "v"(a), "v"(b), "v"(c));
  return r;
}
DEV void mfmac(f32x16& c, bf16x8 a, u32x4 b) { asm volatile("v_mfma_f32_32x32x16_bf16 %0, %1, %2, %0" : "+v"(c) : "v"(a), "v"(b)); }
DEV void touch(unsigned& sink, u32x4 rs, unsigned voff, unsigned soff) {
  asm volatile("buffer_load_dword %0, %2, %1, %3 offen" : "+v"(sink) : "s"(rs), "v"(voff), "s"(soff) : "memory");
}
DEV void touchc(unsigned& sink, u32x4 rs, unsigned voff, unsigned soff) {
  asm volatile("buffer_load_dword %0, %2, %1, %3 offen sc1" : "+v"(sink) : "s"(rs), "v"(voff), "s"(soff) : "memory");
}
DEV void touchl(u32x4 rs, unsigned voff, unsigned soff) {
  asm volatile("buffer_load_dword %1, %0, %2 offen lds" ::"s"(rs), "v"(voff), "s"(soff) : "memory");
}
template <int N> using IC = std::integral_constant<int, N>;
template <int T, int N, class Fn> DEV void unroll(Fn&& f) {
  if constexpr (T < N) { f(std::integral_constant<int, T>()); unroll<T + 1, N>(f); }
}
template <int OFF, class T> DEV void dmap(u32x4 rs, unsigned voff, unsigned soff, T& pin) {
  asm volatile("buffer_load_dwordx4 %2, %1, %3 offen offset:%4 lds" : "+v"(pin) : "s"(rs), "v"(voff), "s"(soff), "n"(OFF) : "memory");
}

template <class Fn> DEV void features(const f32x16& p, bool h, unsigned (&ph)[2][8], Fn&& cb) {
  constexpr float L = 1.4426950408889634f;
  float mx = fmaxf(p[0], p[1]), mn = fminf(p[0], p[1]);
#pragma unroll
  for (int r = 2; r < 12; r += 2) { mx = fmaxf(fmaxf(mx, p[r]), p[r + 1]); mn = fminf(fminf(mn, p[r]), p[r + 1]); }
  cb(IC<0>(), mx);
  float mx2 = fmaxf(fmaxf(fmaxf(fmaxf(mx, p[12]), p[13]), p[14]), p[15]);
  float mn2 = fminf(fminf(fminf(fminf(mn, p[12]), p[13]), p[14]), p[15]);
  mx = h ? mx : mx2; mn = h ? -mn : -mn2;
  swap32(mx, mn);
  float c0 = -L * fmaxf(mx, mn), c1 = c0;
  cb(IC<1>(), c0);
  swap32(c0, c1);
  const float ninf = -__builtin_inff();
  const float d0 = h ? ninf : c0, d1 = h ? ninf : c1;
  float e[2][16], s0 = 0.f, s1 = 0.f;
  unroll<0, 16>([&](auto rr) {
    constexpr int r = rr.value;
    float a0 = fmaf(p[r], L, r < 12 ? c0 : d0);
    if constexpr (r & 1) cb(IC<2 + r / 2>(), a0);
    e[0][r] = __builtin_amdgcn_exp2f(a0);
    e[1][r] = __builtin_amdgcn_exp2f(fmaf(p[r], -L, r < 12 ? c1 : d1));
    s0 += e[0][r]; s1 += e[1][r];
  });
  swap32(s0, s1);
  float i0 = __builtin_amdgcn_rcpf(s0 + s1), i1 = i0;
  swap32(i0, i1);
  unroll<0, 8>([&](auto rr) {
    constexpr int r = rr.value;
    ph[0][r] = pk(e[0][2 * r] * i0, e[0][2 * r + 1] * i0); ph[1][r] = pk(e[1][2 * r] * i1, e[1][2 * r + 1] * i1);
    if constexpr ((r & 3) == 1) cb(IC<10 + r / 4>(), ph[1][r]);
  });
}
DEV bf16x8 pack8(const f32x16& a, int o) {
  u32x4 u = {pk(a[o], a[o + 1]), pk(a[o + 2], a[o + 3]), pk(a[o + 4], a[o + 5]), pk(a[o + 6], a[o + 7])};
  return __builtin_bit_cast(bf16x8, u);
}
DEV float gelu(float a) {
  constexpr float c1 = -2.302208198f, c2 = c1 * 0.044715f;
  return a * __builtin_amdgcn_rcpf(1.f + __builtin_amdgcn_exp2f(a * fmaf(a * a, c2, c1)));
}

#define STAMP(n)

constexpr int woff(int k) { return (k >> 1) * SLOT + (k & 1) * 8192; }
constexpr int xoff(int k) { return (k >> 1) * SLOT + 4096 + (k & 1) * 2048; }
constexpr int G2R = 4, G2POLL = 5;
constexpr int g2ld(int t) { return t < 17 ? 4 : (t == 17 ? 2 : 0); }
constexpr int g2cnt(int s) { return s < 0 ? 4 : g2ld(s + G2R) + (s == G2POLL ? 1 : 0) + (s == 14 ? 4 : 0); }

__global__ __launch_bounds__(256, 1) void fused_kernel(char* __restrict__ ws, const short* __restrict__ x, const short* __restrict__ w1, const short* __restrict__ wqkv,
                                                       const short* __restrict__ b1, const short* __restrict__ wphi, const short* __restrict__ w2,
                                                       const short* __restrict__ wo, const short* __restrict__ b2, short* __restrict__ out) {
  __shared__ __attribute__((aligned(16))) char lds_[163840];
  const ldsp lds = (ldsp)lds_;
  const unsigned lds0 = (unsigned)(uintptr_t)lds;
  const int tid = threadIdx.x, lane = tid & 63, w = __builtin_amdgcn_readfirstlane(tid >> 6);
  asm volatile("s_nop 4" ::"s"(w), "s"(x), "s"(wqkv), "s"(wphi), "s"(w1), "s"(b1), "s"(w2), "s"(ws) : "memory");
  const int xc = blockIdx.x & 7, i = blockIdx.x >> 3;
  const int h = lane >> 5, n = lane & 31, q = (lane & 15) >> 2, pp = lane & 3, blk = (lane >> 4) & 1;
  const u32x4 rsF = mkrs(ws, WS_FLAG_BYTES), rsX = mkrs(x, S * D * 2), rsW2 = mkrs(w2, F * S * 2);
  const unsigned area = lds0 + w * AREA;
  const int tile2 = 28 * (xc & 1) + i - 4, dt2 = tile2 >> 3, st2 = 8 * (xc >> 1) + (tile2 & 7);
  const unsigned swz = (lane >> 3), swc = ((lane & 7) ^ (((lane >> 4) & 1) << 2)) * 16;
  unsigned v2[4];
#pragma unroll
  for (int j = 0; j < 4; j++) v2[j] = i >= 4 ? swz * (S * 2) + swc + j * (8 * S * 2 - 1024) : OOB;
  const unsigned so2 = (w * 448) * (S * 2) + st2 * 128;
  auto pre2 = [&](int t0, int t1, int slot0) {
#pragma unroll 1
    for (int t = t0; t < t1; t++) dma4(rsW2, v2, so2 + t * (32 * S * 2), area + (slot0 + t - t0) * 4096);
  };

  ldsp pb[2];
#pragma unroll
  for (int a = 0; a < 2; a++) pb[a] = lds + w * AREA + h * 1024 + q * 128 + (((4 * a + 2 * blk + (pp >> 1)) ^ ((q >> 1) << 2)) * 16) + (pp & 1) * 8;
  bf16x8 w2r[4][2][2];
  auto pre2w = [&]() {
#pragma unroll
    for (int t = 0; t < 4; t++) asm volatile("s_waitcnt lgkmcnt(0)" : "+v"(w2r[t][0][0]), "+v"(w2r[t][0][1]), "+v"(w2r[t][1][0]), "+v"(w2r[t][1][1])::"memory");
  };
  auto pre2rd = [&]() {
#pragma unroll
    for (int t = 0; t < 4; t++)
#pragma unroll
      for (int ks = 0; ks < 2; ks++)
#pragma unroll
        for (int a = 0; a < 2; a++) w2r[t][ks][a] = cat(vtr(pb[a] + (t + 6) * 4096 + ks * 2048), vtr(pb[a] + (t + 6) * 4096 + ks * 2048 + 512));
  };

  const bool isA = i >= 16;
  const int T = i - 16, pr = xc;
  const u32x4 rsW1 = mkrs(w1, S * F * 2);
  const unsigned vt1 = xc < 7 ? (lane >> 2) * (F * 2) + (lane & 3) * 128 : OOB;
  auto so_t1 = [&](int stage, int wq, int r0) { return (512 * wq + ((64 * xc + 32 * stage + r0) & 511)) * (F * 2) + xc * 512; };
  Epoch ep;
  auto setep = [&](u32x2 gen) {
    const unsigned long long g = (((unsigned long long)__builtin_amdgcn_readfirstlane(gen.y) << 32) | (unsigned)__builtin_amdgcn_readfirstlane(gen.x)) + 1;
    ep.lo = (unsigned)g; ep.hi = (unsigned)(g >> 32);
  };

  if (!isA) {
    const int row0 = 512 * (i >> 2) + 64 * xc + 16 * (i & 3) + 4 * w;
    const bool isG = xc < 7 && i < 14;
    const int ft = 2 * xc + (i >= 7), dt = i >= 7 ? i - 7 : i;
    bf16x8 xv[4];
    u32x2 gen, bb[4];
    unsigned vz = 0;
    tie(vz);
    const unsigned vo = lane < 56 ? lane * 16 : OOB, vt = lane < 32 ? vt1 : OOB, vb = h * 8;
    {
      const unsigned so = row0 * (D * 2);
      xv[0] = ldp<0>(rsX, vo, so); xv[1] = ldp<896>(rsX, vo, so); xv[2] = ldp<1792>(rsX, vo, so); xv[3] = ldp<2688>(rsX, vo, so);
      gen = ldc2(rsF, vz);
      setm0(area + L_STAT);
      touchl(rsW1, vt, so_t1(i >> 2, i & 3, 8 * w));
      const u32x4 rsB = mkrs(b1, F * 2);
      const unsigned sb = (ft * 128 + w * 32) * 2;
      bb[0] = ldp2<0>(rsB, vb, sb); bb[1] = ldp2<16>(rsB, vb, sb); bb[2] = ldp2<32>(rsB, vb, sb); bb[3] = ldp2<48>(rsB, vb, sb);
    }
    unsigned v1[4][4], vx[4][2];
#pragma unroll
    for (int ks = 0; ks < 4; ks++) {
#pragma unroll
      for (int j = 0; j < 4; j++) v1[ks][j] = (16 * ks + 8 * (j & 1) + swz) * (F * 2) + (j >> 1) * 128 + swc - j * 1024;
#pragma unroll
      for (int j = 0; j < 2; j++) vx[ks][j] = (16 * ks + 8 * j + swz) * (D * 2) + swc - (j + 2 * (ks & 1)) * 1024;
    }
    asm volatile("" ::"s"(wo), "s"(b2), "s"(out) : "memory");
    waitv<5>(xv[0], xv[1], xv[2], xv[3]);
    tie(gen);
    STAMP(0);
    setep(gen);
    const unsigned tagB = ep.lo ^ ep.hi, vs = (lane & 15) ? OOB : (row0 + h + 2 * blk) * 16;
    u32x4 rec;
    {
      float s[4] = {}, z[4] = {};
#pragma unroll
      for (int r = 0; r < 4; r++) acc_stats(xv[r], s[r], z[r]);
      swap32(s[0], s[1]); swap32(s[2], s[3]); swap32(z[0], z[1]); swap32(z[2], z[3]);
      float sa = s[0] + s[1], sb = s[2] + s[3], za = z[0] + z[1], zb = z[2] + z[3];
      swap16(sa, sb); swap16(za, zb);
      const float mu = rowsum(sa + sb) * (1.f / D), ms = rowsum(za + zb) * (1.f / D);
      const float var = fmaxf(ms - mu * mu, 0.f);
      float a = __builtin_amdgcn_rsqf(var + 1e-5f);
      a *= __builtin_amdgcn_rsqf(var * a * a + 1e-5f);
      const unsigned a2 = pk(a, a);
      const float c = -mu * blo(a2);
      rec = (u32x4){a2, ep.lo, __builtin_bit_cast(unsigned, c), tagB};
      stc(mkst(ws + WS_ST, SZ_ST), vs, rec);
    }
    asm volatile("s_barrier" ::: "memory");
    hold(vz, vo, vt, vb);
    STAMP(1);

    if (isG) {
      const unsigned b1s = (w * 512) * (F * 2) + ft * 256, bxs = (w * 512) * (D * 2) + dt * 128;
      auto mset = [&](auto kk, auto jj) {
        constexpr int ks = kk.value, j = jj.value;
        if constexpr ((ks & 1) ? j == 2 : j == 0) setm0n(area + woff(ks));
        if constexpr (!(ks & 1) && j == 4) setm0n(area + xoff(ks));
      };
      auto dgrp = [&](auto kk, auto jj, unsigned s1, unsigned sx) {
        constexpr int ks = kk.value, j = jj.value, jw = (ks & 1) ? j - 2 : j, jx = (ks & 1) ? j : j - 4;
        if constexpr (jw >= 0 && jw < 4) dma<jw * 1024>(rsW1, v1[ks][jw], s1);
        else dma<(jx + 2 * (ks & 1)) * 1024>(rsX, vx[ks][jx], sx);
      };
      auto fill = [&](auto kk) {
        unroll<0, 6>([&](auto jj) { mset(kk, jj); asm volatile("s_nop 0"); dgrp(kk, jj, b1s + 64 * xc * (F * 2), bxs + 64 * xc * (D * 2)); });
      };
      const u32x4 rsS = mkrs(ws + WS_ST, SZ_ST);
      auto gather = [&]() {
        bf16x8 g[8];
#pragma unroll
        for (int j = 0; j < 8; j++) g[j] = ldc<0>(rsS, (lane * 16 + (j + xc) * 1024) & 8191, w * 8192);
        waitv<0>();
        bool ok = true;
#pragma unroll
        for (int j = 0; j < 8; j++) {
          tie(g[j]);
          const u32x4 u = __builtin_bit_cast(u32x4, g[j]);
          ok &= u[1] == ep.lo && u[3] == tagB;
          const ldsp p = lds + w * AREA + L_STAT + j * 512 + (lane >> 4) * 128 + (lane & 15) * 4;
          lst(p, u[2]); lst(p + 64, u[0]);
        }
        return (bool)__all(ok);
      };
      unroll<0, 4>(fill);
      hold(vs, rec);
      STAMP(2);
#pragma unroll 1
      for (int it = 0; it < SPIN; ++it)
        if (gather()) break;
      STAMP(3);
      tie(bb[0]); tie(bb[1]); tie(bb[2]); tie(bb[3]);
      f32x16 acc[4][2];
#pragma unroll
      for (int g = 0; g < 4; g++) {
        acc[0][0][4 * g] = blo(bb[g].x); acc[0][0][4 * g + 1] = bhi(bb[g].x); acc[0][0][4 * g + 2] = blo(bb[g].y); acc[0][0][4 * g + 3] = bhi(bb[g].y);
      }
      acc[0][1] = acc[0][0];
#pragma unroll
      for (int a = 1; a < 4; a++) { acc[a][0] = (f32x16)(0.f); acc[a][1] = (f32x16)(0.f); }
      unsigned pa[4], px[2], msk[4], psc = area + L_STAT + h * 32, psa;
      {
        const unsigned rowb = area + (8 * h + q) * 128 + (pp & 1) * 8, ch = 2 * blk + (pp >> 1), fl = (q >> 1) << 2;
#pragma unroll
        for (int a = 0; a < 4; a++) pa[a] = rowb + ((a ^ w) >> 1) * 2048 + ((4 * ((a ^ w) & 1) + ch) ^ fl) * 16;
        px[0] = rowb + (ch ^ fl) * 16; px[1] = rowb + ((4 + ch) ^ fl) * 16;
        const unsigned r = 4 * ((lane >> 3) & 1) + pp, on = (!blk && (q & 1) == h) ? ((r & 1) ? 0xffff0000u : 0xffffu) : 0u;
#pragma unroll
        for (int j = 0; j < 4; j++) { msk[j] = (r >> 1) == j ? on : 0u; tie(msk[j]); }
        psa = area + L_STAT + 64 + (8 * h + r) * 4;
      }
      s16x4 wl[2][4], wh[2][4], xl[2], xh[2];
      u32x4 hf[2][2], dg;
      unsigned av;
      f32x16 hn[2], cc = {};
      tie(cc);
      auto rdX = [&](auto nn, auto dd) {
        constexpr int o = xoff(nn.value & 3), d = dd.value;
        xl[d] = vtr((ldsp)(uintptr_t)px[d] + o); xh[d] = vtr((ldsp)(uintptr_t)px[d] + o + 512);
      };
      auto rdA = [&](auto nn) { av = lld<unsigned>((ldsp)(uintptr_t)psa + nn.value * 128); };
      auto rdC = [&](auto nn) {
        const f32x4 c0 = lld<f32x4>((ldsp)(uintptr_t)psc + nn.value * 128), c1 = lld<f32x4>((ldsp)(uintptr_t)psc + nn.value * 128 + 16);
#pragma unroll
        for (int e = 0; e < 4; e++) { cc[e] = c0[e]; cc[4 + e] = c1[e]; }
      };
      auto rdW = [&](auto nn, auto aa) {
        constexpr int o = woff(nn.value & 3), nx = nn.value & 1, a = aa.value;
        wl[nx][a] = vtr((ldsp)(uintptr_t)pa[a] + o); wh[nx][a] = vtr((ldsp)(uintptr_t)pa[a] + o + 512);
      };
      auto mkD = [&]() { dg = (u32x4){av & msk[0], av & msk[1], av & msk[2], av & msk[3]}; };
      auto nrm = [&](auto dd) { hn[dd.value] = mfmav(dg, cat(xl[dd.value], xh[dd.value]), cc); };
      auto cvt = [&](auto nn, auto dd) {
        constexpr int d = dd.value;
        hf[nn.value & 1][d] = (u32x4){pk(hn[d][0], hn[d][1]), pk(hn[d][2], hn[d][3]), pk(hn[d][4], hn[d][5]), pk(hn[d][6], hn[d][7])};
        SB();
        keep(hn[d]);
      };
      auto step = [&](auto kk, auto wn, auto last, auto&& ms, auto&& gm) {
        constexpr int ks = kk.value;
        constexpr bool more = !last.value;
        using NX = IC<ks + 1>;
        auto mm = [&](auto m_) {
          constexpr int a = more ? m_.value >> 1 : ((m_.value >> 1) + 1) & 3, dh = m_.value & 1;
          if constexpr (a == 0) mfmac(acc[0][dh], cat(wl[ks & 1][0], wh[ks & 1][0]), hf[ks & 1][dh]);
          else acc[a][dh] = MFMA(cat(wl[ks & 1][a], wh[ks & 1][a]), __builtin_bit_cast(bf16x8, hf[ks & 1][dh]), acc[a][dh]);
          SB();
          if constexpr (!more && m_.value >= 2) {
            constexpr int c = m_.value >> 1;
#pragma unroll
            for (int g = 0; g < 4; g++)
              lst(lds + w * AREA + lane * 16 + (c - 1) * 8192 + dh * 4096 + g * 1024, (f32x4){acc[c][dh][4 * g], acc[c][dh][4 * g + 1], acc[c][dh][4 * g + 2], acc[c][dh][4 * g + 3]});
            SB();
          }
        };
        __builtin_amdgcn_s_waitcnt(0x70 | wn.value);
        asm volatile("" ::: "memory");
        SB();
        if constexpr (more) { rdX(NX(), IC<0>()); rdA(NX()); }
        ms(IC<0>());
        SB();
        mm(IC<0>());
        gm(IC<0>());
        if constexpr (more) { rdC(NX()); rdX(NX(), IC<1>()); SB(); }
        mm(IC<1>());
        gm(IC<1>());
        ms(IC<2>());
        if constexpr (more) { rdW(NX(), IC<0>()); rdW(NX(), IC<1>()); SB(); }
        mm(IC<2>());
        if constexpr (more) { mkD(); SB(); }
        gm(IC<2>());
        if constexpr (more) { rdW(NX(), IC<2>()); SB(); nrm(IC<0>()); SB(); rdW(NX(), IC<3>()); SB(); }
        mm(IC<3>());
        if constexpr (more) { nrm(IC<1>()); SB(); }
        gm(IC<3>());
        ms(IC<4>());
        mm(IC<4>());
        if constexpr (more) { cvt(NX(), IC<0>()); SB(); }
        mm(IC<5>());
        gm(IC<4>());
        mm(IC<6>());
        if constexpr (more) { cvt(NX(), IC<1>()); SB(); }
        gm(IC<5>());
        mm(IC<7>());
      };
      asm volatile("s_waitcnt lgkmcnt(0)" ::: "memory");
      rdX(IC<0>(), IC<0>()); rdX(IC<0>(), IC<1>()); rdA(IC<0>()); rdC(IC<0>());
      unroll<0, 4>([&](auto jj) { rdW(IC<0>(), jj); });
      mkD();
      SB();
      asm volatile("s_nop 1");
      nrm(IC<0>()); nrm(IC<1>());
      asm volatile("s_nop 7\n\ts_nop 7");
      cvt(IC<0>(), IC<0>()); cvt(IC<0>(), IC<1>());
      SB();
      unsigned tk = 64 * xc;
#pragma unroll 1
      for (int t = 0; t < 7; t++) {
        tk = (tk + 64) & 511;
        const unsigned s1 = b1s + tk * (F * 2), sx = bxs + tk * (D * 2);
        unroll<0, 4>([&](auto kk) {
          step(kk, IC<12>(), IC<0>(), [&](auto jj) { mset(kk, jj); SB(); }, [&](auto jj) { dgrp(kk, jj, s1, sx); SB(); });
        });
        psc += 512; tie(psc); psa += 512; tie(psa);
        SB();
      }
      {
        unroll<0, 4>([&](auto kk) {
          constexpr int ks = kk.value;
          step(kk, IC<(ks == 3 ? 12 : 12 - 2 * ks)>(), IC<(ks == 3)>(),
               [&](auto jj) { if constexpr (jj.value == 0) { setm0n(area + (6 + ks) * 4096); SB(); } },
               [&](auto jj) { if constexpr (jj.value < 4) { dma<jj.value * 1024>(rsW2, v2[jj.value], so2 + ks * (32 * S * 2)); SB(); } });
        });
      }
      STAMP(4);
      bar();
      STAMP(5);
      {
        const rsrc_t st = mkst(ws + WS_UF, SZ_UF);
        const unsigned vo = (ft * 16 + w * 4 + h) * (D * 16) + (dt * 64 + n) * 16;
        f32x4 v[2][3][4];
        u32x4 res[4];
#pragma unroll
        for (int dh = 0; dh < 2; dh++)
#pragma unroll
          for (int a = 1; a < 4; a++)
#pragma unroll
            for (int g = 0; g < 4; g++) v[dh][a - 1][g] = lld<f32x4>(lds + (w ^ a) * AREA + lane * 16 + (a - 1) * 8192 + dh * 4096 + g * 1024);
        SB();
#pragma unroll
        for (int dh = 0; dh < 2; dh++) {
#pragma unroll
          for (int g = 0; g < 4; g += 2) {
            float r[8];
#pragma unroll
            for (int e = 0; e < 8; e++) {
              const int gg = g + (e >> 2), c = e & 3;
              r[e] = gelu((acc[0][dh][4 * gg + c] + v[dh][0][gg][c]) + (v[dh][1][gg][c] + v[dh][2][gg][c]));
            }
            auto r0 = __builtin_amdgcn_permlane32_swap(pk(r[0], r[1]), pk(r[4], r[5]), false, false);
            auto r1 = __builtin_amdgcn_permlane32_swap(pk(r[2], r[3]), pk(r[6], r[7]), false, false);
            res[2 * dh + g / 2] = (u32x4){r0[0], r1[0], r0[1], r1[1]};
            stc(st, vo + g * (D * 16) + dh * 512, res[2 * dh + g / 2]);
          }
        }
        STAMP(8);
        waitv<4>();
        pre2rd();
        pre2w();
        hold(res[0], res[1], res[2], res[3], vo);
      }
      dma4(rsW2, v2, so2 + 10 * (32 * S * 2), area + 6 * 4096);
      STAMP(6);
      publish<4>(ws, FL_U + dt * 14 + ft, ep);
      STAMP(7);
      pre2(4, 10, 0);
      pre2(11, 14, 7);
    } else {
      if (xc < 7) {
#pragma unroll 1
        for (int m = 4; m < 9; m++) touchl(rsW1, vt1, so_t1(m, 2 * (i - 14) + (w >> 1), 16 * (w & 1)));
      }
      if (i >= 14) {
#pragma unroll 1
        for (int m = 0; m < 4; m++) touchl(mkrs(wo, D * D * 2), (lane >> 2) * (D * 2) + (lane & 3) * 128, (128 * m + 64 * (i - 14) + 16 * w) * (D * 2) + (xc & 1) * 384);
        const int head = 2 * xc + i - 14;
        const u32x4 rsP = mkrs(ws + WS_KVP, SZ_KVP);
        const unsigned so = head * 65536 + w * 1024, vf = (FL_P + xc * 16 + (lane & 15)) * 128;
        bf16x8 pv[16];
        float a[8] = {};
        unroll<0, 5>([&](auto gg) {
          constexpr int t = 4 * gg.value;
          if constexpr (t < 16) {
            if constexpr (t == 0) poll(rsF, vf, ep);
            if constexpr (t == 12) STAMP(3);
            unroll<t, t + 4>([&](auto tt) { pv[tt.value] = ldc<0>(rsP, lane * 16, so + tt.value * 4096); });
          }
          if constexpr (t > 0) {
            waitv<(t < 16 ? 4 : 0)>(pv[t - 4], pv[t - 3], pv[t - 2], pv[t - 1]);
#pragma unroll
            for (int j = t - 4; j < t; j++) {
              const u32x4 u = __builtin_bit_cast(u32x4, pv[j]);
#pragma unroll
              for (int e = 0; e < 4; e++) { const unsigned v = u[e]; a[2 * e] += blo(v); a[2 * e + 1] += bhi(v); }
            }
            SB();
          }
        });
        const rsrc_t st = mkst(ws + WS_KVF, SZ_KVF);
        const u32x4 r0 = {pk(a[0], a[1]), ep.lo, pk(a[2], a[3]), tagB}, r1 = {pk(a[4], a[5]), ep.lo, pk(a[6], a[7]), tagB};
        stc(st, head * 8192 + w * 2048 + lane * 16, r0);
        stc(st, head * 8192 + w * 2048 + 1024 + lane * 16, r1);
        hold(r0, r1);
        STAMP(4);
      } else {
        __builtin_amdgcn_s_sleep(127);
      }
      pre2(0, 4, 6); pre2(4, 10, 0);
      waitv<24>();
      pre2rd();
      pre2w();
      pre2(10, 14, 6);
    }
  } else {
    const unsigned ring = lds0 + L_WQ + w * 12544, sxa = (128 * T + 32 * w) * (D * 2);
    unsigned vxa[4];
#pragma unroll
    for (int j = 0; j < 4; j++) vxa[j] = (8 * j + swz) * (D * 2) + (((lane & 7) ^ ((4 * j + (lane >> 4)) & 7)) * 16) - j * 1024;
    unsigned sink = 0;
    u32x2 gen = ldc2(rsF, 0);
    dma4(rsX, vxa, sxa, ring);
    const u32x4 rsW = mkrs(wqkv, D * 3 * D * 2);
    {
      const unsigned r6 = (lane * 43) >> 8, c6 = lane - 6 * r6;
      touch(sink, rsW, lane < 42 ? r6 * 2688 + (c6 >> 1) * 896 + (c6 & 1) * 108 : OOB, (28 * T + 7 * w) * 2688 + pr * 112);
    }
    dma4(rsX, vxa, sxa + 128, ring + 4096);
    dma4(rsX, vxa, sxa + 256, ring + 8192);
    unsigned vw[8];
#pragma unroll
    for (int s = 0; s < 8; s++) {
      const unsigned c = (s < 7 ? 256 * s + 64 * w : 3072) + lane, r = (c * 9363) >> 16;
      vw[s] = ((r & ~15u) + 8 * ((r >> 1) & 1) + 4 * (r & 1) + ((r >> 2) & 1) + 2 * ((r >> 3) & 1)) * 2688 + (c - 7 * r) * 16;
    }
    const unsigned mw = lds0 + w * 1024;
    auto mW = [&](auto ss, auto bb) {
      if constexpr (ss.value < 12) addm0<bb.value + ss.value * 4096>(mw); else addm0<bb.value + 49152>(lds0);
    };
    auto dmaW = [&](auto ss, unsigned col) {
      constexpr int s = ss.value;
      dma<0>(rsW, vw[s < 12 ? (s + 14) % 7 : 7], col + pr * 112 + (s >= 7 && s < 12 ? 256 * 2688 : 0));
    };
    asm volatile("" ::"s"(wo), "s"(b2), "s"(out) : "memory");
    bf16x8 xa[28];
    float sa = 0.f, sq = 0.f;
    {
      unsigned pr_[4];
#pragma unroll
      for (int k = 0; k < 4; k++) pr_[k] = ring + n * 128 + (((2 * k + h) ^ ((n >> 1) & 7)) * 16);
      unroll<0, 7>([&](auto cc) {
        constexpr int cb = cc.value;
        waitv<(cb == 0 ? 9 : 8)>();
#pragma unroll
        for (int k = 0; k < 4; k++) xa[4 * cb + k] = lld<bf16x8>((ldsp)(uintptr_t)pr_[k] + (cb % 3) * 4096);
        asm volatile("s_waitcnt lgkmcnt(0)" : "+v"(xa[4 * cb]), "+v"(xa[4 * cb + 1]), "+v"(xa[4 * cb + 2]), "+v"(xa[4 * cb + 3])::"memory");
        if constexpr (cb < 4) dma4(rsX, vxa, sxa + (cb + 3) * 128, ring + (cb % 3) * 4096);
        if constexpr (cb == 4) unroll<0, 4>([&](auto ss) { mW(ss, IC<L_WK>()); asm volatile("s_nop 0"); dmaW(ss, 896); });
        if constexpr (cb == 5) unroll<0, 4>([&](auto ss) { mW(ss, IC<L_WV>()); asm volatile("s_nop 0"); dmaW(ss, 1792); });
        if constexpr (cb == 6) {
          setm0(lds0 + L_WPHI + w * 1024);
          dma<0>(mkrs(wphi + pr * 1568, 3136), tid * 16, 0);
        }
        SB();
#pragma unroll
        for (int k = 0; k < 4; k++) acc_stats(xa[4 * cb + k], sa, sq);
        SB();
      });
    }
    STAMP(1);
    hold(vxa[0], vxa[1], vxa[2], vxa[3]);
    tie(gen);
    setep(gen);
    float la, lc;
    {
      float s2 = sa, q2 = sq;
      swap32(sa, s2); swap32(sq, q2);
      const float mu = (sa + s2) * (1.f / D), var = fmaxf((sq + q2) * (1.f / D) - mu * mu, 0.f);
      la = __builtin_amdgcn_rsqf(var + 1e-5f);
      lc = -mu * la;
    }
    auto norm = [&](auto kk, auto ee) {
      constexpr int ks = kk.value, e = ee.value;
      if constexpr (ks < 28) {
        u32x4 u = __builtin_bit_cast(u32x4, xa[ks]);
        u[e] = affine2(u[e], la, lc);
        xa[ks] = __builtin_bit_cast(bf16x8, u);
      }
    };
    unroll<0, 4>([&](auto ee) { norm(IC<0>(), ee); });
    norm(IC<1>(), IC<0>());
    tie(xa[0]); tie(xa[1]);
    waitv<1>();
    bar();
    STAMP(3);
    const unsigned vt2 = (lane >> 3) * (S * 2) + (lane & 7) * 128;
    auto touch2 = [&](int m) { touch(sink, rsW2, vt2, (448 * w + 128 * m + 8 * T) * (S * 2) + (xc >> 1) * 1024); };
    const unsigned wlane = (2 * h + 4 * q) * 112 + 32 * blk + 8 * pp;
    f32x16 kT[2] = {}, vv[2] = {};
    {
      const ldsp pk_ = lds + L_WK + wlane, pv_ = lds + L_WV + wlane;
      s16x4 fr[30][8];
      auto rd = [&](auto kk, auto cc) {
        constexpr int ks = kk.value, c = cc.value;
        if constexpr (ks < 28) fr[ks][c] = vtr(((c & 4) ? pv_ : pk_) + ks * 1792 + ((c >> 1) & 1) * 56 + (c & 1) * 112);
      };
      unroll<0, 8>([&](auto cc) { rd(IC<0>(), cc); });
      unroll<0, 8>([&](auto cc) { rd(IC<1>(), cc); });
      SB();
      unroll<0, 28>([&](auto kk) {
        constexpr int ks = kk.value;
        if constexpr (ks == 7) { waitv<6>(); asm volatile("s_barrier" ::: "memory"); }
        if constexpr (ks == 16) { waitv<1>(); asm volatile("s_barrier" ::: "memory"); }
        __builtin_amdgcn_s_waitcnt(ks < 27 ? 0xc87f : 0xc07f);
        SB();
        unroll<0, 4>([&](auto mm) {
          constexpr int m = mm.value;
          const bf16x8 wv = cat(fr[ks][2 * m], fr[ks][2 * m + 1]);
          if constexpr (m < 2) kT[m] = MFMA(wv, xa[ks], kT[m]);
          else vv[m - 2] = MFMA(xa[ks], wv, vv[m - 2]);
          SB();
          if constexpr (ks < 9 && m == 0) mW(IC<4 + ks>(), IC<L_WK>());
          if constexpr (ks < 9 && m == 2) mW(IC<4 + ks>(), IC<L_WV>());
          rd(IC<ks + 2>(), IC<2 * m>());
          rd(IC<ks + 2>(), IC<2 * m + 1>());
          SB();
          if constexpr (ks < 9 && !(m & 1)) dmaW(IC<4 + ks>(), m ? 1792 : 896);
          if constexpr (ks == 9 && m == 0) touch(sink, rsW1, vt1, so_t1(8 + (T >> 1), (2 * T + (w >> 1)) & 3, 16 * (w & 1)));
          if constexpr (ks == 22 && m == 0) touch2(0);
          SB();
          norm(IC<ks + 1 + (m == 3)>(), IC<(m + 1) & 3>());
          SB();
        });
      });
    }
    bf16x8 wf[2][2];
    {
      const ldsp p = lds + L_WPHI + (4 * h + q) * 56 + 32 * blk + 8 * pp;
#pragma unroll
      for (int hd = 0; hd < 2; hd++) {
        wf[hd][0] = cat(vtr(p + hd * 1568), vtr(p + hd * 1568 + 8 * 56));
        s16x4 z = vtr(p + hd * 1568 + 24 * 56);
        wf[hd][1] = cat(vtr(p + hd * 1568 + 16 * 56), h ? (s16x4){0, 0, 0, 0} : z);
      }
    }
    STAMP(5);
    {
      f32x16 p[2];
#pragma unroll
      for (int hd = 0; hd < 2; hd++) { p[hd] = MFMA(wf[hd][0], pack8(kT[hd], 0), (f32x16)(0.f)); p[hd] = MFMA(wf[hd][1], pack8(kT[hd], 8), p[hd]); }
      bar();
      {
        const bf16x8 ones = {0x3f80, 0x3f80, 0x3f80, 0x3f80, 0x3f80, 0x3f80, 0x3f80, 0x3f80};
        const ldsp pvx = lds + L_VX + w * 2048 + lane * 16;
#pragma unroll
        for (int hd = 0; hd < 2; hd++)
#pragma unroll
          for (int j = 0; j < 2; j++) { bf16x8 f = pack8(vv[hd], 8 * j); lst(pvx + hd * 8192 + j * 1024, n == 28 ? ones : f); }
      }
      const unsigned pb = (32 * w + n) * 64 + ((h ^ ((n >> 1) & 7)) * 8);
      unroll<0, 2>([&](auto hh) {
        constexpr int hd = hh.value;
        unsigned ph[2][8];
        features(p[hd], h, ph, [&](auto kk, auto& pin) {
          constexpr int s = 12 * hd + kk.value;
          if constexpr (s < 13) {
            mW(IC<s>(), IC<L_WQ>());
            asm volatile("s_nop 0");
            dmap<0>(rsW, vw[s < 12 ? s % 7 : 7], pr * 112 + (s >= 7 && s < 12 ? 256 * 2688 : 0), pin);
          }
          if constexpr (s == 14) touch2(1);
        });
#pragma unroll
        for (int sg = 0; sg < 2; sg++)
#pragma unroll
          for (int g = 0; g < 4; g++) lst(lds + L_PHI + (hd * 2 + sg) * 8192 + (pb ^ (g << 4)), (u32x2){ph[sg][2 * g], ph[sg][2 * g + 1]});
      });
    }
    STAMP(6);
    hold(vw[0], vw[1], vw[2], vw[3], vw[4], vw[5], vw[6], vw[7], vt1);
    waitv<1>();
    bar();
    u32x4 kvp[2];
    const unsigned vkv = (2 * pr + (w >> 1)) * 65536 + T * 4096 + (w & 1) * 2048 + lane * 16;
    {
      const unsigned sw = 2 * h + (q >> 1);
      const ldsp pl = lds + L_PHI + w * 8192 + (4 * h + q) * 64 + (((4 * blk + pp) ^ sw) * 8);
      const ldsp ph_ = lds + L_PHI + w * 8192 + (8 + 4 * h + q) * 64 + (((4 * blk + pp) ^ sw ^ 4) * 8);
      const ldsp pvx = lds + L_VX + (w >> 1) * 8192 + lane * 16;
      f32x16 kv = {};
#pragma unroll
      for (int jj = 0; jj < 8; jj++) kv = MFMA(cat(vtr(pl + jj * 1024), vtr(ph_ + jj * 1024)), lld<bf16x8>(pvx + jj * 1024), kv);
      const rsrc_t st = mkst(ws + WS_KVP, SZ_KVP);
      kvp[0] = __builtin_bit_cast(u32x4, pack8(kv, 0)); kvp[1] = __builtin_bit_cast(u32x4, pack8(kv, 8));
      stc(st, vkv, kvp[0]);
      stc(st, vkv + 1024, kvp[1]);
    }
    STAMP(7);
    f32x16 qT[2] = {};
    {
      const ldsp pq_ = lds + L_WQ + wlane;
      s16x4 fr[29][4];
      auto rd = [&](auto kk, auto cc) {
        constexpr int ks = kk.value, c = cc.value;
        if constexpr (ks < 28) fr[ks][c] = vtr(pq_ + ks * 1792 + (c >> 1) * 56 + (c & 1) * 112);
      };
      unroll<0, 4>([&](auto cc) { rd(std::integral_constant<int, 0>(), cc); });
      unroll<0, 28>([&](auto kk) {
        constexpr int ks = kk.value;
        if constexpr (ks == 14) { hold(kvp[0], kvp[1], vkv); publish(ws, FL_P + pr * 16 + T, ep); STAMP(8); touch2(2); touch2(3); SB(); }
        unroll<0, 2>([&](auto mm) {
          constexpr int m = mm.value;
          rd(std::integral_constant<int, ks + 1>(), std::integral_constant<int, 2 * m>());
          rd(std::integral_constant<int, ks + 1>(), std::integral_constant<int, 2 * m + 1>());
          qT[m] = MFMA(cat(fr[ks][2 * m], fr[ks][2 * m + 1]), xa[ks], qT[m]);
          SB();
        });
      });
    }
    unsigned phq[2][2][8];
    {
      f32x16 p[2];
#pragma unroll
      for (int hd = 0; hd < 2; hd++) { p[hd] = MFMA(wf[hd][0], pack8(qT[hd], 0), (f32x16)(0.f)); p[hd] = MFMA(wf[hd][1], pack8(qT[hd], 8), p[hd]); }
      bar();
      STAMP(9);
      unroll<0, 2>([&](auto hh) {
        features(p[hh.value], h, phq[hh.value], [&](auto kk, auto& pin) {
          constexpr int k = 12 * hh.value + kk.value, t = k >> 2;
          if constexpr ((k & 3) == 0) setm0(area + ((t + 6) % 10) * 4096);
          dmap<(k & 3) * 1024>(rsW2, v2[k & 3], so2 + t * (32 * S * 2), pin);
        });
      });
    }
    STAMP(10);
    pre2(6, 9, 2);
    {
      const u32x4 rsK = mkrs(ws + WS_KVF, SZ_KVF);
      const unsigned so = pr * 16384 + w * 2048, tagB = ep.lo ^ ep.hi;
      const ldsp px = lds + 5 * 4096 + lane * 16;
#pragma unroll 1
      for (int it = 0; it < SPIN; ++it) {
        bf16x8 g0 = ldc<0>(rsK, lane * 16, so), g1 = ldc<1024>(rsK, lane * 16, so), g2 = ldc<0>(rsK, lane * 16, so + 8192), g3 = ldc<1024>(rsK, lane * 16, so + 8192);
        waitv<0>(g0, g1, g2, g3);
        const u32x4 u0 = __builtin_bit_cast(u32x4, g0), u1 = __builtin_bit_cast(u32x4, g1), u2 = __builtin_bit_cast(u32x4, g2), u3 = __builtin_bit_cast(u32x4, g3);
        lst(px + w * AREA, (u32x4){u0[0], u0[2], u1[0], u1[2]});
        lst(px + w * AREA + 1024, (u32x4){u2[0], u2[2], u3[0], u3[2]});
        const unsigned bad = ((u0[1] ^ ep.lo) | (u1[1] ^ ep.lo) | (u2[1] ^ ep.lo) | (u3[1] ^ ep.lo)) | ((u0[3] ^ tagB) | (u1[3] ^ tagB) | (u2[3] ^ tagB) | (u3[3] ^ tagB));
        if (__all(bad == 0)) break;
      }
      STAMP(2);
      tie(sink);
      bar();
      STAMP(11);
      bf16x8 kf[2][4];
#pragma unroll
      for (int hd = 0; hd < 2; hd++)
#pragma unroll
        for (int c = 0; c < 4; c++) kf[hd][c] = lld<bf16x8>(px + c * AREA + hd * 1024);
      f32x16 o[2] = {};
#pragma unroll
      for (int hd = 0; hd < 2; hd++) {
#pragma unroll
        for (int c = 0; c < 4; c++) {
          const unsigned* s = &phq[hd][c >> 1][4 * (c & 1)];
          o[hd] = MFMA(kf[hd][c], __builtin_bit_cast(bf16x8, (u32x4){s[0], s[1], s[2], s[3]}), o[hd]);
        }
      }
      float da = o[0][12], db = o[1][12];
      swap32(da, db);
      float ia = __builtin_amdgcn_rcpf(db + 1e-6f), ib = ia;
      swap32(ia, ib);
      unsigned a[8], bq[8];
#pragma unroll
      for (int r = 0; r < 8; r++) { a[r] = pk(o[0][2 * r] * ia, o[0][2 * r + 1] * ia); bq[r] = pk(o[1][2 * r] * ib, o[1][2 * r + 1] * ib); }
      const rsrc_t st = mkst(ws + WS_OF, SZ_OF);
      const unsigned tk = (128 * T + 32 * w + n) * 16, jb = 14 * (pr >> 1);
      const unsigned vo = (jb + 6 * (pr & 1) + h) * (S * 16) + tk;
      const unsigned v1 = vo + 2 * S * 16, v2_ = vo + 4 * S * 16, v3 = h ? OOB : (jb + 12 + (pr & 1)) * (S * 16) + tk;
      const u32x4 r0 = {a[0], a[1], a[2], a[3]}, r1 = {a[4], a[5], bq[0], bq[1]}, r2 = {bq[2], bq[3], bq[4], bq[5]}, r3 = {a[6], a[7], bq[6], bq[7]};
      stc(st, vo, r0); stc(st, v1, r1); stc(st, v2_, r2); stc(st, v3, r3);
      pre2rd();
      pre2w();
      hold(vo, v1, v2_, v3, r0, r1, r2, r3);
    }
    dma4(rsW2, v2, so2 + 10 * (32 * S * 2), area + 6 * 4096);
    publish<4>(ws, FL_O + T * 8 + pr, ep);
    STAMP(12);
    pre2(9, 10, 5);
    pre2(11, 14, 7);
  }

  if (i >= 4) {
    const unsigned orow = st2 * 64 + 16 * w + (lane >> 3);
    const unsigned vo = orow * (D * 2) + dt2 * 128 + (lane & 7) * 16;
    const u32x4 rsB = mkrs(b2, S * 2), rsU = mkrs(ws + WS_UF, SZ_UF), rsO = mkrs(ws + WS_OF, SZ_OF), rsWo = mkrs(wo, D * D * 2);
    u32x4 wor[14];
    unsigned vwo[4];
#pragma unroll
    for (int j = 0; j < 4; j++) vwo[j] = swz * (D * 2) + swc + j * (8 * D * 2 - 1024);
    unroll<0, 14>([&](auto jj) {
      constexpr int j = jj.value & 3;
      wor[jj.value] = lda<j * 1024>(rsWo, vwo[j], w * 112 * (D * 2) + dt2 * 128 + (jj.value >> 2) * (32 * D * 2));
    });
    poll(rsF, (FL_U + dt2 * 14 + ((7 * w) >> 1) + (lane & 3)) * 128, ep);
    hold(vwo[0], vwo[1], vwo[2], vwo[3]);
    STAMP(13);
    const unsigned vu0 = h * (D * 16) + (dt2 * 64 + n) * 16, vu1 = vu0 + 2 * D * 16, sou = w * 56 * (D * 16);
    const unsigned vof = h * (S * 16) + (st2 * 64 + n) * 16, soo = w * 14 * (S * 16);
    const unsigned vfl = (FL_O + (st2 >> 1) * 8 + 2 * w + (lane & 1)) * 128;
    bf16x8 fa[18][4], xr0, xr1;
    unsigned bs0, bs1;
    const unsigned vb2 = orow * 2;
    u32x2 fl;
    auto ldD = [&](auto tt, auto jj) {
      constexpr int t = tt.value, j = jj.value;
      if constexpr (t < 14) fa[t][j] = ldc<(j & 1) * 512>(rsU, (j >> 1) ? vu1 : vu0, sou + t * (4 * D * 16));
      else if constexpr (j < g2ld(t)) fa[t][j] = ldc<(j & 1) * 512>(rsO, vof, soo + (2 * (t - 14) + (j >> 1)) * (2 * S * 16));
    };
    unroll<0, G2R>([&](auto tt) { unroll<0, 4>([&](auto jj) { ldD(tt, jj); }); });
    const ldsp pw[2] = {pb[0] - h * 512, pb[1] - h * 512};
    f32x16 acc[2][2] = {};
    bf16x8 wb[19][2][2];
    auto rdB = [&](auto tt, auto kk, auto aa) {
      constexpr int t = tt.value, ks = kk.value, a = aa.value, j = 2 * (t - 14) + ks;
      if constexpr (t < 4) wb[t][ks][a] = w2r[t][ks][a];
      else if constexpr (t < 14) wb[t][ks][a] = cat(vtr(pb[a] + (t - 4) * 4096 + ks * 2048), vtr(pb[a] + (t - 4) * 4096 + ks * 2048 + 512));
      else if constexpr (j < 6) {
        constexpr int u = j % 3, rl = 56 * (j / 3) + (u == 0 ? 0 : u == 1 ? 16 : 36), rh = 56 * (j / 3) + (u == 0 ? 8 : u == 1 ? 28 : 44);
        wb[t][ks][a] = cat(vtr(pw[a] + rl * 128), vtr(pw[a] + rh * 128));
      } else if constexpr (j == 6) wb[t][ks][a] = cat(vtr(pw[a] + h * (52 * 128) + 24 * 128), vtr(pw[a] + h * (52 * 128) + 52 * 128));
    };
    unroll<0, 4>([&](auto cc) { rdB(IC<0>(), IC<(cc.value >> 1)>(), IC<(cc.value & 1)>()); });
    unroll<0, 18>([&](auto tt) {
      constexpr int t = tt.value, nk = t < 17 ? 2 : 1;
      constexpr int left = g2cnt(t - 3) + g2cnt(t - 2) + g2cnt(t - 1);
      if constexpr (nk == 2) waitv<left>(fa[t][0], fa[t][1], fa[t][2], fa[t][3]); else waitv<left>(fa[t][0], fa[t][1]);
      if constexpr (t == G2POLL + G2R) {
        tie(fl);
        if (!match(fl, ep)) poll(rsF, vfl, ep);
      }
      SB();
      unroll<0, 4 * nk>([&](auto mm) {
        constexpr int m = mm.value, ks = m >> 2, nh = (m >> 1) & 1, mh = m & 1;
        if constexpr (t < 14) acc[nh][mh] = MFMA(fa[t][ks * 2 + mh], wb[t][ks][nh], acc[nh][mh]);
        else acc[nh][mh] = MFMA(wb[t][ks][mh], fa[t][ks * 2 + nh], acc[nh][mh]);
        SB();
        if constexpr (m & 1) rdB(IC<t + 1>(), IC<(m >> 2)>(), IC<((m >> 1) & 1)>());
        else if constexpr (t >= 5 && 4 * (t - 5) + m / 2 < 14) sta<(4 * (t - 5) + m / 2) * 1024>(area + lane * 16, wor[4 * (t - 5) + m / 2]);
        SB();
        if constexpr (!(m & 1)) ldD(IC<t + G2R>(), IC<m / 2>());
        if constexpr (t == G2POLL && m == 7) fl = ldc2(rsF, vfl);
        if constexpr (t == 14 && m == 0) xr0 = ldp<0>(rsX, vo, 0);
        if constexpr (t == 14 && m == 2) xr1 = ldp<0>(rsX, vo, 8 * D * 2);
        if constexpr (t == 14 && m == 4) bs0 = ldu16<0>(rsB, vb2, 0);
        if constexpr (t == 14 && m == 6) bs1 = ldu16<16>(rsB, vb2, 0);
        SB();
      });
    });
    STAMP(14);
#pragma unroll
    for (int nh = 0; nh < 2; nh++)
#pragma unroll
      for (int mh = 0; mh < 2; mh++)
#pragma unroll
        for (int g = 0; g < 4; g++)
          lst(lds + w * AREA + (32 * nh + n) * 256 + (((8 * mh + 2 * g + h) ^ (n & 15)) * 16),
              (f32x4){acc[nh][mh][4 * g], acc[nh][mh][4 * g + 1], acc[nh][mh][4 * g + 2], acc[nh][mh][4 * g + 3]});
    barv();
    tie(xr0); tie(xr1); tie(bs0); tie(bs1);
    hold(vb2);
    u32x4 res[2];
    const rsrc_t sto = mkst(out, S * D * 2);
#pragma unroll
    for (int u = 0; u < 2; u++) {
      const int r = 16 * w + 8 * u + (lane >> 3);
      const unsigned c0 = r * 256 + (((2 * (lane & 7)) ^ (r & 15)) * 16), c1 = c0 ^ 16;
      f32x4 a = lld<f32x4>(lds + c0), bq = lld<f32x4>(lds + c1);
#pragma unroll
      for (int s = 1; s < 4; s++) { a += lld<f32x4>(lds + s * AREA + c0); bq += lld<f32x4>(lds + s * AREA + c1); }
      const u32x4 xu = __builtin_bit_cast(u32x4, u ? xr1 : xr0);
      const float bv = blo(u ? bs1 : bs0);
      res[u] = (u32x4){pk(a[0] + bv + blo(xu[0]), a[1] + bv + bhi(xu[0])), pk(a[2] + bv + blo(xu[1]), a[3] + bv + bhi(xu[1])),
                   pk(bq[0] + bv + blo(xu[2]), bq[1] + bv + bhi(xu[2])), pk(bq[2] + bv + blo(xu[3]), bq[3] + bv + bhi(xu[3]))};
      __builtin_amdgcn_raw_buffer_store_b128(res[u], sto, vo + u * (8 * D * 2), 0, 16);
    }
    hold(res[0], res[1]);
    if (blockIdx.x == 255 && tid == 0) __builtin_amdgcn_raw_buffer_store_b64((u32x2){ep.lo, ep.hi}, mkst(ws, WS_FLAG_BYTES), 0, 0, 16);
  }
}

extern "C" void kernel_launch(void* const* d_in, const int* in_sizes, int n_in, void* d_out, int out_size, void* d_ws, size_t ws_size,
                              hipStream_t stream) {
  fused_kernel<<<256, 256, 0, stream>>>((char*)d_ws, (const short*)d_in[0], (const short*)d_in[5], (const short*)d_in[1], (const short*)d_in[6],
                                        (const short*)d_in[2], (const short*)d_in[7], (const short*)d_in[3], (const short*)d_in[8], (short*)d_out);
}
```

```cpp
#include <hip/hip_runtime.h>
#include <stdint.h>

typedef short bf16x8 __attribute__((ext_vector_type(8)));
typedef short s16x4 __attribute__((ext_vector_type(4)));
typedef float f32x2 __attribute__((ext_vector_type(2)));
typedef float f32x4 __attribute__((ext_vector_type(4)));
typedef float f32x16 __attribute__((ext_vector_type(16)));
typedef unsigned u32x2 __attribute__((ext_vector_type(2)));
typedef unsigned u32x4 __attribute__((ext_vector_type(4)));
typedef unsigned u32x8 __attribute__((ext_vector_type(8)));
typedef __bf16 b16x2 __attribute__((ext_vector_type(2)));
typedef __attribute__((address_space(3))) char* ldsp;
typedef __amdgpu_buffer_rsrc_t rsrc_t;

#define DEV __device__ __forceinline__
#define SB() __builtin_amdgcn_sched_barrier(0)
#define MFMA(a, b, c) __builtin_amdgcn_mfma_f32_32x32x16_bf16(a, b, c, 0, 0, 0)

constexpr int S = 2048, D = 448, F = 1792;
constexpr unsigned FL_P = 8, FL_O = FL_P + 128, FL_U = FL_O + 128;
constexpr unsigned WS_FLAG_BYTES = 65536;
constexpr unsigned WS_ST = WS_FLAG_BYTES, SZ_ST = S * 16;
constexpr unsigned WS_UF = WS_ST + SZ_ST, SZ_UF = 224 * 448 * 16;
constexpr unsigned WS_OF = WS_UF + SZ_UF, SZ_OF = 56 * 2048 * 16;
constexpr unsigned WS_KVP = WS_OF + SZ_OF, SZ_KVP = 16 * 16 * 4096;
constexpr unsigned WS_KVF = WS_KVP + SZ_KVP, SZ_KVF = 16 * 8 * 1024;
constexpr unsigned WS_END = WS_KVF + SZ_KVF;
constexpr unsigned AREA = 40960, L_STAT = 36864, SLOT = 12288;
constexpr unsigned L_WK = 0, L_WV = 50176, L_WQ = 100352, L_WPHI = 150528, L_PHI = 0, L_VX = 32768;
constexpr unsigned OOB = 0x40000000u;
constexpr int SPIN = 1 << 18;

DEV u32x4 mkrs(const void* p, unsigned bytes) {
  unsigned long long a = (unsigned long long)p;
  u32x4 r = {(unsigned)a, (unsigned)(a >> 32), bytes, 0x00020000u};
  return r;
}
DEV rsrc_t mkst(void* p, unsigned bytes) { return __builtin_amdgcn_make_buffer_rsrc(p, 0, bytes, 0x00020000); }
DEV void stc(rsrc_t rs, unsigned voff, u32x4 v) { __builtin_amdgcn_raw_buffer_store_b128(v, rs, voff, 0, 16); }
template <int OFF> DEV bf16x8 ldp(u32x4 rs, unsigned voff, unsigned soff) {
  bf16x8 r;
  asm volatile("buffer_load_dwordx4 %0, %2, %1, %3 offen offset:%4" : "=v"(r) : "s"(rs), "v"(voff), "s"(soff), "n"(OFF) : "memory");
  return r;
}
template <int OFF> DEV bf16x8 ldc(u32x4 rs, unsigned voff, unsigned soff) {
  bf16x8 r;
  asm volatile("buffer_load_dwordx4 %0, %2, %1, %3 offen offset:%4 sc1" : "=v"(r) : "s"(rs), "v"(voff), "s"(soff), "n"(OFF) : "memory");
  return r;
}
template <int OFF> DEV u32x2 ldp2(u32x4 rs, unsigned voff, unsigned soff) {
  u32x2 r;
  asm volatile("buffer_load_dwordx2 %0, %2, %1, %3 offen offset:%4" : "=v"(r) : "s"(rs), "v"(voff), "s"(soff), "n"(OFF) : "memory");
  return r;
}
template <int OFF> DEV u32x4 lda(u32x4 rs, unsigned voff, unsigned soff) {
  u32x4 r;
  asm volatile("buffer_load_dwordx4 %0, %2, %1, %3 offen offset:%4" : "=a"(r) : "s"(rs), "v"(voff), "s"(soff), "n"(OFF) : "memory");
  return r;
}
template <int OFF> DEV void sta(unsigned addr, u32x4 v) { asm volatile("ds_write_b128 %0, %1 offset:%2" ::"v"(addr), "a"(v), "n"(OFF) : "memory"); }
DEV u32x2 ldc2(u32x4 rs, unsigned voff) {
  u32x2 r;
  asm volatile("buffer_load_dwordx2 %0, %2, %1, 0 offen sc1" : "=v"(r) : "s"(rs), "v"(voff) : "memory");
  return r;
}
template <int OFF> DEV unsigned ldu16(u32x4 rs, unsigned voff, unsigned soff) {
  unsigned r;
  asm volatile("buffer_load_ushort %0, %2, %1, %3 offen offset:%4" : "=v"(r) : "s"(rs), "v"(voff), "s"(soff), "n"(OFF) : "memory");
  return r;
}
DEV void setm0(unsigned a) { asm volatile("s_mov_b32 m0, %0\n\ts_nop 0" ::"s"(a) : "memory"); }
DEV void setm0n(unsigned a) { asm volatile("s_mov_b32 m0, %0" ::"s"(a) : "memory"); }
template <int OFF> DEV void addm0(unsigned a) { asm volatile("s_add_u32 m0, %0, %1" ::"s"(a), "n"(OFF) : "memory", "scc"); }
template <int OFF> DEV void dma(u32x4 rs, unsigned voff, unsigned soff) {
  asm volatile("buffer_load_dwordx4 %1, %0, %2 offen offset:%3 lds" ::"s"(rs), "v"(voff), "s"(soff), "n"(OFF) : "memory");
}
DEV void dma4(u32x4 rs, const unsigned (&v)[4], unsigned soff, unsigned m0) {
  setm0(m0); dma<0>(rs, v[0], soff); dma<1024>(rs, v[1], soff); dma<2048>(rs, v[2], soff); dma<3072>(rs, v[3], soff);
}
template <int N> DEV void waitv() { asm volatile("s_waitcnt vmcnt(%0)" ::"n"(N) : "memory"); }
template <int N, class T> DEV void waitv(T& a) { asm volatile("s_waitcnt vmcnt(%1)" : "+v"(a) : "n"(N) : "memory"); }
template <int N, class T> DEV void waitv(T& a, T& b) { asm volatile("s_waitcnt vmcnt(%2)" : "+v"(a), "+v"(b) : "n"(N) : "memory"); }
template <int N, class T> DEV void waitv(T& a, T& b, T& c, T& d) {
  asm volatile("s_waitcnt vmcnt(%4)" : "+v"(a), "+v"(b), "+v"(c), "+v"(d) : "n"(N) : "memory");
}
template <class T> DEV void tie(T& a) { asm volatile("" : "+v"(a)::"memory"); }
template <class T> DEV void hold(T a) { asm volatile("" ::"v"(a)); }
template <class T, class... R> DEV void hold(T a, R... r) { hold(a); hold(r...); }
DEV void keep(f32x16 a) { asm volatile("" ::"v"(a)); }
DEV void bar() { asm volatile("s_waitcnt lgkmcnt(0)\n\ts_barrier" ::: "memory"); }
DEV void barv() { asm volatile("s_waitcnt vmcnt(0) lgkmcnt(0)\n\ts_barrier" ::: "memory"); }
DEV s16x4 vtr(ldsp p) { return __builtin_amdgcn_ds_read_tr16_b64_v4i16((__attribute__((address_space(3))) s16x4*)p); }
DEV bf16x8 cat(s16x4 a, s16x4 b) { return __builtin_shufflevector(a, b, 0, 1, 2, 3, 4, 5, 6, 7); }
DEV unsigned pk(float a, float b) {
  f32x2 v = {a, b};
  return __builtin_bit_cast(unsigned, __builtin_convertvector(v, b16x2));
}
DEV float blo(unsigned u) { return __builtin_bit_cast(float, u << 16); }
DEV float bhi(unsigned u) { return __builtin_bit_cast(float, u & 0xffff0000u); }
DEV void swap32(float& a, float& b) {
  auto r = __builtin_amdgcn_permlane32_swap(__builtin_bit_cast(unsigned, a), __builtin_bit_cast(unsigned, b), false, false);
  const unsigned r0 = r[0], r1 = r[1];
  a = __builtin_bit_cast(float, r0); b = __builtin_bit_cast(float, r1);
}
DEV void swap16(float& a, float& b) {
  auto r = __builtin_amdgcn_permlane16_swap(__builtin_bit_cast(unsigned, a), __builtin_bit_cast(unsigned, b), false, false);
  const unsigned r0 = r[0], r1 = r[1];
  a = __builtin_bit_cast(float, r0); b = __builtin_bit_cast(float, r1);
}
template <int C> DEV float ror(float z) {
  return __builtin_bit_cast(float, __builtin_amdgcn_update_dpp(0, __builtin_bit_cast(int, z), C, 0xf, 0xf, true));
}
DEV float rowsum(float z) { z += ror<0x128>(z); z += ror<0x124>(z); z += ror<0x122>(z); z += ror<0x121>(z); return z; }
DEV float rdl(float z, int l) { return __builtin_bit_cast(float, __builtin_amdgcn_readlane(__builtin_bit_cast(int, z), l)); }
template <class T> DEV void lst(ldsp p, T v) { *(__attribute__((address_space(3))) T*)p = v; }
template <class T> DEV T lld(ldsp p) { return *(__attribute__((address_space(3))) T*)p; }

struct Epoch { unsigned lo, hi; };
template <int N = 0> DEV void publish(char* ws, unsigned flag, Epoch e) {
  asm volatile("s_waitcnt vmcnt(%0) lgkmcnt(0)\n\ts_barrier" ::"n"(N) : "memory");
  if (threadIdx.x == 0) __builtin_amdgcn_raw_buffer_store_b64((u32x2){e.lo, e.hi}, mkst(ws, WS_FLAG_BYTES), flag * 128, 0, 16);
}
DEV bool match(u32x2 f, Epoch e) { return __all(f.x == e.lo && f.y == e.hi); }
DEV void poll(u32x4 rsF, unsigned voff, Epoch e) {
#pragma unroll 1
  for (int it = 0; it < SPIN; ++it) {
    u32x2 f = ldc2(rsF, voff);
    waitv<0>(f);
    if (match(f, e)) break;
    __builtin_amdgcn_s_sleep(1);
  }
}

DEV void acc_stats(bf16x8 v, float& s, float& q) {
  const u32x4 u = __builtin_bit_cast(u32x4, v);
#pragma unroll
  for (int e = 0; e < 4; e++) {
    const unsigned t = u[e];
    const b16x2 p = __builtin_bit_cast(b16x2, t);
    s = __builtin_amdgcn_fdot2_f32_bf16(p, __builtin_bit_cast(b16x2, 0x3f803f80u), s, false);
    q = __builtin_amdgcn_fdot2_f32_bf16(p, p, q, false);
  }
}
DEV unsigned affine2(unsigned u, float a, float c) {
  float t0, t1;
  unsigned r;
  asm("v_lshlrev_b32 %1, 16, %3\n\tv_and_b32 %2, 0xffff0000, %3\n\tv_fma_f32 %1, %1, %4, %5\n\tv_fma_f32 %2, %2, %4, %5\n\tv_cvt_pk_bf16_f32 %0, %1, %2"
      : "=v"(r), "=&v"(t0), "=&v"(t1)
      : "v"(u), "v"(a), "v"(c));
  return r;
}
DEV f32x16 mfmav(u32x4 a, bf16x8 b, f32x16 c) {
  f32x16 r;
  asm volatile("v_mfma_f32_32x32x16_bf16 %0, %1, %2, %3" : "=&v"(r) : "v"(a), "v"(b), "v"(c));
  return r;
}
DEV void mfmac(f32x16& c, bf16x8 a, u32x4 b) { asm volatile("v_mfma_f32_32x32x16_bf16 %0, %1, %2, %0" : "+v"(c) : "v"(a), "v"(b)); }
DEV void touch(unsigned& sink, u32x4 rs, unsigned voff, unsigned soff) {
  asm volatile("buffer_load_dword %0, %2, %1, %3 offen" : "+v"(sink) : "s"(rs), "v"(voff), "s"(soff) : "memory");
}
DEV void touchl(u32x4 rs, unsigned voff, unsigned soff) {
  asm volatile("buffer_load_dword %1, %0, %2 offen lds" ::"s"(rs), "v"(voff), "s"(soff) : "memory");
}
template <int N> using IC = std::integral_constant<int, N>;
template <int T, int N, class Fn> DEV void unroll(Fn&& f) {
  if constexpr (T < N) { f(std::integral_constant<int, T>()); unroll<T + 1, N>(f); }
}
template <int OFF, class T> DEV void dmap(u32x4 rs, unsigned voff, unsigned soff, T& pin) {
  asm volatile("buffer_load_dwordx4 %2, %1, %3 offen offset:%4 lds" : "+v"(pin) : "s"(rs), "v"(voff), "s"(soff), "n"(OFF) : "memory");
}

template <class Fn> DEV void features(const f32x16& p, bool h, unsigned (&ph)[2][8], Fn&& cb) {
  constexpr float L = 1.4426950408889634f;
  float mx = fmaxf(p[0], p[1]), mn = fminf(p[0], p[1]);
#pragma unroll
  for (int r = 2; r < 12; r += 2) { mx = fmaxf(fmaxf(mx, p[r]), p[r + 1]); mn = fminf(fminf(mn, p[r]), p[r + 1]); }
  cb(IC<0>(), mx);
  float mx2 = fmaxf(fmaxf(fmaxf(fmaxf(mx, p[12]), p[13]), p[14]), p[15]);
  float mn2 = fminf(fminf(fminf(fminf(mn, p[12]), p[13]), p[14]), p[15]);
  mx = h ? mx : mx2; mn = h ? -mn : -mn2;
  swap32(mx, mn);
  float c0 = -L * fmaxf(mx, mn), c1 = c0;
  cb(IC<1>(), c0);
  swap32(c0, c1);
  const float ninf = -__builtin_inff();
  const float d0 = h ? ninf : c0, d1 = h ? ninf : c1;
  float e[2][16], s0 = 0.f, s1 = 0.f;
  unroll<0, 16>([&](auto rr) {
    constexpr int r = rr.value;
    float a0 = fmaf(p[r], L, r < 12 ? c0 : d0);
    if constexpr (r & 1) cb(IC<2 + r / 2>(), a0);
    e[0][r] = __builtin_amdgcn_exp2f(a0);
    e[1][r] = __builtin_amdgcn_exp2f(fmaf(p[r], -L, r < 12 ? c1 : d1));
    s0 += e[0][r]; s1 += e[1][r];
  });
  swap32(s0, s1);
  float i0 = __builtin_amdgcn_rcpf(s0 + s1), i1 = i0;
  swap32(i0, i1);
  unroll<0, 8>([&](auto rr) {
    constexpr int r = rr.value;
    ph[0][r] = pk(e[0][2 * r] * i0, e[0][2 * r + 1] * i0); ph[1][r] = pk(e[1][2 * r] * i1, e[1][2 * r + 1] * i1);
    if constexpr ((r & 3) == 1) cb(IC<10 + r / 4>(), ph[1][r]);
  });
}
DEV bf16x8 pack8(const f32x16& a, int o) {
  u32x4 u = {pk(a[o], a[o + 1]), pk(a[o + 2], a[o + 3]), pk(a[o + 4], a[o + 5]), pk(a[o + 6], a[o + 7])};
  return __builtin_bit_cast(bf16x8, u);
}
DEV float gelu(float a) {
  constexpr float c1 = -2.302208198f, c2 = c1 * 0.044715f;
  return a * __builtin_amdgcn_rcpf(1.f + __builtin_amdgcn_exp2f(a * fmaf(a * a, c2, c1)));
}

#define STAMP(n)

constexpr int woff(int k) { return (k >> 1) * SLOT + (k & 1) * 8192; }
constexpr int xoff(int k) { return (k >> 1) * SLOT + 4096 + (k & 1) * 2048; }
constexpr int G2R = 4, G2POLL = 5;
constexpr int g2ld(int t) { return t < 17 ? 4 : (t == 17 ? 2 : 0); }
constexpr int g2cnt(int s) { return s < 0 ? 4 : g2ld(s + G2R) + (s == G2POLL ? 1 : 0) + (s == 14 ? 4 : 0); }

__global__ __launch_bounds__(256, 1) void fused_kernel(const short*, const short*, const short*, const short*, const short*, const short*, const short*,
                                                       const short*, short*, char*) {
  u32x8 ka, kb;
  u32x4 kc;
  asm volatile("s_load_dwordx8 %0, %3, 0x0\n\ts_load_dwordx8 %1, %3, 0x20\n\ts_load_dwordx4 %2, %3, 0x40"
               : "=&s"(ka), "=&s"(kb), "=&s"(kc)
               : "s"(__builtin_amdgcn_kernarg_segment_ptr())
               : "memory");
  SB();
  __shared__ __attribute__((aligned(16))) char lds_[163840];
  const ldsp lds = (ldsp)lds_;
  const unsigned lds0 = (unsigned)(uintptr_t)lds;
  const int tid = threadIdx.x, lane = tid & 63, w = __builtin_amdgcn_readfirstlane(tid >> 6);
  const int xc = blockIdx.x & 7, i = blockIdx.x >> 3;
  const int h = lane >> 5, n = lane & 31, q = (lane & 15) >> 2, pp = lane & 3, blk = (lane >> 4) & 1;
  const unsigned area = lds0 + w * AREA;
  const int tile2 = 28 * (xc & 1) + i - 4, dt2 = tile2 >> 3, st2 = 8 * (xc >> 1) + (tile2 & 7);
  const unsigned swz = (lane >> 3), swc = ((lane & 7) ^ (((lane >> 4) & 1) << 2)) * 16;
  unsigned v2[4];
#pragma unroll
  for (int j = 0; j < 4; j++) v2[j] = i >= 4 ? swz * (S * 2) + swc + j * (8 * S * 2 - 1024) : OOB;
  unsigned so2 = (w * 448) * (S * 2) + st2 * 128;
  const bool isA = i >= 16;
  const int ja = i - 16, T = (ja & 12) + 2 * (xc & 1) + ((ja >> 1) & 1), pr = (xc & 6) + (ja & 1);
  const int tk0 = 256 * (xc & 1) + 64 * (xc >> 1);
  auto stage0 = [&](int st) { return ((tk0 ^ (st << 5)) & 256) + ((tk0 + 32 * st) & 255); };
  const int row0 = 512 * (i >> 2) + tk0 + 16 * (i & 3) + 4 * w;
  unsigned vt1 = xc < 7 ? (lane >> 2) * (F * 2) + (lane & 3) * 128 : OOB;
  unsigned vo1 = lane < 56 ? lane * 16 : OOB, vz = 0, sx0 = (isA ? 128 * T + 32 * w : row0) * (D * 2), ring = lds0 + L_WQ + w * 12544, vxa[4];
#pragma unroll
  for (int j = 0; j < 4; j++) vxa[j] = (8 * j + swz) * (D * 2) + (((lane & 7) ^ ((4 * j + (lane >> 4)) & 7)) * 16) - j * 1024;
  ldsp pb[2];
#pragma unroll
  for (int a = 0; a < 2; a++) pb[a] = lds + w * AREA + h * 1024 + q * 128 + (((4 * a + 2 * blk + (pp >> 1)) ^ ((q >> 1) << 2)) * 16) + (pp & 1) * 8;
  asm volatile("" : "+v"(v2[0]), "+v"(v2[1]), "+v"(v2[2]), "+v"(v2[3]), "+v"(vxa[0]), "+v"(vxa[1]), "+v"(vxa[2]), "+v"(vxa[3]), "+v"(vt1), "+v"(vo1), "+v"(vz),
               "+v"(pb[0]), "+v"(pb[1]), "+s"(so2), "+s"(sx0), "+s"(ring));
  SB();
  asm volatile("s_waitcnt lgkmcnt(0)" : "+s"(ka), "+s"(kb), "+s"(kc)::"memory");
  auto arg = [](unsigned lo, unsigned hi) { return (char*)(((unsigned long long)hi << 32) | lo); };
  const short *x = (short*)arg(ka[0], ka[1]), *wqkv = (short*)arg(ka[2], ka[3]), *wphi = (short*)arg(ka[4], ka[5]), *wo = (short*)arg(ka[6], ka[7]);
  const short *w1 = (short*)arg(kb[0], kb[1]), *b1 = (short*)arg(kb[2], kb[3]), *w2 = (short*)arg(kb[4], kb[5]), *b2 = (short*)arg(kb[6], kb[7]);
  short* out = (short*)arg(kc[0], kc[1]);
  char* ws = arg(kc[2], kc[3]);
  const u32x4 rsF = mkrs(ws, WS_FLAG_BYTES), rsX = mkrs(x, S * D * 2), rsW2 = mkrs(w2, F * S * 2);
  auto pre2 = [&](int t0, int t1, int slot0) {
#pragma unroll 1
    for (int t = t0; t < t1; t++) dma4(rsW2, v2, so2 + t * (32 * S * 2), area + (slot0 + t - t0) * 4096);
  };

  bf16x8 w2r[4][2][2];
  auto pre2w = [&]() {
#pragma unroll
    for (int t = 0; t < 4; t++) asm volatile("s_waitcnt lgkmcnt(0)" : "+v"(w2r[t][0][0]), "+v"(w2r[t][0][1]), "+v"(w2r[t][1][0]), "+v"(w2r[t][1][1])::"memory");
  };
  auto pre2rd = [&]() {
#pragma unroll
    for (int t = 0; t < 4; t++)
#pragma unroll
      for (int ks = 0; ks < 2; ks++)
#pragma unroll
        for (int a = 0; a < 2; a++) w2r[t][ks][a] = cat(vtr(pb[a] + (t + 6) * 4096 + ks * 2048), vtr(pb[a] + (t + 6) * 4096 + ks * 2048 + 512));
  };

  const u32x4 rsW1 = mkrs(w1, S * F * 2);
  auto so_t1 = [&](int stage, int wq, int r0) { return (512 * wq + stage0(stage) + r0) * (F * 2) + xc * 512; };
  Epoch ep;
  auto setep = [&](u32x2 gen) {
    const unsigned long long g = (((unsigned long long)__builtin_amdgcn_readfirstlane(gen.y) << 32) | (unsigned)__builtin_amdgcn_readfirstlane(gen.x)) + 1;
    ep.lo = (unsigned)g; ep.hi = (unsigned)(g >> 32);
  };

  if (!isA) {
    bf16x8 xv[4];
    u32x2 gen, bb[4];
    xv[0] = ldp<0>(rsX, vo1, sx0); xv[1] = ldp<896>(rsX, vo1, sx0); xv[2] = ldp<1792>(rsX, vo1, sx0); xv[3] = ldp<2688>(rsX, vo1, sx0);
    gen = ldc2(rsF, vz);
    SB();
    const bool isG = xc < 7 && i < 14;
    const int ft = 2 * xc + (i >= 7), dt = i >= 7 ? i - 7 : i;
    const unsigned vt = lane < 32 ? vt1 : OOB, vb = h * 8;
    {
      setm0(area + L_STAT);
      touchl(rsW1, vt, so_t1(i >> 2, i & 3, 8 * w));
      const u32x4 rsB = mkrs(b1, F * 2);
      const unsigned sb = (ft * 128 + w * 32) * 2;
      bb[0] = ldp2<0>(rsB, vb, sb); bb[1] = ldp2<16>(rsB, vb, sb); bb[2] = ldp2<32>(rsB, vb, sb); bb[3] = ldp2<48>(rsB, vb, sb);
    }
    unsigned v1[4][4], vx[4][2];
#pragma unroll
    for (int ks = 0; ks < 4; ks++) {
#pragma unroll
      for (int j = 0; j < 4; j++) v1[ks][j] = (16 * ks + 8 * (j & 1) + swz) * (F * 2) + (j >> 1) * 128 + swc - j * 1024;
#pragma unroll
      for (int j = 0; j < 2; j++) vx[ks][j] = (16 * ks + 8 * j + swz) * (D * 2) + swc - (j + 2 * (ks & 1)) * 1024;
    }
    waitv<5>(xv[0], xv[1], xv[2], xv[3]);
    tie(gen);
    STAMP(0);
    setep(gen);
    const unsigned tagB = ep.lo ^ ep.hi, vs = (lane & 15) ? OOB : (row0 + h + 2 * blk) * 16;
    u32x4 rec;
    {
      float s[4] = {}, z[4] = {};
#pragma unroll
      for (int r = 0; r < 4; r++) acc_stats(xv[r], s[r], z[r]);
      swap32(s[0], s[1]); swap32(s[2], s[3]); swap32(z[0], z[1]); swap32(z[2], z[3]);
      float sa = s[0] + s[1], sb = s[2] + s[3], za = z[0] + z[1], zb = z[2] + z[3];
      swap16(sa, sb); swap16(za, zb);
      const float mu = rowsum(sa + sb) * (1.f / D), ms = rowsum(za + zb) * (1.f / D);
      const float var = fmaxf(ms - mu * mu, 0.f);
      float a = __builtin_amdgcn_rsqf(var + 1e-5f);
      a *= __builtin_amdgcn_rsqf(var * a * a + 1e-5f);
      const unsigned a2 = pk(a, a);
      const float c = -mu * blo(a2);
      rec = (u32x4){a2, ep.lo, __builtin_bit_cast(unsigned, c), tagB};
      stc(mkst(ws + WS_ST, SZ_ST), vs, rec);
    }
    asm volatile("s_barrier" ::: "memory");
    hold(vz, vo1, vt, vb);
    STAMP(1);

    if (isG) {
      const unsigned b1s = (w * 512) * (F * 2) + ft * 256, bxs = (w * 512) * (D * 2) + dt * 128;
      auto mset = [&](auto kk, auto jj) {
        constexpr int ks = kk.value, j = jj.value;
        if constexpr ((ks & 1) ? j == 2 : j == 0) setm0n(area + woff(ks));
        if constexpr (!(ks & 1) && j == 4) setm0n(area + xoff(ks));
      };
      auto dgrp = [&](auto kk, auto jj, unsigned s1, unsigned sx) {
        constexpr int ks = kk.value, j = jj.value, jw = (ks & 1) ? j - 2 : j, jx = (ks & 1) ? j : j - 4;
        if constexpr (jw >= 0 && jw < 4) dma<jw * 1024>(rsW1, v1[ks][jw], s1);
        else dma<(jx + 2 * (ks & 1)) * 1024>(rsX, vx[ks][jx], sx);
      };
      auto fill = [&](auto kk) {
        unroll<0, 6>([&](auto jj) { mset(kk, jj); asm volatile("s_nop 0"); dgrp(kk, jj, b1s + tk0 * (F * 2), bxs + tk0 * (D * 2)); });
      };
      const u32x4 rsS = mkrs(ws + WS_ST, SZ_ST);
      auto gather = [&]() {
        bf16x8 g[8];
#pragma unroll
        for (int j = 0; j < 8; j++) g[j] = ldc<0>(rsS, lane * 16, w * 8192 + stage0(2 * j) * 16);
        waitv<0>();
        bool ok = true;
#pragma unroll
        for (int j = 0; j < 8; j++) {
          tie(g[j]);
          const u32x4 u = __builtin_bit_cast(u32x4, g[j]);
          ok &= u[1] == ep.lo && u[3] == tagB;
          const ldsp p = lds + w * AREA + L_STAT + j * 512 + (lane >> 4) * 128 + (lane & 15) * 4;
          lst(p, u[2]); lst(p + 64, u[0]);
        }
        return (bool)__all(ok);
      };
      unroll<0, 4>(fill);
      hold(vs, rec);
      STAMP(2);
#pragma unroll 1
      for (int it = 0; it < SPIN; ++it)
        if (gather()) break;
      STAMP(3);
      tie(bb[0]); tie(bb[1]); tie(bb[2]); tie(bb[3]);
      f32x16 acc[4][2];
#pragma unroll
      for (int g = 0; g < 4; g++) {
        acc[0][0][4 * g] = blo(bb[g].x); acc[0][0][4 * g + 1] = bhi(bb[g].x); acc[0][0][4 * g + 2] = blo(bb[g].y); acc[0][0][4 * g + 3] = bhi(bb[g].y);
      }
      acc[0][1] = acc[0][0];
#pragma unroll
      for (int a = 1; a < 4; a++) { acc[a][0] = (f32x16)(0.f); acc[a][1] = (f32x16)(0.f); }
      unsigned pa[4], px[2], msk[4], psc = area + L_STAT + h * 32, psa;
      {
        const unsigned rowb = area + (8 * h + q) * 128 + (pp & 1) * 8, ch = 2 * blk + (pp >> 1), fl = (q >> 1) << 2;
#pragma unroll
        for (int a = 0; a < 4; a++) pa[a] = rowb + ((a ^ w) >> 1) * 2048 + ((4 * ((a ^ w) & 1) + ch) ^ fl) * 16;
        px[0] = rowb + (ch ^ fl) * 16; px[1] = rowb + ((4 + ch) ^ fl) * 16;
        const unsigned r = 4 * ((lane >> 3) & 1) + pp, on = (!blk && (q & 1) == h) ? ((r & 1) ? 0xffff0000u : 0xffffu) : 0u;
#pragma unroll
        for (int j = 0; j < 4; j++) { msk[j] = (r >> 1) == j ? on : 0u; tie(msk[j]); }
        psa = area + L_STAT + 64 + (8 * h + r) * 4;
      }
      s16x4 wl[2][4], wh[2][4], xl[2], xh[2];
      u32x4 hf[2][2], dg;
      unsigned av;
      f32x16 hn[2], cc = {};
      tie(cc);
      auto rdX = [&](auto nn, auto dd) {
        constexpr int o = xoff(nn.value & 3), d = dd.value;
        xl[d] = vtr((ldsp)(uintptr_t)px[d] + o); xh[d] = vtr((ldsp)(uintptr_t)px[d] + o + 512);
      };
      auto rdA = [&](auto nn) { av = lld<unsigned>((ldsp)(uintptr_t)psa + nn.value * 128); };
      auto rdC = [&](auto nn) {
        const f32x4 c0 = lld<f32x4>((ldsp)(uintptr_t)psc + nn.value * 128), c1 = lld<f32x4>((ldsp)(uintptr_t)psc + nn.value * 128 + 16);
#pragma unroll
        for (int e = 0; e < 4; e++) { cc[e] = c0[e]; cc[4 + e] = c1[e]; }
      };
      auto rdW = [&](auto nn, auto aa) {
        constexpr int o = woff(nn.value & 3), nx = nn.value & 1, a = aa.value;
        wl[nx][a] = vtr((ldsp)(uintptr_t)pa[a] + o); wh[nx][a] = vtr((ldsp)(uintptr_t)pa[a] + o + 512);
      };
      auto mkD = [&]() { dg = (u32x4){av & msk[0], av & msk[1], av & msk[2], av & msk[3]}; };
      auto nrm = [&](auto dd) { hn[dd.value] = mfmav(dg, cat(xl[dd.value], xh[dd.value]), cc); };
      auto cvt = [&](auto nn, auto dd) {
        constexpr int d = dd.value;
        hf[nn.value & 1][d] = (u32x4){pk(hn[d][0], hn[d][1]), pk(hn[d][2], hn[d][3]), pk(hn[d][4], hn[d][5]), pk(hn[d][6], hn[d][7])};
        SB();
        keep(hn[d]);
      };
      auto step = [&](auto kk, auto wn, auto last, auto&& ms, auto&& gm) {
        constexpr int ks = kk.value;
        constexpr bool more = !last.value;
        using NX = IC<ks + 1>;
        auto mm = [&](auto m_) {
          constexpr int a = more ? m_.value >> 1 : ((m_.value >> 1) + 1) & 3, dh = m_.value & 1;
          if constexpr (a == 0) mfmac(acc[0][dh], cat(wl[ks & 1][0], wh[ks & 1][0]), hf[ks & 1][dh]);
          else acc[a][dh] = MFMA(cat(wl[ks & 1][a], wh[ks & 1][a]), __builtin_bit_cast(bf16x8, hf[ks & 1][dh]), acc[a][dh]);
          SB();
          if constexpr (!more && m_.value >= 2) {
            constexpr int c = m_.value >> 1;
#pragma unroll
            for (int g = 0; g < 4; g++)
              lst(lds + w * AREA + lane * 16 + (c - 1) * 8192 + dh * 4096 + g * 1024, (f32x4){acc[c][dh][4 * g], acc[c][dh][4 * g + 1], acc[c][dh][4 * g + 2], acc[c][dh][4 * g + 3]});
            SB();
          }
        };
        __builtin_amdgcn_s_waitcnt(0x70 | wn.value);
        asm volatile("" ::: "memory");
        SB();
        if constexpr (more) { rdX(NX(), IC<0>()); rdA(NX()); }
        ms(IC<0>());
        SB();
        mm(IC<0>());
        gm(IC<0>());
        if constexpr (more) { rdC(NX()); rdX(NX(), IC<1>()); SB(); }
        mm(IC<1>());
        gm(IC<1>());
        ms(IC<2>());
        if constexpr (more) { rdW(NX(), IC<0>()); rdW(NX(), IC<1>()); SB(); }
        mm(IC<2>());
        if constexpr (more) { mkD(); SB(); }
        gm(IC<2>());
        if constexpr (more) { rdW(NX(), IC<2>()); SB(); nrm(IC<0>()); SB(); rdW(NX(), IC<3>()); SB(); }
        mm(IC<3>());
        if constexpr (more) { nrm(IC<1>()); SB(); }
        gm(IC<3>());
        ms(IC<4>());
        mm(IC<4>());
        if constexpr (more) { cvt(NX(), IC<0>()); SB(); }
        mm(IC<5>());
        gm(IC<4>());
        mm(IC<6>());
        if constexpr (more) { cvt(NX(), IC<1>()); SB(); }
        gm(IC<5>());
        mm(IC<7>());
      };
      asm volatile("s_waitcnt lgkmcnt(0)" ::: "memory");
      rdX(IC<0>(), IC<0>()); rdX(IC<0>(), IC<1>()); rdA(IC<0>()); rdC(IC<0>());
      unroll<0, 4>([&](auto jj) { rdW(IC<0>(), jj); });
      mkD();
      SB();
      asm volatile("s_nop 1");
      nrm(IC<0>()); nrm(IC<1>());
      asm volatile("s_nop 7\n\ts_nop 7");
      cvt(IC<0>(), IC<0>()); cvt(IC<0>(), IC<1>());
      SB();
#pragma unroll 1
      for (int t = 2; t < 16; t += 2) {
        const unsigned tk = stage0(t);
        const unsigned s1 = b1s + tk * (F * 2), sx = bxs + tk * (D * 2);
        unroll<0, 4>([&](auto kk) {
          step(kk, IC<12>(), IC<0>(), [&](auto jj) { mset(kk, jj); SB(); }, [&](auto jj) { dgrp(kk, jj, s1, sx); SB(); });
        });
        psc += 512; tie(psc); psa += 512; tie(psa);
        SB();
      }
      {
        unroll<0, 4>([&](auto kk) {
          constexpr int ks = kk.value;
          step(kk, IC<(ks == 3 ? 12 : 12 - 2 * ks)>(), IC<(ks == 3)>(),
               [&](auto jj) { if constexpr (jj.value == 0) { setm0n(area + (6 + ks) * 4096); SB(); } },
               [&](auto jj) { if constexpr (jj.value < 4) { dma<jj.value * 1024>(rsW2, v2[jj.value], so2 + ks * (32 * S * 2)); SB(); } });
        });
      }
      STAMP(4);
      bar();
      STAMP(5);
      {
        const rsrc_t st = mkst(ws + WS_UF, SZ_UF);
        const unsigned vo = (ft * 16 + w * 4 + h) * (D * 16) + (dt * 64 + n) * 16;
        f32x4 v[2][3][4];
        u32x4 res[4];
#pragma unroll
        for (int dh = 0; dh < 2; dh++)
#pragma unroll
          for (int a = 1; a < 4; a++)
#pragma unroll
            for (int g = 0; g < 4; g++) v[dh][a - 1][g] = lld<f32x4>(lds + (w ^ a) * AREA + lane * 16 + (a - 1) * 8192 + dh * 4096 + g * 1024);
        SB();
#pragma unroll
        for (int dh = 0; dh < 2; dh++) {
#pragma unroll
          for (int g = 0; g < 4; g += 2) {
            float r[8];
#pragma unroll
            for (int e = 0; e < 8; e++) {
              const int gg = g + (e >> 2), c = e & 3;
              r[e] = gelu((acc[0][dh][4 * gg + c] + v[dh][0][gg][c]) + (v[dh][1][gg][c] + v[dh][2][gg][c]));
            }
            auto r0 = __builtin_amdgcn_permlane32_swap(pk(r[0], r[1]), pk(r[4], r[5]), false, false);
            auto r1 = __builtin_amdgcn_permlane32_swap(pk(r[2], r[3]), pk(r[6], r[7]), false, false);
            res[2 * dh + g / 2] = (u32x4){r0[0], r1[0], r0[1], r1[1]};
            stc(st, vo + g * (D * 16) + dh * 512, res[2 * dh + g / 2]);
          }
        }
        STAMP(8);
        waitv<4>();
        pre2rd();
        pre2w();
        hold(res[0], res[1], res[2], res[3], vo);
      }
      dma4(rsW2, v2, so2 + 10 * (32 * S * 2), area + 6 * 4096);
      STAMP(6);
      publish<4>(ws, FL_U + dt * 14 + ft, ep);
      STAMP(7);
      pre2(4, 10, 0);
      pre2(11, 14, 7);
    } else {
      if (xc < 7) {
#pragma unroll 1
        for (int m = 4; m < 9; m++) {
          if (m == 4) __builtin_amdgcn_s_sleep(24); else __builtin_amdgcn_s_sleep(10);
          touchl(rsW1, vt1, so_t1(m, 2 * (i - 14) + (w >> 1), 16 * (w & 1)));
        }
      }
      if (i >= 14) {
#pragma unroll 1
        for (int m = 0; m < 4; m++) touchl(mkrs(wo, D * D * 2), (lane >> 2) * (D * 2) + (lane & 3) * 128, (128 * m + 64 * (i - 14) + 16 * w) * (D * 2) + (xc & 1) * 384);
        const int head = 2 * xc + i - 14;
        const u32x4 rsP = mkrs(ws + WS_KVP, SZ_KVP);
        const unsigned so = head * 65536 + w * 1024, vf = (FL_P + xc * 16 + (lane & 15)) * 128;
        bf16x8 pv[16];
        float a[8] = {};
        unroll<0, 5>([&](auto gg) {
          constexpr int t = 4 * gg.value;
          if constexpr (t < 16) {
            if constexpr (t == 0) poll(rsF, vf, ep);
            if constexpr (t == 12) STAMP(3);
            unroll<t, t + 4>([&](auto tt) { pv[tt.value] = ldc<0>(rsP, lane * 16, so + tt.value * 4096); });
          }
          if constexpr (t > 0) {
            waitv<(t < 16 ? 4 : 0)>(pv[t - 4], pv[t - 3], pv[t - 2], pv[t - 1]);
#pragma unroll
            for (int j = t - 4; j < t; j++) {
              const u32x4 u = __builtin_bit_cast(u32x4, pv[j]);
#pragma unroll
              for (int e = 0; e < 4; e++) { const unsigned v = u[e]; a[2 * e] += blo(v); a[2 * e + 1] += bhi(v); }
            }
            SB();
          }
        });
        const rsrc_t st = mkst(ws + WS_KVF, SZ_KVF);
        const u32x4 r0 = {pk(a[0], a[1]), ep.lo, pk(a[2], a[3]), tagB}, r1 = {pk(a[4], a[5]), ep.lo, pk(a[6], a[7]), tagB};
        stc(st, head * 8192 + w * 2048 + lane * 16, r0);
        stc(st, head * 8192 + w * 2048 + 1024 + lane * 16, r1);
        hold(r0, r1);
        STAMP(4);
      } else {
        __builtin_amdgcn_s_sleep(127);
      }
      pre2(0, 4, 6); pre2(4, 10, 0);
      waitv<24>();
      pre2rd();
      pre2w();
      pre2(10, 14, 6);
    }
  } else {
    const unsigned sxa = sx0;
    dma4(rsX, vxa, sxa, ring);
    u32x2 gen = ldc2(rsF, vz);
    SB();
    unsigned sink = 0;
    const u32x4 rsW = mkrs(wqkv, D * 3 * D * 2);
    {
      const unsigned r6 = (lane * 43) >> 8, c6 = lane - 6 * r6;
      touch(sink, rsW, lane < 42 ? r6 * 2688 + 896 + (c6 & 1) * 896 + (c6 >> 1) * 108 : OOB, (28 * ja + 7 * w) * 2688 + (xc & 6) * 112);
    }
    dma4(rsX, vxa, sxa + 128, ring + 4096);
    dma4(rsX, vxa, sxa + 256, ring + 8192);
    unsigned vw[8];
#pragma unroll
    for (int s = 0; s < 8; s++) {
      const unsigned c = (s < 7 ? 256 * s + 64 * w : 3072) + lane, r = (c * 9363) >> 16;
      vw[s] = ((r & ~15u) + 8 * ((r >> 1) & 1) + 4 * (r & 1) + ((r >> 2) & 1) + 2 * ((r >> 3) & 1)) * 2688 + (c - 7 * r) * 16;
    }
    const unsigned mw = lds0 + w * 1024;
    auto mW = [&](auto ss, auto bb) {
      if constexpr (ss.value < 12) addm0<bb.value + ss.value * 4096>(mw); else addm0<bb.value + 49152>(lds0);
    };
    auto dmaW = [&](auto ss, unsigned col) {
      constexpr int s = ss.value;
      dma<0>(rsW, vw[s < 12 ? (s + 14) % 7 : 7], col + pr * 112 + (s >= 7 && s < 12 ? 256 * 2688 : 0));
    };
    bf16x8 xa[28];
    float sa = 0.f, sq = 0.f;
    {
      unsigned pr_[4];
#pragma unroll
      for (int k = 0; k < 4; k++) pr_[k] = ring + n * 128 + (((2 * k + h) ^ ((n >> 1) & 7)) * 16);
      unroll<0, 7>([&](auto cc) {
        constexpr int cb = cc.value;
        waitv<(cb == 0 ? 9 : 8)>();
#pragma unroll
        for (int k = 0; k < 4; k++) xa[4 * cb + k] = lld<bf16x8>((ldsp)(uintptr_t)pr_[k] + (cb % 3) * 4096);
        asm volatile("s_waitcnt lgkmcnt(0)" : "+v"(xa[4 * cb]), "+v"(xa[4 * cb + 1]), "+v"(xa[4 * cb + 2]), "+v"(xa[4 * cb + 3])::"memory");
        if constexpr (cb < 4) dma4(rsX, vxa, sxa + (cb + 3) * 128, ring + (cb % 3) * 4096);
        if constexpr (cb == 4) unroll<0, 4>([&](auto ss) { mW(ss, IC<L_WK>()); asm volatile("s_nop 0"); dmaW(ss, 896); });
        if constexpr (cb == 5) unroll<0, 4>([&](auto ss) { mW(ss, IC<L_WV>()); asm volatile("s_nop 0"); dmaW(ss, 1792); });
        if constexpr (cb == 6) {
          setm0(lds0 + L_WPHI + w * 1024);
          dma<0>(mkrs(wphi + pr * 1568, 3136), tid * 16, 0);
        }
        SB();
#pragma unroll
        for (int k = 0; k < 4; k++) acc_stats(xa[4 * cb + k], sa, sq);
        SB();
      });
    }
    STAMP(1);
    hold(vxa[0], vxa[1], vxa[2], vxa[3], vz);
    tie(gen);
    setep(gen);
    float la, lc;
    {
      float s2 = sa, q2 = sq;
      swap32(sa, s2); swap32(sq, q2);
      const float mu = (sa + s2) * (1.f / D), var = fmaxf((sq + q2) * (1.f / D) - mu * mu, 0.f);
      la = __builtin_amdgcn_rsqf(var + 1e-5f);
      lc = -mu * la;
    }
    auto norm = [&](auto kk, auto ee) {
      constexpr int ks = kk.value, e = ee.value;
      if constexpr (ks < 28) {
        u32x4 u = __builtin_bit_cast(u32x4, xa[ks]);
        u[e] = affine2(u[e], la, lc);
        xa[ks] = __builtin_bit_cast(bf16x8, u);
      }
    };
    unroll<0, 4>([&](auto ee) { norm(IC<0>(), ee); });
    norm(IC<1>(), IC<0>());
    tie(xa[0]); tie(xa[1]);
    waitv<1>();
    bar();
    STAMP(3);
    const unsigned vt2 = (lane >> 3) * (S * 2) + (lane & 7) * 128;
    auto touch2 = [&](int m) { touch(sink, rsW2, vt2, (448 * w + 128 * m + 8 * ja) * (S * 2) + (xc >> 1) * 1024); };
    const unsigned vtq = lane < 28 && (lane & 3) < 3 ? (lane >> 2) * 2688 + (lane & 3) * 108 : OOB;
    const unsigned vtx = xc < 7 && (lane & 7) < 7 ? (lane >> 3) * (D * 2) + (lane & 7) * 128 : OOB;
    const unsigned stx = (512 * ((2 * ja + (w >> 1)) & 3) + stage0(8 + (ja >> 1)) + 16 * (w & 1)) * (D * 2);
    const unsigned wlane = (2 * h + 4 * q) * 112 + 32 * blk + 8 * pp;
    f32x16 kT[2] = {}, vv[2] = {};
    {
      const ldsp pk_ = lds + L_WK + wlane, pv_ = lds + L_WV + wlane;
      s16x4 fr[30][8];
      auto rd = [&](auto kk, auto cc) {
        constexpr int ks = kk.value, c = cc.value;
        if constexpr (ks < 28) fr[ks][c] = vtr(((c & 4) ? pv_ : pk_) + ks * 1792 + ((c >> 1) & 1) * 56 + (c & 1) * 112);
      };
      unroll<0, 8>([&](auto cc) { rd(IC<0>(), cc); });
      unroll<0, 8>([&](auto cc) { rd(IC<1>(), cc); });
      SB();
      unroll<0, 28>([&](auto kk) {
        constexpr int ks = kk.value;
        if constexpr (ks == 7) { waitv<6>(); asm volatile("s_barrier" ::: "memory"); }
        if constexpr (ks == 16) { waitv<4>(); asm volatile("s_barrier" ::: "memory"); }
        __builtin_amdgcn_s_waitcnt(ks < 27 ? 0xc87f : 0xc07f);
        SB();
        unroll<0, 4>([&](auto mm) {
          constexpr int m = mm.value;
          const bf16x8 wv = cat(fr[ks][2 * m], fr[ks][2 * m + 1]);
          if constexpr (m < 2) kT[m] = MFMA(wv, xa[ks], kT[m]);
          else vv[m - 2] = MFMA(xa[ks], wv, vv[m - 2]);
          SB();
          if constexpr (ks < 9 && m == 0) mW(IC<4 + ks>(), IC<L_WK>());
          if constexpr (ks < 9 && m == 2) mW(IC<4 + ks>(), IC<L_WV>());
          rd(IC<ks + 2>(), IC<2 * m>());
          rd(IC<ks + 2>(), IC<2 * m + 1>());
          SB();
          if constexpr (ks < 9 && !(m & 1)) dmaW(IC<4 + ks>(), m ? 1792 : 896);
          if constexpr (ks == 9 && m == 0) touch(sink, rsW1, vt1, so_t1(8 + (ja >> 1), (2 * ja + (w >> 1)) & 3, 16 * (w & 1)));
          if constexpr (ks == 10 && m == 0) touch(sink, rsX, vtx, stx);
          if constexpr (ks == 11 && m == 0) touch(sink, rsX, vtx, stx + 8 * D * 2);
          if constexpr (ks == 14 && m == 0) touch(sink, rsW, vtq, (28 * ja + 7 * w) * 2688 + (xc & 6) * 112);
          if constexpr (ks == 22 && m == 0) touch2(0);
          SB();
          norm(IC<ks + 1 + (m == 3)>(), IC<(m + 1) & 3>());
          SB();
        });
      });
    }
    bf16x8 wf[2][2];
    {
      const ldsp p = lds + L_WPHI + (4 * h + q) * 56 + 32 * blk + 8 * pp;
#pragma unroll
      for (int hd = 0; hd < 2; hd++) {
        wf[hd][0] = cat(vtr(p + hd * 1568), vtr(p + hd * 1568 + 8 * 56));
        s16x4 z = vtr(p + hd * 1568 + 24 * 56);
        wf[hd][1] = cat(vtr(p + hd * 1568 + 16 * 56), h ? (s16x4){0, 0, 0, 0} : z);
      }
    }
    STAMP(5);
    {
      f32x16 p[2];
#pragma unroll
      for (int hd = 0; hd < 2; hd++) { p[hd] = MFMA(wf[hd][0], pack8(kT[hd], 0), (f32x16)(0.f)); p[hd] = MFMA(wf[hd][1], pack8(kT[hd], 8), p[hd]); }
      bar();
      {
        const bf16x8 ones = {0x3f80, 0x3f80, 0x3f80, 0x3f80, 0x3f80, 0x3f80, 0x3f80, 0x3f80};
        const ldsp pvx = lds + L_VX + w * 2048 + lane * 16;
#pragma unroll
        for (int hd = 0; hd < 2; hd++)
#pragma unroll
          for (int j = 0; j < 2; j++) { bf16x8 f = pack8(vv[hd], 8 * j); lst(pvx + hd * 8192 + j * 1024, n == 28 ? ones : f); }
      }
      const unsigned pb = (32 * w + n) * 64 + ((h ^ ((n >> 1) & 7)) * 8);
      unroll<0, 2>([&](auto hh) {
        constexpr int hd = hh.value;
        unsigned ph[2][8];
        features(p[hd], h, ph, [&](auto kk, auto& pin) {
          constexpr int s = 12 * hd + kk.value;
          if constexpr (s < 13) {
            mW(IC<s>(), IC<L_WQ>());
            asm volatile("s_nop 0");
            dmap<0>(rsW, vw[s < 12 ? s % 7 : 7], pr * 112 + (s >= 7 && s < 12 ? 256 * 2688 : 0), pin);
          }
          if constexpr (s == 14) touch2(1);
        });
#pragma unroll
        for (int sg = 0; sg < 2; sg++)
#pragma unroll
          for (int g = 0; g < 4; g++) lst(lds + L_PHI + (hd * 2 + sg) * 8192 + (pb ^ (g << 4)), (u32x2){ph[sg][2 * g], ph[sg][2 * g + 1]});
      });
    }
    STAMP(6);
    hold(vw[0], vw[1], vw[2], vw[3], vw[4], vw[5], vw[6], vw[7], vt1);
    waitv<1>();
    bar();
    u32x4 kvp[2];
    const unsigned vkv = (2 * pr + (w >> 1)) * 65536 + T * 4096 + (w & 1) * 2048 + lane * 16;
    {
      const unsigned sw = 2 * h + (q >> 1);
      const ldsp pl = lds + L_PHI + w * 8192 + (4 * h + q) * 64 + (((4 * blk + pp) ^ sw) * 8);
      const ldsp ph_ = lds + L_PHI + w * 8192 + (8 + 4 * h + q) * 64 + (((4 * blk + pp) ^ sw ^ 4) * 8);
      const ldsp pvx = lds + L_VX + (w >> 1) * 8192 + lane * 16;
      f32x16 kv = {};
#pragma unroll
      for (int jj = 0; jj < 8; jj++) kv = MFMA(cat(vtr(pl + jj * 1024), vtr(ph_ + jj * 1024)), lld<bf16x8>(pvx + jj * 1024), kv);
      const rsrc_t st = mkst(ws + WS_KVP, SZ_KVP);
      kvp[0] = __builtin_bit_cast(u32x4, pack8(kv, 0)); kvp[1] = __builtin_bit_cast(u32x4, pack8(kv, 8));
      stc(st, vkv, kvp[0]);
      stc(st, vkv + 1024, kvp[1]);
    }
    STAMP(7);
    f32x16 qT[2] = {};
    {
      const ldsp pq_ = lds + L_WQ + wlane;
      s16x4 fr[29][4];
      auto rd = [&](auto kk, auto cc) {
        constexpr int ks = kk.value, c = cc.value;
        if constexpr (ks < 28) fr[ks][c] = vtr(pq_ + ks * 1792 + (c >> 1) * 56 + (c & 1) * 112);
      };
      unroll<0, 4>([&](auto cc) { rd(std::integral_constant<int, 0>(), cc); });
      unroll<0, 28>([&](auto kk) {
        constexpr int ks = kk.value;
        if constexpr (ks == 14) { hold(kvp[0], kvp[1], vkv); publish(ws, FL_P + pr * 16 + T, ep); STAMP(8); touch2(2); touch2(3); SB(); }
        unroll<0, 2>([&](auto mm) {
          constexpr int m = mm.value;
          rd(std::integral_constant<int, ks + 1>(), std::integral_constant<int, 2 * m>());
          rd(std::integral_constant<int, ks + 1>(), std::integral_constant<int, 2 * m + 1>());
          qT[m] = MFMA(cat(fr[ks][2 * m], fr[ks][2 * m + 1]), xa[ks], qT[m]);
          SB();
        });
      });
    }
    unsigned phq[2][2][8];
    {
      f32x16 p[2];
#pragma unroll
      for (int hd = 0; hd < 2; hd++) { p[hd] = MFMA(wf[hd][0], pack8(qT[hd], 0), (f32x16)(0.f)); p[hd] = MFMA(wf[hd][1], pack8(qT[hd], 8), p[hd]); }
      bar();
      STAMP(9);
      unroll<0, 2>([&](auto hh) {
        features(p[hh.value], h, phq[hh.value], [&](auto kk, auto& pin) {
          constexpr int k = 12 * hh.value + kk.value, t = k >> 2;
          if constexpr ((k & 3) == 0) setm0(area + ((t + 6) % 10) * 4096);
          dmap<(k & 3) * 1024>(rsW2, v2[k & 3], so2 + t * (32 * S * 2), pin);
        });
      });
    }
    STAMP(10);
    pre2(6, 9, 2);
    {
      const u32x4 rsK = mkrs(ws + WS_KVF, SZ_KVF);
      const unsigned so = pr * 16384 + w * 2048, tagB = ep.lo ^ ep.hi;
      const ldsp px = lds + 5 * 4096 + lane * 16;
#pragma unroll 1
      for (int it = 0; it < SPIN; ++it) {
        bf16x8 g0 = ldc<0>(rsK, lane * 16, so), g1 = ldc<1024>(rsK, lane * 16, so), g2 = ldc<0>(rsK, lane * 16, so + 8192), g3 = ldc<1024>(rsK, lane * 16, so + 8192);
        waitv<0>(g0, g1, g2, g3);
        const u32x4 u0 = __builtin_bit_cast(u32x4, g0), u1 = __builtin_bit_cast(u32x4, g1), u2 = __builtin_bit_cast(u32x4, g2), u3 = __builtin_bit_cast(u32x4, g3);
        lst(px + w * AREA, (u32x4){u0[0], u0[2], u1[0], u1[2]});
        lst(px + w * AREA + 1024, (u32x4){u2[0], u2[2], u3[0], u3[2]});
        const unsigned bad = ((u0[1] ^ ep.lo) | (u1[1] ^ ep.lo) | (u2[1] ^ ep.lo) | (u3[1] ^ ep.lo)) | ((u0[3] ^ tagB) | (u1[3] ^ tagB) | (u2[3] ^ tagB) | (u3[3] ^ tagB));
        if (__all(bad == 0)) break;
      }
      STAMP(2);
      tie(sink);
      bar();
      STAMP(11);
      bf16x8 kf[2][4];
#pragma unroll
      for (int hd = 0; hd < 2; hd++)
#pragma unroll
        for (int c = 0; c < 4; c++) kf[hd][c] = lld<bf16x8>(px + c * AREA + hd * 1024);
      f32x16 o[2] = {};
#pragma unroll
      for (int hd = 0; hd < 2; hd++) {
#pragma unroll
        for (int c = 0; c < 4; c++) {
          const unsigned* s = &phq[hd][c >> 1][4 * (c & 1)];
          o[hd] = MFMA(kf[hd][c], __builtin_bit_cast(bf16x8, (u32x4){s[0], s[1], s[2], s[3]}), o[hd]);
        }
      }
      float da = o[0][12], db = o[1][12];
      swap32(da, db);
      float ia = __builtin_amdgcn_rcpf(db + 1e-6f), ib = ia;
      swap32(ia, ib);
      unsigned a[8], bq[8];
#pragma unroll
      for (int r = 0; r < 8; r++) { a[r] = pk(o[0][2 * r] * ia, o[0][2 * r + 1] * ia); bq[r] = pk(o[1][2 * r] * ib, o[1][2 * r + 1] * ib); }
      const rsrc_t st = mkst(ws + WS_OF, SZ_OF);
      const unsigned tk = (128 * T + 32 * w + n) * 16, jb = 14 * (pr >> 1);
      const unsigned vo = (jb + 6 * (pr & 1) + h) * (S * 16) + tk;
      const unsigned v1 = vo + 2 * S * 16, v2_ = vo + 4 * S * 16, v3 = h ? OOB : (jb + 12 + (pr & 1)) * (S * 16) + tk;
      const u32x4 r0 = {a[0], a[1], a[2], a[3]}, r1 = {a[4], a[5], bq[0], bq[1]}, r2 = {bq[2], bq[3], bq[4], bq[5]}, r3 = {a[6], a[7], bq[6], bq[7]};
      stc(st, vo, r0); stc(st, v1, r1); stc(st, v2_, r2); stc(st, v3, r3);
      pre2rd();
      pre2w();
      hold(vo, v1, v2_, v3, r0, r1, r2, r3);
    }
    dma4(rsW2, v2, so2 + 10 * (32 * S * 2), area + 6 * 4096);
    publish<4>(ws, FL_O + T * 8 + pr, ep);
    STAMP(12);
    pre2(9, 10, 5);
    pre2(11, 14, 7);
  }

  if (i >= 4) {
    const unsigned orow = st2 * 64 + 16 * w + (lane >> 3);
    const unsigned vo = orow * (D * 2) + dt2 * 128 + (lane & 7) * 16;
    const u32x4 rsB = mkrs(b2, S * 2), rsU = mkrs(ws + WS_UF, SZ_UF), rsO = mkrs(ws + WS_OF, SZ_OF), rsWo = mkrs(wo, D * D * 2);
    u32x4 wor[14];
    unsigned vwo[4];
#pragma unroll
    for (int j = 0; j < 4; j++) vwo[j] = swz * (D * 2) + swc + j * (8 * D * 2 - 1024);
    unroll<0, 14>([&](auto jj) {
      constexpr int j = jj.value & 3;
      wor[jj.value] = lda<j * 1024>(rsWo, vwo[j], w * 112 * (D * 2) + dt2 * 128 + (jj.value >> 2) * (32 * D * 2));
    });
    poll(rsF, (FL_U + dt2 * 14 + ((7 * w) >> 1) + (lane & 3)) * 128, ep);
    hold(vwo[0], vwo[1], vwo[2], vwo[3]);
    STAMP(13);
    const unsigned vu0 = h * (D * 16) + (dt2 * 64 + n) * 16, vu1 = vu0 + 2 * D * 16, sou = w * 56 * (D * 16);
    const unsigned vof = h * (S * 16) + (st2 * 64 + n) * 16, soo = w * 14 * (S * 16);
    const unsigned vfl = (FL_O + (st2 >> 1) * 8 + 2 * w + (lane & 1)) * 128;
    bf16x8 fa[18][4], xr0, xr1;
    unsigned bs0, bs1;
    const unsigned vb2 = orow * 2;
    u32x2 fl;
    auto ldD = [&](auto tt, auto jj) {
      constexpr int t = tt.value, j = jj.value;
      if constexpr (t < 14) fa[t][j] = ldc<(j & 1) * 512>(rsU, (j >> 1) ? vu1 : vu0, sou + t * (4 * D * 16));
      else if constexpr (j < g2ld(t)) fa[t][j] = ldc<(j & 1) * 512>(rsO, vof, soo + (2 * (t - 14) + (j >> 1)) * (2 * S * 16));
    };
    unroll<0, G2R>([&](auto tt) { unroll<0, 4>([&](auto jj) { ldD(tt, jj); }); });
    const ldsp pw[2] = {pb[0] - h * 512, pb[1] - h * 512};
    f32x16 acc[2][2] = {};
    bf16x8 wb[19][2][2];
    auto rdB = [&](auto tt, auto kk, auto aa) {
      constexpr int t = tt.value, ks = kk.value, a = aa.value, j = 2 * (t - 14) + ks;
      if constexpr (t < 4) wb[t][ks][a] = w2r[t][ks][a];
      else if constexpr (t < 14) wb[t][ks][a] = cat(vtr(pb[a] + (t - 4) * 4096 + ks * 2048), vtr(pb[a] + (t - 4) * 4096 + ks * 2048 + 512));
      else if constexpr (j < 6) {
        constexpr int u = j % 3, rl = 56 * (j / 3) + (u == 0 ? 0 : u == 1 ? 16 : 36), rh = 56 * (j / 3) + (u == 0 ? 8 : u == 1 ? 28 : 44);
        wb[t][ks][a] = cat(vtr(pw[a] + rl * 128), vtr(pw[a] + rh * 128));
      } else if constexpr (j == 6) wb[t][ks][a] = cat(vtr(pw[a] + h * (52 * 128) + 24 * 128), vtr(pw[a] + h * (52 * 128) + 52 * 128));
    };
    unroll<0, 4>([&](auto cc) { rdB(IC<0>(), IC<(cc.value >> 1)>(), IC<(cc.value & 1)>()); });
    unroll<0, 18>([&](auto tt) {
      constexpr int t = tt.value, nk = t < 17 ? 2 : 1;
      constexpr int left = g2cnt(t - 3) + g2cnt(t - 2) + g2cnt(t - 1);
      if constexpr (nk == 2) waitv<left>(fa[t][0], fa[t][1], fa[t][2], fa[t][3]); else waitv<left>(fa[t][0], fa[t][1]);
      if constexpr (t == G2POLL + G2R) {
        tie(fl);
        if (!match(fl, ep)) poll(rsF, vfl, ep);
      }
      SB();
      unroll<0, 4 * nk>([&](auto mm) {
        constexpr int m = mm.value, ks = m >> 2, nh = (m >> 1) & 1, mh = m & 1;
        if constexpr (t < 14) acc[nh][mh] = MFMA(fa[t][ks * 2 + mh], wb[t][ks][nh], acc[nh][mh]);
        else acc[nh][mh] = MFMA(wb[t][ks][mh], fa[t][ks * 2 + nh], acc[nh][mh]);
        SB();
        if constexpr (m & 1) rdB(IC<t + 1>(), IC<(m >> 2)>(), IC<((m >> 1) & 1)>());
        else if constexpr (t >= 5 && 4 * (t - 5) + m / 2 < 14) sta<(4 * (t - 5) + m / 2) * 1024>(area + lane * 16, wor[4 * (t - 5) + m / 2]);
        SB();
        if constexpr (!(m & 1)) ldD(IC<t + G2R>(), IC<m / 2>());
        if constexpr (t == G2POLL && m == 7) fl = ldc2(rsF, vfl);
        if constexpr (t == 14 && m == 0) xr0 = ldp<0>(rsX, vo, 0);
        if constexpr (t == 14 && m == 2) xr1 = ldp<0>(rsX, vo, 8 * D * 2);
        if constexpr (t == 14 && m == 4) bs0 = ldu16<0>(rsB, vb2, 0);
        if constexpr (t == 14 && m == 6) bs1 = ldu16<16>(rsB, vb2, 0);
        SB();
      });
    });
    STAMP(14);
#pragma unroll
    for (int nh = 0; nh < 2; nh++)
#pragma unroll
      for (int mh = 0; mh < 2; mh++)
#pragma unroll
        for (int g = 0; g < 4; g++)
          lst(lds + w * AREA + (32 * nh + n) * 256 + (((8 * mh + 2 * g + h) ^ (n & 15)) * 16),
              (f32x4){acc[nh][mh][4 * g], acc[nh][mh][4 * g + 1], acc[nh][mh][4 * g + 2], acc[nh][mh][4 * g + 3]});
    barv();
    tie(xr0); tie(xr1); tie(bs0); tie(bs1);
    hold(vb2);
    u32x4 res[2];
    const rsrc_t sto = mkst(out, S * D * 2);
#pragma unroll
    for (int u = 0; u < 2; u++) {
      const int r = 16 * w + 8 * u + (lane >> 3);
      const unsigned c0 = r * 256 + (((2 * (lane & 7)) ^ (r & 15)) * 16), c1 = c0 ^ 16;
      f32x4 a = lld<f32x4>(lds + c0), bq = lld<f32x4>(lds + c1);
#pragma unroll
      for (int s = 1; s < 4; s++) { a += lld<f32x4>(lds + s * AREA + c0); bq += lld<f32x4>(lds + s * AREA + c1); }
      const u32x4 xu = __builtin_bit_cast(u32x4, u ? xr1 : xr0);
      const float bv = blo(u ? bs1 : bs0);
      res[u] = (u32x4){pk(a[0] + bv + blo(xu[0]), a[1] + bv + bhi(xu[0])), pk(a[2] + bv + blo(xu[1]), a[3] + bv + bhi(xu[1])),
                   pk(bq[0] + bv + blo(xu[2]), bq[1] + bv + bhi(xu[2])), pk(bq[2] + bv + blo(xu[3]), bq[3] + bv + bhi(xu[3]))};
      __builtin_amdgcn_raw_buffer_store_b128(res[u], sto, vo + u * (8 * D * 2), 0, 16);
    }
    hold(res[0], res[1]);
    if (blockIdx.x == 255 && tid == 0) __builtin_amdgcn_raw_buffer_store_b64((u32x2){ep.lo, ep.hi}, mkst(ws, WS_FLAG_BYTES), 0, 0, 16);
  }
}

extern "C" void kernel_launch(void* const* d_in, const int* in_sizes, int n_in, void* d_out, int out_size, void* d_ws, size_t ws_size,
                              hipStream_t stream) {
  fused_kernel<<<256, 256, 0, stream>>>((const short*)d_in[0], (const short*)d_in[1], (const short*)d_in[2], (const short*)d_in[3],
                                        (const short*)d_in[5], (const short*)d_in[6], (const short*)d_in[7], (const short*)d_in[8],
                                        (short*)d_out, (char*)d_ws);
}
```

```cpp
#include <hip/hip_runtime.h>
#include <stdint.h>

typedef short bf16x8 __attribute__((ext_vector_type(8)));
typedef short s16x4 __attribute__((ext_vector_type(4)));
typedef float f32x2 __attribute__((ext_vector_type(2)));
typedef float f32x4 __attribute__((ext_vector_type(4)));
typedef float f32x16 __attribute__((ext_vector_type(16)));
typedef unsigned u32x2 __attribute__((ext_vector_type(2)));
typedef unsigned u32x4 __attribute__((ext_vector_type(4)));
typedef unsigned u32x8 __attribute__((ext_vector_type(8)));
typedef __bf16 b16x2 __attribute__((ext_vector_type(2)));
typedef __attribute__((address_space(3))) char* ldsp;
typedef __amdgpu_buffer_rsrc_t rsrc_t;

#define DEV __device__ __forceinline__
#define SB() __builtin_amdgcn_sched_barrier(0)
#define MFMA(a, b, c) __builtin_amdgcn_mfma_f32_32x32x16_bf16(a, b, c, 0, 0, 0)

constexpr int S = 2048, D = 448, F = 1792;
constexpr unsigned FL_P = 8, FL_O = FL_P + 128, FL_U = FL_O + 128;
constexpr unsigned WS_FLAG_BYTES = 65536;
constexpr unsigned WS_ST = WS_FLAG_BYTES, SZ_ST = S * 16;
constexpr unsigned WS_UF = WS_ST + SZ_ST, SZ_UF = 224 * 448 * 16;
constexpr unsigned WS_OF = WS_UF + SZ_UF, SZ_OF = 56 * 2048 * 16;
constexpr unsigned WS_KVP = WS_OF + SZ_OF, SZ_KVP = 16 * 16 * 4096;
constexpr unsigned WS_KVF = WS_KVP + SZ_KVP, SZ_KVF = 16 * 8 * 1024;
constexpr unsigned WS_END = WS_KVF + SZ_KVF;
constexpr unsigned AREA = 40960, L_STAT = 36864, SLOT = 12288;
constexpr unsigned L_WK = 0, L_WV = 50176, L_WQ = 100352, L_WPHI = 150528, L_PHI = 0, L_VX = 32768;
constexpr unsigned OOB = 0x40000000u;
constexpr int SPIN = 1 << 18;

DEV u32x4 mkrs(const void* p, unsigned bytes) {
  unsigned long long a = (unsigned long long)p;
  u32x4 r = {(unsigned)a, (unsigned)(a >> 32), bytes, 0x00020000u};
  return r;
}
DEV rsrc_t mkst(void* p, unsigned bytes) { return __builtin_amdgcn_make_buffer_rsrc(p, 0, bytes, 0x00020000); }
DEV void stc(rsrc_t rs, unsigned voff, u32x4 v) { __builtin_amdgcn_raw_buffer_store_b128(v, rs, voff, 0, 16); }
template <int OFF> DEV bf16x8 ldp(u32x4 rs, unsigned voff, unsigned soff) {
  bf16x8 r;
  asm volatile("buffer_load_dwordx4 %0, %2, %1, %3 offen offset:%4" : "=v"(r) : "s"(rs), "v"(voff), "s"(soff), "n"(OFF) : "memory");
  return r;
}
template <int OFF> DEV bf16x8 ldc(u32x4 rs, unsigned voff, unsigned soff) {
  bf16x8 r;
  asm volatile("buffer_load_dwordx4 %0, %2, %1, %3 offen offset:%4 sc1" : "=v"(r) : "s"(rs), "v"(voff), "s"(soff), "n"(OFF) : "memory");
  return r;
}
template <int OFF> DEV u32x2 ldp2(u32x4 rs, unsigned voff, unsigned soff) {
  u32x2 r;
  asm volatile("buffer_load_dwordx2 %0, %2, %1, %3 offen offset:%4" : "=v"(r) : "s"(rs), "v"(voff), "s"(soff), "n"(OFF) : "memory");
  return r;
}
template <int OFF> DEV u32x4 lda(u32x4 rs, unsigned voff, unsigned soff) {
  u32x4 r;
  asm volatile("buffer_load_dwordx4 %0, %2, %1, %3 offen offset:%4" : "=a"(r) : "s"(rs), "v"(voff), "s"(soff), "n"(OFF) : "memory");
  return r;
}
template <int OFF> DEV void sta(unsigned addr, u32x4 v) { asm volatile("ds_write_b128 %0, %1 offset:%2" ::"v"(addr), "a"(v), "n"(OFF) : "memory"); }
DEV u32x2 ldc2(u32x4 rs, unsigned voff) {
  u32x2 r;
  asm volatile("buffer_load_dwordx2 %0, %2, %1, 0 offen sc1" : "=v"(r) : "s"(rs), "v"(voff) : "memory");
  return r;
}
template <int OFF> DEV unsigned ldu16(u32x4 rs, unsigned voff, unsigned soff) {
  unsigned r;
  asm volatile("buffer_load_ushort %0, %2, %1, %3 offen offset:%4" : "=v"(r) : "s"(rs), "v"(voff), "s"(soff), "n"(OFF) : "memory");
  return r;
}
DEV void setm0(unsigned a) { asm volatile("s_mov_b32 m0, %0\n\ts_nop 0" ::"s"(a) : "memory"); }
DEV void setm0n(unsigned a) { asm volatile("s_mov_b32 m0, %0" ::"s"(a) : "memory"); }
template <int OFF> DEV void addm0(unsigned a) { asm volatile("s_add_u32 m0, %0, %1" ::"s"(a), "n"(OFF) : "memory", "scc"); }
template <int OFF> DEV void dma(u32x4 rs, unsigned voff, unsigned soff) {
  asm volatile("buffer_load_dwordx4 %1, %0, %2 offen offset:%3 lds" ::"s"(rs), "v"(voff), "s"(soff), "n"(OFF) : "memory");
}
DEV void dma4(u32x4 rs, const unsigned (&v)[4], unsigned soff, unsigned m0) {
  setm0(m0); dma<0>(rs, v[0], soff); dma<1024>(rs, v[1], soff); dma<2048>(rs, v[2], soff); dma<3072>(rs, v[3], soff);
}
template <int N> DEV void waitv() { asm volatile("s_waitcnt vmcnt(%0)" ::"n"(N) : "memory"); }
template <int N, class T> DEV void waitv(T& a) { asm volatile("s_waitcnt vmcnt(%1)" : "+v"(a) : "n"(N) : "memory"); }
template <int N, class T> DEV void waitv(T& a, T& b) { asm volatile("s_waitcnt vmcnt(%2)" : "+v"(a), "+v"(b) : "n"(N) : "memory"); }
template <int N, class T> DEV void waitv(T& a, T& b, T& c, T& d) {
  asm volatile("s_waitcnt vmcnt(%4)" : "+v"(a), "+v"(b), "+v"(c), "+v"(d) : "n"(N) : "memory");
}
template <class T> DEV void tie(T& a) { asm volatile("" : "+v"(a)::"memory"); }
DEV void tiea(u32x4& a) { asm volatile("" : "+a"(a)::"memory"); }
template <class T> DEV void hold(T a) { asm volatile("" ::"v"(a)); }
template <class T, class... R> DEV void hold(T a, R... r) { hold(a); hold(r...); }
DEV void keep(f32x16 a) { asm volatile("" ::"v"(a)); }
DEV void bar() { asm volatile("s_waitcnt lgkmcnt(0)\n\ts_barrier" ::: "memory"); }
DEV void barv() { asm volatile("s_waitcnt vmcnt(0) lgkmcnt(0)\n\ts_barrier" ::: "memory"); }
DEV s16x4 vtr(ldsp p) { return __builtin_amdgcn_ds_read_tr16_b64_v4i16((__attribute__((address_space(3))) s16x4*)p); }
DEV bf16x8 cat(s16x4 a, s16x4 b) { return __builtin_shufflevector(a, b, 0, 1, 2, 3, 4, 5, 6, 7); }
DEV unsigned pk(float a, float b) {
  f32x2 v = {a, b};
  return __builtin_bit_cast(unsigned, __builtin_convertvector(v, b16x2));
}
DEV float blo(unsigned u) { return __builtin_bit_cast(float, u << 16); }
DEV float bhi(unsigned u) { return __builtin_bit_cast(float, u & 0xffff0000u); }
DEV void swap32(float& a, float& b) {
  auto r = __builtin_amdgcn_permlane32_swap(__builtin_bit_cast(unsigned, a), __builtin_bit_cast(unsigned, b), false, false);
  const unsigned r0 = r[0], r1 = r[1];
  a = __builtin_bit_cast(float, r0); b = __builtin_bit_cast(float, r1);
}
DEV void swap16(float& a, float& b) {
  auto r = __builtin_amdgcn_permlane16_swap(__builtin_bit_cast(unsigned, a), __builtin_bit_cast(unsigned, b), false, false);
  const unsigned r0 = r[0], r1 = r[1];
  a = __builtin_bit_cast(float, r0); b = __builtin_bit_cast(float, r1);
}
template <int C> DEV float ror(float z) {
  return __builtin_bit_cast(float, __builtin_amdgcn_update_dpp(0, __builtin_bit_cast(int, z), C, 0xf, 0xf, true));
}
DEV float rowsum(float z) { z += ror<0x128>(z); z += ror<0x124>(z); z += ror<0x122>(z); z += ror<0x121>(z); return z; }
DEV float rdl(float z, int l) { return __builtin_bit_cast(float, __builtin_amdgcn_readlane(__builtin_bit_cast(int, z), l)); }
template <class T> DEV void lst(ldsp p, T v) { *(__attribute__((address_space(3))) T*)p = v; }
template <class T> DEV T lld(ldsp p) { return *(__attribute__((address_space(3))) T*)p; }

struct Epoch { unsigned lo, hi; };
template <int N> DEV void publish(u32x4 rsF, unsigned vflag, u32x2 e) {
  asm volatile("s_waitcnt vmcnt(%3) lgkmcnt(0)\n\ts_barrier\n\tbuffer_store_dwordx2 %2, %1, %0, 0 offen sc1" ::"s"(rsF), "v"(vflag), "v"(e), "n"(N) : "memory");
}
template <int N> DEV unsigned missing(u32x2 f, Epoch e) {
  unsigned r;
  asm volatile("v_cmp_ne_u64 vcc, %2, %1\n\ts_and_b32 %0, vcc_lo, %3" : "=s"(r) : "v"(f), "s"(((unsigned long long)e.hi << 32) | e.lo), "n"((1u << N) - 1) : "vcc", "scc");
  return r;
}
template <int N> DEV void poll(u32x4 rsF, unsigned voff, Epoch e) {
#pragma unroll 1
  for (int it = 0; it < SPIN; ++it) {
    u32x2 f = ldc2(rsF, voff);
    waitv<0>(f);
    if (!missing<N>(f, e)) break;
    __builtin_amdgcn_s_sleep(1);
  }
}
DEV void dman(u32x4 rs, const unsigned (&v)[4], unsigned soff, unsigned step, unsigned m0, unsigned n) {
  asm volatile("s_cmp_eq_u32 %1, 0\n\ts_cbranch_scc1 2f\n\ts_mov_b32 m0, %8\n1:\n\ts_sub_u32 %1, %1, 1\n\t"
               "buffer_load_dwordx4 %3, %2, %0 offen lds\n\tbuffer_load_dwordx4 %4, %2, %0 offen offset:1024 lds\n\t"
               "buffer_load_dwordx4 %5, %2, %0 offen offset:2048 lds\n\tbuffer_load_dwordx4 %6, %2, %0 offen offset:3072 lds\n\t"
               "s_add_u32 m0, m0, 0x1000\n\ts_add_u32 %0, %0, %7\n\ts_cmp_lg_u32 %1, 0\n\ts_cbranch_scc1 1b\n2:"
               : "+&s"(soff), "+&s"(n)
               : "s"(rs), "v"(v[0]), "v"(v[1]), "v"(v[2]), "v"(v[3]), "s"(step), "s"(m0)
               : "memory", "scc");
}

DEV void acc_stats(bf16x8 v, float& s, float& q) {
  const u32x4 u = __builtin_bit_cast(u32x4, v);
#pragma unroll
  for (int e = 0; e < 4; e++) {
    const unsigned t = u[e];
    const b16x2 p = __builtin_bit_cast(b16x2, t);
    s = __builtin_amdgcn_fdot2_f32_bf16(p, __builtin_bit_cast(b16x2, 0x3f803f80u), s, false);
    q = __builtin_amdgcn_fdot2_f32_bf16(p, p, q, false);
  }
}
DEV unsigned affine2(unsigned u, float a, float c) {
  float t0, t1;
  unsigned r;
  asm("v_lshlrev_b32 %1, 16, %3\n\tv_and_b32 %2, 0xffff0000, %3\n\tv_fma_f32 %1, %1, %4, %5\n\tv_fma_f32 %2, %2, %4, %5\n\tv_cvt_pk_bf16_f32 %0, %1, %2"
      : "=v"(r), "=&v"(t0), "=&v"(t1)
      : "v"(u), "v"(a), "v"(c));
  return r;
}
DEV f32x16 mfmav(u32x4 a, bf16x8 b, f32x16 c) {
  f32x16 r;
  asm volatile("v_mfma_f32_32x32x16_bf16 %0, %1, %2, %3" : "=&v"(r) : "v"(a), "v"(b), "v"(c));
  return r;
}
DEV void mfmac(f32x16& c, bf16x8 a, u32x4 b) { asm volatile("v_mfma_f32_32x32x16_bf16 %0, %1, %2, %0" : "+v"(c) : "v"(a), "v"(b)); }
DEV void mfma0(f32x16& c, bf16x8 a, u32x4 b) { asm volatile("v_mfma_f32_32x32x16_bf16 %0, %1, %2, 0" : "=&v"(c) : "v"(a), "v"(b)); }
DEV void touch(unsigned& sink, u32x4 rs, unsigned voff, unsigned soff) {
  asm volatile("buffer_load_dword %0, %2, %1, %3 offen" : "+v"(sink) : "s"(rs), "v"(voff), "s"(soff) : "memory");
}
template <class T> DEV void touchp(unsigned& sink, u32x4 rs, unsigned voff, unsigned soff, T& pin) {
  asm volatile("buffer_load_dword %0, %3, %2, %4 offen" : "+v"(sink), "+v"(pin) : "s"(rs), "v"(voff), "s"(soff) : "memory");
}
DEV void touchl(u32x4 rs, unsigned voff, unsigned soff) {
  asm volatile("buffer_load_dword %1, %0, %2 offen lds" ::"s"(rs), "v"(voff), "s"(soff) : "memory");
}
template <int N> using IC = std::integral_constant<int, N>;
template <int T, int N, class Fn> DEV void unroll(Fn&& f) {
  if constexpr (T < N) { f(std::integral_constant<int, T>()); unroll<T + 1, N>(f); }
}
template <int OFF, class T> DEV void dmap(u32x4 rs, unsigned voff, unsigned soff, T& pin) {
  asm volatile("buffer_load_dwordx4 %2, %1, %3 offen offset:%4 lds" : "+v"(pin) : "s"(rs), "v"(voff), "s"(soff), "n"(OFF) : "memory");
}

template <class Fn> DEV void features(const f32x16& p, bool h, unsigned (&ph)[2][8], Fn&& cb) {
  constexpr float L = 1.4426950408889634f;
  float mx = fmaxf(p[0], p[1]), mn = fminf(p[0], p[1]);
#pragma unroll
  for (int r = 2; r < 12; r += 2) { mx = fmaxf(fmaxf(mx, p[r]), p[r + 1]); mn = fminf(fminf(mn, p[r]), p[r + 1]); }
  cb(IC<0>(), mx);
  float mx2 = fmaxf(fmaxf(fmaxf(fmaxf(mx, p[12]), p[13]), p[14]), p[15]);
  float mn2 = fminf(fminf(fminf(fminf(mn, p[12]), p[13]), p[14]), p[15]);
  mx = h ? mx : mx2; mn = h ? -mn : -mn2;
  swap32(mx, mn);
  float c0 = -L * fmaxf(mx, mn), c1 = c0;
  cb(IC<1>(), c0);
  swap32(c0, c1);
  const float ninf = -__builtin_inff();
  const float d0 = h ? ninf : c0, d1 = h ? ninf : c1;
  float e[2][16], s0 = 0.f, s1 = 0.f;
  unroll<0, 16>([&](auto rr) {
    constexpr int r = rr.value;
    float a0 = fmaf(p[r], L, r < 12 ? c0 : d0);
    if constexpr (r & 1) cb(IC<2 + r / 2>(), a0);
    e[0][r] = __builtin_amdgcn_exp2f(a0);
    e[1][r] = __builtin_amdgcn_exp2f(fmaf(p[r], -L, r < 12 ? c1 : d1));
    s0 += e[0][r]; s1 += e[1][r];
  });
  swap32(s0, s1);
  float i0 = __builtin_amdgcn_rcpf(s0 + s1), i1 = i0;
  swap32(i0, i1);
  unroll<0, 8>([&](auto rr) {
    constexpr int r = rr.value;
    ph[0][r] = pk(e[0][2 * r] * i0, e[0][2 * r + 1] * i0); ph[1][r] = pk(e[1][2 * r] * i1, e[1][2 * r + 1] * i1);
    if constexpr ((r & 3) == 1) cb(IC<10 + r / 4>(), ph[1][r]);
  });
}
DEV bf16x8 pack8(const f32x16& a, int o) {
  u32x4 u = {pk(a[o], a[o + 1]), pk(a[o + 2], a[o + 3]), pk(a[o + 4], a[o + 5]), pk(a[o + 6], a[o + 7])};
  return __builtin_bit_cast(bf16x8, u);
}
DEV float gelu(float a) {
  constexpr float c1 = -2.302208198f, c2 = c1 * 0.044715f;
  return a * __builtin_amdgcn_rcpf(1.f + __builtin_amdgcn_exp2f(a * fmaf(a * a, c2, c1)));
}

#define STAMP(n)

constexpr int woff(int k) { return (k >> 1) * SLOT + (k & 1) * 8192; }
constexpr int xoff(int k) { return (k >> 1) * SLOT + 4096 + (k & 1) * 2048; }
#define TA0 22
#define TA1 13
#define TA2 99
#define QP 11
constexpr int G2R = 4, G2POLL = 6;
constexpr int g2ld(int t) { return t < 17 ? 4 : (t == 17 ? 2 : 0); }
constexpr int g2cnt(int s) { return s < 0 ? 4 : g2ld(s + G2R) + (s == G2POLL ? 1 : 0) + (s == 14 ? 4 : 0); }

__global__ __launch_bounds__(256, 1) void fused_kernel(const short*, const short*, const short*, const short*, const short*, const short*, const short*,
                                                       const short*, short*, char*) {
  u32x8 ka, kb;
  u32x4 kc;
  asm volatile("s_load_dwordx8 %0, %3, 0x0\n\ts_load_dwordx8 %1, %3, 0x20\n\ts_load_dwordx4 %2, %3, 0x40"
               : "=&s"(ka), "=&s"(kb), "=&s"(kc)
               : "s"(__builtin_amdgcn_kernarg_segment_ptr())
               : "memory");
  SB();
  __shared__ __attribute__((aligned(16))) char lds_[163840];
  const ldsp lds = (ldsp)lds_;
  const unsigned lds0 = (unsigned)(uintptr_t)lds;
  const int tid = threadIdx.x, lane = tid & 63, w = __builtin_amdgcn_readfirstlane(tid >> 6);
  const int xc = blockIdx.x & 7, i = blockIdx.x >> 3;
  const int h = lane >> 5, n = lane & 31, q = (lane & 15) >> 2, pp = lane & 3, blk = (lane >> 4) & 1;
  const unsigned area = lds0 + w * AREA;
  const int tile2 = 28 * (xc & 1) + i - 4, dt2 = tile2 >> 3, st2 = 8 * (xc >> 1) + (tile2 & 7);
  const unsigned swz = (lane >> 3), swc = ((lane & 7) ^ (((lane >> 4) & 1) << 2)) * 16;
  unsigned v2[4];
#pragma unroll
  for (int j = 0; j < 4; j++) v2[j] = i >= 4 ? swz * (S * 2) + swc + j * (8 * S * 2 - 1024) : OOB;
  unsigned so2 = (w * 448) * (S * 2) + st2 * 128;
  const bool isA = i >= 16;
  const int ja = i - 16, T = (ja & 12) + 2 * (xc & 1) + ((ja >> 1) & 1), pr = (xc & 6) + (ja & 1);
  const int tk0 = 256 * (xc & 1) + 64 * (xc >> 1);
  auto stage0 = [&](int st) { return ((tk0 ^ (st << 5)) & 256) + ((tk0 + 32 * st) & 255); };
  const int row0 = 512 * (i >> 2) + tk0 + 16 * (i & 3) + 4 * w;
  unsigned vt1 = xc < 7 ? (lane >> 2) * (F * 2) + (lane & 3) * 128 : OOB;
  unsigned vo1 = lane < 56 ? lane * 16 : OOB, vz = 0, sx0 = (isA ? 128 * T + 32 * w : row0) * (D * 2), ring = lds0 + L_WQ + w * 12544, vxa[4];
#pragma unroll
  for (int j = 0; j < 4; j++) vxa[j] = (8 * j + swz) * (D * 2) + (((lane & 7) ^ ((4 * j + (lane >> 4)) & 7)) * 16) - j * 1024;
  ldsp pb[2];
#pragma unroll
  for (int a = 0; a < 2; a++) pb[a] = lds + w * AREA + h * 1024 + q * 128 + (((4 * a + 2 * blk + (pp >> 1)) ^ ((q >> 1) << 2)) * 16) + (pp & 1) * 8;
  asm volatile("" : "+v"(v2[0]), "+v"(v2[1]), "+v"(v2[2]), "+v"(v2[3]), "+v"(vxa[0]), "+v"(vxa[1]), "+v"(vxa[2]), "+v"(vxa[3]), "+v"(vt1), "+v"(vo1), "+v"(vz),
               "+v"(pb[0]), "+v"(pb[1]), "+s"(so2), "+s"(sx0), "+s"(ring));
  SB();
  asm volatile("s_waitcnt lgkmcnt(0)" : "+s"(ka), "+s"(kb), "+s"(kc)::"memory");
  auto arg = [](unsigned lo, unsigned hi) { return (char*)(((unsigned long long)hi << 32) | lo); };
  const short *x = (short*)arg(ka[0], ka[1]), *wqkv = (short*)arg(ka[2], ka[3]), *wphi = (short*)arg(ka[4], ka[5]), *wo = (short*)arg(ka[6], ka[7]);
  const short *w1 = (short*)arg(kb[0], kb[1]), *b1 = (short*)arg(kb[2], kb[3]), *w2 = (short*)arg(kb[4], kb[5]), *b2 = (short*)arg(kb[6], kb[7]);
  short* out = (short*)arg(kc[0], kc[1]);
  char* ws = arg(kc[2], kc[3]);
  const u32x4 rsF = mkrs(ws, WS_FLAG_BYTES), rsX = mkrs(x, S * D * 2), rsW2 = mkrs(w2, F * S * 2);
  auto pre2 = [&](int t0, int t1, int slot0) {
#pragma unroll 1
    for (int t = t0; t < t1; t++) dma4(rsW2, v2, so2 + t * (32 * S * 2), area + (slot0 + t - t0) * 4096);
  };

  bf16x8 w2r[4][2][2];
  auto pre2w = [&]() {
#pragma unroll
    for (int t = 0; t < 4; t++) asm volatile("s_waitcnt lgkmcnt(0)" : "+v"(w2r[t][0][0]), "+v"(w2r[t][0][1]), "+v"(w2r[t][1][0]), "+v"(w2r[t][1][1])::"memory");
  };
  auto pre2rd = [&]() {
#pragma unroll
    for (int t = 0; t < 4; t++)
#pragma unroll
      for (int ks = 0; ks < 2; ks++)
#pragma unroll
        for (int a = 0; a < 2; a++) w2r[t][ks][a] = cat(vtr(pb[a] + (t + 6) * 4096 + ks * 2048), vtr(pb[a] + (t + 6) * 4096 + ks * 2048 + 512));
  };

  u32x4 wor[14];
  unsigned vwo[4];
#pragma unroll
  for (int j = 0; j < 4; j++) vwo[j] = i >= 4 ? swz * (D * 2) + swc + j * (8 * D * 2 - 1024) : OOB;
  const u32x4 rsWo = mkrs(wo, D * D * 2);
  const unsigned sow = w * 112 * (D * 2) + dt2 * 128;
  auto ldwo = [&](auto jj) { wor[jj.value] = lda<(jj.value & 3) * 1024>(rsWo, vwo[jj.value & 3], sow + (jj.value >> 2) * (32 * D * 2)); };
  auto tiewo = [&]() { unroll<0, 14>([&](auto jj) { tiea(wor[jj.value]); }); hold(vwo[0], vwo[1], vwo[2], vwo[3]); };
  const u32x4 rsW1 = mkrs(w1, S * F * 2);
  auto so_t1 = [&](int stage, int wq, int r0) { return (512 * wq + stage0(stage) + r0) * (F * 2) + xc * 512; };
  const unsigned vfu = lane < 4 && i >= 4 ? (FL_U + dt2 * 14 + ((7 * w) >> 1) + lane) * 128 : OOB;
  unsigned miss = 1, td = 14;
  Epoch ep;
  auto setep = [&](u32x2 gen) {
    const unsigned long long g = (((unsigned long long)__builtin_amdgcn_readfirstlane(gen.y) << 32) | (unsigned)__builtin_amdgcn_readfirstlane(gen.x)) + 1;
    ep.lo = (unsigned)g; ep.hi = (unsigned)(g >> 32);
  };

  if (!isA) {
    bf16x8 xv[4];
    u32x2 gen, bb[4];
    xv[0] = ldp<0>(rsX, vo1, sx0); xv[1] = ldp<896>(rsX, vo1, sx0); xv[2] = ldp<1792>(rsX, vo1, sx0); xv[3] = ldp<2688>(rsX, vo1, sx0);
    gen = ldc2(rsF, vz);
    SB();
    const bool isG = xc < 7 && i < 14;
    const int ft = 2 * xc + (i >= 7), dt = i >= 7 ? i - 7 : i;
    const unsigned vt = lane < 32 ? vt1 : OOB, vb = h * 8;
    {
      setm0(area + L_STAT);
      touchl(rsW1, vt, so_t1(i >> 2, i & 3, 8 * w));
      const u32x4 rsB = mkrs(b1, F * 2);
      const unsigned sb = (ft * 128 + w * 32) * 2;
      bb[0] = ldp2<0>(rsB, vb, sb); bb[1] = ldp2<16>(rsB, vb, sb); bb[2] = ldp2<32>(rsB, vb, sb); bb[3] = ldp2<48>(rsB, vb, sb);
    }
    unsigned v1[4][4], vx[4][2];
#pragma unroll
    for (int ks = 0; ks < 4; ks++) {
#pragma unroll
      for (int j = 0; j < 4; j++) v1[ks][j] = (16 * ks + 8 * (j & 1) + swz) * (F * 2) + (j >> 1) * 128 + swc - j * 1024;
#pragma unroll
      for (int j = 0; j < 2; j++) vx[ks][j] = (16 * ks + 8 * j + swz) * (D * 2) + swc - (j + 2 * (ks & 1)) * 1024;
    }
    waitv<5>(xv[0], xv[1], xv[2], xv[3]);
    tie(gen);
    STAMP(0);
    setep(gen);
    const unsigned tagB = ep.lo ^ ep.hi, vs = (lane & 15) ? OOB : (row0 + h + 2 * blk) * 16;
    u32x4 rec;
    {
      float s[4] = {}, z[4] = {};
#pragma unroll
      for (int r = 0; r < 4; r++) acc_stats(xv[r], s[r], z[r]);
      swap32(s[0], s[1]); swap32(s[2], s[3]); swap32(z[0], z[1]); swap32(z[2], z[3]);
      float sa = s[0] + s[1], sb = s[2] + s[3], za = z[0] + z[1], zb = z[2] + z[3];
      swap16(sa, sb); swap16(za, zb);
      const float mu = rowsum(sa + sb) * (1.f / D), ms = rowsum(za + zb) * (1.f / D);
      const float var = fmaxf(ms - mu * mu, 0.f);
      float a = __builtin_amdgcn_rsqf(var + 1e-5f);
      a *= __builtin_amdgcn_rsqf(var * a * a + 1e-5f);
      const unsigned a2 = pk(a, a);
      const float c = -mu * blo(a2);
      rec = (u32x4){a2, ep.lo, __builtin_bit_cast(unsigned, c), tagB};
      stc(mkst(ws + WS_ST, SZ_ST), vs, rec);
    }
    asm volatile("s_barrier" ::: "memory");
    hold(vz, vo1, vt, vb);
    STAMP(1);

    if (isG) {
      const unsigned b1s = (w * 512) * (F * 2) + ft * 256, bxs = (w * 512) * (D * 2) + dt * 128;
      auto mset = [&](auto kk, auto jj) {
        constexpr int ks = kk.value, j = jj.value;
        if constexpr ((ks & 1) ? j == 2 : j == 0) setm0n(area + woff(ks));
        if constexpr (!(ks & 1) && j == 4) setm0n(area + xoff(ks));
      };
      auto dgrp = [&](auto kk, auto jj, unsigned s1, unsigned sx) {
        constexpr int ks = kk.value, j = jj.value, jw = (ks & 1) ? j - 2 : j, jx = (ks & 1) ? j : j - 4;
        if constexpr (jw >= 0 && jw < 4) dma<jw * 1024>(rsW1, v1[ks][jw], s1);
        else dma<(jx + 2 * (ks & 1)) * 1024>(rsX, vx[ks][jx], sx);
      };
      auto fill = [&](auto kk) {
        unroll<0, 6>([&](auto jj) { mset(kk, jj); asm volatile("s_nop 0"); dgrp(kk, jj, b1s + tk0 * (F * 2), bxs + tk0 * (D * 2)); });
      };
      const u32x4 rsS = mkrs(ws + WS_ST, SZ_ST);
      auto gather = [&]() {
        bf16x8 g[8];
#pragma unroll
        for (int j = 0; j < 8; j++) g[j] = ldc<0>(rsS, lane * 16, w * 8192 + stage0(2 * j) * 16);
        waitv<0>();
        bool ok = true;
#pragma unroll
        for (int j = 0; j < 8; j++) {
          tie(g[j]);
          const u32x4 u = __builtin_bit_cast(u32x4, g[j]);
          ok &= u[1] == ep.lo && u[3] == tagB;
          const ldsp p = lds + w * AREA + L_STAT + j * 512 + (lane >> 4) * 128 + (lane & 15) * 4;
          lst(p, u[2]); lst(p + 64, u[0]);
        }
        return (bool)__all(ok);
      };
      unroll<0, 4>(fill);
      hold(vs, rec);
      STAMP(2);
#pragma unroll 1
      for (int it = 0; it < SPIN; ++it)
        if (gather()) break;
      STAMP(3);
      tie(bb[0]); tie(bb[1]); tie(bb[2]); tie(bb[3]);
      f32x16 acc[4][2];
#pragma unroll
      for (int g = 0; g < 4; g++) {
        acc[0][0][4 * g] = blo(bb[g].x); acc[0][0][4 * g + 1] = bhi(bb[g].x); acc[0][0][4 * g + 2] = blo(bb[g].y); acc[0][0][4 * g + 3] = bhi(bb[g].y);
      }
      acc[0][1] = acc[0][0];
#pragma unroll
      for (int a = 1; a < 4; a++) { acc[a][0] = (f32x16)(0.f); acc[a][1] = (f32x16)(0.f); }
      unsigned pa[4], px[2], msk[4], psc = area + L_STAT + h * 32, psa;
      {
        const unsigned rowb = area + (8 * h + q) * 128 + (pp & 1) * 8, ch = 2 * blk + (pp >> 1), fl = (q >> 1) << 2;
#pragma unroll
        for (int a = 0; a < 4; a++) pa[a] = rowb + ((a ^ w) >> 1) * 2048 + ((4 * ((a ^ w) & 1) + ch) ^ fl) * 16;
        px[0] = rowb + (ch ^ fl) * 16; px[1] = rowb + ((4 + ch) ^ fl) * 16;
        const unsigned r = 4 * ((lane >> 3) & 1) + pp, on = (!blk && (q & 1) == h) ? ((r & 1) ? 0xffff0000u : 0xffffu) : 0u;
#pragma unroll
        for (int j = 0; j < 4; j++) { msk[j] = (r >> 1) == j ? on : 0u; tie(msk[j]); }
        psa = area + L_STAT + 64 + (8 * h + r) * 4;
      }
      s16x4 wl[2][4], wh[2][4], xl[2], xh[2];
      u32x4 hf[2][2], dg;
      unsigned av;
      f32x16 hn[2], cc = {};
      tie(cc);
      auto rdX = [&](auto nn, auto dd) {
        constexpr int o = xoff(nn.value & 3), d = dd.value;
        xl[d] = vtr((ldsp)(uintptr_t)px[d] + o); xh[d] = vtr((ldsp)(uintptr_t)px[d] + o + 512);
      };
      auto rdA = [&](auto nn) { av = lld<unsigned>((ldsp)(uintptr_t)psa + nn.value * 128); };
      auto rdC = [&](auto nn) {
        const f32x4 c0 = lld<f32x4>((ldsp)(uintptr_t)psc + nn.value * 128), c1 = lld<f32x4>((ldsp)(uintptr_t)psc + nn.value * 128 + 16);
#pragma unroll
        for (int e = 0; e < 4; e++) { cc[e] = c0[e]; cc[4 + e] = c1[e]; }
      };
      auto rdW = [&](auto nn, auto aa) {
        constexpr int o = woff(nn.value & 3), nx = nn.value & 1, a = aa.value;
        wl[nx][a] = vtr((ldsp)(uintptr_t)pa[a] + o); wh[nx][a] = vtr((ldsp)(uintptr_t)pa[a] + o + 512);
      };
      auto mkD = [&]() { dg = (u32x4){av & msk[0], av & msk[1], av & msk[2], av & msk[3]}; };
      auto nrm = [&](auto dd) { hn[dd.value] = mfmav(dg, cat(xl[dd.value], xh[dd.value]), cc); };
      auto cvt = [&](auto nn, auto dd) {
        constexpr int d = dd.value;
        hf[nn.value & 1][d] = (u32x4){pk(hn[d][0], hn[d][1]), pk(hn[d][2], hn[d][3]), pk(hn[d][4], hn[d][5]), pk(hn[d][6], hn[d][7])};
        SB();
        keep(hn[d]);
      };
      auto step = [&](auto kk, auto wn, auto last, auto&& ms, auto&& gm) {
        constexpr int ks = kk.value;
        constexpr bool more = !last.value;
        using NX = IC<ks + 1>;
        auto mm = [&](auto m_) {
          constexpr int a = more ? m_.value >> 1 : ((m_.value >> 1) + 1) & 3, dh = m_.value & 1;
          if constexpr (a == 0) mfmac(acc[0][dh], cat(wl[ks & 1][0], wh[ks & 1][0]), hf[ks & 1][dh]);
          else acc[a][dh] = MFMA(cat(wl[ks & 1][a], wh[ks & 1][a]), __builtin_bit_cast(bf16x8, hf[ks & 1][dh]), acc[a][dh]);
          SB();
          if constexpr (!more && m_.value >= 2) {
            constexpr int c = m_.value >> 1;
#pragma unroll
            for (int g = 0; g < 4; g++)
              lst(lds + w * AREA + lane * 16 + (c - 1) * 8192 + dh * 4096 + g * 1024, (f32x4){acc[c][dh][4 * g], acc[c][dh][4 * g + 1], acc[c][dh][4 * g + 2], acc[c][dh][4 * g + 3]});
            SB();
          }
        };
        __builtin_amdgcn_s_waitcnt(0x70 | (wn.value & 15) | ((wn.value >> 4) << 14));
        asm volatile("" ::: "memory");
        SB();
        if constexpr (more) { rdX(NX(), IC<0>()); rdA(NX()); }
        ms(IC<0>());
        SB();
        mm(IC<0>());
        gm(IC<0>());
        if constexpr (more) { rdC(NX()); rdX(NX(), IC<1>()); SB(); }
        mm(IC<1>());
        gm(IC<1>());
        ms(IC<2>());
        if constexpr (more) { rdW(NX(), IC<0>()); rdW(NX(), IC<1>()); SB(); }
        mm(IC<2>());
        if constexpr (more) { mkD(); SB(); }
        gm(IC<2>());
        if constexpr (more) { rdW(NX(), IC<2>()); SB(); nrm(IC<0>()); SB(); rdW(NX(), IC<3>()); SB(); }
        mm(IC<3>());
        if constexpr (more) { nrm(IC<1>()); SB(); }
        gm(IC<3>());
        ms(IC<4>());
        mm(IC<4>());
        if constexpr (more) { cvt(NX(), IC<0>()); SB(); }
        mm(IC<5>());
        gm(IC<4>());
        mm(IC<6>());
        if constexpr (more) { cvt(NX(), IC<1>()); SB(); }
        gm(IC<5>());
        mm(IC<7>());
      };
      asm volatile("s_waitcnt lgkmcnt(0)" ::: "memory");
      rdX(IC<0>(), IC<0>()); rdX(IC<0>(), IC<1>()); rdA(IC<0>()); rdC(IC<0>());
      unroll<0, 4>([&](auto jj) { rdW(IC<0>(), jj); });
      mkD();
      SB();
      asm volatile("s_nop 1");
      nrm(IC<0>()); nrm(IC<1>());
      asm volatile("s_nop 7\n\ts_nop 7");
      cvt(IC<0>(), IC<0>()); cvt(IC<0>(), IC<1>());
      SB();
#pragma unroll 1
      for (int t = 2; t < 16; t += 2) {
        const unsigned tk = stage0(t);
        const unsigned s1 = b1s + tk * (F * 2), sx = bxs + tk * (D * 2);
        unroll<0, 4>([&](auto kk) {
          step(kk, IC<12>(), IC<0>(), [&](auto jj) { mset(kk, jj); SB(); }, [&](auto jj) { dgrp(kk, jj, s1, sx); SB(); });
        });
        psc += 512; tie(psc); psa += 512; tie(psa);
        SB();
      }
      {
        unroll<0, 4>([&](auto kk) {
          constexpr int ks = kk.value;
          step(kk, IC<(ks == 0 ? 12 : ks == 1 ? 14 : ks == 2 ? 16 : 24)>(), IC<(ks == 3)>(),
               [&](auto jj) { if constexpr (jj.value == 0) { setm0n(area + (6 + ks) * 4096); SB(); } },
               [&](auto jj) {
                 constexpr int j = jj.value;
                 if constexpr (j < 4) dma<j * 1024>(rsW2, v2[j], so2 + ks * (32 * S * 2));
                 if constexpr (j >= 2 && 4 * ks + j - 2 < 14) ldwo(IC<4 * ks + j - 2>());
                 SB();
               });
        });
      }
      STAMP(4);
      bar();
      STAMP(5);
      {
        const rsrc_t st = mkst(ws + WS_UF, SZ_UF);
        const unsigned vo = (ft * 16 + w * 4 + h) * (D * 16) + (dt * 64 + n) * 16;
        f32x4 v[2][3][4];
        u32x4 res[4];
#pragma unroll
        for (int dh = 0; dh < 2; dh++)
#pragma unroll
          for (int g = 0; g < 4; g++)
#pragma unroll
            for (int a = 1; a < 4; a++) { v[dh][a - 1][g] = lld<f32x4>(lds + (w ^ a) * AREA + lane * 16 + (a - 1) * 8192 + dh * 4096 + g * 1024); SB(); }
#pragma unroll
        for (int dh = 0; dh < 2; dh++) {
#pragma unroll
          for (int g = 0; g < 4; g += 2) {
            float r[8];
#pragma unroll
            for (int e = 0; e < 8; e++) {
              const int gg = g + (e >> 2), c = e & 3;
              r[e] = gelu((acc[0][dh][4 * gg + c] + v[dh][0][gg][c]) + (v[dh][1][gg][c] + v[dh][2][gg][c]));
            }
            auto r0 = __builtin_amdgcn_permlane32_swap(pk(r[0], r[1]), pk(r[4], r[5]), false, false);
            auto r1 = __builtin_amdgcn_permlane32_swap(pk(r[2], r[3]), pk(r[6], r[7]), false, false);
            res[2 * dh + g / 2] = (u32x4){r0[0], r1[0], r0[1], r1[1]};
            stc(st, vo + g * (D * 16) + dh * 512, res[2 * dh + g / 2]);
            SB();
          }
        }
        STAMP(8);
        waitv<4>();
        tiewo();
        pre2rd();
        pre2w();
        hold(res[0], res[1], res[2], res[3], vo);
      }
      const unsigned vfg = tid == 0 ? (FL_U + dt * 14 + ft) * 128 : OOB;
      u32x2 epv = {ep.lo, ep.hi};
      tie(epv);
      STAMP(6);
      publish<0>(rsF, vfg, epv);
      STAMP(7);
      u32x2 f0 = ldc2(rsF, vfu);
      pre2(4, 10, 0);
      waitv<24>(f0);
      hold(vfg, epv);
      miss = missing<4>(f0, ep);
      td = 10;
    } else {
      if (xc < 7) {
#pragma unroll 1
        for (int m = 4; m < 9; m++) {
          if (m == 4) __builtin_amdgcn_s_sleep(24); else __builtin_amdgcn_s_sleep(10);
          touchl(rsW1, vt1, so_t1(m, 2 * (i - 14) + (w >> 1), 16 * (w & 1)));
        }
      }
      if (i >= 14) {
#pragma unroll 1
        for (int m = 0; m < 4; m++) touchl(mkrs(wo, D * D * 2), (lane >> 2) * (D * 2) + (lane & 3) * 128, (128 * m + 64 * (i - 14) + 16 * w) * (D * 2) + (xc & 1) * 384);
#pragma unroll 1
        for (int m = 0; m < 4; m++) touchl(rsW2, (lane >> 3) * (S * 2) + (lane & 7) * 128, (448 * m + 384 + 32 * (i - 14) + 8 * w) * (S * 2) + (xc >> 1) * 1024);
        const int head = 2 * xc + i - 14;
        const u32x4 rsP = mkrs(ws + WS_KVP, SZ_KVP);
        const unsigned so = head * 65536 + w * 1024, vf = lane < 16 ? (FL_P + xc * 16 + lane) * 128 : OOB, vp = lane * 16;
        bf16x8 pv[16];
        float a[8] = {};
        poll<16>(rsF, vf, ep);
        STAMP(3);
        unroll<0, 16>([&](auto tt) { pv[tt.value] = ldc<0>(rsP, vp, so + tt.value * 4096); });
        unroll<0, 4>([&](auto gg) {
          constexpr int t = 4 * gg.value;
          waitv<12 - t>(pv[t], pv[t + 1], pv[t + 2], pv[t + 3]);
          if constexpr (t == 0) hold(vp);
#pragma unroll
          for (int j = t; j < t + 4; j++) {
            const u32x4 u = __builtin_bit_cast(u32x4, pv[j]);
#pragma unroll
            for (int e = 0; e < 4; e++) { const unsigned v = u[e]; a[2 * e] += blo(v); a[2 * e + 1] += bhi(v); }
          }
          SB();
        });
        const rsrc_t st = mkst(ws + WS_KVF, SZ_KVF);
        const u32x4 r0 = {pk(a[0], a[1]), ep.lo, pk(a[2], a[3]), tagB}, r1 = {pk(a[4], a[5]), ep.lo, pk(a[6], a[7]), tagB};
        stc(st, head * 8192 + w * 2048 + lane * 16, r0);
        stc(st, head * 8192 + w * 2048 + 1024 + lane * 16, r1);
        hold(r0, r1);
        STAMP(4);
      } else {
        __builtin_amdgcn_s_sleep(127);
      }
      unroll<0, 14>(ldwo);
      pre2(0, 4, 6); pre2(4, 10, 0);
      waitv<24>();
      tiewo();
      pre2rd();
      pre2w();
      pre2(10, 14, 6);
    }
  } else {
    const unsigned sxa = sx0;
    dma4(rsX, vxa, sxa, ring);
    u32x2 gen = ldc2(rsF, vz);
    SB();
    unsigned sink = 0;
    const u32x4 rsW = mkrs(wqkv, D * 3 * D * 2);
    {
      const unsigned r6 = (lane * 43) >> 8, c6 = lane - 6 * r6;
      touch(sink, rsW, lane < 42 ? r6 * 2688 + 896 + (c6 & 1) * 896 + (c6 >> 1) * 108 : OOB, (28 * ja + 7 * w) * 2688 + (xc & 6) * 112);
    }
    dma4(rsX, vxa, sxa + 128, ring + 4096);
    dma4(rsX, vxa, sxa + 256, ring + 8192);
    unsigned vw[8];
#pragma unroll
    for (int s = 0; s < 8; s++) {
      const unsigned c = (s < 7 ? 256 * s + 64 * w : 3072) + lane, r = (c * 9363) >> 16;
      vw[s] = ((r & ~15u) + 8 * ((r >> 1) & 1) + 4 * (r & 1) + ((r >> 2) & 1) + 2 * ((r >> 3) & 1)) * 2688 + (c - 7 * r) * 16;
    }
    const unsigned mw = lds0 + w * 1024;
    auto mW = [&](auto ss, auto bb) {
      if constexpr (ss.value < 12) addm0<bb.value + ss.value * 4096>(mw); else addm0<bb.value + 49152>(lds0);
    };
    auto dmaW = [&](auto ss, unsigned col) {
      constexpr int s = ss.value;
      dma<0>(rsW, vw[s < 12 ? (s + 14) % 7 : 7], col + pr * 112 + (s >= 7 && s < 12 ? 256 * 2688 : 0));
    };
    bf16x8 xa[28];
    float sa = 0.f, sq = 0.f;
    {
      unsigned pr_[4];
#pragma unroll
      for (int k = 0; k < 4; k++) pr_[k] = ring + n * 128 + (((2 * k + h) ^ ((n >> 1) & 7)) * 16);
      unroll<0, 7>([&](auto cc) {
        constexpr int cb = cc.value;
        waitv<(cb == 0 ? 9 : 8)>();
#pragma unroll
        for (int k = 0; k < 4; k++) xa[4 * cb + k] = lld<bf16x8>((ldsp)(uintptr_t)pr_[k] + (cb % 3) * 4096);
        asm volatile("s_waitcnt lgkmcnt(0)" : "+v"(xa[4 * cb]), "+v"(xa[4 * cb + 1]), "+v"(xa[4 * cb + 2]), "+v"(xa[4 * cb + 3])::"memory");
        if constexpr (cb < 4) dma4(rsX, vxa, sxa + (cb + 3) * 128, ring + (cb % 3) * 4096);
        if constexpr (cb == 4) unroll<0, 4>([&](auto ss) { mW(ss, IC<L_WK>()); asm volatile("s_nop 0"); dmaW(ss, 896); });
        if constexpr (cb == 5) unroll<0, 4>([&](auto ss) { mW(ss, IC<L_WV>()); asm volatile("s_nop 0"); dmaW(ss, 1792); });
        if constexpr (cb == 6) {
          setm0(lds0 + L_WPHI + w * 1024);
          dma<0>(mkrs(wphi + pr * 1568, 3136), tid * 16, 0);
        }
        SB();
#pragma unroll
        for (int k = 0; k < 4; k++) acc_stats(xa[4 * cb + k], sa, sq);
        SB();
      });
    }
    STAMP(1);
    hold(vxa[0], vxa[1], vxa[2], vxa[3], vz);
    tie(gen);
    setep(gen);
    float la, lc;
    {
      float s2 = sa, q2 = sq;
      swap32(sa, s2); swap32(sq, q2);
      const float mu = (sa + s2) * (1.f / D), var = fmaxf((sq + q2) * (1.f / D) - mu * mu, 0.f);
      la = __builtin_amdgcn_rsqf(var + 1e-5f);
      lc = -mu * la;
    }
    auto norm = [&](auto kk, auto ee) {
      constexpr int ks = kk.value, e = ee.value;
      if constexpr (ks < 28) {
        u32x4 u = __builtin_bit_cast(u32x4, xa[ks]);
        u[e] = affine2(u[e], la, lc);
        xa[ks] = __builtin_bit_cast(bf16x8, u);
      }
    };
    unroll<0, 4>([&](auto ee) { norm(IC<0>(), ee); });
    norm(IC<1>(), IC<0>());
    tie(xa[0]); tie(xa[1]);
    waitv<1>();
    bar();
    STAMP(3);
    const unsigned vt2 = (lane >> 3) * (S * 2) + (lane & 7) * 128;
    const unsigned st2t = (448 * w + 8 * ja) * (S * 2) + (xc >> 1) * 1024;
    auto touch2 = [&](int m) { touch(sink, rsW2, vt2, st2t + 128 * m * (S * 2)); };
    const unsigned vtq = lane < 28 && (lane & 3) < 3 ? (lane >> 2) * 2688 + (lane & 3) * 108 : OOB;
    const unsigned vtx = xc < 7 && (lane & 7) < 7 ? (lane >> 3) * (D * 2) + (lane & 7) * 128 : OOB;
    const unsigned stx = (512 * ((2 * ja + (w >> 1)) & 3) + stage0(8 + (ja >> 1)) + 16 * (w & 1)) * (D * 2);
    const unsigned wlane = (2 * h + 4 * q) * 112 + 32 * blk + 8 * pp;
    f32x16 kT[2] = {}, vv[2] = {};
    {
      const ldsp pk_ = lds + L_WK + wlane, pv_ = lds + L_WV + wlane;
      s16x4 fr[30][8];
      auto rd = [&](auto kk, auto cc) {
        constexpr int ks = kk.value, c = cc.value;
        if constexpr (ks < 28) fr[ks][c] = vtr(((c & 4) ? pv_ : pk_) + ks * 1792 + ((c >> 1) & 1) * 56 + (c & 1) * 112);
      };
      unroll<0, 8>([&](auto cc) { rd(IC<0>(), cc); });
      unroll<0, 8>([&](auto cc) { rd(IC<1>(), cc); });
      SB();
      unroll<0, 28>([&](auto kk) {
        constexpr int ks = kk.value;
        if constexpr (ks == 7) { waitv<6>(); asm volatile("s_barrier" ::: "memory"); }
        if constexpr (ks == 16) { waitv<4>(); asm volatile("s_barrier" ::: "memory"); }
        __builtin_amdgcn_s_waitcnt(ks < 27 ? 0xc87f : 0xc07f);
        SB();
        unroll<0, 4>([&](auto mm) {
          constexpr int m = mm.value;
          const bf16x8 wv = cat(fr[ks][2 * m], fr[ks][2 * m + 1]);
          if constexpr (m < 2) kT[m] = MFMA(wv, xa[ks], kT[m]);
          else vv[m - 2] = MFMA(xa[ks], wv, vv[m - 2]);
          SB();
          if constexpr (ks < 9 && m == 0) mW(IC<4 + ks>(), IC<L_WK>());
          if constexpr (ks < 9 && m == 2) mW(IC<4 + ks>(), IC<L_WV>());
          rd(IC<ks + 2>(), IC<2 * m>());
          rd(IC<ks + 2>(), IC<2 * m + 1>());
          SB();
          if constexpr (ks < 9 && !(m & 1)) dmaW(IC<4 + ks>(), m ? 1792 : 896);
          if constexpr (ks == 9 && m == 0) touch(sink, rsW1, vt1, so_t1(8 + (ja >> 1), (2 * ja + (w >> 1)) & 3, 16 * (w & 1)));
          if constexpr (ks == 10 && m == 0) touch(sink, rsX, vtx, stx);
          if constexpr (ks == 11 && m == 0) touch(sink, rsX, vtx, stx + 8 * D * 2);
          if constexpr (ks == 14 && m == 0) touch(sink, rsW, vtq, (28 * ja + 7 * w) * 2688 + (xc & 6) * 112);
          if constexpr (ks == 16 && m == 0) hold(vtx);
          if constexpr (ks == 19 && m == 0) hold(vtq);
          if constexpr (ks == TA0 && m == 0) touch2(0);
          SB();
          norm(IC<ks + 1 + (m == 3)>(), IC<(m + 1) & 3>());
          SB();
        });
      });
    }
    bf16x8 wf[2][2];
    {
      const ldsp p = lds + L_WPHI + (4 * h + q) * 56 + 32 * blk + 8 * pp;
#pragma unroll
      for (int hd = 0; hd < 2; hd++) {
        wf[hd][0] = cat(vtr(p + hd * 1568), vtr(p + hd * 1568 + 8 * 56));
        s16x4 z = vtr(p + hd * 1568 + 24 * 56);
        wf[hd][1] = cat(vtr(p + hd * 1568 + 16 * 56), h ? (s16x4){0, 0, 0, 0} : z);
      }
    }
    STAMP(5);
    {
      f32x16 p[2];
#pragma unroll
      for (int hd = 0; hd < 2; hd++) { p[hd] = MFMA(wf[hd][0], pack8(kT[hd], 0), (f32x16)(0.f)); p[hd] = MFMA(wf[hd][1], pack8(kT[hd], 8), p[hd]); }
      bar();
      {
        const bf16x8 ones = {0x3f80, 0x3f80, 0x3f80, 0x3f80, 0x3f80, 0x3f80, 0x3f80, 0x3f80};
        const ldsp pvx = lds + L_VX + w * 2048 + lane * 16;
#pragma unroll
        for (int hd = 0; hd < 2; hd++)
#pragma unroll
          for (int j = 0; j < 2; j++) { bf16x8 f = pack8(vv[hd], 8 * j); lst(pvx + hd * 8192 + j * 1024, n == 28 ? ones : f); }
      }
      const unsigned pb = (32 * w + n) * 64 + ((h ^ ((n >> 1) & 7)) * 8);
      unroll<0, 2>([&](auto hh) {
        constexpr int hd = hh.value;
        unsigned ph[2][8];
        features(p[hd], h, ph, [&](auto kk, auto& pin) {
          constexpr int s = 12 * hd + kk.value;
          if constexpr (s < 13) {
            mW(IC<s>(), IC<L_WQ>());
            asm volatile("s_nop 0");
            dmap<0>(rsW, vw[s < 12 ? s % 7 : 7], pr * 112 + (s >= 7 && s < 12 ? 256 * 2688 : 0), pin);
          }
          if constexpr (s == TA1 || s == TA2) touchp(sink, rsW2, vt2, st2t + 128 * (s == TA1 ? 1 : 2) * (S * 2), pin);
        });
#pragma unroll
        for (int sg = 0; sg < 2; sg++)
#pragma unroll
          for (int g = 0; g < 4; g++) lst(lds + L_PHI + (hd * 2 + sg) * 8192 + (pb ^ (g << 4)), (u32x2){ph[sg][2 * g], ph[sg][2 * g + 1]});
      });
    }
    STAMP(6);
    hold(vw[0], vw[1], vw[2], vw[3], vw[4], vw[5], vw[6], vw[7], vt1, vt2);
    waitv<(TA2 < 24 ? 2 : 1)>();
    bar();
    u32x4 kvp[2];
    const unsigned vkv = (2 * pr + (w >> 1)) * 65536 + T * 4096 + (w & 1) * 2048 + lane * 16;
    {
      const unsigned sw = 2 * h + (q >> 1);
      const ldsp pl = lds + L_PHI + w * 8192 + (4 * h + q) * 64 + (((4 * blk + pp) ^ sw) * 8);
      const ldsp ph_ = lds + L_PHI + w * 8192 + (8 + 4 * h + q) * 64 + (((4 * blk + pp) ^ sw ^ 4) * 8);
      const ldsp pvx = lds + L_VX + (w >> 1) * 8192 + lane * 16;
      f32x16 kv = {};
#pragma unroll
      for (int jj = 0; jj < 8; jj++) kv = MFMA(cat(vtr(pl + jj * 1024), vtr(ph_ + jj * 1024)), lld<bf16x8>(pvx + jj * 1024), kv);
      const rsrc_t st = mkst(ws + WS_KVP, SZ_KVP);
      kvp[0] = __builtin_bit_cast(u32x4, pack8(kv, 0)); kvp[1] = __builtin_bit_cast(u32x4, pack8(kv, 8));
      stc(st, vkv, kvp[0]);
      stc(st, vkv + 1024, kvp[1]);
    }
    STAMP(7);
    const unsigned vfp = tid == 0 ? (FL_P + pr * 16 + T) * 128 : OOB;
    u32x2 epv = {ep.lo, ep.hi};
    tie(epv);
    f32x16 qT[2] = {};
    {
      const ldsp pq_ = lds + L_WQ + wlane;
      s16x4 fr[29][4];
      auto rd = [&](auto kk, auto cc) {
        constexpr int ks = kk.value, c = cc.value;
        if constexpr (ks < 28) fr[ks][c] = vtr(pq_ + ks * 1792 + (c >> 1) * 56 + (c & 1) * 112);
      };
      unroll<0, 4>([&](auto cc) { rd(std::integral_constant<int, 0>(), cc); });
      unroll<0, 28>([&](auto kk) {
        constexpr int ks = kk.value;
        if constexpr (ks == QP) { hold(kvp[0], kvp[1], vkv); publish<0>(rsF, vfp, epv); STAMP(8); SB(); }
        if constexpr (ks == QP + 2 && TA2 >= 24) { touch2(2); SB(); }
        if constexpr (ks == QP + 6) hold(vfp, vt2);
        unroll<0, 2>([&](auto mm) {
          constexpr int m = mm.value;
          rd(std::integral_constant<int, ks + 1>(), std::integral_constant<int, 2 * m>());
          rd(std::integral_constant<int, ks + 1>(), std::integral_constant<int, 2 * m + 1>());
          qT[m] = MFMA(cat(fr[ks][2 * m], fr[ks][2 * m + 1]), xa[ks], qT[m]);
          SB();
        });
      });
    }
    unsigned phq[2][2][8];
    {
      f32x16 p[2];
#pragma unroll
      for (int hd = 0; hd < 2; hd++) { p[hd] = MFMA(wf[hd][0], pack8(qT[hd], 0), (f32x16)(0.f)); p[hd] = MFMA(wf[hd][1], pack8(qT[hd], 8), p[hd]); }
      bar();
      STAMP(9);
      unroll<0, 2>([&](auto hh) {
        features(p[hh.value], h, phq[hh.value], [&](auto kk, auto& pin) {
          constexpr int k = 12 * hh.value + kk.value, t = k >> 2;
          if constexpr ((k & 3) == 0) setm0(area + ((t + 6) % 10) * 4096);
          dmap<(k & 3) * 1024>(rsW2, v2[k & 3], so2 + t * (32 * S * 2), pin);
        });
      });
    }
    u32x4 fq[2][4];
#pragma unroll
    for (int c = 0; c < 8; c++) {
      const unsigned* s = &phq[c >> 2][(c >> 1) & 1][4 * (c & 1)];
      fq[c >> 2][c & 3] = (u32x4){s[0], s[1], s[2], s[3]};
      tie(fq[c >> 2][c & 3]);
    }
    STAMP(10);
    {
      const u32x4 rsK = mkrs(ws + WS_KVF, SZ_KVF);
      const unsigned so = pr * 16384 + w * 2048, tagB = ep.lo ^ ep.hi, vk = lane * 16;
      const ldsp px = lds + 9 * 4096 + lane * 16;
      auto fetch = [&]() {
        bf16x8 g0 = ldc<0>(rsK, vk, so), g1 = ldc<1024>(rsK, vk, so), g2 = ldc<0>(rsK, vk, so + 8192), g3 = ldc<1024>(rsK, vk, so + 8192);
        waitv<0>(g0, g1, g2, g3);
        const u32x4 u0 = __builtin_bit_cast(u32x4, g0), u1 = __builtin_bit_cast(u32x4, g1), u2 = __builtin_bit_cast(u32x4, g2), u3 = __builtin_bit_cast(u32x4, g3);
        lst(px + w * AREA, (u32x4){u0[0], u0[2], u1[0], u1[2]});
        lst(px + w * AREA + 1024, (u32x4){u2[0], u2[2], u3[0], u3[2]});
        const unsigned bad = ((u0[1] ^ ep.lo) | (u1[1] ^ ep.lo) | (u2[1] ^ ep.lo) | (u3[1] ^ ep.lo)) | ((u0[3] ^ tagB) | (u1[3] ^ tagB) | (u2[3] ^ tagB) | (u3[3] ^ tagB));
        return (bool)__all(bad == 0);
      };
      waitv<8>();
      pre2rd();
      pre2(6, 9, 2);
      unroll<0, 14>(ldwo);
#pragma unroll 1
      for (int it = 0; it < SPIN; ++it)
        if (fetch()) break;
      STAMP(2);
      tie(sink);
      tiewo();
      pre2w();
      bar();
      STAMP(11);
      bf16x8 kf[2][4];
#pragma unroll
      for (int hd = 0; hd < 2; hd++)
#pragma unroll
        for (int c = 0; c < 4; c++) kf[hd][c] = lld<bf16x8>(px + c * AREA + hd * 1024);
      SB();
      dma4(rsW2, v2, so2 + 9 * (32 * S * 2), area + 5 * 4096);
      dma4(rsW2, v2, so2 + 10 * (32 * S * 2), area + 6 * 4096);
      setm0(area + 7 * 4096);
      SB();
      f32x16 o[2];
      unroll<0, 8>([&](auto mm) {
        constexpr int m = mm.value, hd = m & 1, c = m >> 1;
        if constexpr (c == 0) mfma0(o[hd], kf[hd][c], fq[hd][c]); else mfmac(o[hd], kf[hd][c], fq[hd][c]);
        dma<(m & 3) * 1024>(rsW2, v2[m & 3], so2 + (11 + (m >> 2)) * (32 * S * 2));
        if constexpr (m == 3) setm0n(area + 8 * 4096);
        SB();
      });
      asm volatile("s_nop 11" : "+v"(o[0]), "+v"(o[1]));
      float da = o[0][12], db = o[1][12];
      swap32(da, db);
      float ia = __builtin_amdgcn_rcpf(db + 1e-6f), ib = ia;
      swap32(ia, ib);
      unsigned a[8], bq[8];
#pragma unroll
      for (int r = 0; r < 8; r++) { a[r] = pk(o[0][2 * r] * ia, o[0][2 * r + 1] * ia); bq[r] = pk(o[1][2 * r] * ib, o[1][2 * r + 1] * ib); }
      const rsrc_t st = mkst(ws + WS_OF, SZ_OF);
      const unsigned tk = (128 * T + 32 * w + n) * 16, jb = 14 * (pr >> 1);
      const unsigned vo = (jb + 6 * (pr & 1) + h) * (S * 16) + tk;
      const unsigned v1 = vo + 2 * S * 16, v2_ = vo + 4 * S * 16, v3 = h ? OOB : (jb + 12 + (pr & 1)) * (S * 16) + tk;
      const u32x4 r0 = {a[0], a[1], a[2], a[3]}, r1 = {a[4], a[5], bq[0], bq[1]}, r2 = {bq[2], bq[3], bq[4], bq[5]}, r3 = {a[6], a[7], bq[6], bq[7]};
      stc(st, vo, r0); stc(st, v1, r1); stc(st, v2_, r2); stc(st, v3, r3);
      STAMP(12);
      u32x2 f0 = ldc2(rsF, vfu);
      const unsigned vfo = tid == 0 ? (FL_O + T * 8 + pr) * 128 : OOB;
      publish<0>(rsF, vfo, epv);
      tie(f0);
      hold(vo, v1, v2_, v3, r0, r1, r2, r3);
      miss = missing<4>(f0, ep);
      td = 13;
      hold(vfo, epv);
    }
  }

  if (i >= 4) {
    const unsigned orow = st2 * 64 + 16 * w + (lane >> 3);
    const unsigned vo = orow * (D * 2) + dt2 * 128 + (lane & 7) * 16;
    const u32x4 rsB = mkrs(b2, S * 2), rsU = mkrs(ws + WS_UF, SZ_UF), rsO = mkrs(ws + WS_OF, SZ_OF);
#pragma unroll 1
    for (; miss && td < 13; td += 2) {
      u32x2 f = ldc2(rsF, vfu);
      dma4(rsW2, v2, so2 + td * (32 * S * 2), area + (td - 4) * 4096);
      dma4(rsW2, v2, so2 + (td + 1) * (32 * S * 2), area + (td - 3) * 4096);
      waitv<8>(f);
      miss = missing<4>(f, ep);
    }
    if (miss) poll<4>(rsF, vfu, ep);
    STAMP(13);
    const unsigned vu0 = h * (D * 16) + (dt2 * 64 + n) * 16, vu1 = vu0 + 2 * D * 16, sou = w * 56 * (D * 16);
    const unsigned vof = h * (S * 16) + (st2 * 64 + n) * 16, soo = w * 14 * (S * 16);
    const unsigned vfl = lane < 2 ? (FL_O + (st2 >> 1) * 8 + 2 * w + lane) * 128 : OOB;
    bf16x8 fa[18][4], xr0, xr1;
    unsigned bs0, bs1;
    const unsigned vb2 = orow * 2;
    u32x2 fl;
    auto ldD = [&](auto tt, auto jj) {
      constexpr int t = tt.value, j = jj.value;
      if constexpr (t < 14) fa[t][j] = ldc<(j & 1) * 512>(rsU, (j >> 1) ? vu1 : vu0, sou + t * (4 * D * 16));
      else if constexpr (j < g2ld(t)) fa[t][j] = ldc<(j & 1) * 512>(rsO, vof, soo + (2 * (t - 14) + (j >> 1)) * (2 * S * 16));
    };
    unroll<0, G2R>([&](auto tt) { unroll<0, 4>([&](auto jj) { ldD(tt, jj); }); });
    dman(rsW2, v2, so2 + td * (32 * S * 2), 32 * S * 2, area + (td - 4) * 4096, 14 - td);
    const ldsp pw[2] = {pb[0] - h * 512, pb[1] - h * 512};
    f32x16 acc[2][2] = {};
    bf16x8 wb[19][2][2];
    auto rdB = [&](auto tt, auto kk, auto aa) {
      constexpr int t = tt.value, ks = kk.value, a = aa.value, j = 2 * (t - 14) + ks;
      if constexpr (t < 4) wb[t][ks][a] = w2r[t][ks][a];
      else if constexpr (t < 14) wb[t][ks][a] = cat(vtr(pb[a] + (t - 4) * 4096 + ks * 2048), vtr(pb[a] + (t - 4) * 4096 + ks * 2048 + 512));
      else if constexpr (j < 6) {
        constexpr int u = j % 3, rl = 56 * (j / 3) + (u == 0 ? 0 : u == 1 ? 16 : 36), rh = 56 * (j / 3) + (u == 0 ? 8 : u == 1 ? 28 : 44);
        wb[t][ks][a] = cat(vtr(pw[a] + rl * 128), vtr(pw[a] + rh * 128));
      } else if constexpr (j == 6) wb[t][ks][a] = cat(vtr(pw[a] + h * (52 * 128) + 24 * 128), vtr(pw[a] + h * (52 * 128) + 52 * 128));
    };
    unroll<0, 4>([&](auto cc) { rdB(IC<0>(), IC<(cc.value >> 1)>(), IC<(cc.value & 1)>()); });
    unroll<0, 18>([&](auto tt) {
      constexpr int t = tt.value, nk = t < 17 ? 2 : 1;
      constexpr int left = g2cnt(t - 3) + g2cnt(t - 2) + g2cnt(t - 1);
      if constexpr (nk == 2) waitv<left>(fa[t][0], fa[t][1], fa[t][2], fa[t][3]); else waitv<left>(fa[t][0], fa[t][1]);
      if constexpr (t == 3) hold(vfu);
      if constexpr (t == G2POLL + G2R) {
        tie(fl);
        if (missing<2>(fl, ep)) poll<2>(rsF, vfl, ep);
      }
      SB();
      unroll<0, 4 * nk>([&](auto mm) {
        constexpr int m = mm.value, ks = m >> 2, nh = (m >> 1) & 1, mh = m & 1;
        if constexpr (t < 14) acc[nh][mh] = MFMA(fa[t][ks * 2 + mh], wb[t][ks][nh], acc[nh][mh]);
        else acc[nh][mh] = MFMA(wb[t][ks][mh], fa[t][ks * 2 + nh], acc[nh][mh]);
        SB();
        if constexpr (m & 1) rdB(IC<t + 1>(), IC<(m >> 2)>(), IC<((m >> 1) & 1)>());
        else if constexpr (t >= 5 && 4 * (t - 5) + m / 2 < 14) sta<(4 * (t - 5) + m / 2) * 1024>(area + lane * 16, wor[4 * (t - 5) + m / 2]);
        SB();
        if constexpr (!(m & 1)) ldD(IC<t + G2R>(), IC<m / 2>());
        if constexpr (t == G2POLL && m == 7) fl = ldc2(rsF, vfl);
        if constexpr (t == 14 && m == 0) xr0 = ldp<0>(rsX, vo, 0);
        if constexpr (t == 14 && m == 2) xr1 = ldp<0>(rsX, vo, 8 * D * 2);
        if constexpr (t == 14 && m == 4) bs0 = ldu16<0>(rsB, vb2, 0);
        if constexpr (t == 14 && m == 6) bs1 = ldu16<16>(rsB, vb2, 0);
        SB();
      });
    });
    STAMP(14);
#pragma unroll
    for (int nh = 0; nh < 2; nh++)
#pragma unroll
      for (int mh = 0; mh < 2; mh++)
#pragma unroll
        for (int g = 0; g < 4; g++)
          lst(lds + w * AREA + (32 * nh + n) * 256 + (((8 * mh + 2 * g + h) ^ (n & 15)) * 16),
              (f32x4){acc[nh][mh][4 * g], acc[nh][mh][4 * g + 1], acc[nh][mh][4 * g + 2], acc[nh][mh][4 * g + 3]});
    barv();
    tie(xr0); tie(xr1); tie(bs0); tie(bs1);
    hold(vb2);
    u32x4 res[2];
    const rsrc_t sto = mkst(out, S * D * 2);
#pragma unroll
    for (int u = 0; u < 2; u++) {
      const int r = 16 * w + 8 * u + (lane >> 3);
      const unsigned c0 = r * 256 + (((2 * (lane & 7)) ^ (r & 15)) * 16), c1 = c0 ^ 16;
      f32x4 a = lld<f32x4>(lds + c0), bq = lld<f32x4>(lds + c1);
#pragma unroll
      for (int s = 1; s < 4; s++) { a += lld<f32x4>(lds + s * AREA + c0); bq += lld<f32x4>(lds + s * AREA + c1); }
      const u32x4 xu = __builtin_bit_cast(u32x4, u ? xr1 : xr0);
      const float bv = blo(u ? bs1 : bs0);
      res[u] = (u32x4){pk(a[0] + bv + blo(xu[0]), a[1] + bv + bhi(xu[0])), pk(a[2] + bv + blo(xu[1]), a[3] + bv + bhi(xu[1])),
                   pk(bq[0] + bv + blo(xu[2]), bq[1] + bv + bhi(xu[2])), pk(bq[2] + bv + blo(xu[3]), bq[3] + bv + bhi(xu[3]))};
      __builtin_amdgcn_raw_buffer_store_b128(res[u], sto, vo + u * (8 * D * 2), 0, 16);
    }
    hold(res[0], res[1]);
    if (blockIdx.x == 255 && tid == 0) __builtin_amdgcn_raw_buffer_store_b64((u32x2){ep.lo, ep.hi}, mkst(ws, WS_FLAG_BYTES), 0, 0, 16);
  }
}

extern "C" void kernel_launch(void* const* d_in, const int* in_sizes, int n_in, void* d_out, int out_size, void* d_ws, size_t ws_size,
                              hipStream_t stream) {
  fused_kernel<<<256, 256, 0, stream>>>((const short*)d_in[0], (const short*)d_in[1], (const short*)d_in[2], (const short*)d_in[3],
                                        (const short*)d_in[5], (const short*)d_in[6], (const short*)d_in[7], (const short*)d_in[8],
                                        (short*)d_out, (char*)d_ws);
}
```

```cpp
#include <hip/hip_runtime.h>
#include <stdint.h>

typedef short bf16x8 __attribute__((ext_vector_type(8)));
typedef short s16x4 __attribute__((ext_vector_type(4)));
typedef float f32x2 __attribute__((ext_vector_type(2)));
typedef float f32x4 __attribute__((ext_vector_type(4)));
typedef float f32x16 __attribute__((ext_vector_type(16)));
typedef unsigned u32x2 __attribute__((ext_vector_type(2)));
typedef unsigned u32x4 __attribute__((ext_vector_type(4)));
typedef unsigned u32x8 __attribute__((ext_vector_type(8)));
typedef __bf16 b16x2 __attribute__((ext_vector_type(2)));
typedef __attribute__((address_space(3))) char* ldsp;
typedef __amdgpu_buffer_rsrc_t rsrc_t;

#define DEV __device__ __forceinline__
#define SB() __builtin_amdgcn_sched_barrier(0)
#define MFMA(a, b, c) __builtin_amdgcn_mfma_f32_32x32x16_bf16(a, b, c, 0, 0, 0)

constexpr int S = 2048, D = 448, F = 1792;
constexpr unsigned FL_P = 8, FL_O = FL_P + 128, FL_U = FL_O + 128;
constexpr unsigned WS_FLAG_BYTES = 65536;
constexpr unsigned WS_ST = WS_FLAG_BYTES, SZ_ST = S * 16;
constexpr unsigned WS_UF = WS_ST + SZ_ST, SZ_UF = 224 * 448 * 16;
constexpr unsigned WS_OF = WS_UF + SZ_UF, SZ_OF = 56 * 2048 * 16;
constexpr unsigned WS_KVP = WS_OF + SZ_OF, SZ_KVP = 16 * 16 * 4096;
constexpr unsigned WS_KVF = WS_KVP + SZ_KVP, SZ_KVF = 16 * 8 * 1024;
constexpr unsigned WS_END = WS_KVF + SZ_KVF;
constexpr unsigned AREA = 40960, L_STAT = 36864, SLOT = 12288;
constexpr unsigned L_WK = 0, L_WV = 50176, L_WQ = 100352, L_WPHI = 150528, L_PHI = 0, L_VX = 32768;
constexpr unsigned OOB = 0x40000000u;
constexpr int SPIN = 1 << 18;

DEV u32x4 mkrs(const void* p, unsigned bytes) {
  unsigned long long a = (unsigned long long)p;
  u32x4 r = {(unsigned)a, (unsigned)(a >> 32), bytes, 0x00020000u};
  return r;
}
DEV rsrc_t mkst(void* p, unsigned bytes) { return __builtin_amdgcn_make_buffer_rsrc(p, 0, bytes, 0x00020000); }
DEV void stc(rsrc_t rs, unsigned voff, u32x4 v) { __builtin_amdgcn_raw_buffer_store_b128(v, rs, voff, 0, 16); }
template <int OFF> DEV bf16x8 ldp(u32x4 rs, unsigned voff, unsigned soff) {
  bf16x8 r;
  asm volatile("buffer_load_dwordx4 %0, %2, %1, %3 offen offset:%4" : "=v"(r) : "s"(rs), "v"(voff), "s"(soff), "n"(OFF) : "memory");
  return r;
}
template <int OFF> DEV bf16x8 ldc(u32x4 rs, unsigned voff, unsigned soff) {
  bf16x8 r;
  asm volatile("buffer_load_dwordx4 %0, %2, %1, %3 offen offset:%4 sc1" : "=v"(r) : "s"(rs), "v"(voff), "s"(soff), "n"(OFF) : "memory");
  return r;
}
template <int OFF> DEV u32x2 ldp2(u32x4 rs, unsigned voff, unsigned soff) {
  u32x2 r;
  asm volatile("buffer_load_dwordx2 %0, %2, %1, %3 offen offset:%4" : "=v"(r) : "s"(rs), "v"(voff), "s"(soff), "n"(OFF) : "memory");
  return r;
}
template <int OFF> DEV u32x4 lda(u32x4 rs, unsigned voff, unsigned soff) {
  u32x4 r;
  asm volatile("buffer_load_dwordx4 %0, %2, %1, %3 offen offset:%4" : "=a"(r) : "s"(rs), "v"(voff), "s"(soff), "n"(OFF) : "memory");
  return r;
}
template <int OFF> DEV void sta(unsigned addr, u32x4 v) { asm volatile("ds_write_b128 %0, %1 offset:%2" ::"v"(addr), "a"(v), "n"(OFF) : "memory"); }
DEV u32x2 ldc2(u32x4 rs, unsigned voff) {
  u32x2 r;
  asm volatile("buffer_load_dwordx2 %0, %2, %1, 0 offen sc1" : "=v"(r) : "s"(rs), "v"(voff) : "memory");
  return r;
}
template <int OFF> DEV unsigned ldu16(u32x4 rs, unsigned voff, unsigned soff) {
  unsigned r;
  asm volatile("buffer_load_ushort %0, %2, %1, %3 offen offset:%4" : "=v"(r) : "s"(rs), "v"(voff), "s"(soff), "n"(OFF) : "memory");
  return r;
}
DEV void setm0(unsigned a) { asm volatile("s_mov_b32 m0, %0\n\ts_nop 0" ::"s"(a) : "memory"); }
DEV void setm0n(unsigned a) { asm volatile("s_mov_b32 m0, %0" ::"s"(a) : "memory"); }
template <int OFF> DEV void addm0(unsigned a) { asm volatile("s_add_u32 m0, %0, %1" ::"s"(a), "n"(OFF) : "memory", "scc"); }
template <int OFF> DEV void dma(u32x4 rs, unsigned voff, unsigned soff) {
  asm volatile("buffer_load_dwordx4 %1, %0, %2 offen offset:%3 lds" ::"s"(rs), "v"(voff), "s"(soff), "n"(OFF) : "memory");
}
DEV void dma4(u32x4 rs, const unsigned (&v)[4], unsigned soff, unsigned m0) {
  setm0(m0); dma<0>(rs, v[0], soff); dma<1024>(rs, v[1], soff); dma<2048>(rs, v[2], soff); dma<3072>(rs, v[3], soff);
}
template <int N> DEV void waitv() { asm volatile("s_waitcnt vmcnt(%0)" ::"n"(N) : "memory"); }
template <int N, class T> DEV void waitv(T& a) { asm volatile("s_waitcnt vmcnt(%1)" : "+v"(a) : "n"(N) : "memory"); }
template <int N, class T> DEV void waitv(T& a, T& b) { asm volatile("s_waitcnt vmcnt(%2)" : "+v"(a), "+v"(b) : "n"(N) : "memory"); }
template <int N, class T> DEV void waitv(T& a, T& b, T& c, T& d) {
  asm volatile("s_waitcnt vmcnt(%4)" : "+v"(a), "+v"(b), "+v"(c), "+v"(d) : "n"(N) : "memory");
}
template <class T> DEV void tie(T& a) { asm volatile("" : "+v"(a)::"memory"); }
DEV void tiea(u32x4& a) { asm volatile("" : "+a"(a)::"memory"); }
template <class T> DEV void hold(T a) { asm volatile("" ::"v"(a)); }
template <class T, class... R> DEV void hold(T a, R... r) { hold(a); hold(r...); }
DEV void keep(f32x16 a) { asm volatile("" ::"v"(a)); }
DEV void bar() { asm volatile("s_waitcnt lgkmcnt(0)\n\ts_barrier" ::: "memory"); }
DEV void barv() { asm volatile("s_waitcnt vmcnt(0) lgkmcnt(0)\n\ts_barrier" ::: "memory"); }
DEV s16x4 vtr(ldsp p) { return __builtin_amdgcn_ds_read_tr16_b64_v4i16((__attribute__((address_space(3))) s16x4*)p); }
DEV bf16x8 cat(s16x4 a, s16x4 b) { return __builtin_shufflevector(a, b, 0, 1, 2, 3, 4, 5, 6, 7); }
DEV unsigned pk(float a, float b) {
  f32x2 v = {a, b};
  return __builtin_bit_cast(unsigned, __builtin_convertvector(v, b16x2));
}
DEV float blo(unsigned u) { return __builtin_bit_cast(float, u << 16); }
DEV float bhi(unsigned u) { return __builtin_bit_cast(float, u & 0xffff0000u); }
DEV void swap32(float& a, float& b) {
  auto r = __builtin_amdgcn_permlane32_swap(__builtin_bit_cast(unsigned, a), __builtin_bit_cast(unsigned, b), false, false);
  const unsigned r0 = r[0], r1 = r[1];
  a = __builtin_bit_cast(float, r0); b = __builtin_bit_cast(float, r1);
}
DEV void swap16(float& a, float& b) {
  auto r = __builtin_amdgcn_permlane16_swap(__builtin_bit_cast(unsigned, a), __builtin_bit_cast(unsigned, b), false, false);
  const unsigned r0 = r[0], r1 = r[1];
  a = __builtin_bit_cast(float, r0); b = __builtin_bit_cast(float, r1);
}
template <int C> DEV float ror(float z) {
  return __builtin_bit_cast(float, __builtin_amdgcn_update_dpp(0, __builtin_bit_cast(int, z), C, 0xf, 0xf, true));
}
DEV float rowsum(float z) { z += ror<0x128>(z); z += ror<0x124>(z); z += ror<0x122>(z); z += ror<0x121>(z); return z; }
DEV float rdl(float z, int l) { return __builtin_bit_cast(float, __builtin_amdgcn_readlane(__builtin_bit_cast(int, z), l)); }
template <class T> DEV void lst(ldsp p, T v) { *(__attribute__((address_space(3))) T*)p = v; }
template <class T> DEV T lld(ldsp p) { return *(__attribute__((address_space(3))) T*)p; }

struct Epoch { unsigned lo, hi; };
template <int N> DEV void publish(u32x4 rsF, unsigned vflag, u32x2 e) {
  asm volatile("s_waitcnt vmcnt(%3) lgkmcnt(0)\n\ts_barrier\n\tbuffer_store_dwordx2 %2, %1, %0, 0 offen sc1" ::"s"(rsF), "v"(vflag), "v"(e), "n"(N) : "memory");
}
template <int N> DEV unsigned missing(u32x2 f, Epoch e) {
  unsigned r;
  asm volatile("v_cmp_ne_u64 vcc, %2, %1\n\ts_and_b32 %0, vcc_lo, %3" : "=s"(r) : "v"(f), "s"(((unsigned long long)e.hi << 32) | e.lo), "n"((1u << N) - 1) : "vcc", "scc");
  return r;
}
template <int N> DEV void poll(u32x4 rsF, unsigned voff, Epoch e) {
#pragma unroll 1
  for (int it = 0; it < SPIN; ++it) {
    u32x2 f = ldc2(rsF, voff);
    waitv<0>(f);
    if (!missing<N>(f, e)) break;
    __builtin_amdgcn_s_sleep(1);
  }
}
DEV void collect(bf16x8 (&p)[16], u32x4 rsF, unsigned vf, u32x4 rsP, unsigned vp, unsigned so, Epoch e) {
  unsigned done = 0, it = SPIN, nw, t;
  u32x2 f;
  asm volatile("1:\n\tbuffer_load_dwordx2 %[f], %[vf], %[rsF], 0 offen sc1\n\ts_waitcnt vmcnt(0)\n\tv_cmp_eq_u64 vcc, %[e], %[f]\n\t"
               "s_lshr_b32 %[nw], vcc_lo, 1\n\ts_and_b32 %[nw], %[nw], vcc_lo\n\ts_and_b32 %[nw], %[nw], 0x5555\n\t"
               "s_andn2_b32 %[nw], %[nw], %[done]\n\ts_or_b32 %[done], %[done], %[nw]\n\t"
               "s_bitcmp1_b32 %[nw], 0\n\ts_cbranch_scc0 10f\n\ts_add_u32 %[t], %[so], 0\n\t"
               "buffer_load_dwordx4 %0, %[vp], %[rsP], %[t] offen sc1\n\tbuffer_load_dwordx4 %1, %[vp], %[rsP], %[t] offen offset:512 sc1\n10:\n\t"
               "s_bitcmp1_b32 %[nw], 2\n\ts_cbranch_scc0 11f\n\ts_add_u32 %[t], %[so], 8192\n\t"
               "buffer_load_dwordx4 %2, %[vp], %[rsP], %[t] offen sc1\n\tbuffer_load_dwordx4 %3, %[vp], %[rsP], %[t] offen offset:512 sc1\n11:\n\t"
               "s_bitcmp1_b32 %[nw], 4\n\ts_cbranch_scc0 12f\n\ts_add_u32 %[t], %[so], 16384\n\t"
               "buffer_load_dwordx4 %4, %[vp], %[rsP], %[t] offen sc1\n\tbuffer_load_dwordx4 %5, %[vp], %[rsP], %[t] offen offset:512 sc1\n12:\n\t"
               "s_bitcmp1_b32 %[nw], 6\n\ts_cbranch_scc0 13f\n\ts_add_u32 %[t], %[so], 24576\n\t"
               "buffer_load_dwordx4 %6, %[vp], %[rsP], %[t] offen sc1\n\tbuffer_load_dwordx4 %7, %[vp], %[rsP], %[t] offen offset:512 sc1\n13:\n\t"
               "s_bitcmp1_b32 %[nw], 8\n\ts_cbranch_scc0 14f\n\ts_add_u32 %[t], %[so], 32768\n\t"
               "buffer_load_dwordx4 %8, %[vp], %[rsP], %[t] offen sc1\n\tbuffer_load_dwordx4 %9, %[vp], %[rsP], %[t] offen offset:512 sc1\n14:\n\t"
               "s_bitcmp1_b32 %[nw], 10\n\ts_cbranch_scc0 15f\n\ts_add_u32 %[t], %[so], 40960\n\t"
               "buffer_load_dwordx4 %10, %[vp], %[rsP], %[t] offen sc1\n\tbuffer_load_dwordx4 %11, %[vp], %[rsP], %[t] offen offset:512 sc1\n15:\n\t"
               "s_bitcmp1_b32 %[nw], 12\n\ts_cbranch_scc0 16f\n\ts_add_u32 %[t], %[so], 49152\n\t"
               "buffer_load_dwordx4 %12, %[vp], %[rsP], %[t] offen sc1\n\tbuffer_load_dwordx4 %13, %[vp], %[rsP], %[t] offen offset:512 sc1\n16:\n\t"
               "s_bitcmp1_b32 %[nw], 14\n\ts_cbranch_scc0 17f\n\ts_add_u32 %[t], %[so], 57344\n\t"
               "buffer_load_dwordx4 %14, %[vp], %[rsP], %[t] offen sc1\n\tbuffer_load_dwordx4 %15, %[vp], %[rsP], %[t] offen offset:512 sc1\n17:\n\t"
               "s_sub_u32 %[it], %[it], 1\n\ts_cbranch_scc1 2f\n\ts_cmp_lg_u32 %[done], 0x5555\n\ts_cbranch_scc1 1b\n2:\n\ts_waitcnt vmcnt(0)"
               : "=&v"(p[0]), "=&v"(p[1]), "=&v"(p[2]), "=&v"(p[3]), "=&v"(p[4]), "=&v"(p[5]), "=&v"(p[6]), "=&v"(p[7]), "=&v"(p[8]), "=&v"(p[9]), "=&v"(p[10]), "=&v"(p[11]), "=&v"(p[12]), "=&v"(p[13]), "=&v"(p[14]), "=&v"(p[15]),
                 [f] "=&v"(f), [done] "+&s"(done), [it] "+&s"(it), [nw] "=&s"(nw), [t] "=&s"(t)
               : [vf] "v"(vf), [rsF] "s"(rsF), [vp] "v"(vp), [rsP] "s"(rsP), [so] "s"(so), [e] "s"(((unsigned long long)e.hi << 32) | e.lo)
               : "memory", "scc", "vcc");
}
DEV void dman(u32x4 rs, const unsigned (&v)[4], unsigned soff, unsigned step, unsigned m0, unsigned n) {
  asm volatile("s_cmp_eq_u32 %1, 0\n\ts_cbranch_scc1 2f\n\ts_mov_b32 m0, %8\n1:\n\ts_sub_u32 %1, %1, 1\n\t"
               "buffer_load_dwordx4 %3, %2, %0 offen lds\n\tbuffer_load_dwordx4 %4, %2, %0 offen offset:1024 lds\n\t"
               "buffer_load_dwordx4 %5, %2, %0 offen offset:2048 lds\n\tbuffer_load_dwordx4 %6, %2, %0 offen offset:3072 lds\n\t"
               "s_add_u32 m0, m0, 0x1000\n\ts_add_u32 %0, %0, %7\n\ts_cmp_lg_u32 %1, 0\n\ts_cbranch_scc1 1b\n2:"
               : "+&s"(soff), "+&s"(n)
               : "s"(rs), "v"(v[0]), "v"(v[1]), "v"(v[2]), "v"(v[3]), "s"(step), "s"(m0)
               : "memory", "scc");
}

DEV void acc_stats(bf16x8 v, float& s, float& q) {
  const u32x4 u = __builtin_bit_cast(u32x4, v);
#pragma unroll
  for (int e = 0; e < 4; e++) {
    const unsigned t = u[e];
    const b16x2 p = __builtin_bit_cast(b16x2, t);
    s = __builtin_amdgcn_fdot2_f32_bf16(p, __builtin_bit_cast(b16x2, 0x3f803f80u), s, false);
    q = __builtin_amdgcn_fdot2_f32_bf16(p, p, q, false);
  }
}
DEV unsigned affine2(unsigned u, float a, float c) {
  float t0, t1;
  unsigned r;
  asm("v_lshlrev_b32 %1, 16, %3\n\tv_and_b32 %2, 0xffff0000, %3\n\tv_fma_f32 %1, %1, %4, %5\n\tv_fma_f32 %2, %2, %4, %5\n\tv_cvt_pk_bf16_f32 %0, %1, %2"
      : "=v"(r), "=&v"(t0), "=&v"(t1)
      : "v"(u), "v"(a), "v"(c));
  return r;
}
DEV f32x16 mfmav(u32x4 a, bf16x8 b, f32x16 c) {
  f32x16 r;
  asm volatile("v_mfma_f32_32x32x16_bf16 %0, %1, %2, %3" : "=&v"(r) : "v"(a), "v"(b), "v"(c));
  return r;
}
DEV void mfmac(f32x16& c, bf16x8 a, u32x4 b) { asm volatile("v_mfma_f32_32x32x16_bf16 %0, %1, %2, %0" : "+v"(c) : "v"(a), "v"(b)); }
DEV void mfma0(f32x16& c, bf16x8 a, u32x4 b) { asm volatile("v_mfma_f32_32x32x16_bf16 %0, %1, %2, 0" : "=&v"(c) : "v"(a), "v"(b)); }
DEV void touch(unsigned& sink, u32x4 rs, unsigned voff, unsigned soff) {
  asm volatile("buffer_load_dword %0, %2, %1, %3 offen" : "+v"(sink) : "s"(rs), "v"(voff), "s"(soff) : "memory");
}
template <class T> DEV void touchp(unsigned& sink, u32x4 rs, unsigned voff, unsigned soff, T& pin) {
  asm volatile("buffer_load_dword %0, %3, %2, %4 offen" : "+v"(sink), "+v"(pin) : "s"(rs), "v"(voff), "s"(soff) : "memory");
}
DEV void touchl(u32x4 rs, unsigned voff, unsigned soff) {
  asm volatile("buffer_load_dword %1, %0, %2 offen lds" ::"s"(rs), "v"(voff), "s"(soff) : "memory");
}
template <int N> using IC = std::integral_constant<int, N>;
template <int T, int N, class Fn> DEV void unroll(Fn&& f) {
  if constexpr (T < N) { f(std::integral_constant<int, T>()); unroll<T + 1, N>(f); }
}
template <int OFF, class T> DEV void dmap(u32x4 rs, unsigned voff, unsigned soff, T& pin) {
  asm volatile("buffer_load_dwordx4 %2, %1, %3 offen offset:%4 lds" : "+v"(pin) : "s"(rs), "v"(voff), "s"(soff), "n"(OFF) : "memory");
}

template <class Fn> DEV void features(const f32x16& p, bool h, unsigned (&ph)[2][8], Fn&& cb) {
  constexpr float L = 1.4426950408889634f;
  float mx = fmaxf(p[0], p[1]), mn = fminf(p[0], p[1]);
#pragma unroll
  for (int r = 2; r < 12; r += 2) { mx = fmaxf(fmaxf(mx, p[r]), p[r + 1]); mn = fminf(fminf(mn, p[r]), p[r + 1]); }
  cb(IC<0>(), mx);
  float mx2 = fmaxf(fmaxf(fmaxf(fmaxf(mx, p[12]), p[13]), p[14]), p[15]);
  float mn2 = fminf(fminf(fminf(fminf(mn, p[12]), p[13]), p[14]), p[15]);
  mx = h ? mx : mx2; mn = h ? -mn : -mn2;
  swap32(mx, mn);
  float c0 = -L * fmaxf(mx, mn), c1 = c0;
  cb(IC<1>(), c0);
  swap32(c0, c1);
  const float ninf = -__builtin_inff();
  const float d0 = h ? ninf : c0, d1 = h ? ninf : c1;
  float e[2][16], s0 = 0.f, s1 = 0.f;
  unroll<0, 16>([&](auto rr) {
    constexpr int r = rr.value;
    float a0 = fmaf(p[r], L, r < 12 ? c0 : d0);
    if constexpr (r & 1) cb(IC<2 + r / 2>(), a0);
    e[0][r] = __builtin_amdgcn_exp2f(a0);
    e[1][r] = __builtin_amdgcn_exp2f(fmaf(p[r], -L, r < 12 ? c1 : d1));
    s0 += e[0][r]; s1 += e[1][r];
  });
  swap32(s0, s1);
  float i0 = __builtin_amdgcn_rcpf(s0 + s1), i1 = i0;
  swap32(i0, i1);
  unroll<0, 8>([&](auto rr) {
    constexpr int r = rr.value;
    ph[0][r] = pk(e[0][2 * r] * i0, e[0][2 * r + 1] * i0); ph[1][r] = pk(e[1][2 * r] * i1, e[1][2 * r + 1] * i1);
    if constexpr ((r & 3) == 1) cb(IC<10 + r / 4>(), ph[1][r]);
  });
}
DEV bf16x8 pack8(const f32x16& a, int o) {
  u32x4 u = {pk(a[o], a[o + 1]), pk(a[o + 2], a[o + 3]), pk(a[o + 4], a[o + 5]), pk(a[o + 6], a[o + 7])};
  return __builtin_bit_cast(bf16x8, u);
}
DEV bf16x8 pack8s(const f32x16& a, int o) {
  float t0 = a[o], t1 = a[o + 1], t2 = a[o + 2], t3 = a[o + 3], t4 = a[o + 4], t5 = a[o + 5], t6 = a[o + 6], t7 = a[o + 7];
  asm volatile("" : "+v"(t0), "+v"(t1), "+v"(t2), "+v"(t3), "+v"(t4), "+v"(t5), "+v"(t6), "+v"(t7));
  u32x4 u = {pk(t0, t1), pk(t2, t3), pk(t4, t5), pk(t6, t7)};
  return __builtin_bit_cast(bf16x8, u);
}
DEV f32x2 gelu2(f32x2 a) {
  constexpr float c1 = -2.302208198f, c2 = c1 * 0.044715f;
  const f32x2 z = a * __builtin_elementwise_fma(a * a, (f32x2)(c2), (f32x2)(c1));
  const f32x2 e = (f32x2){__builtin_amdgcn_exp2f(z.x), __builtin_amdgcn_exp2f(z.y)} + 1.f;
  return a * (f32x2){__builtin_amdgcn_rcpf(e.x), __builtin_amdgcn_rcpf(e.y)};
}

#define STAMP(n) do {} while (0)

constexpr int woff(int k) { return (k >> 1) * SLOT + (k & 1) * 8192; }
constexpr int xoff(int k) { return (k >> 1) * SLOT + 4096 + (k & 1) * 2048; }
#define TA0 22
#define TA1 13
#define TA2 99
#define QP 11
#define NWO 7
#define FW 6
#ifndef NV
#define NV 4
#endif
constexpr int G2R = 4, G2POLL = 6;
constexpr int g2ld(int t) { return t < 17 ? 4 : (t == 17 ? 2 : 0); }
constexpr int g2cnt(int s) { return s < 0 ? 4 : g2ld(s + G2R) + (s == G2POLL ? 1 : 0) + (s == 12 ? 3 : 0); }

__global__ __launch_bounds__(256, 1) void fused_kernel(const short*, const short*, const short*, const short*, const short*, const short*, const short*,
                                                       const short*, short*, char*) {
  u32x8 ka, kb;
  u32x4 kc;
  asm volatile("s_load_dwordx8 %0, %3, 0x0\n\ts_load_dwordx8 %1, %3, 0x20\n\ts_load_dwordx4 %2, %3, 0x40"
               : "=&s"(ka), "=&s"(kb), "=&s"(kc)
               : "s"(__builtin_amdgcn_kernarg_segment_ptr())
               : "memory");
  SB();
  __shared__ __attribute__((aligned(16))) char lds_[163840];
  const ldsp lds = (ldsp)lds_;
  const unsigned lds0 = (unsigned)(uintptr_t)lds;
  const int tid = threadIdx.x, lane = tid & 63, w = __builtin_amdgcn_readfirstlane(tid >> 6);
  const int xc = blockIdx.x & 7, i = blockIdx.x >> 3;
  const int h = lane >> 5, n = lane & 31, q = (lane & 15) >> 2, pp = lane & 3, blk = (lane >> 4) & 1;
  const unsigned area = lds0 + w * AREA;
  const int tile2 = 28 * (xc & 1) + i - 4, dt2 = tile2 >> 3, st2 = 8 * (xc >> 1) + (tile2 & 7);
  const unsigned swz = (lane >> 3), swc = ((lane & 7) ^ (((lane >> 4) & 1) << 2)) * 16;
  unsigned v2[4];
#pragma unroll
  for (int j = 0; j < 4; j++) v2[j] = i >= 4 ? swz * (S * 2) + swc + j * (8 * S * 2 - 1024) : OOB;
  unsigned so2 = (w * 448) * (S * 2) + st2 * 128;
  const bool isA = i >= 16;
  const int ja = i - 16, T = (ja & 12) + 2 * (xc & 1) + ((ja >> 1) & 1), pr = (xc & 6) + (ja & 1);
  const int tk0 = 256 * (xc & 1) + 64 * (xc >> 1);
  auto stage0 = [&](int st) { return ((tk0 ^ (st << 5)) & 256) + ((tk0 + 32 * st) & 255); };
  const int row0 = 512 * (i >> 2) + tk0 + 16 * (i & 3) + 4 * w;
  unsigned vt1 = xc < 7 ? (lane >> 2) * (F * 2) + (lane & 3) * 128 : OOB;
  unsigned vo1 = lane < 56 ? lane * 16 : OOB, vz = 0, sx0 = (isA ? 128 * T + 32 * w : row0) * (D * 2), ring = lds0 + L_WQ + w * 12544, vxa[4];
#pragma unroll
  for (int j = 0; j < 4; j++) vxa[j] = (8 * j + swz) * (D * 2) + (((lane & 7) ^ ((4 * j + (lane >> 4)) & 7)) * 16) - j * 1024;
  ldsp pb[2];
#pragma unroll
  for (int a = 0; a < 2; a++) pb[a] = lds + w * AREA + h * 1024 + q * 128 + (((4 * a + 2 * blk + (pp >> 1)) ^ ((q >> 1) << 2)) * 16) + (pp & 1) * 8;
  asm volatile("" : "+v"(v2[0]), "+v"(v2[1]), "+v"(v2[2]), "+v"(v2[3]), "+v"(vxa[0]), "+v"(vxa[1]), "+v"(vxa[2]), "+v"(vxa[3]), "+v"(vt1), "+v"(vo1), "+v"(vz),
               "+v"(pb[0]), "+v"(pb[1]), "+s"(so2), "+s"(sx0), "+s"(ring));
  SB();
  asm volatile("s_waitcnt lgkmcnt(0)" : "+s"(ka), "+s"(kb), "+s"(kc)::"memory");
  auto arg = [](unsigned lo, unsigned hi) { return (char*)(((unsigned long long)hi << 32) | lo); };
  const short *x = (short*)arg(ka[0], ka[1]), *wqkv = (short*)arg(ka[2], ka[3]), *wphi = (short*)arg(ka[4], ka[5]), *wo = (short*)arg(ka[6], ka[7]);
  const short *w1 = (short*)arg(kb[0], kb[1]), *b1 = (short*)arg(kb[2], kb[3]), *w2 = (short*)arg(kb[4], kb[5]), *b2 = (short*)arg(kb[6], kb[7]);
  short* out = (short*)arg(kc[0], kc[1]);
  char* ws = arg(kc[2], kc[3]);
  const u32x4 rsF = mkrs(ws, WS_FLAG_BYTES), rsX = mkrs(x, S * D * 2), rsW2 = mkrs(w2, F * S * 2);
  auto pre2 = [&](int t0, int t1, int slot0) {
#pragma unroll 1
    for (int t = t0; t < t1; t++) dma4(rsW2, v2, so2 + t * (32 * S * 2), area + (slot0 + t - t0) * 4096);
  };

  bf16x8 w2r[4][2][2];
  auto pre2w = [&]() {
#pragma unroll
    for (int t = 0; t < 4; t++) asm volatile("s_waitcnt lgkmcnt(0)" : "+v"(w2r[t][0][0]), "+v"(w2r[t][0][1]), "+v"(w2r[t][1][0]), "+v"(w2r[t][1][1])::"memory");
  };
  auto pre2rd = [&]() {
#pragma unroll
    for (int t = 0; t < 4; t++)
#pragma unroll
      for (int ks = 0; ks < 2; ks++)
#pragma unroll
        for (int a = 0; a < 2; a++) w2r[t][ks][a] = cat(vtr(pb[a] + (t + 6) * 4096 + ks * 2048), vtr(pb[a] + (t + 6) * 4096 + ks * 2048 + 512));
  };

  u32x4 wor[14];
  unsigned vwo[4];
#pragma unroll
  for (int j = 0; j < 4; j++) vwo[j] = i >= 4 ? swz * (D * 2) + swc + j * (8 * D * 2 - 1024) : OOB;
  const u32x4 rsWo = mkrs(wo, D * D * 2);
  const unsigned sow = w * 112 * (D * 2) + dt2 * 128;
  auto ldwo = [&](auto jj) { wor[jj.value] = lda<(jj.value & 3) * 1024>(rsWo, vwo[jj.value & 3], sow + (jj.value >> 2) * (32 * D * 2)); };
  auto tiewo = [&]() { unroll<0, 14>([&](auto jj) { tiea(wor[jj.value]); }); hold(vwo[0], vwo[1], vwo[2], vwo[3]); };
  const u32x4 rsW1 = mkrs(w1, S * F * 2);
  auto so_t1 = [&](int stage, int wq, int r0) { return (512 * wq + stage0(stage) + r0) * (F * 2) + xc * 512; };
  const unsigned vfu = lane < 4 && i >= 4 ? (FL_U + dt2 * 14 + ((7 * w) >> 1) + lane) * 128 : OOB;
  unsigned miss = 1, td = 14;
  Epoch ep;
  auto setep = [&](u32x2 gen) {
    const unsigned long long g = (((unsigned long long)__builtin_amdgcn_readfirstlane(gen.y) << 32) | (unsigned)__builtin_amdgcn_readfirstlane(gen.x)) + 1;
    ep.lo = (unsigned)g; ep.hi = (unsigned)(g >> 32);
  };

  if (!isA) {
    bf16x8 xv[4];
    u32x2 gen, bb[4];
    xv[0] = ldp<0>(rsX, vo1, sx0); xv[1] = ldp<896>(rsX, vo1, sx0); xv[2] = ldp<1792>(rsX, vo1, sx0); xv[3] = ldp<2688>(rsX, vo1, sx0);
    gen = ldc2(rsF, vz);
    SB();
    const bool isG = xc < 7 && i < 14;
    const int ft = 2 * xc + (i >= 7), dt = i >= 7 ? i - 7 : i;
    const unsigned vt = lane < 32 ? vt1 : OOB, vb = h * 8;
    {
      setm0(area + L_STAT);
      touchl(rsW1, vt, so_t1(i >> 2, i & 3, 8 * w));
      const u32x4 rsB = mkrs(b1, F * 2);
      const unsigned sb = (ft * 128 + w * 32) * 2;
      bb[0] = ldp2<0>(rsB, vb, sb); bb[1] = ldp2<16>(rsB, vb, sb); bb[2] = ldp2<32>(rsB, vb, sb); bb[3] = ldp2<48>(rsB, vb, sb);
    }
    unsigned v1[4], vx[4];
#pragma unroll
    for (int j = 0; j < 4; j++) {
      v1[j] = (8 * (j & 1) + swz) * (F * 2) + (j >> 1) * 128 + swc - j * 1024;
      vx[j] = (8 * j + swz) * (D * 2) + swc - j * 1024;
    }
    waitv<5>(xv[0], xv[1], xv[2], xv[3]);
    tie(gen);
    STAMP(0);
    setep(gen);
    const unsigned tagB = ep.lo ^ ep.hi, vs = (lane & 15) ? OOB : (row0 + h + 2 * blk) * 16;
    u32x4 rec;
    {
      float s[4] = {}, z[4] = {};
#pragma unroll
      for (int r = 0; r < 4; r++) acc_stats(xv[r], s[r], z[r]);
      swap32(s[0], s[1]); swap32(s[2], s[3]); swap32(z[0], z[1]); swap32(z[2], z[3]);
      float sa = s[0] + s[1], sb = s[2] + s[3], za = z[0] + z[1], zb = z[2] + z[3];
      swap16(sa, sb); swap16(za, zb);
      const float mu = rowsum(sa + sb) * (1.f / D), ms = rowsum(za + zb) * (1.f / D);
      const float var = fmaxf(ms - mu * mu, 0.f);
      float a = __builtin_amdgcn_rsqf(var + 1e-5f);
      a *= __builtin_amdgcn_rsqf(var * a * a + 1e-5f);
      const unsigned a2 = pk(a, a);
      const float c = -mu * blo(a2);
      rec = (u32x4){a2, ep.lo, __builtin_bit_cast(unsigned, c), tagB};
      stc(mkst(ws + WS_ST, SZ_ST), vs, rec);
    }
    asm volatile("s_barrier" ::: "memory");
    hold(vz, vo1, vt, vb);
    STAMP(1);

    if (isG) {
      const unsigned b1s = (w * 512) * (F * 2) + ft * 256, bxs = (w * 512) * (D * 2) + dt * 128;
      auto mset = [&](auto kk, auto jj) {
        constexpr int ks = kk.value, j = jj.value;
        if constexpr ((ks & 1) ? j == 2 : j == 0) setm0n(area + woff(ks));
        if constexpr (!(ks & 1) && j == 4) setm0n(area + xoff(ks));
      };
      auto dgrp = [&](auto kk, auto jj, unsigned s1, unsigned sx) {
        constexpr int ks = kk.value, j = jj.value, jw = (ks & 1) ? j - 2 : j, jx = (ks & 1) ? j : j - 4;
        if constexpr (jw >= 0 && jw < 4) dma<jw * 1024>(rsW1, v1[jw], s1 + ks * (16 * F * 2));
        else dma<(jx + 2 * (ks & 1)) * 1024>(rsX, vx[jx + 2 * (ks & 1)], sx + (ks >> 1) * (32 * D * 2));
      };
      auto fill = [&](auto kk) {
        unroll<0, 6>([&](auto jj) { mset(kk, jj); asm volatile("s_nop 0"); dgrp(kk, jj, b1s + tk0 * (F * 2), bxs + tk0 * (D * 2)); });
      };
      const u32x4 rsS = mkrs(ws + WS_ST, SZ_ST);
      auto gather = [&]() {
        bf16x8 g[8];
#pragma unroll
        for (int j = 0; j < 8; j++) g[j] = ldc<0>(rsS, lane * 16, w * 8192 + stage0(2 * j) * 16);
        waitv<0>();
        bool ok = true;
#pragma unroll
        for (int j = 0; j < 8; j++) {
          tie(g[j]);
          const u32x4 u = __builtin_bit_cast(u32x4, g[j]);
          ok &= u[1] == ep.lo && u[3] == tagB;
          const ldsp p = lds + w * AREA + L_STAT + j * 512 + (lane >> 4) * 128 + (lane & 15) * 4;
          lst(p, u[2]); lst(p + 64, u[0]);
        }
        return (bool)__all(ok);
      };
      unroll<0, 4>(fill);
      hold(vs, rec);
      STAMP(2);
#pragma unroll 1
      for (int it = 0; it < SPIN; ++it)
        if (gather()) break;
      STAMP(3);
      tie(bb[0]); tie(bb[1]); tie(bb[2]); tie(bb[3]);
      f32x16 acc[4][2];
#pragma unroll
      for (int g = 0; g < 4; g++) {
        acc[0][0][4 * g] = blo(bb[g].x); acc[0][0][4 * g + 1] = bhi(bb[g].x); acc[0][0][4 * g + 2] = blo(bb[g].y); acc[0][0][4 * g + 3] = bhi(bb[g].y);
      }
      acc[0][1] = acc[0][0];
#pragma unroll
      for (int a = 1; a < 4; a++) { acc[a][0] = (f32x16)(0.f); acc[a][1] = (f32x16)(0.f); }
      unsigned pa[4], px[2], msk[4], psc = area + L_STAT + h * 32, psa;
      {
        const unsigned rowb = area + (8 * h + q) * 128 + (pp & 1) * 8, ch = 2 * blk + (pp >> 1), fl = (q >> 1) << 2;
#pragma unroll
        for (int a = 0; a < 4; a++) pa[a] = rowb + ((a ^ w) >> 1) * 2048 + ((4 * ((a ^ w) & 1) + ch) ^ fl) * 16;
        px[0] = rowb + (ch ^ fl) * 16; px[1] = rowb + ((4 + ch) ^ fl) * 16;
        const unsigned r = 4 * ((lane >> 3) & 1) + pp, on = (!blk && (q & 1) == h) ? ((r & 1) ? 0xffff0000u : 0xffffu) : 0u;
#pragma unroll
        for (int j = 0; j < 4; j++) { msk[j] = (r >> 1) == j ? on : 0u; tie(msk[j]); }
        psa = area + L_STAT + 64 + (8 * h + r) * 4;
      }
      s16x4 wl[2][4], wh[2][4], xl[2], xh[2];
      u32x4 hf[2][2], dg;
      unsigned av;
      bf16x8 xp[2];
      f32x16 hn[2], cc = {};
      tie(cc);
      auto rdX = [&](auto nn, auto dd) {
        constexpr int o = xoff(nn.value & 3), d = dd.value;
        xl[d] = vtr((ldsp)(uintptr_t)px[d] + o); xh[d] = vtr((ldsp)(uintptr_t)px[d] + o + 512);
      };
      auto rdA = [&](auto nn) { av = lld<unsigned>((ldsp)(uintptr_t)psa + nn.value * 128); };
      auto rdC = [&](auto nn) {
        const f32x4 c0 = lld<f32x4>((ldsp)(uintptr_t)psc + nn.value * 128), c1 = lld<f32x4>((ldsp)(uintptr_t)psc + nn.value * 128 + 16);
#pragma unroll
        for (int e = 0; e < 4; e++) { cc[e] = c0[e]; cc[4 + e] = c1[e]; }
      };
      auto rdW = [&](auto nn, auto aa) {
        constexpr int o = woff(nn.value & 3), nx = nn.value & 1, a = aa.value;
        wl[nx][a] = vtr((ldsp)(uintptr_t)pa[a] + o); wh[nx][a] = vtr((ldsp)(uintptr_t)pa[a] + o + 512);
      };
      auto mkD = [&]() { dg = (u32x4){av & msk[0], av & msk[1], av & msk[2], av & msk[3]}; };
      auto nrm = [&](auto dd) { hn[dd.value] = mfmav(dg, cat(xl[dd.value], xh[dd.value]), cc); };
      auto cvt = [&](auto nn, auto dd) {
        constexpr int d = dd.value;
        hf[nn.value & 1][d] = (u32x4){pk(hn[d][0], hn[d][1]), pk(hn[d][2], hn[d][3]), pk(hn[d][4], hn[d][5]), pk(hn[d][6], hn[d][7])};
        SB();
        keep(hn[d]);
      };
      auto step = [&](auto kk, auto wn, auto last, auto&& ms, auto&& gm) {
        constexpr int ks = kk.value;
        constexpr bool more = !last.value;
        using NX = IC<ks + 1>;
        auto mm = [&](auto m_) {
          constexpr int a = more ? m_.value >> 1 : ((m_.value >> 1) + 1) & 3, dh = m_.value & 1;
          if constexpr (a < NV) mfmac(acc[a][dh], cat(wl[ks & 1][a], wh[ks & 1][a]), hf[ks & 1][dh]);
          else acc[a][dh] = MFMA(cat(wl[ks & 1][a], wh[ks & 1][a]), __builtin_bit_cast(bf16x8, hf[ks & 1][dh]), acc[a][dh]);
          SB();
          if constexpr (!more && m_.value >= 2) {
            constexpr int c = m_.value >> 1;
            unroll<0, 2>([&](auto gg) {
              constexpr int g = gg.value, j = 2 * (m_.value - 2) + g;
              if constexpr (j == 0 && NV > 1) asm volatile("s_nop 9" : "+v"(acc[1][0]));
              if constexpr (c < NV) xp[g] = pack8(acc[c][dh], 8 * g); else xp[g] = pack8s(acc[c][dh], 8 * g);
              lst(lds + w * AREA + lane * 16 + (c - 1) * 4096 + dh * 2048 + g * 1024, xp[g]);
              if constexpr (j) hold(xp[g ^ 1]);
              if constexpr (j < NWO) ldwo(IC<j>());
              SB();
            });
          }
        };
        __builtin_amdgcn_s_waitcnt(0x70 | (wn.value & 15) | ((wn.value >> 4) << 14));
        asm volatile("" ::: "memory");
        SB();
        if constexpr (more) { rdX(NX(), IC<0>()); rdA(NX()); }
        ms(IC<0>());
        SB();
        mm(IC<0>());
        gm(IC<0>());
        if constexpr (more) { rdC(NX()); rdX(NX(), IC<1>()); SB(); }
        mm(IC<1>());
        gm(IC<1>());
        ms(IC<2>());
        if constexpr (more) { rdW(NX(), IC<0>()); rdW(NX(), IC<1>()); SB(); }
        mm(IC<2>());
        if constexpr (more) { mkD(); SB(); }
        gm(IC<2>());
        if constexpr (more) { rdW(NX(), IC<2>()); SB(); nrm(IC<0>()); SB(); rdW(NX(), IC<3>()); SB(); }
        mm(IC<3>());
        if constexpr (more) { nrm(IC<1>()); SB(); }
        gm(IC<3>());
        ms(IC<4>());
        mm(IC<4>());
        if constexpr (more) { cvt(NX(), IC<0>()); SB(); }
        mm(IC<5>());
        gm(IC<4>());
        mm(IC<6>());
        if constexpr (more) { cvt(NX(), IC<1>()); SB(); }
        gm(IC<5>());
        mm(IC<7>());
      };
      asm volatile("s_waitcnt lgkmcnt(0)" ::: "memory");
      rdX(IC<0>(), IC<0>()); rdX(IC<0>(), IC<1>()); rdA(IC<0>()); rdC(IC<0>());
      unroll<0, 4>([&](auto jj) { rdW(IC<0>(), jj); });
      mkD();
      SB();
      asm volatile("s_nop 1");
      nrm(IC<0>()); nrm(IC<1>());
      asm volatile("s_nop 7\n\ts_nop 7");
      cvt(IC<0>(), IC<0>()); cvt(IC<0>(), IC<1>());
      SB();
#pragma unroll 1
      for (int t = 2; t < 16; t += 2) {
        const unsigned tk = stage0(t);
        const unsigned s1 = b1s + tk * (F * 2), sx = bxs + tk * (D * 2);
        unroll<0, 4>([&](auto kk) {
          step(kk, IC<12>(), IC<0>(), [&](auto jj) { mset(kk, jj); SB(); }, [&](auto jj) { dgrp(kk, jj, s1, sx); SB(); });
        });
        psc += 512; tie(psc); psa += 512; tie(psa);
        SB();
      }
      {
        unroll<0, 4>([&](auto kk) {
          constexpr int ks = kk.value;
          if constexpr (ks == 0) STAMP(9);
          if constexpr (ks == 1) hold(v1[0], v1[1], v1[2], v1[3], vx[0], vx[1], vx[2], vx[3]);
          if constexpr (ks == 1) STAMP(10);
          if constexpr (ks == 2) STAMP(11);
          if constexpr (ks == 3) STAMP(12);
          step(kk, IC<(ks == 0 ? 12 : ks == 1 ? 10 : ks == 2 ? 8 : 63)>(), IC<(ks == 3)>(),
               [&](auto jj) { if constexpr (jj.value == 0) { setm0n(area + (6 + ks) * 4096); SB(); } },
               [&](auto jj) {
                 constexpr int j = jj.value;
                 if constexpr (j < 4) dma<j * 1024>(rsW2, v2[j], so2 + ks * (32 * S * 2));
                 SB();
               });
        });
      }
      bf16x8 sl[2];
      {
        u32x4 s0, s1;
#pragma unroll
        for (int j = 0; j < 4; j++) {
          const unsigned e = msk[j] & 0x3f803f80u;
          auto r = __builtin_amdgcn_permlane16_swap(0u, e, false, false);
          const unsigned o = r[0];
          s0[j] = e; s1[j] = o;
        }
        sl[0] = __builtin_bit_cast(bf16x8, s0); sl[1] = __builtin_bit_cast(bf16x8, s1);
        tie(sl[0]); tie(sl[1]);
      }
      unsigned vo = (ft * 16 + w * 4 + h) * (D * 16) + (dt * 64 + n) * 16, vo2 = vo + 2 * D * 16, px3[3];
      tie(vo); tie(vo2);
#pragma unroll
      for (int a = 0; a < 3; a++) { px3[a] = lds0 + (w ^ (a + 1)) * AREA + lane * 16; tie(px3[a]); }
      STAMP(4);
      bar();
      STAMP(5);
      {
        const rsrc_t st = mkst(ws + WS_UF, SZ_UF);
        u32x4 v[12], res[4];
#pragma unroll
        for (int c = 0; c < 12; c++) { v[c] = lld<u32x4>((ldsp)(uintptr_t)px3[c % 3] + (c % 3) * 4096 + (c / 3) * 1024); SB(); }
        unroll<0, 12>([&](auto cc_) {
          constexpr int c = cc_.value;
          mfmac(acc[0][c / 6], sl[(c / 3) & 1], v[c]);
          if constexpr (NWO + c < 14) ldwo(IC<NWO + c>());
        });
        asm volatile("s_nop 11" : "+v"(acc[0][0]), "+v"(acc[0][1]));
#pragma unroll
        for (int dh = 0; dh < 2; dh++) {
#pragma unroll
          for (int g = 0; g < 4; g += 2) {
            float r[8];
#pragma unroll
            for (int e = 0; e < 8; e += 2) {
              const f32x2 z = gelu2((f32x2){acc[0][dh][4 * g + e], acc[0][dh][4 * g + e + 1]});
              r[e] = z.x; r[e + 1] = z.y;
            }
            auto r0 = __builtin_amdgcn_permlane32_swap(pk(r[0], r[1]), pk(r[4], r[5]), false, false);
            auto r1 = __builtin_amdgcn_permlane32_swap(pk(r[2], r[3]), pk(r[6], r[7]), false, false);
            res[2 * dh + g / 2] = (u32x4){r0[0], r1[0], r0[1], r1[1]};
            stc(st, (g ? vo2 : vo) + dh * 512, res[2 * dh + g / 2]);
            SB();
          }
        }
        STAMP(8);
        waitv<4>();
        tiewo();
        pre2rd();
        pre2w();
        hold(res[0], res[1], res[2], res[3], vo, vo2);
      }
      const unsigned vfg = tid == 0 ? (FL_U + dt * 14 + ft) * 128 : OOB;
      u32x2 epv = {ep.lo, ep.hi};
      tie(epv);
      STAMP(6);
      publish<0>(rsF, vfg, epv);
      STAMP(7);
      u32x2 f0 = ldc2(rsF, vfu);
      pre2(4, 10, 0);
      waitv<24>(f0);
      hold(vfg, epv);
      miss = missing<4>(f0, ep);
      td = 10;
    } else {
      if (xc < 7) {
#pragma unroll 1
        for (int m = 4; m < 9; m++) {
          if (m == 4) __builtin_amdgcn_s_sleep(24); else __builtin_amdgcn_s_sleep(10);
          touchl(rsW1, vt1, so_t1(m, 2 * (i - 14) + (w >> 1), 16 * (w & 1)));
        }
      }
      if (i >= 14) {
#pragma unroll 1
        for (int m = 0; m < 4; m++) touchl(mkrs(wo, D * D * 2), (lane >> 2) * (D * 2) + (lane & 3) * 128, (128 * m + 64 * (i - 14) + 16 * w) * (D * 2) + (xc & 1) * 384);
#pragma unroll 1
        for (int m = 0; m < 4; m++) touchl(rsW2, (lane >> 3) * (S * 2) + (lane & 7) * 128, (448 * m + 384 + 32 * (i - 14) + 8 * w) * (S * 2) + (xc >> 1) * 1024);
        const int head = 2 * xc + i - 14;
        const u32x4 rsP = mkrs(ws + WS_KVP, SZ_KVP);
        const unsigned so = head * 65536 + w * 1024, vf = lane < 16 ? (FL_P + xc * 16 + lane) * 128 : OOB, vp = h * 4096 + n * 16;
        const unsigned one = ((lane >> 3) & 1) == blk ? ((lane & 1) ? 0x3f800000u : 0x3f80u) : 0u;
        u32x4 id;
#pragma unroll
        for (int j = 0; j < 4; j++) id[j] = ((lane & 7) >> 1) == j ? one : 0u;
        f32x4 a[2] = {};
        tie(id); tie(a[0]); tie(a[1]);
        bf16x8 pv[16];
        collect(pv, rsF, vf, rsP, vp, so, ep);
        STAMP(3);
#pragma unroll
        for (int t = 0; t < 16; t++) asm volatile("v_mfma_f32_16x16x32_bf16 %0, %1, %2, %0" : "+v"(a[t & 1]) : "v"(id), "v"(pv[t]));
        asm volatile("s_nop 7" : "+v"(a[0]), "+v"(a[1]));
        const rsrc_t st = mkst(ws + WS_KVF, SZ_KVF);
        const u32x4 r0 = {pk(a[0][0], a[0][1]), ep.lo, pk(a[0][2], a[0][3]), tagB}, r1 = {pk(a[1][0], a[1][1]), ep.lo, pk(a[1][2], a[1][3]), tagB};
        const unsigned vr = head * 8192 + w * 2048 + blk * 1024 + (16 * h + (lane & 15)) * 16;
        stc(st, vr, r0);
        stc(st, vr + 512, r1);
        hold(r0, r1, vr);
        STAMP(4);
      } else {
        __builtin_amdgcn_s_sleep(127);
      }
      unroll<0, 14>(ldwo);
      pre2(0, 4, 6); pre2(4, 10, 0);
      waitv<24>();
      tiewo();
      pre2rd();
      pre2w();
      pre2(10, 14, 6);
    }
  } else {
    const unsigned sxa = sx0;
    dma4(rsX, vxa, sxa, ring);
    u32x2 gen = ldc2(rsF, vz);
    SB();
    unsigned sink = 0;
    const u32x4 rsW = mkrs(wqkv, D * 3 * D * 2);
    {
      const unsigned r6 = (lane * 43) >> 8, c6 = lane - 6 * r6;
      touch(sink, rsW, lane < 42 ? r6 * 2688 + 896 + (c6 & 1) * 896 + (c6 >> 1) * 108 : OOB, (28 * ja + 7 * w) * 2688 + (xc & 6) * 112);
    }
    dma4(rsX, vxa, sxa + 128, ring + 4096);
    dma4(rsX, vxa, sxa + 256, ring + 8192);
    unsigned vw[8];
#pragma unroll
    for (int s = 0; s < 8; s++) {
      const unsigned c = (s < 7 ? 256 * s + 64 * w : 3072) + lane, r = (c * 9363) >> 16;
      vw[s] = ((r & ~15u) + 8 * ((r >> 1) & 1) + 4 * (r & 1) + ((r >> 2) & 1) + 2 * ((r >> 3) & 1)) * 2688 + (c - 7 * r) * 16;
    }
    const unsigned mw = lds0 + w * 1024;
    auto mW = [&](auto ss, auto bb) {
      if constexpr (ss.value < 12) addm0<bb.value + ss.value * 4096>(mw); else addm0<bb.value + 49152>(lds0);
    };
    auto dmaW = [&](auto ss, unsigned col) {
      constexpr int s = ss.value;
      dma<0>(rsW, vw[s < 12 ? (s + 14) % 7 : 7], col + pr * 112 + (s >= 7 && s < 12 ? 256 * 2688 : 0));
    };
    bf16x8 xa[28];
    float sa = 0.f, sq = 0.f;
    {
      unsigned pr_[4];
#pragma unroll
      for (int k = 0; k < 4; k++) pr_[k] = ring + n * 128 + (((2 * k + h) ^ ((n >> 1) & 7)) * 16);
      unroll<0, 7>([&](auto cc) {
        constexpr int cb = cc.value;
        waitv<(cb == 0 ? 9 : 8)>();
#pragma unroll
        for (int k = 0; k < 4; k++) xa[4 * cb + k] = lld<bf16x8>((ldsp)(uintptr_t)pr_[k] + (cb % 3) * 4096);
        asm volatile("s_waitcnt lgkmcnt(0)" : "+v"(xa[4 * cb]), "+v"(xa[4 * cb + 1]), "+v"(xa[4 * cb + 2]), "+v"(xa[4 * cb + 3])::"memory");
        if constexpr (cb < 4) dma4(rsX, vxa, sxa + (cb + 3) * 128, ring + (cb % 3) * 4096);
        if constexpr (cb == 4) unroll<0, 4>([&](auto ss) { mW(ss, IC<L_WK>()); asm volatile("s_nop 0"); dmaW(ss, 896); });
        if constexpr (cb == 5) unroll<0, 4>([&](auto ss) { mW(ss, IC<L_WV>()); asm volatile("s_nop 0"); dmaW(ss, 1792); });
        if constexpr (cb == 6) {
          setm0(lds0 + L_WPHI + w * 1024);
          dma<0>(mkrs(wphi + pr * 1568, 3136), tid * 16, 0);
        }
        SB();
#pragma unroll
        for (int k = 0; k < 4; k++) acc_stats(xa[4 * cb + k], sa, sq);
        SB();
      });
    }
    STAMP(1);
    hold(vxa[0], vxa[1], vxa[2], vxa[3], vz);
    tie(gen);
    setep(gen);
    float la, lc;
    {
      float s2 = sa, q2 = sq;
      swap32(sa, s2); swap32(sq, q2);
      const float mu = (sa + s2) * (1.f / D), var = fmaxf((sq + q2) * (1.f / D) - mu * mu, 0.f);
      la = __builtin_amdgcn_rsqf(var + 1e-5f);
      lc = -mu * la;
    }
    auto norm = [&](auto kk, auto ee) {
      constexpr int ks = kk.value, e = ee.value;
      if constexpr (ks < 28) {
        u32x4 u = __builtin_bit_cast(u32x4, xa[ks]);
        u[e] = affine2(u[e], la, lc);
        xa[ks] = __builtin_bit_cast(bf16x8, u);
      }
    };
    unroll<0, 4>([&](auto ee) { norm(IC<0>(), ee); });
    norm(IC<1>(), IC<0>());
    tie(xa[0]); tie(xa[1]);
    waitv<1>();
    bar();
    STAMP(3);
    const unsigned vt2 = (lane >> 3) * (S * 2) + (lane & 7) * 128;
    const unsigned st2t = (448 * w + 8 * ja) * (S * 2) + (xc >> 1) * 1024;
    auto touch2 = [&](int m) { touch(sink, rsW2, vt2, st2t + 128 * m * (S * 2)); };
    const unsigned vtq = lane < 28 && (lane & 3) < 3 ? (lane >> 2) * 2688 + (lane & 3) * 108 : OOB;
    const unsigned vtx = xc < 7 && (lane & 7) < 7 ? (lane >> 3) * (D * 2) + (lane & 7) * 128 : OOB;
    const unsigned stx = (512 * ((2 * ja + (w >> 1)) & 3) + stage0(8 + (ja >> 1)) + 16 * (w & 1)) * (D * 2);
    const unsigned wlane = (2 * h + 4 * q) * 112 + 32 * blk + 8 * pp;
    f32x16 kT[2] = {}, vv[2] = {};
    {
      const ldsp pk_ = lds + L_WK + wlane, pv_ = lds + L_WV + wlane;
      s16x4 fr[30][8];
      auto rd = [&](auto kk, auto cc) {
        constexpr int ks = kk.value, c = cc.value;
        if constexpr (ks < 28) fr[ks][c] = vtr(((c & 4) ? pv_ : pk_) + ks * 1792 + ((c >> 1) & 1) * 56 + (c & 1) * 112);
      };
      unroll<0, 8>([&](auto cc) { rd(IC<0>(), cc); });
      unroll<0, 8>([&](auto cc) { rd(IC<1>(), cc); });
      SB();
      unroll<0, 28>([&](auto kk) {
        constexpr int ks = kk.value;
        if constexpr (ks == 7) { waitv<6>(); asm volatile("s_barrier" ::: "memory"); }
        if constexpr (ks == 16) { waitv<4>(); asm volatile("s_barrier" ::: "memory"); }
        __builtin_amdgcn_s_waitcnt(ks < 27 ? 0xc87f : 0xc07f);
        SB();
        unroll<0, 4>([&](auto mm) {
          constexpr int m = mm.value;
          const bf16x8 wv = cat(fr[ks][2 * m], fr[ks][2 * m + 1]);
          if constexpr (m < 2) kT[m] = MFMA(wv, xa[ks], kT[m]);
          else vv[m - 2] = MFMA(xa[ks], wv, vv[m - 2]);
          SB();
          if constexpr (ks < 9 && m == 0) mW(IC<4 + ks>(), IC<L_WK>());
          if constexpr (ks < 9 && m == 2) mW(IC<4 + ks>(), IC<L_WV>());
          rd(IC<ks + 2>(), IC<2 * m>());
          rd(IC<ks + 2>(), IC<2 * m + 1>());
          SB();
          if constexpr (ks < 9 && !(m & 1)) dmaW(IC<4 + ks>(), m ? 1792 : 896);
          if constexpr (ks == 9 && m == 0) touch(sink, rsW1, vt1, so_t1(8 + (ja >> 1), (2 * ja + (w >> 1)) & 3, 16 * (w & 1)));
          if constexpr (ks == 10 && m == 0) touch(sink, rsX, vtx, stx);
          if constexpr (ks == 11 && m == 0) touch(sink, rsX, vtx, stx + 8 * D * 2);
          if constexpr (ks == 14 && m == 0) touch(sink, rsW, vtq, (28 * ja + 7 * w) * 2688 + (xc & 6) * 112);
          if constexpr (ks == 16 && m == 0) hold(vtx);
          if constexpr (ks == 19 && m == 0) hold(vtq);
          if constexpr (ks == TA0 && m == 0) touch2(0);
          SB();
          norm(IC<ks + 1 + (m == 3)>(), IC<(m + 1) & 3>());
          SB();
        });
      });
    }
    bf16x8 wf[2][2];
    {
      const ldsp p = lds + L_WPHI + (4 * h + q) * 56 + 32 * blk + 8 * pp;
#pragma unroll
      for (int hd = 0; hd < 2; hd++) {
        wf[hd][0] = cat(vtr(p + hd * 1568), vtr(p + hd * 1568 + 8 * 56));
        s16x4 z = vtr(p + hd * 1568 + 24 * 56);
        wf[hd][1] = cat(vtr(p + hd * 1568 + 16 * 56), h ? (s16x4){0, 0, 0, 0} : z);
      }
    }
    STAMP(5);
    {
      f32x16 p[2];
#pragma unroll
      for (int hd = 0; hd < 2; hd++) { p[hd] = MFMA(wf[hd][0], pack8(kT[hd], 0), (f32x16)(0.f)); p[hd] = MFMA(wf[hd][1], pack8(kT[hd], 8), p[hd]); }
      bar();
      {
        const bf16x8 ones = {0x3f80, 0x3f80, 0x3f80, 0x3f80, 0x3f80, 0x3f80, 0x3f80, 0x3f80};
        const ldsp pvx = lds + L_VX + w * 2048 + lane * 16;
#pragma unroll
        for (int hd = 0; hd < 2; hd++)
#pragma unroll
          for (int j = 0; j < 2; j++) { bf16x8 f = pack8(vv[hd], 8 * j); lst(pvx + hd * 8192 + j * 1024, n == 28 ? ones : f); }
      }
      const unsigned pb = (32 * w + n) * 64 + ((h ^ ((n >> 1) & 7)) * 8);
      unroll<0, 2>([&](auto hh) {
        constexpr int hd = hh.value;
        unsigned ph[2][8];
        features(p[hd], h, ph, [&](auto kk, auto& pin) {
          constexpr int s = 12 * hd + kk.value;
          if constexpr (s < 13) {
            mW(IC<s>(), IC<L_WQ>());
            asm volatile("s_nop 0");
            dmap<0>(rsW, vw[s < 12 ? s % 7 : 7], pr * 112 + (s >= 7 && s < 12 ? 256 * 2688 : 0), pin);
          }
          if constexpr (s == TA1 || s == TA2) touchp(sink, rsW2, vt2, st2t + 128 * (s == TA1 ? 1 : 2) * (S * 2), pin);
        });
#pragma unroll
        for (int sg = 0; sg < 2; sg++)
#pragma unroll
          for (int g = 0; g < 4; g++) lst(lds + L_PHI + (hd * 2 + sg) * 8192 + (pb ^ (g << 4)), (u32x2){ph[sg][2 * g], ph[sg][2 * g + 1]});
      });
    }
    STAMP(6);
    hold(vw[0], vw[1], vw[2], vw[3], vw[4], vw[5], vw[6], vw[7], vt1, vt2);
    waitv<(TA2 < 24 ? 2 : 1)>();
    bar();
    u32x4 kvp[2];
    const unsigned vkv = (2 * pr + (w >> 1)) * 65536 + T * 4096 + (w & 1) * 2048 + lane * 16;
    {
      const unsigned sw = 2 * h + (q >> 1);
      const ldsp pl = lds + L_PHI + w * 8192 + (4 * h + q) * 64 + (((4 * blk + pp) ^ sw) * 8);
      const ldsp ph_ = lds + L_PHI + w * 8192 + (8 + 4 * h + q) * 64 + (((4 * blk + pp) ^ sw ^ 4) * 8);
      const ldsp pvx = lds + L_VX + (w >> 1) * 8192 + lane * 16;
      f32x16 kv = {};
#pragma unroll
      for (int jj = 0; jj < 8; jj++) kv = MFMA(cat(vtr(pl + jj * 1024), vtr(ph_ + jj * 1024)), lld<bf16x8>(pvx + jj * 1024), kv);
      const rsrc_t st = mkst(ws + WS_KVP, SZ_KVP);
      kvp[0] = __builtin_bit_cast(u32x4, pack8(kv, 0)); kvp[1] = __builtin_bit_cast(u32x4, pack8(kv, 8));
      stc(st, vkv, kvp[0]);
      stc(st, vkv + 1024, kvp[1]);
    }
    STAMP(7);
    const unsigned vfp = tid == 0 ? (FL_P + pr * 16 + T) * 128 : OOB;
    u32x2 epv = {ep.lo, ep.hi};
    tie(epv);
    f32x16 qT[2] = {};
    {
      const ldsp pq_ = lds + L_WQ + wlane;
      s16x4 fr[29][4];
      auto rd = [&](auto kk, auto cc) {
        constexpr int ks = kk.value, c = cc.value;
        if constexpr (ks < 28) fr[ks][c] = vtr(pq_ + ks * 1792 + (c >> 1) * 56 + (c & 1) * 112);
      };
      unroll<0, 4>([&](auto cc) { rd(std::integral_constant<int, 0>(), cc); });
      unroll<0, 28>([&](auto kk) {
        constexpr int ks = kk.value;
        if constexpr (ks == QP) { hold(kvp[0], kvp[1], vkv); publish<0>(rsF, vfp, epv); STAMP(8); SB(); }
        if constexpr (ks == QP + 2 && TA2 >= 24) { touch2(2); SB(); }
        if constexpr (ks == QP + 6) hold(vfp, vt2);
        unroll<0, 2>([&](auto mm) {
          constexpr int m = mm.value;
          rd(std::integral_constant<int, ks + 1>(), std::integral_constant<int, 2 * m>());
          rd(std::integral_constant<int, ks + 1>(), std::integral_constant<int, 2 * m + 1>());
          qT[m] = MFMA(cat(fr[ks][2 * m], fr[ks][2 * m + 1]), xa[ks], qT[m]);
          SB();
        });
      });
    }
    unsigned phq[2][2][8];
    {
      f32x16 p[2];
#pragma unroll
      for (int hd = 0; hd < 2; hd++) { p[hd] = MFMA(wf[hd][0], pack8(qT[hd], 0), (f32x16)(0.f)); p[hd] = MFMA(wf[hd][1], pack8(qT[hd], 8), p[hd]); }
      bar();
      STAMP(9);
      unroll<0, 2>([&](auto hh) {
        features(p[hh.value], h, phq[hh.value], [&](auto kk, auto& pin) {
          constexpr int k = 12 * hh.value + kk.value, t = k >> 2;
          if constexpr ((k & 3) == 0) setm0(area + ((t + 6) % 10) * 4096);
          dmap<(k & 3) * 1024>(rsW2, v2[k & 3], so2 + t * (32 * S * 2), pin);
        });
      });
    }
    u32x4 fq[2][4];
#pragma unroll
    for (int c = 0; c < 8; c++) {
      const unsigned* s = &phq[c >> 2][(c >> 1) & 1][4 * (c & 1)];
      fq[c >> 2][c & 3] = (u32x4){s[0], s[1], s[2], s[3]};
      tie(fq[c >> 2][c & 3]);
    }
    STAMP(10);
    {
      const u32x4 rsK = mkrs(ws + WS_KVF, SZ_KVF);
      const unsigned so = pr * 16384 + w * 2048, tagB = ep.lo ^ ep.hi, vk = lane * 16;
      const ldsp px = lds + 9 * 4096 + lane * 16;
      bf16x8 g0, g1, g2, g3;
      auto fetch = [&]() { g0 = ldc<0>(rsK, vk, so); g1 = ldc<1024>(rsK, vk, so); g2 = ldc<0>(rsK, vk, so + 8192); g3 = ldc<1024>(rsK, vk, so + 8192); };
      auto check = [&]() {
        const u32x4 u0 = __builtin_bit_cast(u32x4, g0), u1 = __builtin_bit_cast(u32x4, g1), u2 = __builtin_bit_cast(u32x4, g2), u3 = __builtin_bit_cast(u32x4, g3);
        lst(px + w * AREA, (u32x4){u0[0], u0[2], u1[0], u1[2]});
        lst(px + w * AREA + 1024, (u32x4){u2[0], u2[2], u3[0], u3[2]});
        const unsigned bad = ((u0[1] ^ ep.lo) | (u1[1] ^ ep.lo) | (u2[1] ^ ep.lo) | (u3[1] ^ ep.lo)) | ((u0[3] ^ tagB) | (u1[3] ^ tagB) | (u2[3] ^ tagB) | (u3[3] ^ tagB));
        return (bool)__all(bad == 0);
      };
      waitv<8>();
      pre2rd();
      pre2(6, 9, 2);
      unroll<0, FW>(ldwo);
      fetch();
      unroll<FW, 14>(ldwo);
      waitv<14 - FW>(g0, g1, g2, g3);
      if (!check()) {
#pragma unroll 1
        for (int it = 0; it < SPIN; ++it) {
          fetch();
          waitv<0>(g0, g1, g2, g3);
          if (check()) break;
        }
      }
      STAMP(2);
      tie(sink);
      pre2w();
      bar();
      STAMP(11);
      bf16x8 kf[2][4];
#pragma unroll
      for (int hd = 0; hd < 2; hd++)
#pragma unroll
        for (int c = 0; c < 4; c++) kf[hd][c] = lld<bf16x8>(px + c * AREA + hd * 1024);
      SB();
      dma4(rsW2, v2, so2 + 9 * (32 * S * 2), area + 5 * 4096);
      dma4(rsW2, v2, so2 + 10 * (32 * S * 2), area + 6 * 4096);
      setm0(area + 7 * 4096);
      SB();
      f32x16 o[2];
      unroll<0, 8>([&](auto mm) {
        constexpr int m = mm.value, hd = m & 1, c = m >> 1;
        if constexpr (c == 0) mfma0(o[hd], kf[hd][c], fq[hd][c]); else mfmac(o[hd], kf[hd][c], fq[hd][c]);
        dma<(m & 3) * 1024>(rsW2, v2[m & 3], so2 + (11 + (m >> 2)) * (32 * S * 2));
        if constexpr (m == 3) setm0n(area + 8 * 4096);
        SB();
      });
      asm volatile("s_nop 11" : "+v"(o[0]), "+v"(o[1]));
      float da = o[0][12], db = o[1][12];
      swap32(da, db);
      float ia = __builtin_amdgcn_rcpf(db + 1e-6f), ib = ia;
      swap32(ia, ib);
      unsigned a[8], bq[8];
#pragma unroll
      for (int r = 0; r < 8; r++) { a[r] = pk(o[0][2 * r] * ia, o[0][2 * r + 1] * ia); bq[r] = pk(o[1][2 * r] * ib, o[1][2 * r + 1] * ib); }
      const rsrc_t st = mkst(ws + WS_OF, SZ_OF);
      const unsigned tk = (128 * T + 32 * w + n) * 16, jb = 14 * (pr >> 1);
      const unsigned vo = (jb + 6 * (pr & 1) + h) * (S * 16) + tk;
      const unsigned v1 = vo + 2 * S * 16, v2_ = vo + 4 * S * 16, v3 = h ? OOB : (jb + 12 + (pr & 1)) * (S * 16) + tk;
      const u32x4 r0 = {a[0], a[1], a[2], a[3]}, r1 = {a[4], a[5], bq[0], bq[1]}, r2 = {bq[2], bq[3], bq[4], bq[5]}, r3 = {a[6], a[7], bq[6], bq[7]};
      stc(st, vo, r0); stc(st, v1, r1); stc(st, v2_, r2); stc(st, v3, r3);
      STAMP(12);
      u32x2 f0 = ldc2(rsF, vfu);
      const unsigned vfo = tid == 0 ? (FL_O + T * 8 + pr) * 128 : OOB;
      publish<0>(rsF, vfo, epv);
      tie(f0);
      tiewo();
      hold(vk);
      hold(vo, v1, v2_, v3, r0, r1, r2, r3);
      miss = missing<4>(f0, ep);
      td = 13;
      hold(vfo, epv);
    }
  }

  if (i >= 4) {
    const unsigned orow = st2 * 64 + 16 * w + (lane & 15);
    const unsigned vo = orow * (D * 2) + dt2 * 128 + (lane >> 4) * 16;
    const u32x4 rsB = mkrs(b2, S * 2), rsU = mkrs(ws + WS_UF, SZ_UF), rsO = mkrs(ws + WS_OF, SZ_OF);
#pragma unroll 1
    for (; miss && td < 13; td += 2) {
      u32x2 f = ldc2(rsF, vfu);
      dma4(rsW2, v2, so2 + td * (32 * S * 2), area + (td - 4) * 4096);
      dma4(rsW2, v2, so2 + (td + 1) * (32 * S * 2), area + (td - 3) * 4096);
      waitv<8>(f);
      miss = missing<4>(f, ep);
    }
    if (miss) poll<4>(rsF, vfu, ep);
    STAMP(13);
    const unsigned vu0 = h * (D * 16) + (dt2 * 64 + n) * 16, vu1 = vu0 + 2 * D * 16, sou = w * 56 * (D * 16);
    const unsigned vof = h * (S * 16) + (st2 * 64 + n) * 16, soo = w * 14 * (S * 16);
    const unsigned vfl = lane < 2 ? (FL_O + (st2 >> 1) * 8 + 2 * w + lane) * 128 : OOB;
    bf16x8 fa[18][4], xr[2];
    unsigned bs, one, sel[4], pr_[2];
    float bv, yv[16];
    const unsigned vb2 = orow * 2;
    u32x2 fl;
    auto ldD = [&](auto tt, auto jj) {
      constexpr int t = tt.value, j = jj.value;
      if constexpr (t < 14) fa[t][j] = ldc<(j & 1) * 512>(rsU, (j >> 1) ? vu1 : vu0, sou + t * (4 * D * 16));
      else if constexpr (j < g2ld(t)) fa[t][j] = ldc<(j & 1) * 512>(rsO, vof, soo + (2 * (t - 14) + (j >> 1)) * (2 * S * 16));
    };
    unroll<0, G2R>([&](auto tt) { unroll<0, 4>([&](auto jj) { ldD(tt, jj); }); });
    dman(rsW2, v2, so2 + td * (32 * S * 2), 32 * S * 2, area + (td - 4) * 4096, 14 - td);
    const ldsp pw[2] = {pb[0] - h * 512, pb[1] - h * 512};
    f32x16 acc[2][2] = {};
    bf16x8 wb[19][2][2];
    auto rdB = [&](auto tt, auto kk, auto aa) {
      constexpr int t = tt.value, ks = kk.value, a = aa.value, j = 2 * (t - 14) + ks;
      if constexpr (t < 4) wb[t][ks][a] = w2r[t][ks][a];
      else if constexpr (t < 14) wb[t][ks][a] = cat(vtr(pb[a] + (t - 4) * 4096 + ks * 2048), vtr(pb[a] + (t - 4) * 4096 + ks * 2048 + 512));
      else if constexpr (j < 6) {
        constexpr int u = j % 3, rl = 56 * (j / 3) + (u == 0 ? 0 : u == 1 ? 16 : 36), rh = 56 * (j / 3) + (u == 0 ? 8 : u == 1 ? 28 : 44);
        wb[t][ks][a] = cat(vtr(pw[a] + rl * 128), vtr(pw[a] + rh * 128));
      } else if constexpr (j == 6) wb[t][ks][a] = cat(vtr(pw[a] + h * (52 * 128) + 24 * 128), vtr(pw[a] + h * (52 * 128) + 52 * 128));
    };
    unroll<0, 4>([&](auto cc) { rdB(IC<0>(), IC<(cc.value >> 1)>(), IC<(cc.value & 1)>()); });
    unroll<0, 18>([&](auto tt) {
      constexpr int t = tt.value, nk = t < 17 ? 2 : 1;
      constexpr int left = g2cnt(t - 3) + g2cnt(t - 2) + g2cnt(t - 1);
      if constexpr (nk == 2) waitv<left>(fa[t][0], fa[t][1], fa[t][2], fa[t][3]); else waitv<left>(fa[t][0], fa[t][1]);
      if constexpr (t == 3) hold(vfu);
      if constexpr (t == 14) { one = ((lane >> 3) & 1) == blk ? ((lane & 1) ? 0x3f800000u : 0x3f80u) : 0u; tie(one); }
      if constexpr (t == 16) { tie(xr[0]); tie(xr[1]); tie(bs); hold(vb2, vof); }
      if constexpr (t == G2POLL + G2R) {
        tie(fl);
        if (missing<2>(fl, ep)) poll<2>(rsF, vfl, ep);
      }
      SB();
      unroll<0, 4 * nk>([&](auto mm) {
        constexpr int m = mm.value, ks = m >> 2, nh = (m >> 1) & 1, mh = m & 1;
        if constexpr (t < 14) acc[nh][mh] = MFMA(fa[t][ks * 2 + mh], wb[t][ks][nh], acc[nh][mh]);
        else acc[nh][mh] = MFMA(wb[t][ks][mh], fa[t][ks * 2 + nh], acc[nh][mh]);
        SB();
        if constexpr (m & 1) rdB(IC<t + 1>(), IC<(m >> 2)>(), IC<((m >> 1) & 1)>());
        else if constexpr (t >= 5 && 4 * (t - 5) + m / 2 < 14) sta<(4 * (t - 5) + m / 2) * 1024>(area + lane * 16, wor[4 * (t - 5) + m / 2]);
        SB();
        if constexpr (!(m & 1)) ldD(IC<t + G2R>(), IC<m / 2>());
        if constexpr (t == G2POLL && m == 7) fl = ldc2(rsF, vfl);
        if constexpr (t == 12 && m == 0) xr[0] = ldp<0>(rsX, vo, 0);
        if constexpr (t == 12 && m == 2) xr[1] = ldp<64>(rsX, vo, 0);
        if constexpr (t == 12 && m == 4) bs = ldu16<0>(rsB, vb2, 0);
        if constexpr (t == 15 && m < 4) { sel[m] = ((lane & 7) >> 1) == m ? one : 0u; tie(sel[m]); }
        if constexpr (t == 15 && m >= 6) { pr_[m - 6] = lds0 + (h + 2 * (m - 6)) * AREA + (w >> 1) * 4096 + blk * 1024 + (16 * (w & 1) + (lane & 15)) * 16; tie(pr_[m - 6]); }
        if constexpr (t == 16 && m == 0) { bv = blo(bs); tie(bv); }
        if constexpr (t == 16) {
          const u32x4 xu = __builtin_bit_cast(u32x4, xr[m >> 2]);
          const unsigned xe = xu[m & 3];
          yv[2 * m] = blo(xe) + bv; yv[2 * m + 1] = bhi(xe) + bv;
          tie(yv[2 * m]); tie(yv[2 * m + 1]);
        }
        SB();
      });
    });
    STAMP(14);
    bf16x8 pt[8];
#pragma unroll
    for (int c = 0; c < 8; c++) {
      pt[c] = pack8s(acc[c >> 2][(c >> 1) & 1], 8 * (c & 1));
      lst(lds + w * AREA + c * 1024 + lane * 16, pt[c]);
      if (c) hold(pt[c - 1]);
      SB();
    }
    f32x4 y[2][2];
#pragma unroll
    for (int c = 0; c < 4; c++) y[c >> 1][c & 1] = (f32x4){yv[4 * c], yv[4 * c + 1], yv[4 * c + 2], yv[4 * c + 3]};
    u32x4 id = {sel[0], sel[1], sel[2], sel[3]};
    asm volatile("s_waitcnt lgkmcnt(0)\n\ts_barrier" : "+v"(y[0][0]), "+v"(y[0][1]), "+v"(y[1][0]), "+v"(y[1][1]), "+v"(id)::"memory");
#pragma unroll
    for (int c = 0; c < 8; c++) { pt[c] = lld<bf16x8>((ldsp)(uintptr_t)pr_[c >> 2] + ((c >> 1) & 1) * 2048 + (c & 1) * 512); SB(); }
#pragma unroll
    for (int c = 0; c < 8; c++) asm volatile("v_mfma_f32_16x16x32_bf16 %0, %1, %2, %0" : "+v"(y[(c >> 1) & 1][c & 1]) : "v"(id), "v"(pt[c]));
    asm volatile("s_nop 7" : "+v"(y[0][0]), "+v"(y[0][1]), "+v"(y[1][0]), "+v"(y[1][1]));
    u32x4 res[2];
    const rsrc_t sto = mkst(out, S * D * 2);
#pragma unroll
    for (int u = 0; u < 2; u++) {
      res[u] = (u32x4){pk(y[u][0][0], y[u][0][1]), pk(y[u][0][2], y[u][0][3]), pk(y[u][1][0], y[u][1][1]), pk(y[u][1][2], y[u][1][3])};
      __builtin_amdgcn_raw_buffer_store_b128(res[u], sto, vo + u * 64, 0, 16);
    }
    if (blockIdx.x == 255 && tid == 0) __builtin_amdgcn_raw_buffer_store_b64((u32x2){ep.lo, ep.hi}, mkst(ws, WS_FLAG_BYTES), 0, 0, 16);
    hold(res[0], res[1], vo);
  }
}

extern "C" void kernel_launch(void* const* d_in, const int* in_sizes, int n_in, void* d_out, int out_size, void* d_ws, size_t ws_size,
                              hipStream_t stream) {
  fused_kernel<<<256, 256, 0, stream>>>((const short*)d_in[0], (const short*)d_in[1], (const short*)d_in[2], (const short*)d_in[3],
                                        (const short*)d_in[5], (const short*)d_in[6], (const short*)d_in[7], (const short*)d_in[8],
                                        (short*)d_out, (char*)d_ws);
}
```

```cpp
#include <hip/hip_runtime.h>
#include <stdint.h>

typedef short bf16x8 __attribute__((ext_vector_type(8)));
typedef short s16x4 __attribute__((ext_vector_type(4)));
typedef float f32x2 __attribute__((ext_vector_type(2)));
typedef float f32x4 __attribute__((ext_vector_type(4)));
typedef float f32x16 __attribute__((ext_vector_type(16)));
typedef unsigned u32x2 __attribute__((ext_vector_type(2)));
typedef unsigned u32x4 __attribute__((ext_vector_type(4)));
typedef unsigned u32x8 __attribute__((ext_vector_type(8)));
typedef __bf16 b16x2 __attribute__((ext_vector_type(2)));
typedef __attribute__((address_space(3))) char* ldsp;
typedef __amdgpu_buffer_rsrc_t rsrc_t;

#define DEV __device__ __forceinline__
#define SB() __builtin_amdgcn_sched_barrier(0)
#define MFMA(a, b, c) __builtin_amdgcn_mfma_f32_32x32x16_bf16(a, b, c, 0, 0, 0)

constexpr int S = 2048, D = 448, F = 1792;
constexpr unsigned FL_P = 8, FL_O = FL_P + 128, FL_U = FL_O + 128;
constexpr unsigned WS_FLAG_BYTES = 65536;
constexpr unsigned WS_ST = WS_FLAG_BYTES, SZ_ST = S * 16;
constexpr unsigned WS_UF = WS_ST + SZ_ST, SZ_UF = 224 * 448 * 16;
constexpr unsigned WS_OF = WS_UF + SZ_UF, SZ_OF = 56 * 2048 * 16;
constexpr unsigned WS_KVP = WS_OF + SZ_OF, SZ_KVP = 16 * 16 * 4096;
constexpr unsigned WS_KVF = WS_KVP + SZ_KVP, SZ_KVF = 16 * 8 * 1024;
constexpr unsigned WS_END = WS_KVF + SZ_KVF;
constexpr unsigned AREA = 40960, L_STAT = 36864, SLOT = 12288;
constexpr unsigned L_WK = 0, L_WV = 50176, L_WQ = 100352, L_WPHI = 150528, L_PHI = 0, L_VX = 32768;
constexpr unsigned OOB = 0x40000000u;
constexpr int SPIN = 1 << 18;

DEV u32x4 mkrs(const void* p, unsigned bytes) {
  unsigned long long a = (unsigned long long)p;
  u32x4 r = {(unsigned)a, (unsigned)(a >> 32), bytes, 0x00020000u};
  return r;
}
DEV rsrc_t mkst(void* p, unsigned bytes) { return __builtin_amdgcn_make_buffer_rsrc(p, 0, bytes, 0x00020000); }
DEV void stc(rsrc_t rs, unsigned voff, u32x4 v) { __builtin_amdgcn_raw_buffer_store_b128(v, rs, voff, 0, 16); }
template <int OFF> DEV bf16x8 ldp(u32x4 rs, unsigned voff, unsigned soff) {
  bf16x8 r;
  asm volatile("buffer_load_dwordx4 %0, %2, %1, %3 offen offset:%4" : "=v"(r) : "s"(rs), "v"(voff), "s"(soff), "n"(OFF) : "memory");
  return r;
}
template <int OFF> DEV bf16x8 ldc(u32x4 rs, unsigned voff, unsigned soff) {
  bf16x8 r;
  asm volatile("buffer_load_dwordx4 %0, %2, %1, %3 offen offset:%4 sc1" : "=v"(r) : "s"(rs), "v"(voff), "s"(soff), "n"(OFF) : "memory");
  return r;
}
template <int OFF> DEV u32x2 ldp2(u32x4 rs, unsigned voff, unsigned soff) {
  u32x2 r;
  asm volatile("buffer_load_dwordx2 %0, %2, %1, %3 offen offset:%4" : "=v"(r) : "s"(rs), "v"(voff), "s"(soff), "n"(OFF) : "memory");
  return r;
}
template <int OFF> DEV u32x4 lda(u32x4 rs, unsigned voff, unsigned soff) {
  u32x4 r;
  asm volatile("buffer_load_dwordx4 %0, %2, %1, %3 offen offset:%4" : "=a"(r) : "s"(rs), "v"(voff), "s"(soff), "n"(OFF) : "memory");
  return r;
}
template <int OFF> DEV void sta(unsigned addr, u32x4 v) { asm volatile("ds_write_b128 %0, %1 offset:%2" ::"v"(addr), "a"(v), "n"(OFF) : "memory"); }
DEV u32x2 ldc2(u32x4 rs, unsigned voff) {
  u32x2 r;
  asm volatile("buffer_load_dwordx2 %0, %2, %1, 0 offen sc1" : "=v"(r) : "s"(rs), "v"(voff) : "memory");
  return r;
}
template <int OFF> DEV unsigned ldu16(u32x4 rs, unsigned voff, unsigned soff) {
  unsigned r;
  asm volatile("buffer_load_ushort %0, %2, %1, %3 offen offset:%4" : "=v"(r) : "s"(rs), "v"(voff), "s"(soff), "n"(OFF) : "memory");
  return r;
}
DEV void setm0(unsigned a) { asm volatile("s_mov_b32 m0, %0\n\ts_nop 0" ::"s"(a) : "memory"); }
DEV void setm0n(unsigned a) { asm volatile("s_mov_b32 m0, %0" ::"s"(a) : "memory"); }
template <int OFF> DEV void addm0(unsigned a) { asm volatile("s_add_u32 m0, %0, %1" ::"s"(a), "n"(OFF) : "memory", "scc"); }
template <int OFF> DEV void dma(u32x4 rs, unsigned voff, unsigned soff) {
  asm volatile("buffer_load_dwordx4 %1, %0, %2 offen offset:%3 lds" ::"s"(rs), "v"(voff), "s"(soff), "n"(OFF) : "memory");
}
DEV void dma4(u32x4 rs, const unsigned (&v)[4], unsigned soff, unsigned m0) {
  setm0(m0); dma<0>(rs, v[0], soff); dma<1024>(rs, v[1], soff); dma<2048>(rs, v[2], soff); dma<3072>(rs, v[3], soff);
}
template <int N> DEV void waitv() { asm volatile("s_waitcnt vmcnt(%0)" ::"n"(N) : "memory"); }
template <int N, class T> DEV void waitv(T& a) { asm volatile("s_waitcnt vmcnt(%1)" : "+v"(a) : "n"(N) : "memory"); }
template <int N, class T> DEV void waitv(T& a, T& b) { asm volatile("s_waitcnt vmcnt(%2)" : "+v"(a), "+v"(b) : "n"(N) : "memory"); }
template <int N, class T> DEV void waitv(T& a, T& b, T& c, T& d) {
  asm volatile("s_waitcnt vmcnt(%4)" : "+v"(a), "+v"(b), "+v"(c), "+v"(d) : "n"(N) : "memory");
}
template <class T> DEV void tie(T& a) { asm volatile("" : "+v"(a)::"memory"); }
DEV void tiea(u32x4& a) { asm volatile("" : "+a"(a)::"memory"); }
template <class T> DEV void hold(T a) { asm volatile("" ::"v"(a)); }
template <class T, class... R> DEV void hold(T a, R... r) { hold(a); hold(r...); }
DEV void keep(f32x16 a) { asm volatile("" ::"v"(a)); }
DEV void bar() { asm volatile("s_waitcnt lgkmcnt(0)\n\ts_barrier" ::: "memory"); }
DEV void barv() { asm volatile("s_waitcnt vmcnt(0) lgkmcnt(0)\n\ts_barrier" ::: "memory"); }
DEV s16x4 vtr(ldsp p) { return __builtin_amdgcn_ds_read_tr16_b64_v4i16((__attribute__((address_space(3))) s16x4*)p); }
DEV bf16x8 cat(s16x4 a, s16x4 b) { return __builtin_shufflevector(a, b, 0, 1, 2, 3, 4, 5, 6, 7); }
DEV unsigned pk(float a, float b) {
  f32x2 v = {a, b};
  return __builtin_bit_cast(unsigned, __builtin_convertvector(v, b16x2));
}
DEV float blo(unsigned u) { return __builtin_bit_cast(float, u << 16); }
DEV float bhi(unsigned u) { return __builtin_bit_cast(float, u & 0xffff0000u); }
DEV void swap32(float& a, float& b) {
  auto r = __builtin_amdgcn_permlane32_swap(__builtin_bit_cast(unsigned, a), __builtin_bit_cast(unsigned, b), false, false);
  const unsigned r0 = r[0], r1 = r[1];
  a = __builtin_bit_cast(float, r0); b = __builtin_bit_cast(float, r1);
}
DEV void swap16(float& a, float& b) {
  auto r = __builtin_amdgcn_permlane16_swap(__builtin_bit_cast(unsigned, a), __builtin_bit_cast(unsigned, b), false, false);
  const unsigned r0 = r[0], r1 = r[1];
  a = __builtin_bit_cast(float, r0); b = __builtin_bit_cast(float, r1);
}
template <int C> DEV float ror(float z) {
  return __builtin_bit_cast(float, __builtin_amdgcn_update_dpp(0, __builtin_bit_cast(int, z), C, 0xf, 0xf, true));
}
DEV float rowsum(float z) { z += ror<0x128>(z); z += ror<0x124>(z); z += ror<0x122>(z); z += ror<0x121>(z); return z; }
DEV float rdl(float z, int l) { return __builtin_bit_cast(float, __builtin_amdgcn_readlane(__builtin_bit_cast(int, z), l)); }
template <class T> DEV void lst(ldsp p, T v) { *(__attribute__((address_space(3))) T*)p = v; }
template <class T> DEV T lld(ldsp p) { return *(__attribute__((address_space(3))) T*)p; }

struct Epoch { unsigned lo, hi; };
template <int N> DEV void publish(u32x4 rsF, unsigned vflag, u32x2 e) {
  asm volatile("s_waitcnt vmcnt(%3) lgkmcnt(0)\n\ts_barrier\n\tbuffer_store_dwordx2 %2, %1, %0, 0 offen sc1" ::"s"(rsF), "v"(vflag), "v"(e), "n"(N) : "memory");
}
template <int N> DEV unsigned missing(u32x2 f, Epoch e) {
  unsigned r;
  asm volatile("v_cmp_ne_u64 vcc, %2, %1\n\ts_and_b32 %0, vcc_lo, %3" : "=s"(r) : "v"(f), "s"(((unsigned long long)e.hi << 32) | e.lo), "n"((1u << N) - 1) : "vcc", "scc");
  return r;
}
template <int N> DEV void poll(u32x4 rsF, unsigned voff, Epoch e) {
#pragma unroll 1
  for (int it = 0; it < SPIN; ++it) {
    u32x2 f = ldc2(rsF, voff);
    waitv<0>(f);
    if (!missing<N>(f, e)) break;
    __builtin_amdgcn_s_sleep(1);
  }
}
DEV void collect(bf16x8 (&p)[16], u32x4 rsF, unsigned vf, u32x4 rsP, unsigned vp, unsigned so, Epoch e) {
  unsigned done = 0, it = SPIN, nw, t;
  u32x2 f;
  asm volatile("1:\n\tbuffer_load_dwordx2 %[f], %[vf], %[rsF], 0 offen sc1\n\ts_waitcnt vmcnt(0)\n\tv_cmp_eq_u64 vcc, %[e], %[f]\n\t"
               "s_lshr_b32 %[nw], vcc_lo, 1\n\ts_and_b32 %[nw], %[nw], vcc_lo\n\ts_and_b32 %[nw], %[nw], 0x5555\n\t"
               "s_andn2_b32 %[nw], %[nw], %[done]\n\ts_or_b32 %[done], %[done], %[nw]\n\t"
               "s_bitcmp1_b32 %[nw], 0\n\ts_cbranch_scc0 10f\n\ts_add_u32 %[t], %[so], 0\n\t"
               "buffer_load_dwordx4 %0, %[vp], %[rsP], %[t] offen sc1\n\tbuffer_load_dwordx4 %1, %[vp], %[rsP], %[t] offen offset:512 sc1\n10:\n\t"
               "s_bitcmp1_b32 %[nw], 2\n\ts_cbranch_scc0 11f\n\ts_add_u32 %[t], %[so], 8192\n\t"
               "buffer_load_dwordx4 %2, %[vp], %[rsP], %[t] offen sc1\n\tbuffer_load_dwordx4 %3, %[vp], %[rsP], %[t] offen offset:512 sc1\n11:\n\t"
               "s_bitcmp1_b32 %[nw], 4\n\ts_cbranch_scc0 12f\n\ts_add_u32 %[t], %[so], 16384\n\t"
               "buffer_load_dwordx4 %4, %[vp], %[rsP], %[t] offen sc1\n\tbuffer_load_dwordx4 %5, %[vp], %[rsP], %[t] offen offset:512 sc1\n12:\n\t"
               "s_bitcmp1_b32 %[nw], 6\n\ts_cbranch_scc0 13f\n\ts_add_u32 %[t], %[so], 24576\n\t"
               "buffer_load_dwordx4 %6, %[vp], %[rsP], %[t] offen sc1\n\tbuffer_load_dwordx4 %7, %[vp], %[rsP], %[t] offen offset:512 sc1\n13:\n\t"
               "s_bitcmp1_b32 %[nw], 8\n\ts_cbranch_scc0 14f\n\ts_add_u32 %[t], %[so], 32768\n\t"
               "buffer_load_dwordx4 %8, %[vp], %[rsP], %[t] offen sc1\n\tbuffer_load_dwordx4 %9, %[vp], %[rsP], %[t] offen offset:512 sc1\n14:\n\t"
               "s_bitcmp1_b32 %[nw], 10\n\ts_cbranch_scc0 15f\n\ts_add_u32 %[t], %[so], 40960\n\t"
               "buffer_load_dwordx4 %10, %[vp], %[rsP], %[t] offen sc1\n\tbuffer_load_dwordx4 %11, %[vp], %[rsP], %[t] offen offset:512 sc1\n15:\n\t"
               "s_bitcmp1_b32 %[nw], 12\n\ts_cbranch_scc0 16f\n\ts_add_u32 %[t], %[so], 49152\n\t"
               "buffer_load_dwordx4 %12, %[vp], %[rsP], %[t] offen sc1\n\tbuffer_load_dwordx4 %13, %[vp], %[rsP], %[t] offen offset:512 sc1\n16:\n\t"
               "s_bitcmp1_b32 %[nw], 14\n\ts_cbranch_scc0 17f\n\ts_add_u32 %[t], %[so], 57344\n\t"
               "buffer_load_dwordx4 %14, %[vp], %[rsP], %[t] offen sc1\n\tbuffer_load_dwordx4 %15, %[vp], %[rsP], %[t] offen offset:512 sc1\n17:\n\t"
               "s_sub_u32 %[it], %[it], 1\n\ts_cbranch_scc1 2f\n\ts_cmp_lg_u32 %[done], 0x5555\n\ts_cbranch_scc1 1b\n2:\n\ts_waitcnt vmcnt(0)"
               : "=&v"(p[0]), "=&v"(p[1]), "=&v"(p[2]), "=&v"(p[3]), "=&v"(p[4]), "=&v"(p[5]), "=&v"(p[6]), "=&v"(p[7]), "=&v"(p[8]), "=&v"(p[9]), "=&v"(p[10]), "=&v"(p[11]), "=&v"(p[12]), "=&v"(p[13]), "=&v"(p[14]), "=&v"(p[15]),
                 [f] "=&v"(f), [done] "+&s"(done), [it] "+&s"(it), [nw] "=&s"(nw), [t] "=&s"(t)
               : [vf] "v"(vf), [rsF] "s"(rsF), [vp] "v"(vp), [rsP] "s"(rsP), [so] "s"(so), [e] "s"(((unsigned long long)e.hi << 32) | e.lo)
               : "memory", "scc", "vcc");
}
DEV void dman(u32x4 rs, const unsigned (&v)[4], unsigned soff, unsigned step, unsigned m0, unsigned n) {
  asm volatile("s_cmp_eq_u32 %1, 0\n\ts_cbranch_scc1 2f\n\ts_mov_b32 m0, %8\n1:\n\ts_sub_u32 %1, %1, 1\n\t"
               "buffer_load_dwordx4 %3, %2, %0 offen lds\n\tbuffer_load_dwordx4 %4, %2, %0 offen offset:1024 lds\n\t"
               "buffer_load_dwordx4 %5, %2, %0 offen offset:2048 lds\n\tbuffer_load_dwordx4 %6, %2, %0 offen offset:3072 lds\n\t"
               "s_add_u32 m0, m0, 0x1000\n\ts_add_u32 %0, %0, %7\n\ts_cmp_lg_u32 %1, 0\n\ts_cbranch_scc1 1b\n2:"
               : "+&s"(soff), "+&s"(n)
               : "s"(rs), "v"(v[0]), "v"(v[1]), "v"(v[2]), "v"(v[3]), "s"(step), "s"(m0)
               : "memory", "scc");
}

DEV void acc_stats(bf16x8 v, float& s, float& q) {
  const u32x4 u = __builtin_bit_cast(u32x4, v);
#pragma unroll
  for (int e = 0; e < 4; e++) {
    const unsigned t = u[e];
    const b16x2 p = __builtin_bit_cast(b16x2, t);
    s = __builtin_amdgcn_fdot2_f32_bf16(p, __builtin_bit_cast(b16x2, 0x3f803f80u), s, false);
    q = __builtin_amdgcn_fdot2_f32_bf16(p, p, q, false);
  }
}
DEV unsigned affine2(unsigned u, float a, float c) {
  float t0, t1;
  unsigned r;
  asm("v_lshlrev_b32 %1, 16, %3\n\tv_and_b32 %2, 0xffff0000, %3\n\tv_fma_f32 %1, %1, %4, %5\n\tv_fma_f32 %2, %2, %4, %5\n\tv_cvt_pk_bf16_f32 %0, %1, %2"
      : "=v"(r), "=&v"(t0), "=&v"(t1)
      : "v"(u), "v"(a), "v"(c));
  return r;
}
DEV f32x16 mfmav(u32x4 a, bf16x8 b, f32x16 c) {
  f32x16 r;
  asm volatile("v_mfma_f32_32x32x16_bf16 %0, %1, %2, %3" : "=&v"(r) : "v"(a), "v"(b), "v"(c));
  return r;
}
DEV void mfmac(f32x16& c, bf16x8 a, u32x4 b) { asm volatile("v_mfma_f32_32x32x16_bf16 %0, %1, %2, %0" : "+v"(c) : "v"(a), "v"(b)); }
DEV void mfma0(f32x16& c, bf16x8 a, u32x4 b) { asm volatile("v_mfma_f32_32x32x16_bf16 %0, %1, %2, 0" : "=&v"(c) : "v"(a), "v"(b)); }
template <bool first> DEV void mfmab(f32x16& c, bf16x8 a, bf16x8 b) {
  if constexpr (first) mfma0(c, a, __builtin_bit_cast(u32x4, b)); else mfmac(c, a, __builtin_bit_cast(u32x4, b));
}
DEV void touch(unsigned& sink, u32x4 rs, unsigned voff, unsigned soff) {
  asm volatile("buffer_load_dword %0, %2, %1, %3 offen" : "+v"(sink) : "s"(rs), "v"(voff), "s"(soff) : "memory");
}
template <class T> DEV void touchp(unsigned& sink, u32x4 rs, unsigned voff, unsigned soff, T& pin) {
  asm volatile("buffer_load_dword %0, %3, %2, %4 offen" : "+v"(sink), "+v"(pin) : "s"(rs), "v"(voff), "s"(soff) : "memory");
}
DEV void touchl(u32x4 rs, unsigned voff, unsigned soff) {
  asm volatile("buffer_load_dword %1, %0, %2 offen lds" ::"s"(rs), "v"(voff), "s"(soff) : "memory");
}
template <int N> using IC = std::integral_constant<int, N>;
template <int T, int N, class Fn> DEV void unroll(Fn&& f) {
  if constexpr (T < N) { f(std::integral_constant<int, T>()); unroll<T + 1, N>(f); }
}
template <int OFF, class T> DEV void dmap(u32x4 rs, unsigned voff, unsigned soff, T& pin) {
  asm volatile("buffer_load_dwordx4 %2, %1, %3 offen offset:%4 lds" : "+v"(pin) : "s"(rs), "v"(voff), "s"(soff), "n"(OFF) : "memory");
}

DEV void minmax4(float& mx, float& mn, float x0, float n0, float a, float b, float c, float d) {
  asm("v_max3_f32 %0, %2, %4, %5\n\tv_min3_f32 %1, %3, %4, %5\n\tv_max3_f32 %0, %0, %6, %7\n\tv_min3_f32 %1, %1, %6, %7"
      : "=&v"(mx), "=&v"(mn)
      : "v"(x0), "v"(n0), "v"(a), "v"(b), "v"(c), "v"(d));
}
template <class Fn> DEV void features(const f32x16& p, bool h, unsigned (&ph)[2][8], Fn&& cb) {
  constexpr float L = 1.4426950408889634f;
  float x0, n0, x1, n1, mx, mn, mx2, mn2;
  minmax4(x0, n0, p[0], p[0], p[0], p[1], p[2], p[3]);
  minmax4(x1, n1, x0, n0, p[4], p[5], p[6], p[7]);
  minmax4(mx, mn, x1, n1, p[8], p[9], p[10], p[11]);
  cb(IC<0>(), mx);
  minmax4(mx2, mn2, mx, mn, p[12], p[13], p[14], p[15]);
  mx = h ? mx : mx2; mn = h ? -mn : -mn2;
  swap32(mx, mn);
  float c0 = fminf(-L * mx, -L * mn), c1 = c0;
  cb(IC<1>(), c0);
  swap32(c0, c1);
  const float ninf = -__builtin_inff();
  const float d0 = h ? ninf : c0, d1 = h ? ninf : c1;
  f32x2 e[2][8], s0, s1;
  unroll<0, 8>([&](auto rr) {
    constexpr int r = rr.value;
    const f32x2 v = {p[2 * r], p[2 * r + 1]};
    const f32x2 a0 = __builtin_elementwise_fma(v, (f32x2)(L), (f32x2)(r < 6 ? c0 : d0));
    const f32x2 a1 = __builtin_elementwise_fma(v, (f32x2)(-L), (f32x2)(r < 6 ? c1 : d1));
    e[0][r] = (f32x2){__builtin_amdgcn_exp2f(a0.x), __builtin_amdgcn_exp2f(a0.y)};
    e[1][r] = (f32x2){__builtin_amdgcn_exp2f(a1.x), __builtin_amdgcn_exp2f(a1.y)};
    if constexpr (r == 0) { s0 = e[0][0]; s1 = e[1][0]; } else { s0 += e[0][r]; s1 += e[1][r]; }
    cb(IC<2 + r>(), s1);
    SB();
  });
  float t0 = s0.x + s0.y, t1 = s1.x + s1.y;
  swap32(t0, t1);
  float i0 = __builtin_amdgcn_rcpf(t0 + t1), i1 = i0;
  swap32(i0, i1);
  unroll<0, 8>([&](auto rr) {
    constexpr int r = rr.value;
    const f32x2 u0 = e[0][r] * i0, u1 = e[1][r] * i1;
    ph[0][r] = pk(u0.x, u0.y); ph[1][r] = pk(u1.x, u1.y);
    if constexpr ((r & 3) == 1) cb(IC<10 + r / 4>(), ph[1][r]);
  });
}
DEV bf16x8 pack8(const f32x16& a, int o) {
  u32x4 u = {pk(a[o], a[o + 1]), pk(a[o + 2], a[o + 3]), pk(a[o + 4], a[o + 5]), pk(a[o + 6], a[o + 7])};
  return __builtin_bit_cast(bf16x8, u);
}
DEV bf16x8 pack8s(const f32x16& a, int o) {
  float t0 = a[o], t1 = a[o + 1], t2 = a[o + 2], t3 = a[o + 3], t4 = a[o + 4], t5 = a[o + 5], t6 = a[o + 6], t7 = a[o + 7];
  asm volatile("" : "+v"(t0), "+v"(t1), "+v"(t2), "+v"(t3), "+v"(t4), "+v"(t5), "+v"(t6), "+v"(t7));
  u32x4 u = {pk(t0, t1), pk(t2, t3), pk(t4, t5), pk(t6, t7)};
  return __builtin_bit_cast(bf16x8, u);
}
DEV f32x2 gelu2(f32x2 a) {
  constexpr float c1 = -2.302208198f, c2 = c1 * 0.044715f;
  const f32x2 z = a * __builtin_elementwise_fma(a * a, (f32x2)(c2), (f32x2)(c1));
  const f32x2 e = (f32x2){__builtin_amdgcn_exp2f(z.x), __builtin_amdgcn_exp2f(z.y)} + 1.f;
  return a * (f32x2){__builtin_amdgcn_rcpf(e.x), __builtin_amdgcn_rcpf(e.y)};
}

#define STAMP(n) do {} while (0)

constexpr int woff(int k) { return (k >> 1) * SLOT + (k & 1) * 8192; }
constexpr int xoff(int k) { return (k >> 1) * SLOT + 4096 + (k & 1) * 2048; }
#define TA0 22
#define TA1 13
#define TA2 99
#define QP 11
#define NWO 7
#define FW 6
#ifndef NV
#define NV 4
#endif
constexpr int G2R = 4, G2POLL = 6;
constexpr int g2ld(int t) { return t < 17 ? 4 : (t == 17 ? 2 : 0); }
constexpr int g2cnt(int s) { return s < 0 ? 4 : g2ld(s + G2R) + (s == G2POLL ? 1 : 0) + (s == 12 ? 3 : 0); }

__global__ __launch_bounds__(256, 1) void fused_kernel(const short*, const short*, const short*, const short*, const short*, const short*, const short*,
                                                       const short*, short*, char*) {
  u32x8 ka, kb;
  u32x4 kc;
  asm volatile("s_load_dwordx8 %0, %3, 0x0\n\ts_load_dwordx8 %1, %3, 0x20\n\ts_load_dwordx4 %2, %3, 0x40"
               : "=&s"(ka), "=&s"(kb), "=&s"(kc)
               : "s"(__builtin_amdgcn_kernarg_segment_ptr())
               : "memory");
  SB();
  __shared__ __attribute__((aligned(16))) char lds_[163840];
  const ldsp lds = (ldsp)lds_;
  const unsigned lds0 = (unsigned)(uintptr_t)lds;
  const int tid = threadIdx.x, lane = tid & 63, w = __builtin_amdgcn_readfirstlane(tid >> 6);
  const int xc = blockIdx.x & 7, i = blockIdx.x >> 3;
  const int h = lane >> 5, n = lane & 31, q = (lane & 15) >> 2, pp = lane & 3, blk = (lane >> 4) & 1;
  const unsigned area = lds0 + w * AREA;
  const int tile2 = 28 * (xc & 1) + i - 4, dt2 = tile2 >> 3, st2 = 8 * (xc >> 1) + (tile2 & 7);
  const unsigned swz = (lane >> 3), swc = ((lane & 7) ^ (((lane >> 4) & 1) << 2)) * 16;
  unsigned v2[4];
#pragma unroll
  for (int j = 0; j < 4; j++) v2[j] = i >= 4 ? swz * (S * 2) + swc + j * (8 * S * 2 - 1024) : OOB;
  unsigned so2 = (w * 448) * (S * 2) + st2 * 128;
  const bool isA = i >= 16;
  const int ja = i - 16, T = (ja & 12) + 2 * (xc & 1) + ((ja >> 1) & 1), pr = (xc & 6) + (ja & 1);
  const int tk0 = 256 * (xc & 1) + 64 * (xc >> 1);
  auto stage0 = [&](int st) { return ((tk0 ^ (st << 5)) & 256) + ((tk0 + 32 * st) & 255); };
  const int row0 = 512 * (i >> 2) + tk0 + 16 * (i & 3) + 4 * w;
  unsigned vt1 = xc < 7 ? (lane >> 2) * (F * 2) + (lane & 3) * 128 : OOB;
  unsigned vo1 = lane < 56 ? lane * 16 : OOB, vz = 0, sx0 = (isA ? 128 * T + 32 * w : row0) * (D * 2), ring = lds0 + L_WQ + w * 12544, vxa[4];
#pragma unroll
  for (int j = 0; j < 4; j++) vxa[j] = (8 * j + swz) * (D * 2) + (((lane & 7) ^ ((4 * j + (lane >> 4)) & 7)) * 16) - j * 1024;
  ldsp pb[2];
#pragma unroll
  for (int a = 0; a < 2; a++) pb[a] = lds + w * AREA + h * 1024 + q * 128 + (((4 * a + 2 * blk + (pp >> 1)) ^ ((q >> 1) << 2)) * 16) + (pp & 1) * 8;
  asm volatile("" : "+v"(v2[0]), "+v"(v2[1]), "+v"(v2[2]), "+v"(v2[3]), "+v"(vxa[0]), "+v"(vxa[1]), "+v"(vxa[2]), "+v"(vxa[3]), "+v"(vt1), "+v"(vo1), "+v"(vz),
               "+v"(pb[0]), "+v"(pb[1]), "+s"(so2), "+s"(sx0), "+s"(ring));
  SB();
  asm volatile("s_waitcnt lgkmcnt(0)" : "+s"(ka), "+s"(kb), "+s"(kc)::"memory");
  auto arg = [](unsigned lo, unsigned hi) { return (char*)(((unsigned long long)hi << 32) | lo); };
  const short *x = (short*)arg(ka[0], ka[1]), *wqkv = (short*)arg(ka[2], ka[3]), *wphi = (short*)arg(ka[4], ka[5]), *wo = (short*)arg(ka[6], ka[7]);
  const short *w1 = (short*)arg(kb[0], kb[1]), *b1 = (short*)arg(kb[2], kb[3]), *w2 = (short*)arg(kb[4], kb[5]), *b2 = (short*)arg(kb[6], kb[7]);
  short* out = (short*)arg(kc[0], kc[1]);
  char* ws = arg(kc[2], kc[3]);
  const u32x4 rsF = mkrs(ws, WS_FLAG_BYTES), rsX = mkrs(x, S * D * 2), rsW2 = mkrs(w2, F * S * 2);
  auto pre2 = [&](int t0, int t1, int slot0) {
#pragma unroll 1
    for (int t = t0; t < t1; t++) dma4(rsW2, v2, so2 + t * (32 * S * 2), area + (slot0 + t - t0) * 4096);
  };

  bf16x8 w2r[4][2][2];
  auto pre2w = [&]() {
#pragma unroll
    for (int t = 0; t < 4; t++) asm volatile("s_waitcnt lgkmcnt(0)" : "+v"(w2r[t][0][0]), "+v"(w2r[t][0][1]), "+v"(w2r[t][1][0]), "+v"(w2r[t][1][1])::"memory");
  };
  auto pre2rd = [&]() {
#pragma unroll
    for (int t = 0; t < 4; t++)
#pragma unroll
      for (int ks = 0; ks < 2; ks++)
#pragma unroll
        for (int a = 0; a < 2; a++) w2r[t][ks][a] = cat(vtr(pb[a] + (t + 6) * 4096 + ks * 2048), vtr(pb[a] + (t + 6) * 4096 + ks * 2048 + 512));
  };

  u32x4 wor[14];
  unsigned vwo[4];
#pragma unroll
  for (int j = 0; j < 4; j++) vwo[j] = i >= 4 ? swz * (D * 2) + swc + j * (8 * D * 2 - 1024) : OOB;
  const u32x4 rsWo = mkrs(wo, D * D * 2);
  const unsigned sow = w * 112 * (D * 2) + dt2 * 128;
  auto ldwo = [&](auto jj) { wor[jj.value] = lda<(jj.value & 3) * 1024>(rsWo, vwo[jj.value & 3], sow + (jj.value >> 2) * (32 * D * 2)); };
  auto tiewo = [&]() { unroll<0, 14>([&](auto jj) { tiea(wor[jj.value]); }); hold(vwo[0], vwo[1], vwo[2], vwo[3]); };
  const u32x4 rsW1 = mkrs(w1, S * F * 2);
  auto so_t1 = [&](int stage, int wq, int r0) { return (512 * wq + stage0(stage) + r0) * (F * 2) + xc * 512; };
  const unsigned vfu = lane < 4 && i >= 4 ? (FL_U + dt2 * 14 + ((7 * w) >> 1) + lane) * 128 : OOB;
  unsigned miss = 1, td = 14;
  Epoch ep;
  auto setep = [&](u32x2 gen) {
    const unsigned long long g = (((unsigned long long)__builtin_amdgcn_readfirstlane(gen.y) << 32) | (unsigned)__builtin_amdgcn_readfirstlane(gen.x)) + 1;
    ep.lo = (unsigned)g; ep.hi = (unsigned)(g >> 32);
  };

  if (!isA) {
    bf16x8 xv[4];
    u32x2 gen, bb[4];
    xv[0] = ldp<0>(rsX, vo1, sx0); xv[1] = ldp<896>(rsX, vo1, sx0); xv[2] = ldp<1792>(rsX, vo1, sx0); xv[3] = ldp<2688>(rsX, vo1, sx0);
    gen = ldc2(rsF, vz);
    SB();
    const bool isG = xc < 7 && i < 14;
    const int ft = 2 * xc + (i >= 7), dt = i >= 7 ? i - 7 : i;
    const unsigned vt = lane < 32 ? vt1 : OOB, vb = h * 8;
    {
      setm0(area + L_STAT);
      touchl(rsW1, vt, so_t1(i >> 2, i & 3, 8 * w));
      const u32x4 rsB = mkrs(b1, F * 2);
      const unsigned sb = (ft * 128 + w * 32) * 2;
      bb[0] = ldp2<0>(rsB, vb, sb); bb[1] = ldp2<16>(rsB, vb, sb); bb[2] = ldp2<32>(rsB, vb, sb); bb[3] = ldp2<48>(rsB, vb, sb);
    }
    unsigned v1[4], vx[4];
#pragma unroll
    for (int j = 0; j < 4; j++) {
      v1[j] = (8 * (j & 1) + swz) * (F * 2) + (j >> 1) * 128 + swc - j * 1024;
      vx[j] = (8 * j + swz) * (D * 2) + swc - j * 1024;
    }
    waitv<5>(xv[0], xv[1], xv[2], xv[3]);
    tie(gen);
    STAMP(0);
    setep(gen);
    const unsigned tagB = ep.lo ^ ep.hi, vs = (lane & 15) ? OOB : (row0 + h + 2 * blk) * 16;
    u32x4 rec;
    {
      float s[4] = {}, z[4] = {};
#pragma unroll
      for (int r = 0; r < 4; r++) acc_stats(xv[r], s[r], z[r]);
      swap32(s[0], s[1]); swap32(s[2], s[3]); swap32(z[0], z[1]); swap32(z[2], z[3]);
      float sa = s[0] + s[1], sb = s[2] + s[3], za = z[0] + z[1], zb = z[2] + z[3];
      swap16(sa, sb); swap16(za, zb);
      const float mu = rowsum(sa + sb) * (1.f / D), ms = rowsum(za + zb) * (1.f / D);
      const float var = fmaxf(ms - mu * mu, 0.f);
      float a = __builtin_amdgcn_rsqf(var + 1e-5f);
      a *= __builtin_amdgcn_rsqf(var * a * a + 1e-5f);
      const unsigned a2 = pk(a, a);
      const float c = -mu * blo(a2);
      rec = (u32x4){a2, ep.lo, __builtin_bit_cast(unsigned, c), tagB};
      stc(mkst(ws + WS_ST, SZ_ST), vs, rec);
    }
    asm volatile("s_barrier" ::: "memory");
    hold(vz, vo1, vt, vb);
    STAMP(1);

    if (isG) {
      const unsigned b1s = (w * 512) * (F * 2) + ft * 256, bxs = (w * 512) * (D * 2) + dt * 128;
      auto mset = [&](auto kk, auto jj) {
        constexpr int ks = kk.value, j = jj.value;
        if constexpr ((ks & 1) ? j == 2 : j == 0) setm0n(area + woff(ks));
        if constexpr (!(ks & 1) && j == 4) setm0n(area + xoff(ks));
      };
      auto dgrp = [&](auto kk, auto jj, unsigned s1, unsigned sx) {
        constexpr int ks = kk.value, j = jj.value, jw = (ks & 1) ? j - 2 : j, jx = (ks & 1) ? j : j - 4;
        if constexpr (jw >= 0 && jw < 4) dma<jw * 1024>(rsW1, v1[jw], s1 + ks * (16 * F * 2));
        else dma<(jx + 2 * (ks & 1)) * 1024>(rsX, vx[jx + 2 * (ks & 1)], sx + (ks >> 1) * (32 * D * 2));
      };
      auto fill = [&](auto kk) {
        unroll<0, 6>([&](auto jj) { mset(kk, jj); asm volatile("s_nop 0"); dgrp(kk, jj, b1s + tk0 * (F * 2), bxs + tk0 * (D * 2)); });
      };
      const u32x4 rsS = mkrs(ws + WS_ST, SZ_ST);
      auto gather = [&]() {
        bf16x8 g[8];
#pragma unroll
        for (int j = 0; j < 8; j++) g[j] = ldc<0>(rsS, lane * 16, w * 8192 + stage0(2 * j) * 16);
        waitv<0>();
        bool ok = true;
#pragma unroll
        for (int j = 0; j < 8; j++) {
          tie(g[j]);
          const u32x4 u = __builtin_bit_cast(u32x4, g[j]);
          ok &= u[1] == ep.lo && u[3] == tagB;
          const ldsp p = lds + w * AREA + L_STAT + j * 512 + (lane >> 4) * 128 + (lane & 15) * 4;
          lst(p, u[2]); lst(p + 64, u[0]);
        }
        return (bool)__all(ok);
      };
      unroll<0, 4>(fill);
      hold(vs, rec);
      STAMP(2);
#pragma unroll 1
      for (int it = 0; it < SPIN; ++it)
        if (gather()) break;
      STAMP(3);
      tie(bb[0]); tie(bb[1]); tie(bb[2]); tie(bb[3]);
      f32x16 acc[4][2];
#pragma unroll
      for (int g = 0; g < 4; g++) {
        acc[0][0][4 * g] = blo(bb[g].x); acc[0][0][4 * g + 1] = bhi(bb[g].x); acc[0][0][4 * g + 2] = blo(bb[g].y); acc[0][0][4 * g + 3] = bhi(bb[g].y);
      }
      acc[0][1] = acc[0][0];
#pragma unroll
      for (int a = 1; a < 4; a++) { acc[a][0] = (f32x16)(0.f); acc[a][1] = (f32x16)(0.f); }
      unsigned pa[4], px[2], msk[4], psc = area + L_STAT + h * 32, psa;
      {
        const unsigned rowb = area + (8 * h + q) * 128 + (pp & 1) * 8, ch = 2 * blk + (pp >> 1), fl = (q >> 1) << 2;
#pragma unroll
        for (int a = 0; a < 4; a++) pa[a] = rowb + ((a ^ w) >> 1) * 2048 + ((4 * ((a ^ w) & 1) + ch) ^ fl) * 16;
        px[0] = rowb + (ch ^ fl) * 16; px[1] = rowb + ((4 + ch) ^ fl) * 16;
        const unsigned r = 4 * ((lane >> 3) & 1) + pp, on = (!blk && (q & 1) == h) ? ((r & 1) ? 0xffff0000u : 0xffffu) : 0u;
#pragma unroll
        for (int j = 0; j < 4; j++) { msk[j] = (r >> 1) == j ? on : 0u; tie(msk[j]); }
        psa = area + L_STAT + 64 + (8 * h + r) * 4;
      }
      s16x4 wl[2][4], wh[2][4], xl[2], xh[2];
      u32x4 hf[2][2], dg;
      unsigned av;
      bf16x8 xp[2];
      f32x16 hn[2], cc = {};
      tie(cc);
      auto rdX = [&](auto nn, auto dd) {
        constexpr int o = xoff(nn.value & 3), d = dd.value;
        xl[d] = vtr((ldsp)(uintptr_t)px[d] + o); xh[d] = vtr((ldsp)(uintptr_t)px[d] + o + 512);
      };
      auto rdA = [&](auto nn) { av = lld<unsigned>((ldsp)(uintptr_t)psa + nn.value * 128); };
      auto rdC = [&](auto nn) {
        const f32x4 c0 = lld<f32x4>((ldsp)(uintptr_t)psc + nn.value * 128), c1 = lld<f32x4>((ldsp)(uintptr_t)psc + nn.value * 128 + 16);
#pragma unroll
        for (int e = 0; e < 4; e++) { cc[e] = c0[e]; cc[4 + e] = c1[e]; }
      };
      auto rdW = [&](auto nn, auto aa) {
        constexpr int o = woff(nn.value & 3), nx = nn.value & 1, a = aa.value;
        wl[nx][a] = vtr((ldsp)(uintptr_t)pa[a] + o); wh[nx][a] = vtr((ldsp)(uintptr_t)pa[a] + o + 512);
      };
      auto mkD = [&]() { dg = (u32x4){av & msk[0], av & msk[1], av & msk[2], av & msk[3]}; };
      auto nrm = [&](auto dd) { hn[dd.value] = mfmav(dg, cat(xl[dd.value], xh[dd.value]), cc); };
      auto cvt = [&](auto nn, auto dd) {
        constexpr int d = dd.value;
        hf[nn.value & 1][d] = (u32x4){pk(hn[d][0], hn[d][1]), pk(hn[d][2], hn[d][3]), pk(hn[d][4], hn[d][5]), pk(hn[d][6], hn[d][7])};
        SB();
        keep(hn[d]);
      };
      auto step = [&](auto kk, auto wn, auto last, auto&& ms, auto&& gm) {
        constexpr int ks = kk.value;
        constexpr bool more = !last.value;
        using NX = IC<ks + 1>;
        auto mm = [&](auto m_) {
          constexpr int a = more ? m_.value >> 1 : ((m_.value >> 1) + 1) & 3, dh = m_.value & 1;
          if constexpr (a < NV) mfmac(acc[a][dh], cat(wl[ks & 1][a], wh[ks & 1][a]), hf[ks & 1][dh]);
          else acc[a][dh] = MFMA(cat(wl[ks & 1][a], wh[ks & 1][a]), __builtin_bit_cast(bf16x8, hf[ks & 1][dh]), acc[a][dh]);
          SB();
          if constexpr (!more && m_.value >= 2) {
            constexpr int c = m_.value >> 1;
            unroll<0, 2>([&](auto gg) {
              constexpr int g = gg.value, j = 2 * (m_.value - 2) + g;
              if constexpr (j == 0 && NV > 1) asm volatile("s_nop 9" : "+v"(acc[1][0]));
              if constexpr (c < NV) xp[g] = pack8(acc[c][dh], 8 * g); else xp[g] = pack8s(acc[c][dh], 8 * g);
              lst(lds + w * AREA + lane * 16 + (c - 1) * 4096 + dh * 2048 + g * 1024, xp[g]);
              if constexpr (j) hold(xp[g ^ 1]);
              if constexpr (j < NWO) ldwo(IC<j>());
              SB();
            });
          }
        };
        __builtin_amdgcn_s_waitcnt(0x70 | (wn.value & 15) | ((wn.value >> 4) << 14));
        asm volatile("" ::: "memory");
        SB();
        if constexpr (more) { rdX(NX(), IC<0>()); rdA(NX()); }
        ms(IC<0>());
        SB();
        mm(IC<0>());
        gm(IC<0>());
        if constexpr (more) { rdC(NX()); rdX(NX(), IC<1>()); SB(); }
        mm(IC<1>());
        gm(IC<1>());
        ms(IC<2>());
        if constexpr (more) { rdW(NX(), IC<0>()); rdW(NX(), IC<1>()); SB(); }
        mm(IC<2>());
        if constexpr (more) { mkD(); SB(); }
        gm(IC<2>());
        if constexpr (more) { rdW(NX(), IC<2>()); SB(); nrm(IC<0>()); SB(); rdW(NX(), IC<3>()); SB(); }
        mm(IC<3>());
        if constexpr (more) { nrm(IC<1>()); SB(); }
        gm(IC<3>());
        ms(IC<4>());
        mm(IC<4>());
        if constexpr (more) { cvt(NX(), IC<0>()); SB(); }
        mm(IC<5>());
        gm(IC<4>());
        mm(IC<6>());
        if constexpr (more) { cvt(NX(), IC<1>()); SB(); }
        gm(IC<5>());
        mm(IC<7>());
      };
      asm volatile("s_waitcnt lgkmcnt(0)" ::: "memory");
      rdX(IC<0>(), IC<0>()); rdX(IC<0>(), IC<1>()); rdA(IC<0>()); rdC(IC<0>());
      unroll<0, 4>([&](auto jj) { rdW(IC<0>(), jj); });
      mkD();
      SB();
      asm volatile("s_nop 1");
      nrm(IC<0>()); nrm(IC<1>());
      asm volatile("s_nop 7\n\ts_nop 7");
      cvt(IC<0>(), IC<0>()); cvt(IC<0>(), IC<1>());
      SB();
#pragma unroll 1
      for (int t = 2; t < 16; t += 2) {
        const unsigned tk = stage0(t);
        const unsigned s1 = b1s + tk * (F * 2), sx = bxs + tk * (D * 2);
        unroll<0, 4>([&](auto kk) {
          step(kk, IC<12>(), IC<0>(), [&](auto jj) { mset(kk, jj); SB(); }, [&](auto jj) { dgrp(kk, jj, s1, sx); SB(); });
        });
        psc += 512; tie(psc); psa += 512; tie(psa);
        SB();
      }
      {
        unroll<0, 4>([&](auto kk) {
          constexpr int ks = kk.value;
          if constexpr (ks == 0) STAMP(9);
          if constexpr (ks == 1) hold(v1[0], v1[1], v1[2], v1[3], vx[0], vx[1], vx[2], vx[3]);
          if constexpr (ks == 1) STAMP(10);
          if constexpr (ks == 2) STAMP(11);
          if constexpr (ks == 3) STAMP(12);
          step(kk, IC<(ks == 0 ? 12 : ks == 1 ? 10 : ks == 2 ? 8 : 63)>(), IC<(ks == 3)>(),
               [&](auto jj) { if constexpr (jj.value == 0) { setm0n(area + (6 + ks) * 4096); SB(); } },
               [&](auto jj) {
                 constexpr int j = jj.value;
                 if constexpr (j < 4) dma<j * 1024>(rsW2, v2[j], so2 + ks * (32 * S * 2));
                 SB();
               });
        });
      }
      bf16x8 sl[2];
      {
        u32x4 s0, s1;
#pragma unroll
        for (int j = 0; j < 4; j++) {
          const unsigned e = msk[j] & 0x3f803f80u;
          auto r = __builtin_amdgcn_permlane16_swap(0u, e, false, false);
          const unsigned o = r[0];
          s0[j] = e; s1[j] = o;
        }
        sl[0] = __builtin_bit_cast(bf16x8, s0); sl[1] = __builtin_bit_cast(bf16x8, s1);
        tie(sl[0]); tie(sl[1]);
      }
      unsigned vo = (ft * 16 + w * 4 + h) * (D * 16) + (dt * 64 + n) * 16, vo2 = vo + 2 * D * 16, px3[3];
      tie(vo); tie(vo2);
#pragma unroll
      for (int a = 0; a < 3; a++) { px3[a] = lds0 + (w ^ (a + 1)) * AREA + lane * 16; tie(px3[a]); }
      STAMP(4);
      bar();
      STAMP(5);
      {
        const rsrc_t st = mkst(ws + WS_UF, SZ_UF);
        u32x4 v[12], res[4];
#pragma unroll
        for (int c = 0; c < 12; c++) { v[c] = lld<u32x4>((ldsp)(uintptr_t)px3[c % 3] + (c % 3) * 4096 + (c / 3) * 1024); SB(); }
        unroll<0, 12>([&](auto cc_) {
          constexpr int c = cc_.value;
          mfmac(acc[0][c / 6], sl[(c / 3) & 1], v[c]);
          if constexpr (NWO + c < 14) ldwo(IC<NWO + c>());
        });
        asm volatile("s_nop 11" : "+v"(acc[0][0]), "+v"(acc[0][1]));
#pragma unroll
        for (int dh = 0; dh < 2; dh++) {
#pragma unroll
          for (int g = 0; g < 4; g += 2) {
            float r[8];
#pragma unroll
            for (int e = 0; e < 8; e += 2) {
              const f32x2 z = gelu2((f32x2){acc[0][dh][4 * g + e], acc[0][dh][4 * g + e + 1]});
              r[e] = z.x; r[e + 1] = z.y;
            }
            auto r0 = __builtin_amdgcn_permlane32_swap(pk(r[0], r[1]), pk(r[4], r[5]), false, false);
            auto r1 = __builtin_amdgcn_permlane32_swap(pk(r[2], r[3]), pk(r[6], r[7]), false, false);
            res[2 * dh + g / 2] = (u32x4){r0[0], r1[0], r0[1], r1[1]};
            stc(st, (g ? vo2 : vo) + dh * 512, res[2 * dh + g / 2]);
            SB();
          }
        }
        STAMP(8);
        waitv<4>();
        tiewo();
        pre2rd();
        pre2w();
        hold(res[0], res[1], res[2], res[3], vo, vo2);
      }
      const unsigned vfg = tid == 0 ? (FL_U + dt * 14 + ft) * 128 : OOB;
      u32x2 epv = {ep.lo, ep.hi};
      tie(epv);
      STAMP(6);
      publish<0>(rsF, vfg, epv);
      STAMP(7);
      u32x2 f0 = ldc2(rsF, vfu);
      pre2(4, 10, 0);
      waitv<24>(f0);
      hold(vfg, epv);
      miss = missing<4>(f0, ep);
      td = 10;
    } else {
      if (xc < 7) {
#pragma unroll 1
        for (int m = 4; m < 9; m++) {
          if (m == 4) __builtin_amdgcn_s_sleep(24); else __builtin_amdgcn_s_sleep(10);
          touchl(rsW1, vt1, so_t1(m, 2 * (i - 14) + (w >> 1), 16 * (w & 1)));
        }
      }
      if (i >= 14) {
#pragma unroll 1
        for (int m = 0; m < 4; m++) touchl(mkrs(wo, D * D * 2), (lane >> 2) * (D * 2) + (lane & 3) * 128, (128 * m + 64 * (i - 14) + 16 * w) * (D * 2) + (xc & 1) * 384);
#pragma unroll 1
        for (int m = 0; m < 4; m++) touchl(rsW2, (lane >> 3) * (S * 2) + (lane & 7) * 128, (448 * m + 384 + 32 * (i - 14) + 8 * w) * (S * 2) + (xc >> 1) * 1024);
        const int head = 2 * xc + i - 14;
        const u32x4 rsP = mkrs(ws + WS_KVP, SZ_KVP);
        const unsigned so = head * 65536 + w * 1024, vf = lane < 16 ? (FL_P + xc * 16 + lane) * 128 : OOB, vp = h * 4096 + n * 16;
        const unsigned one = ((lane >> 3) & 1) == blk ? ((lane & 1) ? 0x3f800000u : 0x3f80u) : 0u;
        u32x4 id;
#pragma unroll
        for (int j = 0; j < 4; j++) id[j] = ((lane & 7) >> 1) == j ? one : 0u;
        f32x4 a[2] = {};
        tie(id); tie(a[0]); tie(a[1]);
        bf16x8 pv[16];
        collect(pv, rsF, vf, rsP, vp, so, ep);
        STAMP(3);
#pragma unroll
        for (int t = 0; t < 16; t++) asm volatile("v_mfma_f32_16x16x32_bf16 %0, %1, %2, %0" : "+v"(a[t & 1]) : "v"(id), "v"(pv[t]));
        asm volatile("s_nop 7" : "+v"(a[0]), "+v"(a[1]));
        const rsrc_t st = mkst(ws + WS_KVF, SZ_KVF);
        const u32x4 r0 = {pk(a[0][0], a[0][1]), ep.lo, pk(a[0][2], a[0][3]), tagB}, r1 = {pk(a[1][0], a[1][1]), ep.lo, pk(a[1][2], a[1][3]), tagB};
        const unsigned vr = head * 8192 + w * 2048 + blk * 1024 + (16 * h + (lane & 15)) * 16;
        stc(st, vr, r0);
        stc(st, vr + 512, r1);
        hold(r0, r1, vr);
        STAMP(4);
      } else {
        __builtin_amdgcn_s_sleep(127);
      }
      unroll<0, 14>(ldwo);
      pre2(0, 4, 6); pre2(4, 10, 0);
      waitv<24>();
      tiewo();
      pre2rd();
      pre2w();
      pre2(10, 14, 6);
    }
  } else {
    const unsigned sxa = sx0;
    dma4(rsX, vxa, sxa, ring);
    u32x2 gen = ldc2(rsF, vz);
    SB();
    unsigned sink = 0;
    const u32x4 rsW = mkrs(wqkv, D * 3 * D * 2);
    {
      const unsigned r6 = (lane * 43) >> 8, c6 = lane - 6 * r6;
      touch(sink, rsW, lane < 42 ? r6 * 2688 + 896 + (c6 & 1) * 896 + (c6 >> 1) * 108 : OOB, (28 * ja + 7 * w) * 2688 + (xc & 6) * 112);
    }
    dma4(rsX, vxa, sxa + 128, ring + 4096);
    dma4(rsX, vxa, sxa + 256, ring + 8192);
    unsigned vw[8];
#pragma unroll
    for (int s = 0; s < 8; s++) {
      const unsigned c = (s < 7 ? 256 * s + 64 * w : 3072) + lane, r = (c * 9363) >> 16;
      vw[s] = ((r & ~15u) + 8 * ((r >> 1) & 1) + 4 * (r & 1) + ((r >> 2) & 1) + 2 * ((r >> 3) & 1)) * 2688 + (c - 7 * r) * 16;
    }
    const unsigned mw = lds0 + w * 1024;
    auto mW = [&](auto ss, auto bb) {
      if constexpr (ss.value < 12) addm0<bb.value + ss.value * 4096>(mw); else addm0<bb.value + 49152>(lds0);
    };
    auto dmaW = [&](auto ss, unsigned col) {
      constexpr int s = ss.value;
      dma<0>(rsW, vw[s < 12 ? (s + 14) % 7 : 7], col + pr * 112 + (s >= 7 && s < 12 ? 256 * 2688 : 0));
    };
    bf16x8 xa[28];
    float sa = 0.f, sq = 0.f;
    {
      unsigned pr_[4];
#pragma unroll
      for (int k = 0; k < 4; k++) pr_[k] = ring + n * 128 + (((2 * k + h) ^ ((n >> 1) & 7)) * 16);
      unroll<0, 7>([&](auto cc) {
        constexpr int cb = cc.value;
        waitv<(cb == 0 ? 9 : 8)>();
#pragma unroll
        for (int k = 0; k < 4; k++) xa[4 * cb + k] = lld<bf16x8>((ldsp)(uintptr_t)pr_[k] + (cb % 3) * 4096);
        asm volatile("s_waitcnt lgkmcnt(0)" : "+v"(xa[4 * cb]), "+v"(xa[4 * cb + 1]), "+v"(xa[4 * cb + 2]), "+v"(xa[4 * cb + 3])::"memory");
        if constexpr (cb < 4) dma4(rsX, vxa, sxa + (cb + 3) * 128, ring + (cb % 3) * 4096);
        if constexpr (cb == 4) unroll<0, 4>([&](auto ss) { mW(ss, IC<L_WK>()); asm volatile("s_nop 0"); dmaW(ss, 896); });
        if constexpr (cb == 5) unroll<0, 4>([&](auto ss) { mW(ss, IC<L_WV>()); asm volatile("s_nop 0"); dmaW(ss, 1792); });
        if constexpr (cb == 6) {
          setm0(lds0 + L_WPHI + w * 1024);
          dma<0>(mkrs(wphi + pr * 1568, 3136), tid * 16, 0);
        }
        SB();
#pragma unroll
        for (int k = 0; k < 4; k++) acc_stats(xa[4 * cb + k], sa, sq);
        SB();
      });
    }
    STAMP(1);
    hold(vxa[0], vxa[1], vxa[2], vxa[3], vz);
    tie(gen);
    setep(gen);
    float la, lc;
    {
      float s2 = sa, q2 = sq;
      swap32(sa, s2); swap32(sq, q2);
      const float mu = (sa + s2) * (1.f / D), var = fmaxf((sq + q2) * (1.f / D) - mu * mu, 0.f);
      la = __builtin_amdgcn_rsqf(var + 1e-5f);
      lc = -mu * la;
    }
    auto norm = [&](auto kk, auto ee) {
      constexpr int ks = kk.value, e = ee.value;
      if constexpr (ks < 28) {
        u32x4 u = __builtin_bit_cast(u32x4, xa[ks]);
        u[e] = affine2(u[e], la, lc);
        xa[ks] = __builtin_bit_cast(bf16x8, u);
      }
    };
    unroll<0, 4>([&](auto ee) { norm(IC<0>(), ee); });
    norm(IC<1>(), IC<0>());
    tie(xa[0]); tie(xa[1]);
    waitv<1>();
    bar();
    STAMP(3);
    const unsigned vt2 = (lane >> 3) * (S * 2) + (lane & 7) * 128;
    const unsigned st2t = (448 * w + 8 * ja) * (S * 2) + (xc >> 1) * 1024;
    auto touch2 = [&](int m) { touch(sink, rsW2, vt2, st2t + 128 * m * (S * 2)); };
    const unsigned vtq = lane < 28 && (lane & 3) < 3 ? (lane >> 2) * 2688 + (lane & 3) * 108 : OOB;
    const unsigned vtx = xc < 7 && (lane & 7) < 7 ? (lane >> 3) * (D * 2) + (lane & 7) * 128 : OOB;
    const unsigned stx = (512 * ((2 * ja + (w >> 1)) & 3) + stage0(8 + (ja >> 1)) + 16 * (w & 1)) * (D * 2);
    const unsigned wlane = (2 * h + 4 * q) * 112 + 32 * blk + 8 * pp;
    f32x16 kT[2], vv[2];
    {
      const ldsp pk_ = lds + L_WK + wlane, pv_ = lds + L_WV + wlane;
      s16x4 fr[30][8];
      auto rd = [&](auto kk, auto cc) {
        constexpr int ks = kk.value, c = cc.value;
        if constexpr (ks < 28) fr[ks][c] = vtr(((c & 4) ? pv_ : pk_) + ks * 1792 + ((c >> 1) & 1) * 56 + (c & 1) * 112);
      };
      unroll<0, 8>([&](auto cc) { rd(IC<0>(), cc); });
      unroll<0, 8>([&](auto cc) { rd(IC<1>(), cc); });
      SB();
      unroll<0, 28>([&](auto kk) {
        constexpr int ks = kk.value;
        if constexpr (ks == 7) { waitv<6>(); asm volatile("s_barrier" ::: "memory"); }
        if constexpr (ks == 16) { waitv<4>(); asm volatile("s_barrier" ::: "memory"); }
        __builtin_amdgcn_s_waitcnt(ks < 27 ? 0xc87f : 0xc07f);
        SB();
        unroll<0, 4>([&](auto mm) {
          constexpr int m = mm.value;
          const bf16x8 wv = cat(fr[ks][2 * m], fr[ks][2 * m + 1]);
          if constexpr (m < 2) mfmab<ks == 0>(kT[m], wv, xa[ks]);
          else mfmab<ks == 0>(vv[m - 2], xa[ks], wv);
          SB();
          if constexpr (ks < 9 && m == 0) mW(IC<4 + ks>(), IC<L_WK>());
          if constexpr (ks < 9 && m == 2) mW(IC<4 + ks>(), IC<L_WV>());
          rd(IC<ks + 2>(), IC<2 * m>());
          rd(IC<ks + 2>(), IC<2 * m + 1>());
          SB();
          if constexpr (ks < 9 && !(m & 1)) dmaW(IC<4 + ks>(), m ? 1792 : 896);
          if constexpr (ks == 9 && m == 0) touch(sink, rsW1, vt1, so_t1(8 + (ja >> 1), (2 * ja + (w >> 1)) & 3, 16 * (w & 1)));
          if constexpr (ks == 10 && m == 0) touch(sink, rsX, vtx, stx);
          if constexpr (ks == 11 && m == 0) touch(sink, rsX, vtx, stx + 8 * D * 2);
          if constexpr (ks == 14 && m == 0) touch(sink, rsW, vtq, (28 * ja + 7 * w) * 2688 + (xc & 6) * 112);
          if constexpr (ks == 16 && m == 0) hold(vtx);
          if constexpr (ks == 19 && m == 0) hold(vtq);
          if constexpr (ks == TA0 && m == 0) touch2(0);
          SB();
          norm(IC<ks + 1 + (m == 3)>(), IC<(m + 1) & 3>());
          SB();
        });
      });
    }
    bf16x8 wf[2][2];
    {
      const ldsp p = lds + L_WPHI + (4 * h + q) * 56 + 32 * blk + 8 * pp;
#pragma unroll
      for (int hd = 0; hd < 2; hd++) {
        wf[hd][0] = cat(vtr(p + hd * 1568), vtr(p + hd * 1568 + 8 * 56));
        s16x4 z = vtr(p + hd * 1568 + 24 * 56);
        wf[hd][1] = cat(vtr(p + hd * 1568 + 16 * 56), h ? (s16x4){0, 0, 0, 0} : z);
      }
    }
    auto phi = [&](f32x16 (&t)[2], f32x16 (&p)[2]) {
      asm volatile("s_nop 9" : "+v"(t[0]), "+v"(t[1]));
      bf16x8 b[4] = {pack8(t[0], 0), pack8(t[0], 8), pack8(t[1], 0), pack8(t[1], 8)};
      asm volatile("s_nop 1" : "+v"(b[0]), "+v"(b[1]), "+v"(b[2]), "+v"(b[3]), "+v"(wf[0][1]), "+v"(wf[1][1]));
      mfmab<true>(p[0], wf[0][0], b[0]); mfmab<false>(p[0], wf[0][1], b[1]);
      mfmab<true>(p[1], wf[1][0], b[2]); mfmab<false>(p[1], wf[1][1], b[3]);
    };
    STAMP(5);
    {
      f32x16 p[2];
      phi(kT, p);
      asm volatile("s_waitcnt lgkmcnt(0)\n\ts_barrier" : "+v"(vv[0]), "+v"(vv[1])::"memory");
      {
        const bf16x8 ones = {0x3f80, 0x3f80, 0x3f80, 0x3f80, 0x3f80, 0x3f80, 0x3f80, 0x3f80};
        const ldsp pvx = lds + L_VX + w * 2048 + lane * 16;
#pragma unroll
        for (int hd = 0; hd < 2; hd++)
#pragma unroll
          for (int j = 0; j < 2; j++) { bf16x8 f = pack8(vv[hd], 8 * j); lst(pvx + hd * 8192 + j * 1024, n == 28 ? ones : f); }
      }
      mW(IC<0>(), IC<L_WQ>());
      asm volatile("" : "+v"(p[0]), "+v"(p[1])::"memory");
      const unsigned pb = (32 * w + n) * 64 + ((h ^ ((n >> 1) & 7)) * 8);
      unroll<0, 2>([&](auto hh) {
        constexpr int hd = hh.value;
        unsigned ph[2][8];
        features(p[hd], h, ph, [&](auto kk, auto& pin) {
          constexpr int s = 12 * hd + kk.value;
          if constexpr (s < 13) {
            dmap<0>(rsW, vw[s < 12 ? s % 7 : 7], pr * 112 + (s >= 7 && s < 12 ? 256 * 2688 : 0), pin);
            if constexpr (s < 12) mW(IC<s + 1>(), IC<L_WQ>());
          }
          if constexpr (s == TA1 || s == TA2) touchp(sink, rsW2, vt2, st2t + 128 * (s == TA1 ? 1 : 2) * (S * 2), pin);
        });
#pragma unroll
        for (int sg = 0; sg < 2; sg++)
#pragma unroll
          for (int g = 0; g < 4; g++) lst(lds + L_PHI + (hd * 2 + sg) * 8192 + (pb ^ (g << 4)), (u32x2){ph[sg][2 * g], ph[sg][2 * g + 1]});
      });
    }
    STAMP(6);
    hold(vw[0], vw[1], vw[2], vw[3], vw[4], vw[5], vw[6], vw[7], vt1, vt2);
    waitv<(TA2 < 24 ? 2 : 1)>();
    bar();
    u32x4 kvp[2];
    const unsigned vkv = (2 * pr + (w >> 1)) * 65536 + T * 4096 + (w & 1) * 2048 + lane * 16;
    {
      const unsigned sw = 2 * h + (q >> 1);
      const ldsp pl = lds + L_PHI + w * 8192 + (4 * h + q) * 64 + (((4 * blk + pp) ^ sw) * 8);
      const ldsp ph_ = lds + L_PHI + w * 8192 + (8 + 4 * h + q) * 64 + (((4 * blk + pp) ^ sw ^ 4) * 8);
      const ldsp pvx = lds + L_VX + (w >> 1) * 8192 + lane * 16;
      f32x16 kv;
      bf16x8 ka_[8], kb_[8];
#pragma unroll
      for (int jj = 0; jj < 8; jj++) { ka_[jj] = cat(vtr(pl + jj * 1024), vtr(ph_ + jj * 1024)); kb_[jj] = lld<bf16x8>(pvx + jj * 1024); SB(); }
      unroll<0, 8>([&](auto jj) { mfmab<jj.value == 0>(kv, ka_[jj.value], kb_[jj.value]); });
      asm volatile("s_nop 11" : "+v"(kv));
      const rsrc_t st = mkst(ws + WS_KVP, SZ_KVP);
      kvp[0] = __builtin_bit_cast(u32x4, pack8(kv, 0)); kvp[1] = __builtin_bit_cast(u32x4, pack8(kv, 8));
      stc(st, vkv, kvp[0]);
      stc(st, vkv + 1024, kvp[1]);
    }
    STAMP(7);
    const unsigned vfp = tid == 0 ? (FL_P + pr * 16 + T) * 128 : OOB;
    u32x2 epv = {ep.lo, ep.hi};
    tie(epv);
    f32x16 qT[2];
    {
      const ldsp pq_ = lds + L_WQ + wlane;
      s16x4 fr[29][4];
      auto rd = [&](auto kk, auto cc) {
        constexpr int ks = kk.value, c = cc.value;
        if constexpr (ks < 28) fr[ks][c] = vtr(pq_ + ks * 1792 + (c >> 1) * 56 + (c & 1) * 112);
      };
      unroll<0, 4>([&](auto cc) { rd(std::integral_constant<int, 0>(), cc); });
      unroll<0, 28>([&](auto kk) {
        constexpr int ks = kk.value;
        if constexpr (ks == QP) { hold(kvp[0], kvp[1], vkv); publish<0>(rsF, vfp, epv); STAMP(8); SB(); }
        if constexpr (ks == QP + 2 && TA2 >= 24) { touch2(2); SB(); }
        if constexpr (ks == QP + 6) hold(vfp, vt2);
        unroll<0, 2>([&](auto mm) {
          constexpr int m = mm.value;
          rd(std::integral_constant<int, ks + 1>(), std::integral_constant<int, 2 * m>());
          rd(std::integral_constant<int, ks + 1>(), std::integral_constant<int, 2 * m + 1>());
          mfmab<ks == 0>(qT[m], cat(fr[ks][2 * m], fr[ks][2 * m + 1]), xa[ks]);
          SB();
        });
      });
    }
    unsigned phq[2][2][8];
    {
      f32x16 p[2];
      asm volatile("s_nop 1" : "+v"(qT[0]), "+v"(qT[1]));
      phi(qT, p);
      setm0n(area + 6 * 4096);
      asm volatile("s_waitcnt lgkmcnt(0)\n\ts_barrier\n\ts_nop 9" : "+v"(p[0]), "+v"(p[1])::"memory");
      STAMP(9);
      unroll<0, 2>([&](auto hh) {
        features(p[hh.value], h, phq[hh.value], [&](auto kk, auto& pin) {
          constexpr int k = 12 * hh.value + kk.value, t = k >> 2;
          dmap<(k & 3) * 1024>(rsW2, v2[k & 3], so2 + t * (32 * S * 2), pin);
          if constexpr ((k & 3) == 3 && k < 23) setm0n(area + ((t + 7) % 10) * 4096);
        });
      });
    }
    u32x4 fq[2][4];
#pragma unroll
    for (int c = 0; c < 8; c++) {
      const unsigned* s = &phq[c >> 2][(c >> 1) & 1][4 * (c & 1)];
      fq[c >> 2][c & 3] = (u32x4){s[0], s[1], s[2], s[3]};
      tie(fq[c >> 2][c & 3]);
    }
    STAMP(10);
    {
      const u32x4 rsK = mkrs(ws + WS_KVF, SZ_KVF);
      const unsigned so = pr * 16384 + w * 2048, tagB = ep.lo ^ ep.hi, vk = lane * 16;
      const ldsp px = lds + 9 * 4096 + lane * 16;
      bf16x8 g0, g1, g2, g3;
      auto fetch = [&]() { g0 = ldc<0>(rsK, vk, so); g1 = ldc<1024>(rsK, vk, so); g2 = ldc<0>(rsK, vk, so + 8192); g3 = ldc<1024>(rsK, vk, so + 8192); };
      auto check = [&]() {
        const u32x4 u0 = __builtin_bit_cast(u32x4, g0), u1 = __builtin_bit_cast(u32x4, g1), u2 = __builtin_bit_cast(u32x4, g2), u3 = __builtin_bit_cast(u32x4, g3);
        lst(px + w * AREA, (u32x4){u0[0], u0[2], u1[0], u1[2]});
        lst(px + w * AREA + 1024, (u32x4){u2[0], u2[2], u3[0], u3[2]});
        const unsigned bad = ((u0[1] ^ ep.lo) | (u1[1] ^ ep.lo) | (u2[1] ^ ep.lo) | (u3[1] ^ ep.lo)) | ((u0[3] ^ tagB) | (u1[3] ^ tagB) | (u2[3] ^ tagB) | (u3[3] ^ tagB));
        return (bool)__all(bad == 0);
      };
      waitv<8>();
      pre2rd();
      pre2(6, 9, 2);
      unroll<0, FW>(ldwo);
      fetch();
      unroll<FW, 14>(ldwo);
      waitv<14 - FW>(g0, g1, g2, g3);
      if (!check()) {
#pragma unroll 1
        for (int it = 0; it < SPIN; ++it) {
          fetch();
          waitv<0>(g0, g1, g2, g3);
          if (check()) break;
        }
      }
      STAMP(2);
      tie(sink);
      pre2w();
      bar();
      STAMP(11);
      bf16x8 kf[2][4];
#pragma unroll
      for (int hd = 0; hd < 2; hd++)
#pragma unroll
        for (int c = 0; c < 4; c++) kf[hd][c] = lld<bf16x8>(px + c * AREA + hd * 1024);
      SB();
      dma4(rsW2, v2, so2 + 9 * (32 * S * 2), area + 5 * 4096);
      dma4(rsW2, v2, so2 + 10 * (32 * S * 2), area + 6 * 4096);
      setm0(area + 7 * 4096);
      SB();
      f32x16 o[2];
      unroll<0, 8>([&](auto mm) {
        constexpr int m = mm.value, hd = m & 1, c = m >> 1;
        if constexpr (c == 0) mfma0(o[hd], kf[hd][c], fq[hd][c]); else mfmac(o[hd], kf[hd][c], fq[hd][c]);
        dma<(m & 3) * 1024>(rsW2, v2[m & 3], so2 + (11 + (m >> 2)) * (32 * S * 2));
        if constexpr (m == 3) setm0n(area + 8 * 4096);
        SB();
      });
      asm volatile("s_nop 11" : "+v"(o[0]), "+v"(o[1]));
      float da = o[0][12], db = o[1][12];
      swap32(da, db);
      float ia = __builtin_amdgcn_rcpf(db + 1e-6f), ib = ia;
      swap32(ia, ib);
      unsigned a[8], bq[8];
#pragma unroll
      for (int r = 0; r < 8; r++) { a[r] = pk(o[0][2 * r] * ia, o[0][2 * r + 1] * ia); bq[r] = pk(o[1][2 * r] * ib, o[1][2 * r + 1] * ib); }
      const rsrc_t st = mkst(ws + WS_OF, SZ_OF);
      const unsigned tk = (128 * T + 32 * w + n) * 16, jb = 14 * (pr >> 1);
      const unsigned vo = (jb + 6 * (pr & 1) + h) * (S * 16) + tk;
      const unsigned v1 = vo + 2 * S * 16, v2_ = vo + 4 * S * 16, v3 = h ? OOB : (jb + 12 + (pr & 1)) * (S * 16) + tk;
      const u32x4 r0 = {a[0], a[1], a[2], a[3]}, r1 = {a[4], a[5], bq[0], bq[1]}, r2 = {bq[2], bq[3], bq[4], bq[5]}, r3 = {a[6], a[7], bq[6], bq[7]};
      stc(st, vo, r0); stc(st, v1, r1); stc(st, v2_, r2); stc(st, v3, r3);
      STAMP(12);
      u32x2 f0 = ldc2(rsF, vfu);
      const unsigned vfo = tid == 0 ? (FL_O + T * 8 + pr) * 128 : OOB;
      publish<0>(rsF, vfo, epv);
      tie(f0);
      tiewo();
      hold(vk);
      hold(vo, v1, v2_, v3, r0, r1, r2, r3);
      miss = missing<4>(f0, ep);
      td = 13;
      hold(vfo, epv);
    }
  }

  if (i >= 4) {
    const unsigned orow = st2 * 64 + 16 * w + (lane & 15);
    const unsigned vo = orow * (D * 2) + dt2 * 128 + (lane >> 4) * 16;
    const u32x4 rsB = mkrs(b2, S * 2), rsU = mkrs(ws + WS_UF, SZ_UF), rsO = mkrs(ws + WS_OF, SZ_OF);
#pragma unroll 1
    for (; miss && td < 13; td += 2) {
      u32x2 f = ldc2(rsF, vfu);
      dma4(rsW2, v2, so2 + td * (32 * S * 2), area + (td - 4) * 4096);
      dma4(rsW2, v2, so2 + (td + 1) * (32 * S * 2), area + (td - 3) * 4096);
      waitv<8>(f);
      miss = missing<4>(f, ep);
    }
    if (miss) poll<4>(rsF, vfu, ep);
    STAMP(13);
    const unsigned vu0 = h * (D * 16) + (dt2 * 64 + n) * 16, vu1 = vu0 + 2 * D * 16, sou = w * 56 * (D * 16);
    const unsigned vof = h * (S * 16) + (st2 * 64 + n) * 16, soo = w * 14 * (S * 16);
    const unsigned vfl = lane < 2 ? (FL_O + (st2 >> 1) * 8 + 2 * w + lane) * 128 : OOB;
    bf16x8 fa[18][4], xr[2];
    unsigned bs, one, sel[4], pr_[2];
    float bv, yv[16];
    const unsigned vb2 = orow * 2;
    u32x2 fl;
    auto ldD = [&](auto tt, auto jj) {
      constexpr int t = tt.value, j = jj.value;
      if constexpr (t < 14) fa[t][j] = ldc<(j & 1) * 512>(rsU, (j >> 1) ? vu1 : vu0, sou + t * (4 * D * 16));
      else if constexpr (j < g2ld(t)) fa[t][j] = ldc<(j & 1) * 512>(rsO, vof, soo + (2 * (t - 14) + (j >> 1)) * (2 * S * 16));
    };
    unroll<0, G2R>([&](auto tt) { unroll<0, 4>([&](auto jj) { ldD(tt, jj); }); });
    dman(rsW2, v2, so2 + td * (32 * S * 2), 32 * S * 2, area + (td - 4) * 4096, 14 - td);
    const ldsp pw[2] = {pb[0] - h * 512, pb[1] - h * 512};
    f32x16 acc[2][2] = {};
    bf16x8 wb[19][2][2];
    auto rdB = [&](auto tt, auto kk, auto aa) {
      constexpr int t = tt.value, ks = kk.value, a = aa.value, j = 2 * (t - 14) + ks;
      if constexpr (t < 4) wb[t][ks][a] = w2r[t][ks][a];
      else if constexpr (t < 14) wb[t][ks][a] = cat(vtr(pb[a] + (t - 4) * 4096 + ks * 2048), vtr(pb[a] + (t - 4) * 4096 + ks * 2048 + 512));
      else if constexpr (j < 6) {
        constexpr int u = j % 3, rl = 56 * (j / 3) + (u == 0 ? 0 : u == 1 ? 16 : 36), rh = 56 * (j / 3) + (u == 0 ? 8 : u == 1 ? 28 : 44);
        wb[t][ks][a] = cat(vtr(pw[a] + rl * 128), vtr(pw[a] + rh * 128));
      } else if constexpr (j == 6) wb[t][ks][a] = cat(vtr(pw[a] + h * (52 * 128) + 24 * 128), vtr(pw[a] + h * (52 * 128) + 52 * 128));
    };
    unroll<0, 4>([&](auto cc) { rdB(IC<0>(), IC<(cc.value >> 1)>(), IC<(cc.value & 1)>()); });
    unroll<0, 18>([&](auto tt) {
      constexpr int t = tt.value, nk = t < 17 ? 2 : 1;
      constexpr int left = g2cnt(t - 3) + g2cnt(t - 2) + g2cnt(t - 1);
      if constexpr (nk == 2) waitv<left>(fa[t][0], fa[t][1], fa[t][2], fa[t][3]); else waitv<left>(fa[t][0], fa[t][1]);
      if constexpr (t == 3) hold(vfu);
      if constexpr (t == 14) { one = ((lane >> 3) & 1) == blk ? ((lane & 1) ? 0x3f800000u : 0x3f80u) : 0u; tie(one); }
      if constexpr (t == 16) { tie(xr[0]); tie(xr[1]); tie(bs); hold(vb2, vof); }
      if constexpr (t == G2POLL + G2R) {
        tie(fl);
        if (missing<2>(fl, ep)) poll<2>(rsF, vfl, ep);
      }
      SB();
      unroll<0, 4 * nk>([&](auto mm) {
        constexpr int m = mm.value, ks = m >> 2, nh = (m >> 1) & 1, mh = m & 1;
        if constexpr (t < 14) acc[nh][mh] = MFMA(fa[t][ks * 2 + mh], wb[t][ks][nh], acc[nh][mh]);
        else acc[nh][mh] = MFMA(wb[t][ks][mh], fa[t][ks * 2 + nh], acc[nh][mh]);
        SB();
        if constexpr (m & 1) rdB(IC<t + 1>(), IC<(m >> 2)>(), IC<((m >> 1) & 1)>());
        else if constexpr (t >= 5 && 4 * (t - 5) + m / 2 < 14) sta<(4 * (t - 5) + m / 2) * 1024>(area + lane * 16, wor[4 * (t - 5) + m / 2]);
        SB();
        if constexpr (!(m & 1)) ldD(IC<t + G2R>(), IC<m / 2>());
        if constexpr (t == G2POLL && m == 7) fl = ldc2(rsF, vfl);
        if constexpr (t == 12 && m == 0) xr[0] = ldp<0>(rsX, vo, 0);
        if constexpr (t == 12 && m == 2) xr[1] = ldp<64>(rsX, vo, 0);
        if constexpr (t == 12 && m == 4) bs = ldu16<0>(rsB, vb2, 0);
        if constexpr (t == 15 && m < 4) { sel[m] = ((lane & 7) >> 1) == m ? one : 0u; tie(sel[m]); }
        if constexpr (t == 15 && m >= 6) { pr_[m - 6] = lds0 + (h + 2 * (m - 6)) * AREA + (w >> 1) * 4096 + blk * 1024 + (16 * (w & 1) + (lane & 15)) * 16; tie(pr_[m - 6]); }
        if constexpr (t == 16 && m == 0) { bv = blo(bs); tie(bv); }
        if constexpr (t == 16) {
          const u32x4 xu = __builtin_bit_cast(u32x4, xr[m >> 2]);
          const unsigned xe = xu[m & 3];
          yv[2 * m] = blo(xe) + bv; yv[2 * m + 1] = bhi(xe) + bv;
          tie(yv[2 * m]); tie(yv[2 * m + 1]);
        }
        SB();
      });
    });
    STAMP(14);
    bf16x8 pt[8];
#pragma unroll
    for (int c = 0; c < 8; c++) {
      pt[c] = pack8s(acc[c >> 2][(c >> 1) & 1], 8 * (c & 1));
      lst(lds + w * AREA + c * 1024 + lane * 16, pt[c]);
      if (c) hold(pt[c - 1]);
      SB();
    }
    f32x4 y[2][2];
#pragma unroll
    for (int c = 0; c < 4; c++) y[c >> 1][c & 1] = (f32x4){yv[4 * c], yv[4 * c + 1], yv[4 * c + 2], yv[4 * c + 3]};
    u32x4 id = {sel[0], sel[1], sel[2], sel[3]};
    asm volatile("s_waitcnt lgkmcnt(0)\n\ts_barrier" : "+v"(y[0][0]), "+v"(y[0][1]), "+v"(y[1][0]), "+v"(y[1][1]), "+v"(id)::"memory");
#pragma unroll
    for (int c = 0; c < 8; c++) { pt[c] = lld<bf16x8>((ldsp)(uintptr_t)pr_[c >> 2] + ((c >> 1) & 1) * 2048 + (c & 1) * 512); SB(); }
#pragma unroll
    for (int c = 0; c < 8; c++) asm volatile("v_mfma_f32_16x16x32_bf16 %0, %1, %2, %0" : "+v"(y[(c >> 1) & 1][c & 1]) : "v"(id), "v"(pt[c]));
    asm volatile("s_nop 7" : "+v"(y[0][0]), "+v"(y[0][1]), "+v"(y[1][0]), "+v"(y[1][1]));
    u32x4 res[2];
    const rsrc_t sto = mkst(out, S * D * 2);
#pragma unroll
    for (int u = 0; u < 2; u++) {
      res[u] = (u32x4){pk(y[u][0][0], y[u][0][1]), pk(y[u][0][2], y[u][0][3]), pk(y[u][1][0], y[u][1][1]), pk(y[u][1][2], y[u][1][3])};
      __builtin_amdgcn_raw_buffer_store_b128(res[u], sto, vo + u * 64, 0, 16);
    }
    if (blockIdx.x == 255 && tid == 0) __builtin_amdgcn_raw_buffer_store_b64((u32x2){ep.lo, ep.hi}, mkst(ws, WS_FLAG_BYTES), 0, 0, 16);
    hold(res[0], res[1], vo);
  }
}

extern "C" void kernel_launch(void* const* d_in, const int* in_sizes, int n_in, void* d_out, int out_size, void* d_ws, size_t ws_size,
                              hipStream_t stream) {
  fused_kernel<<<256, 256, 0, stream>>>((const short*)d_in[0], (const short*)d_in[1], (const short*)d_in[2], (const short*)d_in[3],
                                        (const short*)d_in[5], (const short*)d_in[6], (const short*)d_in[7], (const short*)d_in[8],
                                        (short*)d_out, (char*)d_ws);
}
```

```cpp
#include <hip/hip_runtime.h>
#include <stdint.h>

typedef short bf16x8 __attribute__((ext_vector_type(8)));
typedef short s16x4 __attribute__((ext_vector_type(4)));
typedef float f32x2 __attribute__((ext_vector_type(2)));
typedef float f32x4 __attribute__((ext_vector_type(4)));
typedef float f32x16 __attribute__((ext_vector_type(16)));
typedef unsigned u32x2 __attribute__((ext_vector_type(2)));
typedef unsigned u32x4 __attribute__((ext_vector_type(4)));
typedef unsigned u32x8 __attribute__((ext_vector_type(8)));
typedef __bf16 b16x2 __attribute__((ext_vector_type(2)));
typedef __attribute__((address_space(3))) char* ldsp;
typedef __amdgpu_buffer_rsrc_t rsrc_t;

#define DEV __device__ __forceinline__
#define SB() __builtin_amdgcn_sched_barrier(0)
#define MFMA(a, b, c) __builtin_amdgcn_mfma_f32_32x32x16_bf16(a, b, c, 0, 0, 0)

constexpr int S = 2048, D = 448, F = 1792;
constexpr unsigned FL_P = 8, FL_O = FL_P + 128, FL_U = FL_O + 128;
constexpr unsigned WS_FLAG_BYTES = 65536;
constexpr unsigned WS_ST = WS_FLAG_BYTES, SZ_ST = S * 16;
constexpr unsigned WS_UF = WS_ST + SZ_ST, SZ_UF = 224 * 448 * 16;
constexpr unsigned WS_OF = WS_UF + SZ_UF, SZ_OF = 56 * 2048 * 16;
constexpr unsigned WS_KVP = WS_OF + SZ_OF, SZ_KVP = 16 * 16 * 4096;
constexpr unsigned WS_KVF = WS_KVP + SZ_KVP, SZ_KVF = 16 * 8 * 1024;
constexpr unsigned WS_END = WS_KVF + SZ_KVF;
constexpr unsigned AREA = 40960, L_STAT = 36864, SLOT = 12288;
constexpr unsigned L_WK = 0, L_WV = 50176, L_WQ = 100352, L_WPHI = 150528, L_PHI = 0, L_VX = 32768;
constexpr unsigned OOB = 0x40000000u;
constexpr int SPIN = 1 << 18;

DEV u32x4 mkrs(const void* p, unsigned bytes) {
  unsigned long long a = (unsigned long long)p;
  u32x4 r = {(unsigned)a, (unsigned)(a >> 32), bytes, 0x00020000u};
  return r;
}
DEV rsrc_t mkst(void* p, unsigned bytes) { return __builtin_amdgcn_make_buffer_rsrc(p, 0, bytes, 0x00020000); }
DEV void stc(rsrc_t rs, unsigned voff, u32x4 v) { __builtin_amdgcn_raw_buffer_store_b128(v, rs, voff, 0, 16); }
template <int OFF> DEV bf16x8 ldp(u32x4 rs, unsigned voff, unsigned soff) {
  bf16x8 r;
  asm volatile("buffer_load_dwordx4 %0, %2, %1, %3 offen offset:%4" : "=v"(r) : "s"(rs), "v"(voff), "s"(soff), "n"(OFF) : "memory");
  return r;
}
template <int OFF> DEV bf16x8 ldc(u32x4 rs, unsigned voff, unsigned soff) {
  bf16x8 r;
  asm volatile("buffer_load_dwordx4 %0, %2, %1, %3 offen offset:%4 sc1" : "=v"(r) : "s"(rs), "v"(voff), "s"(soff), "n"(OFF) : "memory");
  return r;
}
template <int OFF> DEV u32x2 ldp2(u32x4 rs, unsigned voff, unsigned soff) {
  u32x2 r;
  asm volatile("buffer_load_dwordx2 %0, %2, %1, %3 offen offset:%4" : "=v"(r) : "s"(rs), "v"(voff), "s"(soff), "n"(OFF) : "memory");
  return r;
}
template <int OFF> DEV u32x4 lda(u32x4 rs, unsigned voff, unsigned soff) {
  u32x4 r;
  asm volatile("buffer_load_dwordx4 %0, %2, %1, %3 offen offset:%4" : "=a"(r) : "s"(rs), "v"(voff), "s"(soff), "n"(OFF) : "memory");
  return r;
}
template <int OFF> DEV void sta(unsigned addr, u32x4 v) { asm volatile("ds_write_b128 %0, %1 offset:%2" ::"v"(addr), "a"(v), "n"(OFF) : "memory"); }
DEV u32x2 ldc2(u32x4 rs, unsigned voff) {
  u32x2 r;
  asm volatile("buffer_load_dwordx2 %0, %2, %1, 0 offen sc1" : "=v"(r) : "s"(rs), "v"(voff) : "memory");
  return r;
}
template <int OFF> DEV unsigned ldu16(u32x4 rs, unsigned voff, unsigned soff) {
  unsigned r;
  asm volatile("buffer_load_ushort %0, %2, %1, %3 offen offset:%4" : "=v"(r) : "s"(rs), "v"(voff), "s"(soff), "n"(OFF) : "memory");
  return r;
}
DEV void setm0(unsigned a) { asm volatile("s_mov_b32 m0, %0\n\ts_nop 0" ::"s"(a) : "memory"); }
DEV void setm0n(unsigned a) { asm volatile("s_mov_b32 m0, %0" ::"s"(a) : "memory"); }
template <int OFF> DEV void addm0(unsigned a) { asm volatile("s_add_u32 m0, %0, %1" ::"s"(a), "n"(OFF) : "memory", "scc"); }
template <int OFF> DEV void dma(u32x4 rs, unsigned voff, unsigned soff) {
  asm volatile("buffer_load_dwordx4 %1, %0, %2 offen offset:%3 lds" ::"s"(rs), "v"(voff), "s"(soff), "n"(OFF) : "memory");
}
DEV void dma4(u32x4 rs, const unsigned (&v)[4], unsigned soff, unsigned m0) {
  setm0(m0); dma<0>(rs, v[0], soff); dma<1024>(rs, v[1], soff); dma<2048>(rs, v[2], soff); dma<3072>(rs, v[3], soff);
}
template <int N> DEV void waitv() { asm volatile("s_waitcnt vmcnt(%0)" ::"n"(N) : "memory"); }
template <int N, class T> DEV void waitv(T& a) { asm volatile("s_waitcnt vmcnt(%1)" : "+v"(a) : "n"(N) : "memory"); }
template <int N, class T> DEV void waitv(T& a, T& b) { asm volatile("s_waitcnt vmcnt(%2)" : "+v"(a), "+v"(b) : "n"(N) : "memory"); }
template <int N, class T> DEV void waitv(T& a, T& b, T& c, T& d) {
  asm volatile("s_waitcnt vmcnt(%4)" : "+v"(a), "+v"(b), "+v"(c), "+v"(d) : "n"(N) : "memory");
}
template <class T> DEV void tie(T& a) { asm volatile("" : "+v"(a)::"memory"); }
DEV void tiea(u32x4& a) { asm volatile("" : "+a"(a)::"memory"); }
template <class T> DEV void hold(T a) { asm volatile("" ::"v"(a)); }
template <class T, class... R> DEV void hold(T a, R... r) { hold(a); hold(r...); }
DEV void keep(f32x16 a) { asm volatile("" ::"v"(a)); }
DEV void bar() { asm volatile("s_waitcnt lgkmcnt(0)\n\ts_barrier" ::: "memory"); }
DEV s16x4 vtr(ldsp p) { return __builtin_amdgcn_ds_read_tr16_b64_v4i16((__attribute__((address_space(3))) s16x4*)p); }
DEV bf16x8 cat(s16x4 a, s16x4 b) { return __builtin_shufflevector(a, b, 0, 1, 2, 3, 4, 5, 6, 7); }
DEV unsigned pk(float a, float b) {
  f32x2 v = {a, b};
  return __builtin_bit_cast(unsigned, __builtin_convertvector(v, b16x2));
}
DEV float blo(unsigned u) { return __builtin_bit_cast(float, u << 16); }
DEV float bhi(unsigned u) { return __builtin_bit_cast(float, u & 0xffff0000u); }
DEV void swap32(float& a, float& b) {
  auto r = __builtin_amdgcn_permlane32_swap(__builtin_bit_cast(unsigned, a), __builtin_bit_cast(unsigned, b), false, false);
  const unsigned r0 = r[0], r1 = r[1];
  a = __builtin_bit_cast(float, r0); b = __builtin_bit_cast(float, r1);
}
DEV void swap16(float& a, float& b) {
  auto r = __builtin_amdgcn_permlane16_swap(__builtin_bit_cast(unsigned, a), __builtin_bit_cast(unsigned, b), false, false);
  const unsigned r0 = r[0], r1 = r[1];
  a = __builtin_bit_cast(float, r0); b = __builtin_bit_cast(float, r1);
}
template <int C> DEV float ror(float z) {
  return __builtin_bit_cast(float, __builtin_amdgcn_update_dpp(0, __builtin_bit_cast(int, z), C, 0xf, 0xf, true));
}
DEV float rowsum(float z) { z += ror<0x128>(z); z += ror<0x124>(z); z += ror<0x122>(z); z += ror<0x121>(z); return z; }
template <class T> DEV void lst(ldsp p, T v) { *(__attribute__((address_space(3))) T*)p = v; }
template <class T> DEV T lld(ldsp p) { return *(__attribute__((address_space(3))) T*)p; }

struct Epoch { unsigned lo, hi; };
template <int N> DEV void publish(u32x4 rsF, unsigned vflag, u32x2 e) {
  asm volatile("s_waitcnt vmcnt(%3) lgkmcnt(0)\n\ts_barrier\n\tbuffer_store_dwordx2 %2, %1, %0, 0 offen sc1" ::"s"(rsF), "v"(vflag), "v"(e), "n"(N) : "memory");
}
template <int N> DEV unsigned missing(u32x2 f, Epoch e) {
  unsigned r;
  asm volatile("v_cmp_ne_u64 vcc, %2, %1\n\ts_and_b32 %0, vcc_lo, %3" : "=s"(r) : "v"(f), "s"(((unsigned long long)e.hi << 32) | e.lo), "n"((1u << N) - 1) : "vcc", "scc");
  return r;
}
template <int N> DEV void poll(u32x4 rsF, unsigned voff, Epoch e) {
#pragma unroll 1
  for (int it = 0; it < SPIN; ++it) {
    u32x2 f = ldc2(rsF, voff);
    waitv<0>(f);
    if (!missing<N>(f, e)) break;
    __builtin_amdgcn_s_sleep(1);
  }
}
DEV void collect(bf16x8 (&p)[16], u32x4 rsF, unsigned vf, u32x4 rsP, unsigned vp, unsigned so, Epoch e) {
  unsigned done = 0, it = SPIN, nw, t;
  u32x2 f;
  asm volatile("1:\n\tbuffer_load_dwordx2 %[f], %[vf], %[rsF], 0 offen sc1\n\ts_waitcnt vmcnt(0)\n\tv_cmp_eq_u64 vcc, %[e], %[f]\n\t"
               "s_lshr_b32 %[nw], vcc_lo, 1\n\ts_and_b32 %[nw], %[nw], vcc_lo\n\ts_and_b32 %[nw], %[nw], 0x5555\n\t"
               "s_andn2_b32 %[nw], %[nw], %[done]\n\ts_or_b32 %[done], %[done], %[nw]\n\t"
               "s_bitcmp1_b32 %[nw], 0\n\ts_cbranch_scc0 10f\n\ts_add_u32 %[t], %[so], 0\n\t"
               "buffer_load_dwordx4 %0, %[vp], %[rsP], %[t] offen sc1\n\tbuffer_load_dwordx4 %1, %[vp], %[rsP], %[t] offen offset:512 sc1\n10:\n\t"
               "s_bitcmp1_b32 %[nw], 2\n\ts_cbranch_scc0 11f\n\ts_add_u32 %[t], %[so], 8192\n\t"
               "buffer_load_dwordx4 %2, %[vp], %[rsP], %[t] offen sc1\n\tbuffer_load_dwordx4 %3, %[vp], %[rsP], %[t] offen offset:512 sc1\n11:\n\t"
               "s_bitcmp1_b32 %[nw], 4\n\ts_cbranch_scc0 12f\n\ts_add_u32 %[t], %[so], 16384\n\t"
               "buffer_load_dwordx4 %4, %[vp], %[rsP], %[t] offen sc1\n\tbuffer_load_dwordx4 %5, %[vp], %[rsP], %[t] offen offset:512 sc1\n12:\n\t"
               "s_bitcmp1_b32 %[nw], 6\n\ts_cbranch_scc0 13f\n\ts_add_u32 %[t], %[so], 24576\n\t"
               "buffer_load_dwordx4 %6, %[vp], %[rsP], %[t] offen sc1\n\tbuffer_load_dwordx4 %7, %[vp], %[rsP], %[t] offen offset:512 sc1\n13:\n\t"
               "s_bitcmp1_b32 %[nw], 8\n\ts_cbranch_scc0 14f\n\ts_add_u32 %[t], %[so], 32768\n\t"
               "buffer_load_dwordx4 %8, %[vp], %[rsP], %[t] offen sc1\n\tbuffer_load_dwordx4 %9, %[vp], %[rsP], %[t] offen offset:512 sc1\n14:\n\t"
               "s_bitcmp1_b32 %[nw], 10\n\ts_cbranch_scc0 15f\n\ts_add_u32 %[t], %[so], 40960\n\t"
               "buffer_load_dwordx4 %10, %[vp], %[rsP], %[t] offen sc1\n\tbuffer_load_dwordx4 %11, %[vp], %[rsP], %[t] offen offset:512 sc1\n15:\n\t"
               "s_bitcmp1_b32 %[nw], 12\n\ts_cbranch_scc0 16f\n\ts_add_u32 %[t], %[so], 49152\n\t"
               "buffer_load_dwordx4 %12, %[vp], %[rsP], %[t] offen sc1\n\tbuffer_load_dwordx4 %13, %[vp], %[rsP], %[t] offen offset:512 sc1\n16:\n\t"
               "s_bitcmp1_b32 %[nw], 14\n\ts_cbranch_scc0 17f\n\ts_add_u32 %[t], %[so], 57344\n\t"
               "buffer_load_dwordx4 %14, %[vp], %[rsP], %[t] offen sc1\n\tbuffer_load_dwordx4 %15, %[vp], %[rsP], %[t] offen offset:512 sc1\n17:\n\t"
               "s_sub_u32 %[it], %[it], 1\n\ts_cbranch_scc1 2f\n\ts_cmp_lg_u32 %[done], 0x5555\n\ts_cbranch_scc1 1b\n2:\n\ts_waitcnt vmcnt(0)"
               : "=&v"(p[0]), "=&v"(p[1]), "=&v"(p[2]), "=&v"(p[3]), "=&v"(p[4]), "=&v"(p[5]), "=&v"(p[6]), "=&v"(p[7]), "=&v"(p[8]), "=&v"(p[9]), "=&v"(p[10]), "=&v"(p[11]), "=&v"(p[12]), "=&v"(p[13]), "=&v"(p[14]), "=&v"(p[15]),
                 [f] "=&v"(f), [done] "+&s"(done), [it] "+&s"(it), [nw] "=&s"(nw), [t] "=&s"(t)
               : [vf] "v"(vf), [rsF] "s"(rsF), [vp] "v"(vp), [rsP] "s"(rsP), [so] "s"(so), [e] "s"(((unsigned long long)e.hi << 32) | e.lo)
               : "memory", "scc", "vcc");
}
DEV void dman(u32x4 rs, const unsigned (&v)[4], unsigned soff, unsigned step, unsigned m0, unsigned n) {
  asm volatile("s_cmp_eq_u32 %1, 0\n\ts_cbranch_scc1 2f\n\ts_mov_b32 m0, %8\n1:\n\ts_sub_u32 %1, %1, 1\n\t"
               "buffer_load_dwordx4 %3, %2, %0 offen lds\n\tbuffer_load_dwordx4 %4, %2, %0 offen offset:1024 lds\n\t"
               "buffer_load_dwordx4 %5, %2, %0 offen offset:2048 lds\n\tbuffer_load_dwordx4 %6, %2, %0 offen offset:3072 lds\n\t"
               "s_add_u32 m0, m0, 0x1000\n\ts_add_u32 %0, %0, %7\n\ts_cmp_lg_u32 %1, 0\n\ts_cbranch_scc1 1b\n2:"
               : "+&s"(soff), "+&s"(n)
               : "s"(rs), "v"(v[0]), "v"(v[1]), "v"(v[2]), "v"(v[3]), "s"(step), "s"(m0)
               : "memory", "scc");
}

DEV void acc_stats(bf16x8 v, float& s, float& q) {
  const u32x4 u = __builtin_bit_cast(u32x4, v);
#pragma unroll
  for (int e = 0; e < 4; e++) {
    const unsigned t = u[e];
    const b16x2 p = __builtin_bit_cast(b16x2, t);
    s = __builtin_amdgcn_fdot2_f32_bf16(p, __builtin_bit_cast(b16x2, 0x3f803f80u), s, false);
    q = __builtin_amdgcn_fdot2_f32_bf16(p, p, q, false);
  }
}
DEV unsigned affine2(unsigned u, float a, float c) {
  float t0, t1;
  unsigned r;
  asm("v_lshlrev_b32 %1, 16, %3\n\tv_and_b32 %2, 0xffff0000, %3\n\tv_fma_f32 %1, %1, %4, %5\n\tv_fma_f32 %2, %2, %4, %5\n\tv_cvt_pk_bf16_f32 %0, %1, %2"
      : "=v"(r), "=&v"(t0), "=&v"(t1)
      : "v"(u), "v"(a), "v"(c));
  return r;
}
DEV f32x16 mfmav(u32x4 a, bf16x8 b, f32x16 c) {
  f32x16 r;
  asm volatile("v_mfma_f32_32x32x16_bf16 %0, %1, %2, %3" : "=&v"(r) : "v"(a), "v"(b), "v"(c));
  return r;
}
DEV void mfmac(f32x16& c, bf16x8 a, u32x4 b) { asm volatile("v_mfma_f32_32x32x16_bf16 %0, %1, %2, %0" : "+v"(c) : "v"(a), "v"(b)); }
DEV void mfma0(f32x16& c, bf16x8 a, u32x4 b) { asm volatile("v_mfma_f32_32x32x16_bf16 %0, %1, %2, 0" : "=&v"(c) : "v"(a), "v"(b)); }
template <bool first> DEV void mfmab(f32x16& c, bf16x8 a, bf16x8 b) {
  if constexpr (first) mfma0(c, a, __builtin_bit_cast(u32x4, b)); else mfmac(c, a, __builtin_bit_cast(u32x4, b));
}
DEV void touch(unsigned& sink, u32x4 rs, unsigned voff, unsigned soff) {
  asm volatile("buffer_load_dword %0, %2, %1, %3 offen" : "+v"(sink) : "s"(rs), "v"(voff), "s"(soff) : "memory");
}
template <class T> DEV void touchp(unsigned& sink, u32x4 rs, unsigned voff, unsigned soff, T& pin) {
  asm volatile("buffer_load_dword %0, %3, %2, %4 offen" : "+v"(sink), "+v"(pin) : "s"(rs), "v"(voff), "s"(soff) : "memory");
}
DEV void touchl(u32x4 rs, unsigned voff, unsigned soff) {
  asm volatile("buffer_load_dword %1, %0, %2 offen lds" ::"s"(rs), "v"(voff), "s"(soff) : "memory");
}
template <int N> using IC = std::integral_constant<int, N>;
template <int T, int N, class Fn> DEV void unroll(Fn&& f) {
  if constexpr (T < N) { f(std::integral_constant<int, T>()); unroll<T + 1, N>(f); }
}
template <int OFF, class T> DEV void dmap(u32x4 rs, unsigned voff, unsigned soff, T& pin) {
  asm volatile("buffer_load_dwordx4 %2, %1, %3 offen offset:%4 lds" : "+v"(pin) : "s"(rs), "v"(voff), "s"(soff), "n"(OFF) : "memory");
}

DEV void minmax4(float& mx, float& mn, float x0, float n0, float a, float b, float c, float d) {
  asm("v_max3_f32 %0, %2, %4, %5\n\tv_min3_f32 %1, %3, %4, %5\n\tv_max3_f32 %0, %0, %6, %7\n\tv_min3_f32 %1, %1, %6, %7"
      : "=&v"(mx), "=&v"(mn)
      : "v"(x0), "v"(n0), "v"(a), "v"(b), "v"(c), "v"(d));
}
template <class Fn> DEV void features(const f32x16& p, bool h, unsigned (&ph)[2][8], Fn&& cb) {
  constexpr float L = 1.4426950408889634f;
  float x0, n0, x1, n1, mx, mn, mx2, mn2;
  minmax4(x0, n0, p[0], p[0], p[0], p[1], p[2], p[3]);
  minmax4(x1, n1, x0, n0, p[4], p[5], p[6], p[7]);
  minmax4(mx, mn, x1, n1, p[8], p[9], p[10], p[11]);
  cb(IC<0>(), mx);
  minmax4(mx2, mn2, mx, mn, p[12], p[13], p[14], p[15]);
  mx = h ? mx : mx2; mn = h ? -mn : -mn2;
  swap32(mx, mn);
  float c0 = fminf(-L * mx, -L * mn), c1 = c0;
  cb(IC<1>(), c0);
  swap32(c0, c1);
  const float ninf = -__builtin_inff();
  const float d0 = h ? ninf : c0, d1 = h ? ninf : c1;
  f32x2 e[2][8], s0, s1;
  unroll<0, 8>([&](auto rr) {
    constexpr int r = rr.value;
    const f32x2 v = {p[2 * r], p[2 * r + 1]};
    const f32x2 a0 = __builtin_elementwise_fma(v, (f32x2)(L), (f32x2)(r < 6 ? c0 : d0));
    const f32x2 a1 = __builtin_elementwise_fma(v, (f32x2)(-L), (f32x2)(r < 6 ? c1 : d1));
    e[0][r] = (f32x2){__builtin_amdgcn_exp2f(a0.x), __builtin_amdgcn_exp2f(a0.y)};
    e[1][r] = (f32x2){__builtin_amdgcn_exp2f(a1.x), __builtin_amdgcn_exp2f(a1.y)};
    if constexpr (r == 0) { s0 = e[0][0]; s1 = e[1][0]; } else { s0 += e[0][r]; s1 += e[1][r]; }
    cb(IC<2 + r>(), s1);
    SB();
  });
  float t0 = s0.x + s0.y, t1 = s1.x + s1.y;
  swap32(t0, t1);
  float i0 = __builtin_amdgcn_rcpf(t0 + t1), i1 = i0;
  swap32(i0, i1);
  unroll<0, 8>([&](auto rr) {
    constexpr int r = rr.value;
    const f32x2 u0 = e[0][r] * i0, u1 = e[1][r] * i1;
    ph[0][r] = pk(u0.x, u0.y); ph[1][r] = pk(u1.x, u1.y);
    if constexpr ((r & 3) == 1) cb(IC<10 + r / 4>(), ph[1][r]);
  });
}
DEV bf16x8 pack8(const f32x16& a, int o) {
  u32x4 u = {pk(a[o], a[o + 1]), pk(a[o + 2], a[o + 3]), pk(a[o + 4], a[o + 5]), pk(a[o + 6], a[o + 7])};
  return __builtin_bit_cast(bf16x8, u);
}
DEV bf16x8 pack8s(const f32x16& a, int o) {
  float t0 = a[o], t1 = a[o + 1], t2 = a[o + 2], t3 = a[o + 3], t4 = a[o + 4], t5 = a[o + 5], t6 = a[o + 6], t7 = a[o + 7];
  asm volatile("" : "+v"(t0), "+v"(t1), "+v"(t2), "+v"(t3), "+v"(t4), "+v"(t5), "+v"(t6), "+v"(t7));
  u32x4 u = {pk(t0, t1), pk(t2, t3), pk(t4, t5), pk(t6, t7)};
  return __builtin_bit_cast(bf16x8, u);
}
DEV f32x2 gelu2(f32x2 a) {
  constexpr float c1 = -2.302208198f, c2 = c1 * 0.044715f;
  const f32x2 z = a * __builtin_elementwise_fma(a * a, (f32x2)(c2), (f32x2)(c1));
  const f32x2 e = (f32x2){__builtin_amdgcn_exp2f(z.x), __builtin_amdgcn_exp2f(z.y)} + 1.f;
  return a * (f32x2){__builtin_amdgcn_rcpf(e.x), __builtin_amdgcn_rcpf(e.y)};
}

constexpr int woff(int k) { return (k >> 1) * SLOT + (k & 1) * 8192; }
constexpr int xoff(int k) { return (k >> 1) * SLOT + 4096 + (k & 1) * 2048; }
constexpr int TA0 = 22, TA1 = 13, QP = 9, NWO = 7, DE = 2, NE = 8, FW = 8, G2R = 4, G2POLL = 6;
constexpr int g2ld(int t) { return t < 17 ? 4 : (t == 17 ? 2 : 0); }
constexpr int g2cnt(int s) { return s < 0 ? 4 : g2ld(s + G2R) + (s == G2POLL ? 1 : 0) + (s == 12 ? 3 : 0); }

__global__ __launch_bounds__(256, 1) void fused_kernel(const short*, const short*, const short*, const short*, const short*, const short*, const short*,
                                                       const short*, short*, char*) {
  u32x8 ka, kb;
  u32x4 kc;
  asm volatile("s_load_dwordx8 %0, %3, 0x0\n\ts_load_dwordx8 %1, %3, 0x20\n\ts_load_dwordx4 %2, %3, 0x40"
               : "=&s"(ka), "=&s"(kb), "=&s"(kc)
               : "s"(__builtin_amdgcn_kernarg_segment_ptr())
               : "memory");
  SB();
  __shared__ __attribute__((aligned(16))) char lds_[163840];
  const ldsp lds = (ldsp)lds_;
  const unsigned lds0 = (unsigned)(uintptr_t)lds;
  const int tid = threadIdx.x, lane = tid & 63, w = __builtin_amdgcn_readfirstlane(tid >> 6);
  const int xc = blockIdx.x & 7, i = blockIdx.x >> 3;
  const int h = lane >> 5, n = lane & 31, q = (lane & 15) >> 2, pp = lane & 3, blk = (lane >> 4) & 1;
  const unsigned area = lds0 + w * AREA;
  const int tile2 = 28 * (xc & 1) + i - 4, dt2 = tile2 >> 3, st2 = 8 * (xc >> 1) + (tile2 & 7);
  const unsigned swz = (lane >> 3), swc = ((lane & 7) ^ (((lane >> 4) & 1) << 2)) * 16;
  unsigned v2[4];
#pragma unroll
  for (int j = 0; j < 4; j++) v2[j] = i >= 4 ? swz * (S * 2) + swc + j * (8 * S * 2 - 1024) : OOB;
  unsigned so2 = (w * 448) * (S * 2) + st2 * 128;
  const bool isA = i >= 16;
  const int ja = i - 16, T = (ja & 12) + 2 * (xc & 1) + ((ja >> 1) & 1), pr = (xc & 6) + (ja & 1);
  const int tk0 = 256 * (xc & 1) + 64 * (xc >> 1);
  auto stage0 = [&](int st) { return ((tk0 ^ (st << 5)) & 256) + ((tk0 + 32 * st) & 255); };
  const int row0 = 512 * (i >> 2) + tk0 + 16 * (i & 3) + 4 * w;
  unsigned vt1 = xc < 7 ? (lane >> 2) * (F * 2) + (lane & 3) * 128 : OOB;
  unsigned vo1 = lane < 56 ? lane * 16 : OOB, vz = 0, sx0 = (isA ? 128 * T + 32 * w : row0) * (D * 2), ring = lds0 + L_WQ + w * 12544, vxa[4];
#pragma unroll
  for (int j = 0; j < 4; j++) vxa[j] = (8 * j + swz) * (D * 2) + (((lane & 7) ^ ((4 * j + (lane >> 4)) & 7)) * 16) - j * 1024;
  ldsp pb[2];
#pragma unroll
  for (int a = 0; a < 2; a++) pb[a] = lds + w * AREA + h * 1024 + q * 128 + (((4 * a + 2 * blk + (pp >> 1)) ^ ((q >> 1) << 2)) * 16) + (pp & 1) * 8;
  asm volatile("" : "+v"(v2[0]), "+v"(v2[1]), "+v"(v2[2]), "+v"(v2[3]), "+v"(vxa[0]), "+v"(vxa[1]), "+v"(vxa[2]), "+v"(vxa[3]), "+v"(vt1), "+v"(vo1), "+v"(vz),
               "+v"(pb[0]), "+v"(pb[1]), "+s"(so2), "+s"(sx0), "+s"(ring));
  SB();
  asm volatile("s_waitcnt lgkmcnt(0)" : "+s"(ka), "+s"(kb), "+s"(kc)::"memory");
  auto arg = [](unsigned lo, unsigned hi) { return (char*)(((unsigned long long)hi << 32) | lo); };
  const short *x = (short*)arg(ka[0], ka[1]), *wqkv = (short*)arg(ka[2], ka[3]), *wphi = (short*)arg(ka[4], ka[5]), *wo = (short*)arg(ka[6], ka[7]);
  const short *w1 = (short*)arg(kb[0], kb[1]), *b1 = (short*)arg(kb[2], kb[3]), *w2 = (short*)arg(kb[4], kb[5]), *b2 = (short*)arg(kb[6], kb[7]);
  short* out = (short*)arg(kc[0], kc[1]);
  char* ws = arg(kc[2], kc[3]);
  const u32x4 rsF = mkrs(ws, WS_FLAG_BYTES), rsX = mkrs(x, S * D * 2), rsW2 = mkrs(w2, F * S * 2);
  auto pre2 = [&](int t0, int t1, int slot0) {
#pragma unroll 1
    for (int t = t0; t < t1; t++) dma4(rsW2, v2, so2 + t * (32 * S * 2), area + (slot0 + t - t0) * 4096);
  };

  bf16x8 w2r[4][2][2];
  auto pre2w = [&]() {
#pragma unroll
    for (int t = 0; t < 4; t++) asm volatile("s_waitcnt lgkmcnt(0)" : "+v"(w2r[t][0][0]), "+v"(w2r[t][0][1]), "+v"(w2r[t][1][0]), "+v"(w2r[t][1][1])::"memory");
  };
  auto pre2rd = [&]() {
#pragma unroll
    for (int t = 0; t < 4; t++)
#pragma unroll
      for (int ks = 0; ks < 2; ks++)
#pragma unroll
        for (int a = 0; a < 2; a++) w2r[t][ks][a] = cat(vtr(pb[a] + (t + 6) * 4096 + ks * 2048), vtr(pb[a] + (t + 6) * 4096 + ks * 2048 + 512));
  };

  u32x4 wor[14];
  unsigned vwo[4];
#pragma unroll
  for (int j = 0; j < 4; j++) vwo[j] = i >= 4 ? swz * (D * 2) + swc + j * (8 * D * 2 - 1024) : OOB;
  const u32x4 rsWo = mkrs(wo, D * D * 2);
  const unsigned sow = w * 112 * (D * 2) + dt2 * 128;
  auto ldwo = [&](auto jj) { wor[jj.value] = lda<(jj.value & 3) * 1024>(rsWo, vwo[jj.value & 3], sow + (jj.value >> 2) * (32 * D * 2)); };
  auto tiewo = [&]() { unroll<0, 14>([&](auto jj) { tiea(wor[jj.value]); }); hold(vwo[0], vwo[1], vwo[2], vwo[3]); };
  const u32x4 rsW1 = mkrs(w1, S * F * 2);
  auto so_t1 = [&](int stage, int wq, int r0) { return (512 * wq + stage0(stage) + r0) * (F * 2) + xc * 512; };
  const unsigned vfu = lane < 4 && i >= 4 ? (FL_U + dt2 * 14 + ((7 * w) >> 1) + lane) * 128 : OOB;
  unsigned miss = 1, td = 14;
  Epoch ep;
  auto setep = [&](u32x2 gen) {
    const unsigned long long g = (((unsigned long long)__builtin_amdgcn_readfirstlane(gen.y) << 32) | (unsigned)__builtin_amdgcn_readfirstlane(gen.x)) + 1;
    ep.lo = (unsigned)g; ep.hi = (unsigned)(g >> 32);
  };

  if (!isA) {
    bf16x8 xv[4];
    u32x2 gen, bb[4];
    xv[0] = ldp<0>(rsX, vo1, sx0); xv[1] = ldp<896>(rsX, vo1, sx0); xv[2] = ldp<1792>(rsX, vo1, sx0); xv[3] = ldp<2688>(rsX, vo1, sx0);
    gen = ldc2(rsF, vz);
    SB();
    const bool isG = xc < 7 && i < 14;
    const int ft = 2 * xc + (i >= 7), dt = i >= 7 ? i - 7 : i;
    const unsigned vt = lane < 32 ? vt1 : OOB, vb = h * 8;
    {
      setm0(area + L_STAT);
      touchl(rsW1, vt, so_t1(i >> 2, i & 3, 8 * w));
      const u32x4 rsB = mkrs(b1, F * 2);
      const unsigned sb = (ft * 128 + w * 32) * 2;
      bb[0] = ldp2<0>(rsB, vb, sb); bb[1] = ldp2<16>(rsB, vb, sb); bb[2] = ldp2<32>(rsB, vb, sb); bb[3] = ldp2<48>(rsB, vb, sb);
    }
    unsigned v1[4], vx[4];
#pragma unroll
    for (int j = 0; j < 4; j++) {
      v1[j] = (8 * (j & 1) + swz) * (F * 2) + (j >> 1) * 128 + swc - j * 1024;
      vx[j] = (8 * j + swz) * (D * 2) + swc - j * 1024;
    }
    waitv<5>(xv[0], xv[1], xv[2], xv[3]);
    tie(gen);
    setep(gen);
    const unsigned tagB = ep.lo ^ ep.hi, vs = (lane & 15) ? OOB : (row0 + h + 2 * blk) * 16;
    u32x4 rec;
    {
      float s[4] = {}, z[4] = {};
#pragma unroll
      for (int r = 0; r < 4; r++) acc_stats(xv[r], s[r], z[r]);
      swap32(s[0], s[1]); swap32(s[2], s[3]); swap32(z[0], z[1]); swap32(z[2], z[3]);
      float sa = s[0] + s[1], sb = s[2] + s[3], za = z[0] + z[1], zb = z[2] + z[3];
      swap16(sa, sb); swap16(za, zb);
      const float mu = rowsum(sa + sb) * (1.f / D), ms = rowsum(za + zb) * (1.f / D);
      const float var = fmaxf(ms - mu * mu, 0.f);
      float a = __builtin_amdgcn_rsqf(var + 1e-5f);
      a *= __builtin_amdgcn_rsqf(var * a * a + 1e-5f);
      const unsigned a2 = pk(a, a);
      const float c = -mu * blo(a2);
      rec = (u32x4){a2, ep.lo, __builtin_bit_cast(unsigned, c), tagB};
      stc(mkst(ws + WS_ST, SZ_ST), vs, rec);
    }
    asm volatile("s_barrier" ::: "memory");
    hold(vz, vo1, vt, vb);

    if (isG) {
      const unsigned b1s = (w * 512) * (F * 2) + ft * 256, bxs = (w * 512) * (D * 2) + dt * 128;
      auto mset = [&](auto kk, auto jj) {
        constexpr int ks = kk.value, j = jj.value;
        if constexpr ((ks & 1) ? j == 2 : j == 0) setm0n(area + woff(ks));
        if constexpr (!(ks & 1) && j == 4) setm0n(area + xoff(ks));
      };
      auto dgrp = [&](auto kk, auto jj, unsigned s1, unsigned sx) {
        constexpr int ks = kk.value, j = jj.value, jw = (ks & 1) ? j - 2 : j, jx = (ks & 1) ? j : j - 4;
        if constexpr (jw >= 0 && jw < 4) dma<jw * 1024>(rsW1, v1[jw], s1 + ks * (16 * F * 2));
        else dma<(jx + 2 * (ks & 1)) * 1024>(rsX, vx[jx + 2 * (ks & 1)], sx + (ks >> 1) * (32 * D * 2));
      };
      auto fill = [&](auto kk) {
        unroll<0, 6>([&](auto jj) { mset(kk, jj); asm volatile("s_nop 0"); dgrp(kk, jj, b1s + tk0 * (F * 2), bxs + tk0 * (D * 2)); });
      };
      const u32x4 rsS = mkrs(ws + WS_ST, SZ_ST);
      auto gather = [&]() {
        bf16x8 g[8];
#pragma unroll
        for (int j = 0; j < 8; j++) g[j] = ldc<0>(rsS, lane * 16, w * 8192 + stage0(2 * j) * 16);
        waitv<0>();
        bool ok = true;
#pragma unroll
        for (int j = 0; j < 8; j++) {
          tie(g[j]);
          const u32x4 u = __builtin_bit_cast(u32x4, g[j]);
          ok &= u[1] == ep.lo && u[3] == tagB;
          const ldsp p = lds + w * AREA + L_STAT + j * 512 + (lane >> 4) * 128 + (lane & 15) * 4;
          lst(p, u[2]); lst(p + 64, u[0]);
        }
        return (bool)__all(ok);
      };
      unroll<0, 4>(fill);
      hold(vs, rec);
#pragma unroll 1
      for (int it = 0; it < SPIN; ++it)
        if (gather()) break;
      tie(bb[0]); tie(bb[1]); tie(bb[2]); tie(bb[3]);
      f32x16 acc[4][2];
#pragma unroll
      for (int g = 0; g < 4; g++) {
        acc[0][0][4 * g] = blo(bb[g].x); acc[0][0][4 * g + 1] = bhi(bb[g].x); acc[0][0][4 * g + 2] = blo(bb[g].y); acc[0][0][4 * g + 3] = bhi(bb[g].y);
      }
      acc[0][1] = acc[0][0];
#pragma unroll
      for (int a = 1; a < 4; a++) { acc[a][0] = (f32x16)(0.f); acc[a][1] = (f32x16)(0.f); }
      unsigned pa[4], px[2], msk[4], psc = area + L_STAT + h * 32, psa;
      {
        const unsigned rowb = area + (8 * h + q) * 128 + (pp & 1) * 8, ch = 2 * blk + (pp >> 1), fl = (q >> 1) << 2;
#pragma unroll
        for (int a = 0; a < 4; a++) pa[a] = rowb + ((a ^ w) >> 1) * 2048 + ((4 * ((a ^ w) & 1) + ch) ^ fl) * 16;
        px[0] = rowb + (ch ^ fl) * 16; px[1] = rowb + ((4 + ch) ^ fl) * 16;
        const unsigned r = 4 * ((lane >> 3) & 1) + pp, on = (!blk && (q & 1) == h) ? ((r & 1) ? 0xffff0000u : 0xffffu) : 0u;
#pragma unroll
        for (int j = 0; j < 4; j++) { msk[j] = (r >> 1) == j ? on : 0u; tie(msk[j]); }
        psa = area + L_STAT + 64 + (8 * h + r) * 4;
      }
      s16x4 wl[2][4], wh[2][4], xl[2], xh[2];
      u32x4 hf[2][2], dg;
      unsigned av;
      bf16x8 xp[2];
      f32x16 hn[2], cc = {};
      tie(cc);
      auto rdX = [&](auto nn, auto dd) {
        constexpr int o = xoff(nn.value & 3), d = dd.value;
        xl[d] = vtr((ldsp)(uintptr_t)px[d] + o); xh[d] = vtr((ldsp)(uintptr_t)px[d] + o + 512);
      };
      auto rdA = [&](auto nn) { av = lld<unsigned>((ldsp)(uintptr_t)psa + nn.value * 128); };
      auto rdC = [&](auto nn) {
        const f32x4 c0 = lld<f32x4>((ldsp)(uintptr_t)psc + nn.value * 128), c1 = lld<f32x4>((ldsp)(uintptr_t)psc + nn.value * 128 + 16);
#pragma unroll
        for (int e = 0; e < 4; e++) { cc[e] = c0[e]; cc[4 + e] = c1[e]; }
      };
      auto rdW = [&](auto nn, auto aa) {
        constexpr int o = woff(nn.value & 3), nx = nn.value & 1, a = aa.value;
        wl[nx][a] = vtr((ldsp)(uintptr_t)pa[a] + o); wh[nx][a] = vtr((ldsp)(uintptr_t)pa[a] + o + 512);
      };
      auto mkD = [&]() { dg = (u32x4){av & msk[0], av & msk[1], av & msk[2], av & msk[3]}; };
      auto nrm = [&](auto dd) { hn[dd.value] = mfmav(dg, cat(xl[dd.value], xh[dd.value]), cc); };
      auto cvt = [&](auto nn, auto dd) {
        constexpr int d = dd.value;
        hf[nn.value & 1][d] = (u32x4){pk(hn[d][0], hn[d][1]), pk(hn[d][2], hn[d][3]), pk(hn[d][4], hn[d][5]), pk(hn[d][6], hn[d][7])};
        SB();
        keep(hn[d]);
      };
      auto early = [&](auto kk) {
        constexpr int k = kk.value;
        if constexpr (k < NE) {
          if constexpr (!(k & 3)) setm0(area + (5 - (k >> 2)) * 4096);
          dma<(k & 3) * 1024>(rsW2, v2[k & 3], so2 + (9 - (k >> 2)) * (32 * S * 2));
        }
      };
      auto step = [&](auto kk, auto wn, auto last, auto&& ms, auto&& gm) {
        constexpr int ks = kk.value;
        constexpr bool more = !last.value;
        using NX = IC<ks + 1>;
        auto mm = [&](auto m_) {
          constexpr int a = more ? m_.value >> 1 : ((m_.value >> 1) + 1) & 3, dh = m_.value & 1;
          mfmac(acc[a][dh], cat(wl[ks & 1][a], wh[ks & 1][a]), hf[ks & 1][dh]);
          SB();
          if constexpr (!more && m_.value >= 2) {
            constexpr int c = m_.value >> 1;
            unroll<0, 2>([&](auto gg) {
              constexpr int g = gg.value, j = 2 * (m_.value - 2) + g;
              if constexpr (j == 0) asm volatile("s_nop 9" : "+v"(acc[1][0]));
              xp[g] = pack8(acc[c][dh], 8 * g);
              lst(lds + w * AREA + lane * 16 + (c - 1) * 4096 + dh * 2048 + g * 1024, xp[g]);
              if constexpr (j) hold(xp[g ^ 1]);
              if constexpr (j < NWO) ldwo(IC<j>());
              else early(IC<j - NWO>());
              SB();
            });
          }
        };
        __builtin_amdgcn_s_waitcnt(0x70 | (wn.value & 15) | ((wn.value >> 4) << 14));
        asm volatile("" ::: "memory");
        SB();
        if constexpr (more) { rdX(NX(), IC<0>()); rdA(NX()); }
        ms(IC<0>());
        SB();
        mm(IC<0>());
        gm(IC<0>());
        if constexpr (more) { rdC(NX()); rdX(NX(), IC<1>()); SB(); }
        mm(IC<1>());
        gm(IC<1>());
        ms(IC<2>());
        if constexpr (more) { rdW(NX(), IC<0>()); rdW(NX(), IC<1>()); SB(); }
        mm(IC<2>());
        if constexpr (more) { mkD(); SB(); }
        gm(IC<2>());
        if constexpr (more) { rdW(NX(), IC<2>()); SB(); nrm(IC<0>()); SB(); rdW(NX(), IC<3>()); SB(); }
        mm(IC<3>());
        if constexpr (more) { nrm(IC<1>()); SB(); }
        gm(IC<3>());
        ms(IC<4>());
        mm(IC<4>());
        if constexpr (more) { cvt(NX(), IC<0>()); SB(); }
        mm(IC<5>());
        gm(IC<4>());
        mm(IC<6>());
        if constexpr (more) { cvt(NX(), IC<1>()); SB(); }
        gm(IC<5>());
        mm(IC<7>());
      };
      asm volatile("s_waitcnt lgkmcnt(0)" ::: "memory");
      rdX(IC<0>(), IC<0>()); rdX(IC<0>(), IC<1>()); rdA(IC<0>()); rdC(IC<0>());
      unroll<0, 4>([&](auto jj) { rdW(IC<0>(), jj); });
      mkD();
      SB();
      asm volatile("s_nop 1");
      nrm(IC<0>()); nrm(IC<1>());
      asm volatile("s_nop 7\n\ts_nop 7");
      cvt(IC<0>(), IC<0>()); cvt(IC<0>(), IC<1>());
      SB();
#pragma unroll 1
      for (int t = 2; t < 16; t += 2) {
        const unsigned tk = stage0(t);
        const unsigned s1 = b1s + tk * (F * 2), sx = bxs + tk * (D * 2);
        unroll<0, 4>([&](auto kk) {
          step(kk, IC<12>(), IC<0>(), [&](auto jj) { mset(kk, jj); SB(); }, [&](auto jj) { dgrp(kk, jj, s1, sx); SB(); });
        });
        psc += 512; tie(psc); psa += 512; tie(psa);
        SB();
      }
      {
        unroll<0, 4>([&](auto kk) {
          constexpr int ks = kk.value;
          if constexpr (ks == 1) hold(v1[0], v1[1], v1[2], v1[3], vx[0], vx[1], vx[2], vx[3]);
          step(kk, IC<(ks == 0 ? 12 : ks == 1 ? 10 : ks == 2 ? 8 : 63)>(), IC<(ks == 3)>(),
               [&](auto jj) { if constexpr (jj.value == 0) { setm0n(area + (6 + ks) * 4096); SB(); } },
               [&](auto jj) {
                 constexpr int j = jj.value;
                 if constexpr (j < 4) dma<j * 1024>(rsW2, v2[j], so2 + ks * (32 * S * 2));
                 SB();
               });
        });
      }
      bf16x8 sl[2];
      {
        u32x4 s0, s1;
#pragma unroll
        for (int j = 0; j < 4; j++) {
          const unsigned e = msk[j] & 0x3f803f80u;
          auto r = __builtin_amdgcn_permlane16_swap(0u, e, false, false);
          const unsigned o = r[0];
          s0[j] = e; s1[j] = o;
        }
        sl[0] = __builtin_bit_cast(bf16x8, s0); sl[1] = __builtin_bit_cast(bf16x8, s1);
        tie(sl[0]); tie(sl[1]);
      }
      unsigned vo = (ft * 16 + w * 4 + h) * (D * 16) + (dt * 64 + n) * 16, vo2 = vo + 2 * D * 16, px3[3];
      tie(vo); tie(vo2);
#pragma unroll
      for (int a = 0; a < 3; a++) { px3[a] = lds0 + (w ^ (a + 1)) * AREA + lane * 16; tie(px3[a]); }
      bar();
      {
        const rsrc_t st = mkst(ws + WS_UF, SZ_UF);
        u32x4 v[12], res[4];
#pragma unroll
        for (int c = 0; c < 12; c++) { v[c] = lld<u32x4>((ldsp)(uintptr_t)px3[c % 3] + (c % 3) * 4096 + (c / 3) * 1024); SB(); }
        unroll<0, 12>([&](auto cc_) {
          constexpr int c = cc_.value;
          mfmac(acc[0][c / 6], sl[(c / 3) & 1], v[c]);
          if constexpr (NWO + c < 14) ldwo(IC<NWO + c>());
          else early(IC<c - 2>());
        });
        unroll<10, 12>(early);
        asm volatile("s_nop 11" : "+v"(acc[0][0]), "+v"(acc[0][1]));
#pragma unroll
        for (int dh = 0; dh < 2; dh++) {
#pragma unroll
          for (int g = 0; g < 4; g += 2) {
            float r[8];
#pragma unroll
            for (int e = 0; e < 8; e += 2) {
              const f32x2 z = gelu2((f32x2){acc[0][dh][4 * g + e], acc[0][dh][4 * g + e + 1]});
              r[e] = z.x; r[e + 1] = z.y;
            }
            auto r0 = __builtin_amdgcn_permlane32_swap(pk(r[0], r[1]), pk(r[4], r[5]), false, false);
            auto r1 = __builtin_amdgcn_permlane32_swap(pk(r[2], r[3]), pk(r[6], r[7]), false, false);
            res[2 * dh + g / 2] = (u32x4){r0[0], r1[0], r0[1], r1[1]};
            stc(st, (g ? vo2 : vo) + dh * 512, res[2 * dh + g / 2]);
            SB();
          }
        }
        waitv<4>();
        tiewo();
        pre2rd();
        pre2w();
        hold(res[0], res[1], res[2], res[3], vo, vo2);
      }
      const unsigned vfg = tid == 0 ? (FL_U + dt * 14 + ft) * 128 : OOB;
      u32x2 epv = {ep.lo, ep.hi};
      tie(epv);
      publish<0>(rsF, vfg, epv);
      u32x2 f0 = ldc2(rsF, vfu);
      pre2(4, 10 - NE / 4, 0);
      waitv<24 - NE>(f0);
      hold(vfg, epv);
      miss = missing<4>(f0, ep);
      td = 10;
    } else {
      if (xc < 7) {
#pragma unroll 1
        for (int m = 4; m < 9; m++) {
          if (m == 4) __builtin_amdgcn_s_sleep(24); else __builtin_amdgcn_s_sleep(10);
          touchl(rsW1, vt1, so_t1(m, 2 * (i - 14) + (w >> 1), 16 * (w & 1)));
        }
      }
      if (i >= 14) {
#pragma unroll 1
        for (int m = 0; m < 4; m++) touchl(mkrs(wo, D * D * 2), (lane >> 2) * (D * 2) + (lane & 3) * 128, (128 * m + 64 * (i - 14) + 16 * w) * (D * 2) + (xc & 1) * 384);
#pragma unroll 1
        for (int m = 0; m < 4; m++) touchl(rsW2, (lane >> 3) * (S * 2) + (lane & 7) * 128, (448 * m + 384 + 32 * (i - 14) + 8 * w) * (S * 2) + (xc >> 1) * 1024);
        touchl(mkrs(b2, S * 2), lane * 128, 0);
        const int head = 2 * xc + i - 14;
        const u32x4 rsP = mkrs(ws + WS_KVP, SZ_KVP);
        const unsigned so = head * 65536 + w * 1024, vf = lane < 16 ? (FL_P + xc * 16 + lane) * 128 : OOB, vp = h * 4096 + n * 16;
        const unsigned one = ((lane >> 3) & 1) == blk ? ((lane & 1) ? 0x3f800000u : 0x3f80u) : 0u;
        u32x4 id;
#pragma unroll
        for (int j = 0; j < 4; j++) id[j] = ((lane & 7) >> 1) == j ? one : 0u;
        f32x4 a[2] = {};
        tie(id); tie(a[0]); tie(a[1]);
        bf16x8 pv[16];
        collect(pv, rsF, vf, rsP, vp, so, ep);
#pragma unroll
        for (int t = 0; t < 16; t++) asm volatile("v_mfma_f32_16x16x32_bf16 %0, %1, %2, %0" : "+v"(a[t & 1]) : "v"(id), "v"(pv[t]));
        asm volatile("s_nop 7" : "+v"(a[0]), "+v"(a[1]));
        const rsrc_t st = mkst(ws + WS_KVF, SZ_KVF);
        const u32x4 r0 = {pk(a[0][0], a[0][1]), ep.lo, pk(a[0][2], a[0][3]), tagB}, r1 = {pk(a[1][0], a[1][1]), ep.lo, pk(a[1][2], a[1][3]), tagB};
        const unsigned vr = head * 8192 + w * 2048 + blk * 1024 + (16 * h + (lane & 15)) * 16;
        stc(st, vr, r0);
        stc(st, vr + 512, r1);
        hold(r0, r1, vr);
      } else {
        __builtin_amdgcn_s_sleep(127);
      }
      unroll<0, 14>(ldwo);
      pre2(0, 4, 6); pre2(4, 10, 0);
      waitv<24>();
      tiewo();
      pre2rd();
      pre2w();
      pre2(10, 14, 6);
    }
  } else {
    const unsigned sxa = sx0;
    dma4(rsX, vxa, sxa, ring);
    u32x2 gen = ldc2(rsF, vz);
    SB();
    unsigned sink = 0;
    const u32x4 rsW = mkrs(wqkv, D * 3 * D * 2);
    {
      const unsigned r6 = (lane * 43) >> 8, c6 = lane - 6 * r6;
      touch(sink, rsW, lane < 42 ? r6 * 2688 + 896 + (c6 & 1) * 896 + (c6 >> 1) * 108 : OOB, (28 * ja + 7 * w) * 2688 + (xc & 6) * 112);
    }
    dma4(rsX, vxa, sxa + 128, ring + 4096);
    dma4(rsX, vxa, sxa + 256, ring + 8192);
    unsigned vw[8];
#pragma unroll
    for (int s = 0; s < 8; s++) {
      const unsigned c = (s < 7 ? 256 * s + 64 * w : 3072) + lane, r = (c * 9363) >> 16;
      vw[s] = ((r & ~15u) + 8 * ((r >> 1) & 1) + 4 * (r & 1) + ((r >> 2) & 1) + 2 * ((r >> 3) & 1)) * 2688 + (c - 7 * r) * 16;
    }
    const unsigned mw = lds0 + w * 1024;
    auto mW = [&](auto ss, auto bb) {
      if constexpr (ss.value < 12) addm0<bb.value + ss.value * 4096>(mw); else addm0<bb.value + 49152>(lds0);
    };
    auto dmaW = [&](auto ss, unsigned col) {
      constexpr int s = ss.value;
      dma<0>(rsW, vw[s < 12 ? (s + 14) % 7 : 7], col + pr * 112 + (s >= 7 && s < 12 ? 256 * 2688 : 0));
    };
    bf16x8 xa[28];
    float sa = 0.f, sq = 0.f;
    {
      unsigned pr_[4];
#pragma unroll
      for (int k = 0; k < 4; k++) pr_[k] = ring + n * 128 + (((2 * k + h) ^ ((n >> 1) & 7)) * 16);
      unroll<0, 7>([&](auto cc) {
        constexpr int cb = cc.value;
        waitv<(cb == 0 ? 9 : 8)>();
#pragma unroll
        for (int k = 0; k < 4; k++) xa[4 * cb + k] = lld<bf16x8>((ldsp)(uintptr_t)pr_[k] + (cb % 3) * 4096);
        asm volatile("s_waitcnt lgkmcnt(0)" : "+v"(xa[4 * cb]), "+v"(xa[4 * cb + 1]), "+v"(xa[4 * cb + 2]), "+v"(xa[4 * cb + 3])::"memory");
        if constexpr (cb < 4) dma4(rsX, vxa, sxa + (cb + 3) * 128, ring + (cb % 3) * 4096);
        if constexpr (cb == 4) unroll<0, 4>([&](auto ss) { mW(ss, IC<L_WK>()); asm volatile("s_nop 0"); dmaW(ss, 896); });
        if constexpr (cb == 5) unroll<0, 4>([&](auto ss) { mW(ss, IC<L_WV>()); asm volatile("s_nop 0"); dmaW(ss, 1792); });
        if constexpr (cb == 6) {
          setm0(lds0 + L_WPHI + w * 1024);
          dma<0>(mkrs(wphi + pr * 1568, 3136), tid * 16, 0);
        }
        SB();
#pragma unroll
        for (int k = 0; k < 4; k++) acc_stats(xa[4 * cb + k], sa, sq);
        SB();
      });
    }
    hold(vxa[0], vxa[1], vxa[2], vxa[3], vz);
    tie(gen);
    setep(gen);
    float la, lc;
    {
      float s2 = sa, q2 = sq;
      swap32(sa, s2); swap32(sq, q2);
      const float mu = (sa + s2) * (1.f / D), var = fmaxf((sq + q2) * (1.f / D) - mu * mu, 0.f);
      la = __builtin_amdgcn_rsqf(var + 1e-5f);
      lc = -mu * la;
    }
    auto norm = [&](auto kk, auto ee) {
      constexpr int ks = kk.value, e = ee.value;
      if constexpr (ks < 28) {
        u32x4 u = __builtin_bit_cast(u32x4, xa[ks]);
        u[e] = affine2(u[e], la, lc);
        xa[ks] = __builtin_bit_cast(bf16x8, u);
      }
    };
    unroll<0, 4>([&](auto ee) { norm(IC<0>(), ee); });
    norm(IC<1>(), IC<0>());
    tie(xa[0]); tie(xa[1]);
    waitv<1>();
    bar();
    const unsigned vt2 = (lane >> 3) * (S * 2) + (lane & 7) * 128;
    const unsigned st2t = (448 * w + 8 * ja) * (S * 2) + (xc >> 1) * 1024;
    auto touch2 = [&](int m) { touch(sink, rsW2, vt2, st2t + 128 * m * (S * 2)); };
    const unsigned vtq = lane < 28 && (lane & 3) < 3 ? (lane >> 2) * 2688 + (lane & 3) * 108 : OOB;
    const unsigned vtx = xc < 7 && (lane & 7) < 7 ? (lane >> 3) * (D * 2) + (lane & 7) * 128 : OOB;
    const unsigned stx = (512 * ((2 * ja + (w >> 1)) & 3) + stage0(8 + (ja >> 1)) + 16 * (w & 1)) * (D * 2);
    const unsigned wlane = (2 * h + 4 * q) * 112 + 32 * blk + 8 * pp;
    f32x16 kT[2], vv[2];
    {
      const ldsp pk_ = lds + L_WK + wlane, pv_ = lds + L_WV + wlane;
      s16x4 fr[30][8];
      auto rd = [&](auto kk, auto cc) {
        constexpr int ks = kk.value, c = cc.value;
        if constexpr (ks < 28) fr[ks][c] = vtr(((c & 4) ? pv_ : pk_) + ks * 1792 + ((c >> 1) & 1) * 56 + (c & 1) * 112);
      };
      unroll<0, 8>([&](auto cc) { rd(IC<0>(), cc); });
      unroll<0, 8>([&](auto cc) { rd(IC<1>(), cc); });
      SB();
      unroll<0, 28>([&](auto kk) {
        constexpr int ks = kk.value;
        if constexpr (ks == 7) { waitv<6>(); asm volatile("s_barrier" ::: "memory"); }
        if constexpr (ks == 16) { waitv<4>(); asm volatile("s_barrier" ::: "memory"); }
        __builtin_amdgcn_s_waitcnt(ks < 27 ? 0xc87f : 0xc07f);
        SB();
        unroll<0, 4>([&](auto mm) {
          constexpr int m = mm.value;
          const bf16x8 wv = cat(fr[ks][2 * m], fr[ks][2 * m + 1]);
          if constexpr (m < 2) mfmab<ks == 0>(kT[m], wv, xa[ks]);
          else mfmab<ks == 0>(vv[m - 2], xa[ks], wv);
          SB();
          if constexpr (ks < 9 && m == 0) mW(IC<4 + ks>(), IC<L_WK>());
          if constexpr (ks < 9 && m == 2) mW(IC<4 + ks>(), IC<L_WV>());
          rd(IC<ks + 2>(), IC<2 * m>());
          rd(IC<ks + 2>(), IC<2 * m + 1>());
          SB();
          if constexpr (ks < 9 && !(m & 1)) dmaW(IC<4 + ks>(), m ? 1792 : 896);
          if constexpr (ks == 9 && m == 0) touch(sink, rsW1, vt1, so_t1(8 + (ja >> 1), (2 * ja + (w >> 1)) & 3, 16 * (w & 1)));
          if constexpr (ks == 10 && m == 0) touch(sink, rsX, vtx, stx);
          if constexpr (ks == 11 && m == 0) touch(sink, rsX, vtx, stx + 8 * D * 2);
          if constexpr (ks == 14 && m == 0) touch(sink, rsW, vtq, (28 * ja + 7 * w) * 2688 + (xc & 6) * 112);
          if constexpr (ks == 16 && m == 0) hold(vtx);
          if constexpr (ks == 19 && m == 0) hold(vtq);
          if constexpr (ks == TA0 && m == 0) touch2(0);
          SB();
          norm(IC<ks + 1 + (m == 3)>(), IC<(m + 1) & 3>());
          SB();
        });
      });
    }
    bf16x8 wf[2][2];
    {
      const ldsp p = lds + L_WPHI + (4 * h + q) * 56 + 32 * blk + 8 * pp;
#pragma unroll
      for (int hd = 0; hd < 2; hd++) {
        wf[hd][0] = cat(vtr(p + hd * 1568), vtr(p + hd * 1568 + 8 * 56));
        s16x4 z = vtr(p + hd * 1568 + 24 * 56);
        wf[hd][1] = cat(vtr(p + hd * 1568 + 16 * 56), h ? (s16x4){0, 0, 0, 0} : z);
      }
    }
    auto phi = [&](f32x16 (&t)[2], f32x16 (&p)[2]) {
      asm volatile("s_nop 9" : "+v"(t[0]), "+v"(t[1]));
      bf16x8 b[4] = {pack8(t[0], 0), pack8(t[0], 8), pack8(t[1], 0), pack8(t[1], 8)};
      asm volatile("s_nop 1" : "+v"(b[0]), "+v"(b[1]), "+v"(b[2]), "+v"(b[3]), "+v"(wf[0][1]), "+v"(wf[1][1]));
      mfmab<true>(p[0], wf[0][0], b[0]); mfmab<false>(p[0], wf[0][1], b[1]);
      mfmab<true>(p[1], wf[1][0], b[2]); mfmab<false>(p[1], wf[1][1], b[3]);
    };
    {
      f32x16 p[2];
      phi(kT, p);
      asm volatile("s_waitcnt lgkmcnt(0)\n\ts_barrier" : "+v"(vv[0]), "+v"(vv[1])::"memory");
      {
        const bf16x8 ones = {0x3f80, 0x3f80, 0x3f80, 0x3f80, 0x3f80, 0x3f80, 0x3f80, 0x3f80};
        const ldsp pvx = lds + L_VX + w * 2048 + lane * 16;
#pragma unroll
        for (int hd = 0; hd < 2; hd++)
#pragma unroll
          for (int j = 0; j < 2; j++) { bf16x8 f = pack8(vv[hd], 8 * j); lst(pvx + hd * 8192 + j * 1024, n == 28 ? ones : f); }
      }
      mW(IC<0>(), IC<L_WQ>());
      asm volatile("" : "+v"(p[0]), "+v"(p[1])::"memory");
      const unsigned pb = (32 * w + n) * 64 + ((h ^ ((n >> 1) & 7)) * 8);
      unroll<0, 2>([&](auto hh) {
        constexpr int hd = hh.value;
        unsigned ph[2][8];
        features(p[hd], h, ph, [&](auto kk, auto& pin) {
          constexpr int s = 12 * hd + kk.value;
          if constexpr (s < 13) {
            dmap<0>(rsW, vw[s < 12 ? s % 7 : 7], pr * 112 + (s >= 7 && s < 12 ? 256 * 2688 : 0), pin);
            if constexpr (s < 12) mW(IC<s + 1>(), IC<L_WQ>());
          }
          if constexpr (s == TA1) touchp(sink, rsW2, vt2, st2t + 128 * (S * 2), pin);
        });
#pragma unroll
        for (int sg = 0; sg < 2; sg++)
#pragma unroll
          for (int g = 0; g < 4; g++) lst(lds + L_PHI + (hd * 2 + sg) * 8192 + (pb ^ (g << 4)), (u32x2){ph[sg][2 * g], ph[sg][2 * g + 1]});
      });
    }
    hold(vw[0], vw[1], vw[2], vw[3], vw[4], vw[5], vw[6], vw[7], vt1, vt2);
    bar();
    u32x4 kvp[2];
    const unsigned vkv = (2 * pr + (w >> 1)) * 65536 + T * 4096 + (w & 1) * 2048 + lane * 16;
    {
      const unsigned sw = 2 * h + (q >> 1);
      const ldsp pl = lds + L_PHI + w * 8192 + (4 * h + q) * 64 + (((4 * blk + pp) ^ sw) * 8);
      const ldsp ph_ = lds + L_PHI + w * 8192 + (8 + 4 * h + q) * 64 + (((4 * blk + pp) ^ sw ^ 4) * 8);
      const ldsp pvx = lds + L_VX + (w >> 1) * 8192 + lane * 16;
      f32x16 kv;
      bf16x8 ka_[8], kb_[8];
#pragma unroll
      for (int jj = 0; jj < 8; jj++) { ka_[jj] = cat(vtr(pl + jj * 1024), vtr(ph_ + jj * 1024)); kb_[jj] = lld<bf16x8>(pvx + jj * 1024); SB(); }
      unroll<0, 8>([&](auto jj) { mfmab<jj.value == 0>(kv, ka_[jj.value], kb_[jj.value]); });
      asm volatile("s_nop 11" : "+v"(kv));
      const rsrc_t st = mkst(ws + WS_KVP, SZ_KVP);
      kvp[0] = __builtin_bit_cast(u32x4, pack8(kv, 0)); kvp[1] = __builtin_bit_cast(u32x4, pack8(kv, 8));
      stc(st, vkv, kvp[0]);
      stc(st, vkv + 1024, kvp[1]);
    }
    asm volatile("s_waitcnt vmcnt(3)\n\ts_barrier" ::: "memory");
    const unsigned vfp = tid == 0 ? (FL_P + pr * 16 + T) * 128 : OOB;
    u32x2 epv = {ep.lo, ep.hi};
    tie(epv);
    f32x16 qT[2];
    {
      const ldsp pq_ = lds + L_WQ + wlane;
      s16x4 fr[29][4];
      auto rd = [&](auto kk, auto cc) {
        constexpr int ks = kk.value, c = cc.value;
        if constexpr (ks < 28) fr[ks][c] = vtr(pq_ + ks * 1792 + (c >> 1) * 56 + (c & 1) * 112);
      };
      unroll<0, 4>([&](auto cc) { rd(std::integral_constant<int, 0>(), cc); });
      unroll<0, 28>([&](auto kk) {
        constexpr int ks = kk.value;
        if constexpr (ks == QP) { hold(kvp[0], kvp[1], vkv); publish<0>(rsF, vfp, epv); SB(); }
        if constexpr (ks == QP + 2) { touch2(2); SB(); }
        if constexpr (ks == QP + 6) hold(vfp, vt2);
        unroll<0, 2>([&](auto mm) {
          constexpr int m = mm.value;
          rd(std::integral_constant<int, ks + 1>(), std::integral_constant<int, 2 * m>());
          rd(std::integral_constant<int, ks + 1>(), std::integral_constant<int, 2 * m + 1>());
          mfmab<ks == 0>(qT[m], cat(fr[ks][2 * m], fr[ks][2 * m + 1]), xa[ks]);
          SB();
        });
      });
    }
    unsigned phq[2][2][8];
    {
      f32x16 p[2];
      asm volatile("s_nop 1" : "+v"(qT[0]), "+v"(qT[1]));
      phi(qT, p);
      setm0n(area + 6 * 4096);
      asm volatile("s_waitcnt lgkmcnt(0)\n\ts_barrier\n\ts_nop 9" : "+v"(p[0]), "+v"(p[1])::"memory");
      unroll<0, 2>([&](auto hh) {
        features(p[hh.value], h, phq[hh.value], [&](auto kk, auto& pin) {
          constexpr int k = 12 * hh.value + kk.value, t = k >> 2;
          dmap<(k & 3) * 1024>(rsW2, v2[k & 3], so2 + t * (32 * S * 2), pin);
          if constexpr ((k & 3) == 3 && k < 23) setm0n(area + ((t + 7) % 10) * 4096);
        });
      });
    }
    u32x4 fq[2][4];
#pragma unroll
    for (int c = 0; c < 8; c++) {
      const unsigned* s = &phq[c >> 2][(c >> 1) & 1][4 * (c & 1)];
      fq[c >> 2][c & 3] = (u32x4){s[0], s[1], s[2], s[3]};
      tie(fq[c >> 2][c & 3]);
    }
    {
      const u32x4 rsK = mkrs(ws + WS_KVF, SZ_KVF);
      const unsigned so = pr * 16384 + w * 2048, tagB = ep.lo ^ ep.hi, vk = lane * 16;
      const ldsp px = lds + 9 * 4096 + lane * 16;
      bf16x8 g0, g1, g2, g3;
      auto fetch = [&]() { g0 = ldc<0>(rsK, vk, so); g1 = ldc<1024>(rsK, vk, so); g2 = ldc<0>(rsK, vk, so + 8192); g3 = ldc<1024>(rsK, vk, so + 8192); };
      auto check = [&]() {
        const u32x4 u0 = __builtin_bit_cast(u32x4, g0), u1 = __builtin_bit_cast(u32x4, g1), u2 = __builtin_bit_cast(u32x4, g2), u3 = __builtin_bit_cast(u32x4, g3);
        lst(px + w * AREA, (u32x4){u0[0], u0[2], u1[0], u1[2]});
        lst(px + w * AREA + 1024, (u32x4){u2[0], u2[2], u3[0], u3[2]});
        const unsigned bad = ((u0[1] ^ ep.lo) | (u1[1] ^ ep.lo) | (u2[1] ^ ep.lo) | (u3[1] ^ ep.lo)) | ((u0[3] ^ tagB) | (u1[3] ^ tagB) | (u2[3] ^ tagB) | (u3[3] ^ tagB));
        return (bool)__all(bad == 0);
      };
      waitv<8>();
      pre2rd();
      pre2(6, 9, 2);
      unroll<0, FW>(ldwo);
      fetch();
      unroll<FW, 14>(ldwo);
      waitv<14 - FW>(g0, g1, g2, g3);
      if (!check()) {
#pragma unroll 1
        for (int it = 0; it < SPIN; ++it) {
          fetch();
          waitv<0>(g0, g1, g2, g3);
          if (check()) break;
        }
      }
      tie(sink);
      pre2w();
      bar();
      bf16x8 kf[2][4];
#pragma unroll
      for (int hd = 0; hd < 2; hd++)
#pragma unroll
        for (int c = 0; c < 4; c++) kf[hd][c] = lld<bf16x8>(px + c * AREA + hd * 1024);
      SB();
      unroll<0, DE>([&](auto tt) { dma4(rsW2, v2, so2 + (9 + tt.value) * (32 * S * 2), area + (5 + tt.value) * 4096); });
      SB();
      f32x16 o[2];
      unroll<0, 8>([&](auto mm) {
        constexpr int m = mm.value, hd = m & 1, c = m >> 1;
        if constexpr (c == 0) mfma0(o[hd], kf[hd][c], fq[hd][c]); else mfmac(o[hd], kf[hd][c], fq[hd][c]);
        SB();
      });
      asm volatile("s_nop 11" : "+v"(o[0]), "+v"(o[1]));
      float da = o[0][12], db = o[1][12];
      swap32(da, db);
      float ia = __builtin_amdgcn_rcpf(db + 1e-6f), ib = ia;
      swap32(ia, ib);
      unsigned a[8], bq[8];
#pragma unroll
      for (int r = 0; r < 8; r++) { a[r] = pk(o[0][2 * r] * ia, o[0][2 * r + 1] * ia); bq[r] = pk(o[1][2 * r] * ib, o[1][2 * r + 1] * ib); }
      const rsrc_t st = mkst(ws + WS_OF, SZ_OF);
      const unsigned tk = (128 * T + 32 * w + n) * 16, jb = 14 * (pr >> 1);
      const unsigned vo = (jb + 6 * (pr & 1) + h) * (S * 16) + tk;
      const unsigned v1 = vo + 2 * S * 16, v2_ = vo + 4 * S * 16, v3 = h ? OOB : (jb + 12 + (pr & 1)) * (S * 16) + tk;
      const u32x4 r0 = {a[0], a[1], a[2], a[3]}, r1 = {a[4], a[5], bq[0], bq[1]}, r2 = {bq[2], bq[3], bq[4], bq[5]}, r3 = {a[6], a[7], bq[6], bq[7]};
      stc(st, vo, r0); stc(st, v1, r1); stc(st, v2_, r2); stc(st, v3, r3);
      unroll<DE, 4>([&](auto tt) { dma4(rsW2, v2, so2 + (9 + tt.value) * (32 * S * 2), area + (5 + tt.value) * 4096); });
      u32x2 f0 = ldc2(rsF, vfu);
      const unsigned vfo = tid == 0 ? (FL_O + T * 8 + pr) * 128 : OOB;
      publish<0>(rsF, vfo, epv);
      tie(f0);
      tiewo();
      hold(vk);
      hold(vo, v1, v2_, v3, r0, r1, r2, r3);
      miss = missing<4>(f0, ep);
      td = 13;
      hold(vfo, epv);
    }
  }

  if (i >= 4) {
    const unsigned orow = st2 * 64 + 16 * w + (lane & 15);
    const unsigned vo = orow * (D * 2) + dt2 * 128 + (lane >> 4) * 16;
    const u32x4 rsB = mkrs(b2, S * 2), rsU = mkrs(ws + WS_UF, SZ_UF), rsO = mkrs(ws + WS_OF, SZ_OF);
#pragma unroll 1
    for (; miss && td < 13; td += 2) {
      u32x2 f = ldc2(rsF, vfu);
      dma4(rsW2, v2, so2 + td * (32 * S * 2), area + (td - 4) * 4096);
      dma4(rsW2, v2, so2 + (td + 1) * (32 * S * 2), area + (td - 3) * 4096);
      waitv<8>(f);
      miss = missing<4>(f, ep);
    }
    if (miss) poll<4>(rsF, vfu, ep);
    const unsigned vu0 = h * (D * 16) + (dt2 * 64 + n) * 16, vu1 = vu0 + 2 * D * 16, sou = w * 56 * (D * 16);
    const unsigned vof = h * (S * 16) + (st2 * 64 + n) * 16, soo = w * 14 * (S * 16);
    const unsigned vfl = lane < 2 ? (FL_O + (st2 >> 1) * 8 + 2 * w + lane) * 128 : OOB;
    bf16x8 fa[18][4], xr[2];
    unsigned bs, one, sel[4], pr_[2];
    float bv, yv[16];
    const unsigned vb2 = orow * 2;
    u32x2 fl;
    auto ldD = [&](auto tt, auto jj) {
      constexpr int t = tt.value, j = jj.value;
      if constexpr (t < 14) fa[t][j] = ldc<(j & 1) * 512>(rsU, (j >> 1) ? vu1 : vu0, sou + t * (4 * D * 16));
      else if constexpr (j < g2ld(t)) fa[t][j] = ldc<(j & 1) * 512>(rsO, vof, soo + (2 * (t - 14) + (j >> 1)) * (2 * S * 16));
    };
    unroll<0, G2R>([&](auto tt) { unroll<0, 4>([&](auto jj) { ldD(tt, jj); }); });
    dman(rsW2, v2, so2 + td * (32 * S * 2), 32 * S * 2, area + (td - 4) * 4096, 14 - td);
    const ldsp pw[2] = {pb[0] - h * 512, pb[1] - h * 512};
    f32x16 acc[2][2] = {};
    bf16x8 wb[19][2][2];
    auto rdB = [&](auto tt, auto kk, auto aa) {
      constexpr int t = tt.value, ks = kk.value, a = aa.value, j = 2 * (t - 14) + ks;
      if constexpr (t < 4) wb[t][ks][a] = w2r[t][ks][a];
      else if constexpr (t < 14) wb[t][ks][a] = cat(vtr(pb[a] + (t - 4) * 4096 + ks * 2048), vtr(pb[a] + (t - 4) * 4096 + ks * 2048 + 512));
      else if constexpr (j < 6) {
        constexpr int u = j % 3, rl = 56 * (j / 3) + (u == 0 ? 0 : u == 1 ? 16 : 36), rh = 56 * (j / 3) + (u == 0 ? 8 : u == 1 ? 28 : 44);
        wb[t][ks][a] = cat(vtr(pw[a] + rl * 128), vtr(pw[a] + rh * 128));
      } else if constexpr (j == 6) wb[t][ks][a] = cat(vtr(pw[a] + h * (52 * 128) + 24 * 128), vtr(pw[a] + h * (52 * 128) + 52 * 128));
    };
    unroll<0, 4>([&](auto cc) { rdB(IC<0>(), IC<(cc.value >> 1)>(), IC<(cc.value & 1)>()); });
    unroll<0, 18>([&](auto tt) {
      constexpr int t = tt.value, nk = t < 17 ? 2 : 1;
      constexpr int left = g2cnt(t - 3) + g2cnt(t - 2) + g2cnt(t - 1);
      if constexpr (nk == 2) waitv<left>(fa[t][0], fa[t][1], fa[t][2], fa[t][3]); else waitv<left>(fa[t][0], fa[t][1]);
      if constexpr (t == 3) hold(vfu);
      if constexpr (t == 14) { one = ((lane >> 3) & 1) == blk ? ((lane & 1) ? 0x3f800000u : 0x3f80u) : 0u; tie(one); }
      if constexpr (t == 16) { tie(xr[0]); tie(xr[1]); tie(bs); hold(vb2, vof); }
      if constexpr (t == G2POLL + G2R) {
        tie(fl);
        if (missing<2>(fl, ep)) poll<2>(rsF, vfl, ep);
      }
      SB();
      unroll<0, 4 * nk>([&](auto mm) {
        constexpr int m = mm.value, ks = m >> 2, nh = (m >> 1) & 1, mh = m & 1;
        if constexpr (t < 14) acc[nh][mh] = MFMA(fa[t][ks * 2 + mh], wb[t][ks][nh], acc[nh][mh]);
        else acc[nh][mh] = MFMA(wb[t][ks][mh], fa[t][ks * 2 + nh], acc[nh][mh]);
        SB();
        if constexpr (m & 1) rdB(IC<t + 1>(), IC<(m >> 2)>(), IC<((m >> 1) & 1)>());
        else if constexpr (t >= 5 && 4 * (t - 5) + m / 2 < 14) sta<(4 * (t - 5) + m / 2) * 1024>(area + lane * 16, wor[4 * (t - 5) + m / 2]);
        SB();
        if constexpr (!(m & 1)) ldD(IC<t + G2R>(), IC<m / 2>());
        if constexpr (t == G2POLL && m == 7) fl = ldc2(rsF, vfl);
        if constexpr (t == 12 && m == 0) xr[0] = ldp<0>(rsX, vo, 0);
        if constexpr (t == 12 && m == 2) xr[1] = ldp<64>(rsX, vo, 0);
        if constexpr (t == 12 && m == 4) bs = ldu16<0>(rsB, vb2, 0);
        if constexpr (t == 15 && m < 4) { sel[m] = ((lane & 7) >> 1) == m ? one : 0u; tie(sel[m]); }
        if constexpr (t == 15 && m >= 6) { pr_[m - 6] = lds0 + (h + 2 * (m - 6)) * AREA + (w >> 1) * 4096 + blk * 1024 + (16 * (w & 1) + (lane & 15)) * 16; tie(pr_[m - 6]); }
        if constexpr (t == 16 && m == 0) { bv = blo(bs); tie(bv); }
        if constexpr (t == 16) {
          const u32x4 xu = __builtin_bit_cast(u32x4, xr[m >> 2]);
          const unsigned xe = xu[m & 3];
          yv[2 * m] = blo(xe) + bv; yv[2 * m + 1] = bhi(xe) + bv;
          tie(yv[2 * m]); tie(yv[2 * m + 1]);
        }
        SB();
      });
    });
    bf16x8 pt[8];
#pragma unroll
    for (int c = 0; c < 8; c++) {
      pt[c] = pack8s(acc[c >> 2][(c >> 1) & 1], 8 * (c & 1));
      lst(lds + w * AREA + c * 1024 + lane * 16, pt[c]);
      if (c) hold(pt[c - 1]);
      SB();
    }
    f32x4 y[2][2];
#pragma unroll
    for (int c = 0; c < 4; c++) y[c >> 1][c & 1] = (f32x4){yv[4 * c], yv[4 * c + 1], yv[4 * c + 2], yv[4 * c + 3]};
    u32x4 id = {sel[0], sel[1], sel[2], sel[3]};
    asm volatile("s_waitcnt lgkmcnt(0)\n\ts_barrier" : "+v"(y[0][0]), "+v"(y[0][1]), "+v"(y[1][0]), "+v"(y[1][1]), "+v"(id)::"memory");
#pragma unroll
    for (int c = 0; c < 8; c++) { pt[c] = lld<bf16x8>((ldsp)(uintptr_t)pr_[c >> 2] + ((c >> 1) & 1) * 2048 + (c & 1) * 512); SB(); }
#pragma unroll
    for (int c = 0; c < 8; c++) asm volatile("v_mfma_f32_16x16x32_bf16 %0, %1, %2, %0" : "+v"(y[(c >> 1) & 1][c & 1]) : "v"(id), "v"(pt[c]));
    asm volatile("s_nop 7" : "+v"(y[0][0]), "+v"(y[0][1]), "+v"(y[1][0]), "+v"(y[1][1]));
    u32x4 res[2];
    const rsrc_t sto = mkst(out, S * D * 2);
#pragma unroll
    for (int u = 0; u < 2; u++) {
      res[u] = (u32x4){pk(y[u][0][0], y[u][0][1]), pk(y[u][0][2], y[u][0][3]), pk(y[u][1][0], y[u][1][1]), pk(y[u][1][2], y[u][1][3])};
      __builtin_amdgcn_raw_buffer_store_b128(res[u], sto, vo + u * 64, 0, 16);
    }
    if (blockIdx.x == 255 && tid == 0) __builtin_amdgcn_raw_buffer_store_b64((u32x2){ep.lo, ep.hi}, mkst(ws, WS_FLAG_BYTES), 0, 0, 16);
    hold(res[0], res[1], vo);
  }
}

extern "C" void kernel_launch(void* const* d_in, const int* in_sizes, int n_in, void* d_out, int out_size, void* d_ws, size_t ws_size,
                              hipStream_t stream) {
  fused_kernel<<<256, 256, 0, stream>>>((const short*)d_in[0], (const short*)d_in[1], (const short*)d_in[2], (const short*)d_in[3],
                                        (const short*)d_in[5], (const short*)d_in[6], (const short*)d_in[7], (const short*)d_in[8],
                                        (short*)d_out, (char*)d_ws);
}
```
